```python
import torch

def _softmax(x, axis=-1):
    return torch.softmax(x, dim=axis)
def _gelu(x):
    return torch.nn.functional.gelu(x, approximate="tanh")
def _silu(x):
    return torch.nn.functional.silu(x)
def _sigmoid(x):
    return torch.sigmoid(x)
def _layernorm(x, axis=-1, eps=1e-5):
    m = x.mean(dim=axis, keepdim=True)
    v = x.var(dim=axis, keepdim=True, unbiased=False)
    return (x - m) / torch.sqrt(v + eps)
def _scatter_add(tgt, idx, src, axis=0):
    return tgt.clone().index_add_(axis, idx, src)
def _segment_sum(src, seg, n):
    out = torch.zeros((n,) + src.shape[1:], dtype=src.dtype)
    return out.index_add_(0, seg, src)
def _pad(x, pw):
    flat = tuple(v for pair in reversed(pw) for v in pair)
    return torch.nn.functional.pad(x, flat)

kernel_name = 'recurrent_scan__ssm_conv_s4__moe_arch_mod__prompt'

def setup_inputs():

    _w = (lambda a: (lambda t: t.to(torch.bfloat16) if t.is_floating_point() else t)(torch.as_tensor(a)))
    def _A_setup():
        B, L, D, N = 1, 2048, 576, 128
        A_re = -(lambda _x: torch.exp(torch.as_tensor(_x)))((lambda *s: torch.randn(*s, dtype=torch.bfloat16) * 6.4)(D, N))
        A_im = torch.pi * (lambda *s: torch.randn(*s, dtype=torch.bfloat16) * 0.05)(D, N)
        B_re = (lambda *s: torch.randn(*s, dtype=torch.bfloat16) * 0.1)(D, N)
        B_im = (lambda *s: torch.randn(*s, dtype=torch.bfloat16) * 0.1)(D, N)
        C_re = (lambda *s: torch.randn(*s, dtype=torch.bfloat16) * 0.1)(D, N)
        C_im = (lambda *s: torch.randn(*s, dtype=torch.bfloat16) * 0.1)(D, N)
        log_dt = (lambda *s: torch.randn(*s, dtype=torch.bfloat16) * 0.05)(D) * 0.5 - 4.0
        Dskip = (lambda *s: torch.randn(*s, dtype=torch.bfloat16) * 0.05)(D)
        return {
            "u": _w((lambda *s: torch.randn(*s, dtype=torch.bfloat16) * 0.05)(B, L, D)),
            "A_re": _w(A_re),
            "A_im": _w(A_im),
            "B_re": _w(B_re),
            "B_im": _w(B_im),
            "C_re": _w(C_re),
            "C_im": _w(C_im),
            "log_dt": _w(log_dt),
            "Dskip": _w(Dskip),
        }
    _A_in = _A_setup()

    def _B_setup():
        B, S, D, F, C = 1, 2048, 576, 4608, 8
        return {
            "x": _w((lambda *s: torch.randn(*s, dtype=torch.bfloat16) * 0.05)(B, S, D)),
            "w_r": _w((lambda *s: torch.randn(*s, dtype=torch.bfloat16) * 0.05)(D)),
            "w_gate": _w((lambda *s: torch.randn(*s, dtype=torch.bfloat16) * 0.05)(D, F)),
            "w_up": _w((lambda *s: torch.randn(*s, dtype=torch.bfloat16) * 0.05)(D, F)),
            "w_down": _w((lambda *s: torch.randn(*s, dtype=torch.bfloat16) * 0.05)(F, D)),
        }
    _B_in = _B_setup()

    return {
        "A_u": _A_in["u"],
        "A_A_re": _A_in["A_re"],
        "A_A_im": _A_in["A_im"],
        "A_B_re": _A_in["B_re"],
        "A_B_im": _A_in["B_im"],
        "A_C_re": _A_in["C_re"],
        "A_C_im": _A_in["C_im"],
        "A_log_dt": _A_in["log_dt"],
        "A_Dskip": _A_in["Dskip"],
        "B_x": _B_in["x"],
        "B_w_r": _B_in["w_r"],
        "B_w_gate": _B_in["w_gate"],
        "B_w_up": _B_in["w_up"],
        "B_w_down": _B_in["w_down"],
    }

def reference(A_u, A_A_re, A_A_im, A_B_re, A_B_im, A_C_re, A_C_im, A_log_dt, A_Dskip, B_x, B_w_r, B_w_gate, B_w_up, B_w_down):
    def _A_ref(u, A_re, A_im, B_re, B_im, C_re, C_im, log_dt, Dskip):
        Bsz, L, D = u.shape
        dt = torch.exp(log_dt)[:, None]
        A = A_re + 1j * A_im
        Bc = B_re + 1j * B_im
        Cc = C_re + 1j * C_im
        logdA = dt * A
        dB = dt * Bc
        l = (lambda x, dt: x.to(dt))(torch.arange(L), A_re.dtype)
        P = torch.exp(logdA[:, :, None] * l[None, None, :])
        K = torch.einsum("dn,dnl,dn->dl", Cc, P, dB).real
        L2 = 2 * L
        Kp = _pad(K, ((0, 0), (0, L)))
        up = (lambda x, *d: x.permute(*d))(u, 0, 2, 1)
        up = _pad(up, ((0, 0), (0, 0), (0, L)))
        Kf = (lambda x, axes=(-2,-1): torch.fft.rfft2(x.float(), dim=axes))(Kp[:, None, :], axes=(-2, -1))
        Uf = (lambda x, axes=(-2,-1): torch.fft.rfft2(x.float(), dim=axes))(up[:, :, None, :], axes=(-2, -1))
        Yf = Uf * Kf[None, :, :, :]
        y = (lambda x, s, axes=(-2,-1): torch.fft.irfft2(x, s=s, dim=axes).to(torch.bfloat16))(Yf, (1, L2), axes=(-2, -1))[:, :, 0, :L]
        y = (lambda x, *d: x.permute(*d))(y, 0, 2, 1) + u * Dskip[None, None, :]
        return y

    def _B_ref(x, w_r, w_gate, w_up, w_down):
        B, S, D = x.shape
        C = 8
        r = x @ w_r
        idx = torch.argsort(-r, axis=-1)[:, :C]
        idx_e = (lambda x, n, axis: torch.repeat_interleave(x, n, dim=axis))(idx.reshape(B, C, 1), D, axis=2)
        x_sel = (lambda x, i, axis: torch.take_along_dim(x, i, dim=axis))(x, idx_e, axis=1)
        g = _silu(x_sel @ w_gate)
        u = x_sel @ w_up
        y = (g * u) @ w_down
        r_sel = _sigmoid((lambda x, i, axis: torch.take_along_dim(x, i, dim=axis))(r, idx, axis=1))
        y = y * r_sel.reshape(B, C, 1)
        b_off = (torch.arange(B) * S).reshape(B, 1)
        flat_idx = (idx + b_off).reshape(B * C)
        zero = (x * 0.0).reshape(B * S, D)
        upd = _scatter_add(zero, flat_idx, y.reshape(B * C, D), axis=0)
        return x + upd.reshape(B, S, D)

    x = A_u
    xn = _layernorm(x, axis=-1)
    s = _A_ref(xn, A_A_re, A_A_im, A_B_re, A_B_im, A_C_re, A_C_im, A_log_dt, A_Dskip)
    g = _silu(_B_ref(xn, B_w_r, B_w_gate, B_w_up, B_w_down))
    return x + s * g
```

```cpp
#include <hip/hip_runtime.h>
#include <hip/hip_bf16.h>
#include <stdint.h>
#if defined(__HIP_DEVICE_COMPILE__)
#define NOPK __attribute__((target("no-packed-fp32-ops")))
#else
#define NOPK
#endif
#define DEV __device__ NOPK __forceinline__
typedef unsigned short u16;
typedef unsigned int u32;
typedef unsigned long long u64;
typedef short bf16x8 __attribute__((ext_vector_type(8)));
typedef _Float16 h8 __attribute__((ext_vector_type(8)));
typedef _Float16 h2 __attribute__((ext_vector_type(2)));
typedef float f32x4 __attribute__((ext_vector_type(4)));
typedef float f32x2 __attribute__((ext_vector_type(2)));
typedef u32 u32x4 __attribute__((ext_vector_type(4)));
typedef u32 u32x2 __attribute__((ext_vector_type(2)));
typedef __bf16 bf2 __attribute__((ext_vector_type(2)));
typedef short v4s __attribute__((ext_vector_type(4)));
typedef __amdgpu_buffer_rsrc_t rsrc_t;
#define GAS __attribute__((address_space(1)))
#define LAS __attribute__((address_space(3)))
typedef LAS char lch;
#define L_ 2048
#define D_ 576
#define F_ 4608
#define BC(T,v) __builtin_bit_cast(T,v)
#define TID ((int)__builtin_amdgcn_workitem_id_x())
#define BID ((int)__builtin_amdgcn_workgroup_id_x())
#define RFL(x) __builtin_amdgcn_readfirstlane(x)
#define RLX_AGENT __ATOMIC_RELAXED, __HIP_MEMORY_SCOPE_AGENT
#define LD(T,p) (*(const LAS T*)(p))
#define ST(T,p) (*(LAS T*)(p))
#define SPINCAP (1<<17)
#define DRAIN() asm volatile("s_waitcnt vmcnt(0)" ::: "memory")
#define GLD(p) __hip_atomic_load((const GAS u64*)(p),RLX_AGENT)
#define GST(p,v) __hip_atomic_store((GAS u64*)(p),(u64)(v),RLX_AGENT)
#define BALLOT(c) __builtin_amdgcn_ballot_w64(c)
#define SC1 16
#define LANE(l) int l; { int t2_=TID; asm volatile("" : "+v"(t2_)); l=t2_&63; }
#define SCHED() __builtin_amdgcn_sched_barrier(0)
#define RSRC(p,n) __builtin_amdgcn_make_buffer_rsrc((void*)(p),0,(n),0x00020000)
#define BLD128 __builtin_amdgcn_raw_buffer_load_b128
#define BLD32 __builtin_amdgcn_raw_buffer_load_b32
#define BST128 __builtin_amdgcn_raw_buffer_store_b128
#define PRIO(n) __builtin_amdgcn_s_setprio(n)

DEV float b2f(u32 v){ return BC(float,v<<16); }
DEV float blo(u32 v){ return BC(float,v<<16); }
DEV float bhi(u32 v){ return BC(float,v&0xffff0000u); }
DEV u32 pk(float a,float b){ f32x2 v={a,b}; bf2 r=__builtin_convertvector(v,bf2); return BC(u32,r); }
DEV u32 pkh(float a,float b){ f32x2 v={a,b}; h2 r=__builtin_convertvector(v,h2); return BC(u32,r); }
DEV float rb(float f){ return BC(float,pk(f,f)<<16); }
DEV float rcpf_(float x){ return __builtin_amdgcn_rcpf(x); }
DEV float expf_(float x){
  const float L2E=1.4426950216293335f, L2El=1.9259629911266175e-8f; float h=x*L2E; float l=__builtin_fmaf(x,L2E,-h)+x*L2El;
  float e=__builtin_amdgcn_exp2f(h); return __builtin_fmaf(e*l,0.6931471805599453f,e); }
DEV float sigm_f(float x){ return rcpf_(1.f+__builtin_amdgcn_exp2f(-x*1.4426950408889634f)); }
DEV float silu_f(float x){ return x*sigm_f(x); }
DEV u32 umax_(u32 a,u32 b){ return a>b?a:b; }
DEV u32 umin_(u32 a,u32 b){ return a<b?a:b; }

#define YSH 3
#define PRE1 6
#define PRE3 4
#define PRE2 5
#define W1 5
#define W3 3
#define W2_ 4
#define W1B 20
#define W2B 12
#define LAST1 4
#define RGATE 4
#define UGATE 7
#define R2D 12
#define PFN 32
#define CPF 8
#define NRG 8
#define W_EPOCH 0
#define W_RG 16384
#define W_ST (W_RG+32*8192)
#define W_U (W_ST+32768)
#define UST (D_*16+128)
#define YST (L_*2+128)
#define TL 1
#define GST_ ((D_*16<<TL)+128)
#define W_Y (W_U+256*UST)
#define W_PART (W_Y+D_*YST)
#define W_GU (W_PART+8*9*16*256)
#define W_FL 9216
#define W_IDX 1024
#define CTLB 0
struct KP { const u16 *Are,*Aim,*Bre,*Bim,*Cre,*Cim,*ldt,*pad; };

template<int CTRL> DEV float dppf(float v){ return BC(float,__builtin_amdgcn_update_dpp(0u,BC(u32,v),CTRL,0xf,0xf,false)); }
template<int CTRL> DEV u32 dppu(u32 v){ return (u32)__builtin_amdgcn_update_dpp(0u,v,CTRL,0xf,0xf,false); }
DEV float sum16(float v){ v+=dppf<0xB1>(v); v+=dppf<0x4E>(v); v+=dppf<0x141>(v); v+=dppf<0x140>(v); return v; }
DEV float add16x(float v){ auto r=__builtin_amdgcn_permlane16_swap(BC(u32,v),BC(u32,v),false,false); u32 a=r[0], b=r[1]; return BC(float,a)+BC(float,b); }
DEV u32 wmax(u32 v){
  v=umax_(v,(u32)__builtin_amdgcn_update_dpp(0u,v,0x111,0xf,0xf,false));
  v=umax_(v,(u32)__builtin_amdgcn_update_dpp(0u,v,0x112,0xf,0xf,false));
  v=umax_(v,(u32)__builtin_amdgcn_update_dpp(0u,v,0x114,0xf,0xf,false));
  v=umax_(v,(u32)__builtin_amdgcn_update_dpp(0u,v,0x118,0xf,0xf,false));
  v=umax_(v,(u32)__builtin_amdgcn_update_dpp(0u,v,0x142,0xa,0xf,false));
  v=umax_(v,(u32)__builtin_amdgcn_update_dpp(0u,v,0x143,0xc,0xf,false));
  return (u32)__builtin_amdgcn_readlane((int)v,63); }

DEV void garrive(lch* sm,int c){ u32 ad=(u32)(size_t)sm+c*4, one=1;
  asm volatile("s_mov_b64 exec, 1\n\tds_add_u32 %0, %1\n\ts_mov_b64 exec, -1" :: "v"(ad),"v"(one) : "memory"); }
template<int SLP> DEV void gwait(lch* sm,int c,u32 target){ u32 ad=(u32)(size_t)sm+c*4, tv, cnt, sv; target=RFL(target);
  asm volatile("s_mov_b32 %1, 0x20000\n"
    "1:\n\tds_read_b32 %0, %3\n\ts_waitcnt lgkmcnt(0)\n\tv_readfirstlane_b32 %2, %0\n\ts_cmp_ge_u32 %2, %4\n\ts_cbranch_scc1 2f\n\ts_sleep %5\n\ts_sub_u32 %1, %1, 1\n\ts_cbranch_scc0 1b\n"
    "2:" : "=&v"(tv),"=&s"(cnt),"=&s"(sv) : "v"(ad),"s"(target),"i"(SLP) : "memory","scc"); }
template<int ST_,class T> DEV void gwait4(int c,u32 target,const lch* p,T& a,T& b,T& d,T& e){ u32 tv,cnt,sv;
  asm volatile("s_mov_b32 %1, 0x20000\n1:\n\tds_read_b32 %0, %7\n\tds_read_b128 %3, %8\n\tds_read_b128 %4, %8 offset:%10\n\tds_read_b128 %5, %8 offset:%11\n\tds_read_b128 %6, %8 offset:%12\n\ts_waitcnt lgkmcnt(4)\n\t"
    "v_readfirstlane_b32 %2, %0\n\ts_cmp_ge_u32 %2, %9\n\ts_cbranch_scc1 2f\n\ts_sleep 1\n\ts_sub_u32 %1, %1, 1\n\ts_cbranch_scc0 1b\n2:\n\ts_waitcnt lgkmcnt(0)"
    : "=&v"(tv),"=&s"(cnt),"=&s"(sv),"=&v"(a),"=&v"(b),"=&v"(d),"=&v"(e) : "v"(c*4),"v"(p),"i"(target),"i"(ST_),"i"(2*ST_),"i"(3*ST_) : "memory","scc"); }
DEV void gsync(lch* sm,int c,u32 target){ garrive(sm,c); gwait<0>(sm,c,target); }
template<int SH> DEV u64 poll1(const char* p,u64 want){ u64 f=GLD(p);
  _Pragma("unroll 1") for(int sp=0;sp<SPINCAP;sp++){ bool bad=((f^want)>>SH)!=0ull; if(BALLOT(bad)==0ull) break; __builtin_amdgcn_s_sleep(1); if(bad) f=GLD(p); }
  __builtin_amdgcn_fence(__ATOMIC_ACQUIRE,"wavefront"); return f; }
#define STR_(x) #x
#define STR(x) STR_(x)
DEV u32x4 mkrs(const void* p,u32 n){ u64 a=(u64)p; return u32x4{(u32)RFL((int)(u32)a),(u32)RFL((int)((u32)(a>>32)&0xffffu)),n,0x00020000u}; }
#define VMW(n) asm volatile("s_waitcnt vmcnt(%0)" :: "i"(n) : "memory")
#define YSLP 1
#define GSLP 1
#define QSLP 1
#define PSLP 3
#define POLLASM(LOADS,CHECK,K) "s_mov_b32 %[cnt], 0x20000\n1:\n\ts_waitcnt vmcnt(0)\n\t" CHECK "s_cbranch_vccz 9f\n\ts_sleep " STR(K) "\n\ts_mov_b64 exec, vcc\n\t" LOADS \
  "s_mov_b64 exec, -1\n\ts_sub_u32 %[cnt], %[cnt], 1\n\ts_cbranch_scc0 1b\n9:\n\t"
#define LD_DUP2 "buffer_load_dwordx4 v[112:115], %[of], %[rs], 0 offen sc1\n\tbuffer_load_dwordx4 v[116:119], %[of], %[rs], %[so] offen sc1\n\t"
#define MX2(a,b) "v_max3_u32 %[t0], %[t0], v" #a ", v" #b "\n\t"
#define CHK_I0 "v_max_u32 %[t0], v112, v113\n\t" MX2(114,115)
#define CHK_IE "v_cmp_eq_u32 vcc, -1, %[t0]\n\t"
#define PCLOB "memory","vcc","scc"
#define UMASK 0x40074007u
DEV u32 upat(u32 tag,int k){ return ((((tag*0xea959c21u)>>(8*k))&255u)*0x04000801u)&UMASK; }
DEV u32 pkz(float a,float b){ return BC(u32,__builtin_amdgcn_cvt_pkrtz(a,b)); }
#define INV 0xffffffffu
DEV u32 nv(u32 v){ return umin_(v,0xfffeffffu); }
DEV u32x4 nv4(u32x4 v){ return u32x4{nv(v[0]),nv(v[1]),nv(v[2]),nv(v[3])}; }
#define OOR 0x40000000

#define CHSZ 36864
#define O_CH 512
#define O_CND 256
#define SLSZ 38080
#define O_R2 (O_CH+2*SLSZ)
#define O_X2 (O_R2+CHSZ)
#define O_TILE (O_X2+CHSZ)
#define XSTR 1168
#define TSTR2 1216
#define O_ONE (O_TILE+9728+16)
#define O_CAND (O_ONE+1168+48)
#define LDS_TOTAL (O_CAND+128)
#define C_G 0
#define C_U 8
#define C_L 4
#define C_D 5
#define C_X 12
#define C_P 13
#define C_H 14
#define C_I 17
#define C_E 18
#define C_IX 19
#define C_PR 21
#define O_IDX 96
#define O_EXP 144
#define O_RMX 192
#define O_RV 128
#define O_MD 400
#define O_RC (O_CAND+64)

struct C2 { float r,i; };
DEV C2 cmul(C2 a,C2 b){ C2 c; c.r=__builtin_fmaf(a.r,b.r,-(a.i*b.i)); c.i=__builtin_fmaf(a.r,b.i,a.i*b.r); return c; }
DEV h8 ldA(const lch* p){ u32x2 a=*(const volatile LAS u32x2*)p, b=*(const volatile LAS u32x2*)(p+8); return BC(h8,(u32x4{a[0],a[1],b[0],b[1]})); }
DEV bf16x8 trfrag(const lch* p0,const lch* p1){
  u32x2 b0=BC(u32x2,__builtin_amdgcn_ds_read_tr16_b64_v4i16((LAS v4s*)p0)), b1=BC(u32x2,__builtin_amdgcn_ds_read_tr16_b64_v4i16((LAS v4s*)p1));
  return BC(bf16x8,(u32x4{b0[0],b0[1],b1[0],b1[1]})); }

struct FP { float gg[24], bs[24]; };
DEV void final_pre(FP& f,int w4,const u32x4* xq,const u32x4* dq,f32x2 rc){
  float ea=rc[0]*-1.4426950408889634f, eb=rc[1]*-1.4426950408889634f;
  #pragma unroll
  for(int z=0;z<3;z++) if(z<2||w4==0){
    #pragma unroll
    for(int e=0;e<4;e++){ u32 xw=xq[z][e], dw=dq[z][e]; int i=8*z+2*e; float a=blo(xw), b=bhi(xw);
      float pa=__builtin_fmaf(a,rc[0],rc[1]), pb=__builtin_fmaf(b,rc[0],rc[1]);
      float ga=pa*rcpf_(1.f+__builtin_amdgcn_exp2f(__builtin_fmaf(a,ea,eb))), gb=pb*rcpf_(1.f+__builtin_amdgcn_exp2f(__builtin_fmaf(b,ea,eb)));
      f.gg[i]=ga; f.gg[i+1]=gb; f.bs[i]=__builtin_fmaf(pa*blo(dw),ga,a); f.bs[i+1]=__builtin_fmaf(pb*bhi(dw),gb,b);
      asm volatile("" : "+v"(f.gg[i]),"+v"(f.gg[i+1]),"+v"(f.bs[i]),"+v"(f.bs[i+1])); } }
}
DEV void final_post(lch* sm,const FP& f,int w4,int nch,u16* __restrict__ out,char* ws){
  LANE(ln); int bid=BID, xg=bid&7, lb=bid>>3, g=ln>>4, li=ln&15, qq=li>>2, pq=li&3;
  int cg=lb&3, T=8*xg+(lb>>2), trow=32*T+8*g+(li&7);
  int ob=(trow*D_+144*cg+16*w4+(li>>3)*8)*2, yo=(144*cg+16*w4+(ln>>2))*YST+(32*T+8*(ln&3))*2;
  #define ZCH 64
  { gwait<2>(sm,C_IX,1); u32x4 iq=LD(u32x4,sm+O_IDX); u32 tt=(u32)trow*0x10001u; bool routed=false;
    #pragma unroll
    for(int q=0;q<4;q++){ u32 z=iq[q]^tt; routed|=((z&0xffffu)==0u)|((z>>16)==0u); }
    ob=routed? OOR:ob; }
  lch* fb=sm+O_CH+w4*3072; lch* wp=fb+((ln&3)*16+(ln>>2))*16; const lch* tp=fb+(16*g+8*(pq>>1)+qq)*16+(pq&1)*8;
  u32x4 rs=mkrs(ws+W_Y,D_*YST); rsrc_t ro=RSRC(out,L_*D_*2);
  gwait<4>(sm,C_E,4*nch);
  if(lb>=8&&lb<14) gwait<4>(sm,C_H,4);
  PRIO(3);
  { u32 t0,cnt;
    #define LD_Y "buffer_load_dwordx4 v[112:115], %[of], %[rs], 0 offen sc1\n\tbuffer_load_dwordx4 v[116:119], %[of], %[rs], %[s1] offen sc1\n\tbuffer_load_dwordx4 v[120:123], %[o3], %[rs], 0 offen sc1\n\t"
    asm volatile(LD_Y POLLASM(LD_Y,CHK_I0 MX2(116,117) MX2(118,119) MX2(120,121) MX2(122,123) CHK_IE,YSLP)
      "ds_write_b128 %[wp], v[112:115]\n\tds_write_b128 %[wp], v[116:119] offset:1024\n\tds_write_b128 %[wp], v[120:123] offset:2048"
      : [t0]"=&v"(t0),[cnt]"=&s"(cnt) : [of]"v"(yo),[rs]"s"(rs),[s1]"s"(ZCH*YST),[o3]"v"(w4==0? yo+2*ZCH*YST:OOR),[wp]"v"(wp) : PCLOB); }
  if(lb>=14) garrive(sm,C_PR);
  u32x4 r[3];
  #pragma unroll
  for(int z=0;z<3;z++) r[z]=BC(u32x4,trfrag(tp+z*1024,tp+z*1024+64));
  #pragma unroll
  for(int z=0;z<3;z++) if(z<2||w4==0){ u32x4 ov;
    #pragma unroll
    for(int e=0;e<4;e++){ u32 rw=r[z][e]; int i=8*z+2*e; ov[e]=pk(__builtin_fmaf(blo(rw),f.gg[i],f.bs[i]),__builtin_fmaf(bhi(rw),f.gg[i+1],f.bs[i+1])); }
    BST128(ov,ro,ob+z*(2*ZCH),0,0); SCHED(); }
}

#define TROW 528
#define O_TB 4640
#define O_DE 34752
#define O_DO 35968
#define O_R2K 37120
#define O_R3K 37632
#define O_DUM 37952
#define O_RED O_TB
#define MH(a,b,c) __builtin_amdgcn_mfma_f32_16x16x32_f16(a,b,c,0,0,0)
#define PIN(x) asm volatile("" : "+v"(x))
DEV h8 ld4w(const lch* p){ return BC(h8,(u32x4{LD(u32,p),LD(u32,p+4),LD(u32,p+8),LD(u32,p+12)})); }
DEV u32 __umul24_(u32 a,u32 b){ u32 r; asm("v_mul_u32_u24 %0, %1, %2" : "=v"(r) : "s"(b),"v"(a)); return r; }
DEV u32x4 band(u32 mask,int g,u32 val){ u32 by=__builtin_amdgcn_ubfe(mask,8*g,8), X=by|(by<<15), k=0x10001u; u32x4 r; asm volatile("" : "+s"(k),"+s"(val));
  #pragma unroll
  for(int d=0;d<4;d++) r[d]=__umul24_((X>>(2*d))&k,val);
  return r; }
DEV h8 zpack(f32x4 e,f32x4 o){ return BC(h8,(u32x4{pkh(e[0],o[0]),pkh(e[1],o[1]),pkh(e[2],o[2]),pkh(e[3],o[3])})); }
struct C3 { float r,i,n; };
DEV C3 c3(C2 a){ C3 c; c.r=a.r; c.i=a.i; PIN(c.i); c.n=-c.i; PIN(c.n); return c; }
DEV C2 cm(C2 a,C3 b){ C2 c; c.r=__builtin_fmaf(a.r,b.r,a.i*b.n); c.i=__builtin_fmaf(a.r,b.i,a.i*b.r); return c; }
DEV u32 ph(C2 a){ return pkh(a.r,a.i); }
#define W2(p,a,b) asm volatile("ds_write2_b32 %0, %1, %2 offset1:132" :: "v"(p),"v"(a),"v"(b) : "memory")
struct CP { const lch *x0,*x1,*bd; lch *p0,*p1,*p2,*wp; u32x4 Be,Bo,AE; int yo; };
template<int ROLE> DEV void lanes(CP& c,lch* cb,int ln){ int m=ln&15, g=ln>>4;
  const lch* ad=cb+((m&1)? O_DO+2*527 : O_DE+2*536)+2*(8*g-m);
  const lch* bd=cb+2*(256+16*m+8*g); lch* dum=cb+O_DUM;
  if(ROLE==0){ int l0=128*(g>>1)+8*m+4*(g&1); c.p0=cb+O_DE+(549-l0)*2; PIN(c.p0); c.p1=c.p0+6; PIN(c.p1); c.p2=c.p0+(O_DO-O_DE-18); c.x0=ad-640; c.bd=bd-64; c.x1=cb+O_R3K+2*(126-2*m+8*g)-192; PIN(c.x1);
    c.Be=band(0x1ffu<<(m+4),g,0x3c00u); PIN(c.Be); }
  if(ROLE==1){ lch* r=cb+O_R2K+(192-64*(g-2)-2*m)*2; c.p0= g<2? dum : r-66; c.p1= ln==0? cb+O_DE+(552-256)*2 : (ln<=32? dum : r); c.p2=cb+O_DE+(544-256-8*ln)*2; c.x0=ad-128-640; c.bd=bd-128-128; PIN(c.p0); PIN(c.p1); PIN(c.p2); }
  if(ROLE==2){ int i0=16*m+4*g; c.p0= m<8? cb+O_R3K+(125-i0)*2 : dum+2; c.p1= ln==0? cb+O_R2K+128 : (ln==8? cb+O_R3K : c.p0+6); c.x0=ad-320-640; c.bd=bd-320-128; PIN(c.p0); PIN(c.p1); }
  if(ROLE==3){ c.x0=cb+O_R2K+2*(188-4*m+8*g)-256; c.bd=cb+2*(248+8*m+8*g);
    c.Be=band(0x1fu<<(m+6),g,0x3c00u); c.Bo=band(0x7u<<(m+11),g,0x3c00u); PIN(c.Be); PIN(c.Bo); }
  PIN(c.x0); PIN(c.bd); }
template<int ROLE> DEV void lanes2(CP& c,lch* cb,int ln){ int m=ln&15, g=ln>>4;
  if(ROLE==0){ c.Bo=band(0x7fu<<(m+5),g,0x3c00u); c.AE=band(3u<<(m+12),g,0xffffu); PIN(c.Bo); PIN(c.AE); }
  c.wp=cb+O_RED+ln*16; c.yo=(g&1)*512+m*32+(g>>1)*16; PIN(c.wp); PIN(c.yo); }
template<int ROLE> DEV void chan2(lch* sm,lch* cb,const lch*& fa,const lch*& fb,int grp,int d,int one_,int emax,u64 tag,char* ws){
  CP c;
  const f32x4 Z4={0,0,0,0}; h8 A[6]; f32x4 acc[8];
  #define LD_U "buffer_load_dwordx4 v[112:115], %[of], %[rs], 0 offen sc1\n\tbuffer_load_dwordx4 v[116:119], %[of], %[rs], %[s1] offen sc1\n\tbuffer_load_dwordx4 v[120:123], %[of], %[rs], %[s2] offen sc1\n\tbuffer_load_dwordx4 v[124:127], %[of], %[rs], %[s3] offen sc1\n\t"
  #define UC(t,a,b,c) "v_bitop3_b32 " t ", v" #a ", v" #b ", %[" #c "] bitop3:0x7e\n\t"
  #define UD(t,a,c) "v_bitop3_b32 " t ", " t ", v" #a ", %[" #c "] bitop3:0xf6\n\t"
  #define CHK_U UC("%[t0]",112,116,c0) UC("%[t1]",113,117,c1) UD("%[t0]",120,c0) UD("%[t1]",121,c1) UD("%[t0]",124,c0) UD("%[t1]",125,c1) UD("%[t0]",114,c2) UD("%[t1]",115,c3) UD("%[t0]",118,c2) UD("%[t1]",119,c3) \
    UD("%[t0]",122,c2) UD("%[t1]",123,c3) UD("%[t0]",126,c2) UD("%[t1]",127,c3) "v_bitop3_b32 %[t0], %[t0], %[t1], %[mk] bitop3:0xa8\n\tv_cmp_ne_u32 vcc, 0, %[t0]\n\t"
  #define UX(a,b,c,d,o) "v_xor_b32 v" #a ", %[e0], v" #a "\n\tv_xor_b32 v" #b ", %[e1], v" #b "\n\tv_xor_b32 v" #c ", %[e2], v" #c "\n\tv_xor_b32 v" #d ", %[e3], v" #d "\n\tds_write_b128 %[up], v[" #a ":" #d "] offset:" #o "\n\t"
  #define UPOLL { u32 t0,t1,cnt; asm volatile(POLLASM(LD_U,CHK_U,PSLP) UX(112,113,114,115,544) UX(116,117,118,119,1056) UX(120,121,122,123,1568) UX(124,125,126,127,2080) \
      : [t0]"=&v"(t0),[t1]"=&v"(t1),[cnt]"=&s"(cnt) : [of]"v"(c.p0),[rs]"s"(rsU),[s1]"s"((32>>TL)*GST_),[s2]"s"((64>>TL)*GST_),[s3]"s"((96>>TL)*GST_),[c0]"s"(c0),[c1]"s"(c1),[c2]"s"(c2),[c3]"s"(c3_), \
        [e0]"s"(c0^0x40004u),[e1]"s"(c1^0x40004u),[e2]"s"(c2^0x40004u),[e3]"s"(c3_^0x40004u),[mk]"s"(UMASK),[up]"v"(c.p1) : PCLOB); garrive(sm,C_U+one_); }
  if(ROLE<3){
    h8 a[8], b[8];
    #pragma unroll
    for(int ks=0;ks<8;ks++){ a[ks]=LD(h8,fa+ks*64); b[ks]=LD(h8,fb+ks*64); }
    SCHED();
    LANE(ln); lanes<ROLE>(c,cb,ln); float fz= ln==32? 0.f : 4.76837158203125e-07f; if(ROLE==1) PIN(fz);
    SCHED();
    f32x4 k=Z4;
    #pragma unroll
    for(int ks=0;ks<8;ks++) k=MH(a[ks],b[ks],k);
    const float f=4.76837158203125e-07f;
    float k0=k[0]*f, k1=k[1]*f, k2=k[2]*f, k3=k[3]*f;
    u32 h32=pkh(k3,k2), h10=pkh(k1,k0);
    if(ROLE!=1){
      ST(u16,c.p0)=(u16)h32; ST(u32,c.p0+2)=pkh(k2,k1); ST(u16,c.p1)=(u16)(h10>>16);
      if(ROLE==0) ST(u32x2,c.p2)=u32x2{h32,h10};
    } else {
      float n0=dppf<0x130>(k[0])*fz, z1= fz==0.f? k0:k1, z2=k[2]*fz, z3=k[3]*fz; u32 h22=pkh(z2,z2);
      ST(u16,c.p0+64)=(u16)h10; ST(u16,c.p0+2)=(u16)(h32>>16); ST(u16,c.p0)=(u16)h32; ST(u16,c.p1)=(u16)(h10>>16);
      u32x4 vo={pkh(n0,z3),h22,pkh(z2,z1),pkh(k0,k0)}, ve={pkh(n0,n0),pkh(z3,z2),h22,pkh(z1,k0)};
      asm volatile("s_mov_b32 exec_hi, 1\n\tds_write_b128 %0, %1\n\tds_write_b128 %0, %2 offset:1200\n\ts_mov_b32 exec_hi, -1" :: "v"(c.p2),"v"(ve),"v"(vo) : "memory"); }
    gsync(sm,C_G+grp,7);
    u32 kv; A[0]=ld4w(c.x0+640); A[1]=ld4w(c.x0+128);
    if(ROLE==0){ A[2]=ld4w(c.x1+192); A[3]=ld4w(c.x1+128); A[4]=ld4w(c.x1+64); A[5]=ld4w(c.x1); kv=LD(u16,cb+O_R2K+128); }
    else { A[2]=ld4w(c.x0+576); A[3]=ld4w(c.x0+64); A[4]=ld4w(c.x0+512); A[5]=ld4w(c.x0); }
    SCHED(); lanes2<ROLE>(c,cb,ln); SCHED();
    if(ROLE==0){ kv|=kv<<16; c.AE=u32x4{c.AE[0]&kv,c.AE[1]&kv,c.AE[2]&kv,c.AE[3]&kv}; PIN(c.AE); }
    PRIO(2);
    gwait<1>(sm,C_U+one_,2-one_);
  } else {
    u32 tg=(u32)tag, c0=upat(tg,0), c1=upat(tg,1), c2=upat(tg,2), c3_=upat(tg,3); u32x4 rsU=mkrs(ws+W_U,256*UST);
    if(!one_) PRIO(2);
    LANE(ln); c.p0=(lch*)fa; c.p1=(lch*)fb;
    bool late= grp==0||BID>=112;
    if(!late) asm volatile(LD_U :: [of]"v"(c.p0),[rs]"s"(rsU),[s1]"s"((32>>TL)*GST_),[s2]"s"((64>>TL)*GST_),[s3]"s"((96>>TL)*GST_) : "memory");
    asm volatile("s_mov_b64 exec, 1\n\tglobal_store_dwordx2 %0, %1, %2 sc1\n\ts_mov_b64 exec, -1" :: "v"(W_FL+d*8),"v"(tag),"s"(ws) : "memory");
    lanes<3>(c,cb,ln); lanes2<3>(c,cb,ln);
    #pragma unroll
    for(int j=2;j<8;j++){ acc[j]=Z4; PIN(acc[j]); }
    if(late){ gwait<2>(sm,C_G,UGATE); if(BID>=112) gwait<1>(sm,C_D,4); asm volatile(LD_U :: [of]"v"(c.p0),[rs]"s"(rsU),[s1]"s"((32>>TL)*GST_),[s2]"s"((64>>TL)*GST_),[s3]"s"((96>>TL)*GST_) : "memory"); }
    if(!late) UPOLL
    gwait<1>(sm,C_G+grp,7);
    #pragma unroll
    for(int i=0;i<6;i++) A[i]=ldA(c.x0+320-64*i);
    if(late) UPOLL
    PRIO(2);
    gwait<1>(sm,C_U+one_,2-one_);
  }
  const lch* bd=c.bd; h8 Be=BC(h8,c.Be), Bo=BC(h8,c.Bo);
  #pragma unroll
  for(int j=0;j<(ROLE==3? 2:8);j++) acc[j]=Z4;
  #define RW(j) if((j)>=0&&((j)>>1)!=ROLE&&!(ROLE==3&&(j)<2)){ ST(f32x4,c.wp+((j)>>1)*6144+((ROLE-((j)>>1)-1)&3)*2048+((j)&1)*1024)=acc[(j)<0? 0:(j)]; SCHED(); }
  if(ROLE==1||ROLE==2){
    h8 B[8];
    #pragma unroll
    for(int q=0;q<8;q++) B[q]=LD(h8,bd+128+512*q);
    #pragma unroll
    for(int r=0;r<3;r++){
      #pragma unroll
      for(int dl=0;dl<8;dl++){ if(dl<7) acc[dl+1]=MH(A[2*r+1],B[dl],acc[dl+1]); SCHED(); acc[dl]=MH(A[2*r],B[dl],acc[dl]); SCHED();
        if(r<2){ B[dl]=LD(h8,bd+64-64*r+512*dl); SCHED(); } else RW(dl-2) }
      } }
  if(ROLE==0){
    h8 B[8], ue[4];
    #pragma unroll
    for(int kt=0;kt<4;kt++){ ue[kt]=LD(h8,bd+48+512*kt); B[kt]=LD(h8,bd+64+512*kt); }
    f32x4 ce[2],co[2]; h8 Z[2];
    ce[0]=MH(ue[0],Be,Z4); co[0]=MH(B[0],Bo,Z4); ce[1]=MH(ue[1],Be,Z4); co[1]=MH(B[1],Bo,Z4); Z[0]=zpack(ce[0],co[0]);
    #pragma unroll
    for(int kt=0;kt<4;kt++){
      SCHED();
      if(kt+2<4){ ce[kt&1]=MH(ue[kt+2],Be,Z4); co[kt&1]=MH(B[kt+2],Bo,Z4); }
      if(kt+1<4) Z[(kt+1)&1]=zpack(ce[(kt+1)&1],co[(kt+1)&1]);
      #pragma unroll
      for(int rho=0;rho+kt<4;rho++) acc[4+kt+rho]=MH(Z[kt&1],A[2+rho],acc[4+kt+rho]);
      SCHED(); B[4+kt]=LD(h8,bd+64+512*(4+kt)); if(kt==0) A[5]=BC(h8,c.AE); if(kt==1) A[4]=ld4w(c.x0-384); if(kt==2) A[3]=ld4w(c.x0+64); if(kt==3) A[2]=ld4w(c.x0+576); SCHED(); }
    #pragma unroll
    for(int dl=0;dl<8;dl++){ if(dl<4) acc[dl+4]=MH(A[5],B[dl],acc[dl+4]); SCHED(); if(dl<6) acc[dl+2]=MH(A[4],B[dl],acc[dl+2]); SCHED(); if(dl<7) acc[dl+1]=MH(A[1],B[dl],acc[dl+1]); SCHED(); acc[dl]=MH(A[0],B[dl],acc[dl]);
      SCHED(); B[dl]=LD(h8,bd+512*dl); SCHED(); }
    #pragma unroll
    for(int dl=0;dl<8;dl++){ if(dl<7) acc[dl+1]=MH(A[3],B[dl],acc[dl+1]); SCHED(); acc[dl]=MH(A[2],B[dl],acc[dl]); SCHED(); RW(dl-2) } }
  if(ROLE==3){
    h8 U[12];
    #pragma unroll
    for(int kt=0;kt<6;kt++) U[kt]=LD(h8,bd+256*kt);
    f32x4 ce[2],co[2]; h8 Z[2];
    ce[0]=MH(U[0],Be,Z4); co[0]=MH(U[0],Bo,Z4); ce[1]=MH(U[1],Be,Z4); co[1]=MH(U[1],Bo,Z4); Z[0]=zpack(ce[0],co[0]);
    #pragma unroll
    for(int kt=0;kt<12;kt++){
      SCHED();
      if(kt+2<12){ ce[kt&1]=MH(U[kt+2],Be,Z4); co[kt&1]=MH(U[kt+2],Bo,Z4); }
      if(kt+1<12) Z[(kt+1)&1]=zpack(ce[(kt+1)&1],co[(kt+1)&1]);
      #pragma unroll
      for(int rho=-1;rho<5;rho++){ int j=(kt+4+rho)>>1; if(((kt+rho)&1)==0&&j>=2&&j<8) acc[j]=MH(Z[kt&1],A[rho+1],acc[j]); }
      if(kt<6){ SCHED(); U[kt+6]=LD(h8,bd+256*(kt+6)); }
      if(kt>=3&&kt<=9&&(kt&1)){ SCHED(); RW((kt+1)/2) } }
  }
  PRIO(3);
  RW(6) RW(7)
  gsync(sm,C_G+grp,11);
  { const lch* rp=c.wp+ROLE*6144; f32x4 a[3], b[3], o0=acc[2*ROLE], o1=acc[2*ROLE+1];
    #pragma unroll
    for(int q=0;q<(ROLE==0? 2:3);q++){ a[q]=LD(f32x4,rp+q*2048); b[q]=LD(f32x4,rp+q*2048+1024); }
    SCHED();
    float sc=__builtin_amdgcn_ldexpf(1.f,emax-YSH), s0[4], s1[4];
    #pragma unroll
    for(int e=0;e<4;e++){ s0[e]=((o0[e]+a[0][e])+(ROLE==0? a[1][e] : a[1][e]+a[2][e]))*sc; s1[e]=((o1[e]+b[0][e])+(ROLE==0? b[1][e] : b[1][e]+b[2][e]))*sc; }
    auto r0=__builtin_amdgcn_permlane16_swap(nv(pk(s0[0],s0[1])),nv(pk(s1[0],s1[1])),false,false); auto r1=__builtin_amdgcn_permlane16_swap(nv(pk(s0[2],s0[3])),nv(pk(s1[2],s1[3])),false,false);
    BST128(u32x4{r0[0],r1[0],r0[1],r1[1]},RSRC(ws+W_Y+d*YST,4096),c.yo+ROLE*1024,0,SC1); }
  garrive(sm,C_E);

  asm volatile("s_endpgm" : "=v"(fa),"=v"(fb) :: "memory");
}
#define GETEP asm volatile("s_waitcnt lgkmcnt(0)\n\ts_mov_b64 %0, s[96:97]\n\ts_mov_b32 %1, s98" : "=s"(ep),"=s"(ldw));
DEV void chan(lch* sm,int grp,int w4,int d,int one_,char* ws,u32x4 q0,u32x4 q1,u32x4 q2,u32x4 q3,u32x4 q4){
  lch* cb=sm+O_CH+grp*SLSZ; int role=w4, emax;
  #define LP(v) ((lch*)(size_t)(u32)(v))
  const lch* fa=LP(q4[0]); const lch* fb=LP(q4[1]);
  u64 tag;
  { float a2,bp,br,bi,cr,ci;
    u32 fq=q3[2], fr=q3[3], one=q4[2]; float f64=BC(float,fq), f128=BC(float,fr);
    lch *t8[4]={LP(q0[0]),LP(q0[1]),LP(q0[2]),LP(q0[3])}, *tB[4]={LP(q1[0]),LP(q1[1]),LP(q1[2]),LP(q1[3])}, *t4[2]={LP(q2[0]),LP(q2[1])}, *t2[2]={LP(q2[2]),LP(q2[3])}, *t1=LP(q3[0]), *te=LP(q3[1]);
    if(grp!=0) PRIO(1);
    u64 ep; u32 ldw; GETEP tag=ep+1ull;
    float dt=rb(expf_((d&1)? bhi(ldw):blo(ldw))), ca=dt*1.4426950408889634f, cb_=dt*0.15915494309189535f;
    asm volatile("s_waitcnt vmcnt(1)\n\tv_mul_f32 %2, %8, v114\n\tv_mul_f32 %3, %8, v115\n\tv_mul_f32 %0, %6, v112\n\tv_mul_f32 %1, %7, v113\n\tv_mov_b32 %4, v116\n\tv_mov_b32 %5, v117"
      : "=&v"(a2),"=&v"(bp),"=&v"(br),"=&v"(bi),"=&v"(cr),"=&v"(ci) : "v"(ca),"v"(cb_),"v"(dt) : "memory");
    C2 W; W.r=cr*br-ci*bi; W.i=cr*bi+ci*br;
    u32 mx=BC(u32,__builtin_fmaxf(__builtin_fabsf(W.r),__builtin_fabsf(W.i)));
    #define MXS(C) mx=umax_(mx,dppu<C>(mx));
    float e8=a2*8.f, eB=__builtin_fmaf(a2,f64,14.f), p8=bp*8.f, pB=bp*f64; SCHED();
    MXS(0xB1) p8=__builtin_amdgcn_fractf(p8); pB=__builtin_amdgcn_fractf(pB); e8=__builtin_amdgcn_exp2f(e8); SCHED();
    MXS(0x4E) eB=__builtin_amdgcn_exp2f(eB); float c8=__builtin_amdgcn_cosf(p8); SCHED();
    MXS(0x141) float s8=__builtin_amdgcn_sinf(p8), cB=__builtin_amdgcn_cosf(pB); SCHED();
    float sB=__builtin_amdgcn_sinf(pB); PIN(sB); MXS(0x140) float e1=__builtin_amdgcn_exp2f(a2); PIN(e1); SCHED();
    asm volatile("s_mov_b64 exec, 0x10001\n\tds_max_u32 %0, %1\n\tds_add_u32 %0, %2 offset:4\n\ts_mov_b64 exec, -1" :: "v"(te),"v"(mx),"v"(one) : "memory");
    C3 z8; z8.r=e8*c8; z8.n=e8*s8; z8.i=-z8.n; PIN(z8.i); C2 B; B.r=eB*cB; B.i=-(eB*sB);
    float p1=__builtin_amdgcn_fractf(bp), pE=__builtin_amdgcn_fractf(bp*256.f), pV=__builtin_amdgcn_fractf(bp*f128), eE=a2*256.f, eV=a2*f128, tr[8];
    #pragma unroll
    for(int k=0;k<4;k++){ SCHED(); u32 h0=ph(B); B=cm(B,z8);
      if(k==0){ tr[0]=__builtin_amdgcn_cosf(pV); tr[1]=__builtin_amdgcn_sinf(pV); } if(k==1){ tr[2]=__builtin_amdgcn_exp2f(eV); tr[3]=__builtin_amdgcn_cosf(p1); }
      if(k==2){ tr[4]=__builtin_amdgcn_sinf(p1); tr[5]=__builtin_amdgcn_exp2f(eE); } if(k==3){ tr[6]=__builtin_amdgcn_cosf(pE); tr[7]=__builtin_amdgcn_sinf(pE); }
      PIN(tr[2*k]); PIN(tr[2*k+1]);
      u32 h1=ph(B); if(k<3) B=cm(B,z8); W2(tB[k],h0,h1); }
    asm volatile("ds_read_b64 v[118:119], %0" :: "v"(te) : "memory");
    SCHED();
    C3 z1=c3(C2{e1*tr[3],e1*tr[4]}), E=c3(C2{tr[5]*tr[6],tr[5]*tr[7]}); C2 V[8]; V[0]=cmul(W,C2{tr[2]*tr[0],tr[2]*tr[1]});
    { u32 cnt,n; PIN(V[0].r); PIN(V[0].i); PIN(z1.r); PIN(E.r);
      asm volatile("s_mov_b32 %1, 0x20000\n1:\n\ts_waitcnt lgkmcnt(0)\n\tv_readfirstlane_b32 %2, v119\n\tv_readfirstlane_b32 %0, v118\n\ts_cmp_eq_u32 %2, 8\n\ts_cbranch_scc1 2f\n\tds_read_b64 v[118:119], %3\n\ts_sub_u32 %1, %1, 1\n\ts_cbranch_scc0 1b\n2:"
        : "=&s"(emax),"=&s"(cnt),"=&s"(n) : "v"(te) : "memory","scc");
      emax=RFL(emax); emax=(emax>>23)-126; }
    V[0].r=__builtin_amdgcn_ldexpf(V[0].r,14-emax); V[0].i=__builtin_amdgcn_ldexpf(V[0].i,14-emax);
    C2 Y=cm(V[0],E), Y4; u32 y0=ph(Y);
    V[1]=cm(V[0],z1); W2(t8[0],ph(V[0]),ph(V[1])); Y=cm(Y,E); u32 y1=ph(Y);
    V[2]=cm(V[1],z1); Y=cm(Y,E); u32 y2=ph(Y);
    V[3]=cm(V[2],z1); W2(t8[1],ph(V[2]),ph(V[3])); W2(t4[0],y0,ph(cm(V[2],E)));
    V[4]=cm(V[3],z1); Y=cm(Y,E); ST(u32,t1)=ph(Y);
    V[5]=cm(V[4],z1); W2(t8[2],ph(V[4]),ph(V[5])); Y4=cm(V[4],E); u32 y4=ph(Y4);
    V[6]=cm(V[5],z1); Y=cm(Y,E); ST(u32,t1+2*TROW)=ph(Y);
    V[7]=cm(V[6],z1); W2(t8[3],ph(V[6]),ph(V[7])); Y4=cm(Y4,E); W2(t2[0],y1,ph(Y4));
    W2(t4[1],y4,ph(cm(V[6],E))); Y=cm(Y,E); ST(u32,t1+4*TROW)=ph(Y);
    Y4=cm(Y4,E); W2(t2[1],y2,ph(Y4)); Y=cm(Y,E); ST(u32,t1+6*TROW)=ph(Y);
    Y=cm(Y,E); asm volatile("s_mov_b32 exec_hi, 0\n\tds_write_b32 %0, %1 offset:4224\n\ts_mov_b32 exec_hi, -1" :: "v"(t1),"v"(ph(Y)) : "memory");
  }
  if(one_) PRIO(3);
  DRAIN();
  gsync(sm,C_G+grp,4);
  if(role==0) chan2<0>(sm,cb,fa,fb,grp,d,one_,emax,tag,ws);
  if(role==1) chan2<1>(sm,cb,fa,fb,grp,d,one_,emax,tag,ws);
  if(role==2) chan2<2>(sm,cb,fa,fb,grp,d,one_,emax,tag,ws);
  chan2<3>(sm,cb,fa,fb,grp,d,one_,emax,tag,ws);
}

DEV u32 med3u(u32 a,u32 b,u32 c){ u32 r; asm("v_med3_u32 %0, %1, %2, %3" : "=v"(r) : "v"(a),"v"(b),"v"(c)); return r; }
DEV u32 max3u(u32 a,u32 b,u32 c){ return umax_(umax_(a,b),c); }
#define GTC(r,s) "v_cmp_gt_u32 %[" #s "], v" #r ", %[c]\n\t"
#define GTA(s) "v_addc_co_u32 %[rk], vcc, 0, %[rk], %[" #s "]\n\t"
#define GT4(a,b,c,d) GTC(a,s0) GTC(b,s1) GTC(c,s2) GTC(d,s3) GTA(s0) GTA(s1) GTA(s2) GTA(s3)
#define GTS [s0]"=&s"(q0),[s1]"=&s"(q1),[s2]"=&s"(q2),[s3]"=&s"(q3)
DEV void top8(lch* sm,int w4,int ln,u32 M,char* ws,u32& rank_,u32& key_){
  u32 k[8], rc[8], hm=0xffff0000u; u64 em=0x0001000100010001ull,q0,q1,q2,q3;
  u32x4 rs=mkrs(ws+W_RG+(BID&(NRG-1))*8192,8192); int ro=w4*1024+ln*16;
  #pragma unroll
  for(int e=0;e<8;e++){ rc[e]=(u32)(0xfff-(w4*256+ln*4+(e&3)+(e>>2)*1024)); PIN(rc[e]); }
  lch *aW=sm+O_RMX+(w4*4+(ln>>4))*4, *aC=sm+O_RMX+(ln>>2)*4, *aO=sm+O_RMX+(ln&3)*16, *dC=sm+O_CND+(ln>>1)*4, *dO=sm+O_CND+(ln&1)*64; PIN(aW); PIN(aC); PIN(aO); PIN(dC); PIN(dO); PIN(ro); PIN(ln);
  asm volatile("" : "+s"(hm),"+s"(em),"+s"(rs));
  gwait<2>(sm,C_G,RGATE);
  { u32 cnt;
    #define KL(e,r) "v_lshl_or_b32 %[k" #e "], v" #r ", 16, %[k" #e "]\n\t"
    #define KH(e,r) "v_and_or_b32 %[k" #e "], v" #r ", %[hm], %[k" #e "]\n\t"
    #define CHK_R "v_bitop3_b32 %[t0], v113, v115, %[M] bitop3:0x7e\n\tv_bitop3_b32 %[t1], v117, v119, %[M] bitop3:0x7e\n\tv_or_b32 %[t0], %[t0], %[t1]\n\tv_cmp_ne_u32 vcc, 0, %[t0]\n\t"
    u32 t0,t1;
    asm volatile(LD_DUP2
      "s_sleep " STR(R2D) "\n\tbuffer_load_dwordx4 v[120:123], %[of], %[rs], 0 offen sc1\n\tbuffer_load_dwordx4 v[124:127], %[of], %[rs], %[so] offen sc1\n\ts_mov_b32 %[cnt], 0x20000\n\ts_waitcnt vmcnt(2)\n\t" CHK_R "s_cbranch_vccz 9f\n\t"
      "s_waitcnt vmcnt(0)\n\tv_mov_b64 v[112:113], v[120:121]\n\tv_mov_b64 v[114:115], v[122:123]\n\tv_mov_b64 v[116:117], v[124:125]\n\tv_mov_b64 v[118:119], v[126:127]\n\t"
      POLLASM(LD_DUP2,CHK_R,PSLP)
      KL(0,112) KH(1,112) KL(2,114) KH(3,114) KL(4,116) KH(5,116) KL(6,118) KH(7,118)
      : [k0]"+v"(rc[0]),[k1]"+v"(rc[1]),[k2]"+v"(rc[2]),[k3]"+v"(rc[3]),[k4]"+v"(rc[4]),[k5]"+v"(rc[5]),[k6]"+v"(rc[6]),[k7]"+v"(rc[7]),[cnt]"=&s"(cnt),[t0]"=&v"(t0),[t1]"=&v"(t1) : [of]"v"(ro),[rs]"s"(rs),[so]"s"(4096),[M]"s"(M),[hm]"s"(hm) : PCLOB); }
  #pragma unroll
  for(int e=0;e<8;e++) k[e]=rc[e];
  u32 h=max3u(k[0],k[1],k[2]), m1=max3u(max3u(k[3],k[4],k[5]),max3u(k[6],k[7],h),h), mx=m1, l=med3u(k[0],k[1],k[2]), t=umin_(umin_(k[0],k[1]),k[2]);
  SCHED();
  #define INS(e) t=med3u(t,l,k[e]); l=med3u(h,l,k[e]); if(e<7) h=umax_(h,k[e]); PIN(t); PIN(l); SCHED();
  mx=umax_(mx,dppu<0xB1>(mx)); INS(3) mx=umax_(mx,dppu<0x4E>(mx)); INS(4) mx=umax_(mx,dppu<0x141>(mx)); INS(5) mx=umax_(mx,dppu<0x140>(mx));
  asm volatile("s_mov_b64 exec, %2\n\tds_write_b32 %0, %1\n\ts_mov_b64 exec, -1\n\tds_read_b32 v116, %3\n\tds_read_b128 v[112:115], %4" :: "v"(aW),"v"(mx),"s"(em),"v"(aC),"v"(aO) : "memory");
  INS(6) INS(7)
  u32 m2=l, m3=t, T;
  { u32 c,rk,cnt;
    asm volatile("s_mov_b32 %[cnt], 0x20000\n1:\n\ts_waitcnt lgkmcnt(0)\n\tv_cmp_eq_u32 vcc, 0, v116\n\ts_cbranch_vccz 2f\n\tds_read_b32 v116, %[aC]\n\tds_read_b128 v[112:115], %[aO]\n\ts_sub_u32 %[cnt], %[cnt], 1\n\ts_cbranch_scc0 1b\n2:\n\t"
      "v_mov_b32 %[c], v116\n\tv_mov_b32 %[rk], 0\n\t" GT4(112,113,114,115) "s_nop 1\n\tv_add_u32_dpp %[rk], %[rk], %[rk] quad_perm:[1,0,3,2] row_mask:0xf bank_mask:0xf\n\ts_nop 1\n\tv_add_u32_dpp %[rk], %[rk], %[rk] quad_perm:[2,3,0,1] row_mask:0xf bank_mask:0xf\n\t"
      "v_cmp_eq_u32 vcc, 7, %[rk]\n\ts_ff1_i32_b64 %[cnt], vcc\n\ts_nop 0\n\tv_readlane_b32 %[T], %[c], %[cnt]\n\ts_nop 1"
      : [c]"=&v"(c),[rk]"=&v"(rk),[cnt]"=&s"(cnt),[T]"=&s"(T),GTS : [aC]"v"(aC),[aO]"v"(aO) : PCLOB); T=RFL(T); }
  { u64 b1=BALLOT(m1>=T), b2=BALLOT(m2>=T), b3=BALLOT(m3>=T); int n1=__builtin_popcountll(b1), n2=n1+__builtin_popcountll(b2);
    lch* cw=sm+O_CND+w4*32;
    if(__builtin_expect(b3==0ull&&n2<=8,1)){
      int p1=(int)__builtin_amdgcn_mbcnt_hi((u32)(b1>>32),__builtin_amdgcn_mbcnt_lo((u32)b1,0u)), pf=n2+ln-p1; bool c1=m1>=T;
      if(__builtin_expect(b2!=0ull,0)){ int p2=(int)__builtin_amdgcn_mbcnt_hi((u32)(b2>>32),__builtin_amdgcn_mbcnt_lo((u32)b2,(u32)n1)); if(m2>=T) ST(u32,cw+p2*4)=m2; }
      int ps= c1? p1:pf; ps= ps<8? ps:32-8*w4; ST(u32,cw+ps*4)= c1? m1:1u;
    } else {
      u32 mine=0;
      _Pragma("unroll 1") for(int c=0;c<8;c++){ u32 lm=umax_(umax_(umax_(k[0],k[1]),umax_(k[2],k[3])),umax_(umax_(k[4],k[5]),umax_(k[6],k[7]))); u32 wm=wmax(lm); if(ln==c) mine=wm;
        #pragma unroll
        for(int e=0;e<8;e++) k[e]=(k[e]==wm)?0u:k[e]; }
      if(ln<8) ST(u32,cw+ln*4)=mine; } }
  { u32 c,rk,cnt;
    asm volatile("s_mov_b32 %[cnt], 0x20000\n\tv_mov_b32 %[rk], 0\n\ts_waitcnt vmcnt(0)\n1:\n\tds_read_b32 %[c], %[dC]\n\tds_read_b128 v[112:115], %[dO]\n\tds_read_b128 v[116:119], %[dO] offset:16\n\tds_read_b128 v[120:123], %[dO] offset:32\n\tds_read_b128 v[124:127], %[dO] offset:48\n\t"
      "s_waitcnt lgkmcnt(4)\n\tv_cmp_eq_u32 vcc, 0, %[c]\n\ts_cbranch_vccz 2f\n\ts_sub_u32 %[cnt], %[cnt], 1\n\ts_cbranch_scc0 1b\n2:\n\t"
      "s_waitcnt lgkmcnt(3)\n\t" GT4(112,113,114,115) "s_waitcnt lgkmcnt(2)\n\t" GT4(116,117,118,119) "s_waitcnt lgkmcnt(1)\n\t" GT4(120,121,122,123) "s_waitcnt lgkmcnt(0)\n\t" GT4(124,125,126,127)
      "s_nop 1\n\tv_add_u32_dpp %[rk], %[rk], %[rk] quad_perm:[1,0,3,2] row_mask:0xf bank_mask:0xf"
      : [c]"=&v"(c),[rk]"=&v"(rk),[cnt]"=&s"(cnt),GTS : [dC]"v"(dC),[dO]"v"(dO) : PCLOB);
    rank_=rk; key_=c; }
}

#define FILL_(n,WAIT) "s_mov_b32 m0, %[ld]\n\ts_mov_b32 %[c], " STR(n) "-1\n3:\n\t" WAIT "buffer_load_dwordx4 %[vo], %[rs], %[so] offen nt lds\n\ts_add_u32 m0, m0, 0x1000\n\ts_add_u32 %[so], %[so], %[st]\n\t" \
  "s_sub_u32 %[c], %[c], 1\n\ts_cbranch_scc0 3b\n\ts_mov_b32 %[ld], m0\n\t"
#define FILL(n,WAIT) do{ u32 c_; asm volatile(FILL_(n,WAIT) : [c]"=&s"(c_),[so]"+s"(wso),[ld]"+s"(wld) : [vo]"v"(wvo),[rs]"s"(wrs),[st]"s"(wst) : "memory","scc","m0"); }while(0)
#define FILLD(n,WN,WW_) do{ u32 c_,t_,s_; asm volatile(FILL_(n,"ds_read_b32 %[t], %[ad]\n\ts_waitcnt lgkmcnt(0)\n\tv_readfirstlane_b32 %[s], %[t]\n\ts_cmp_ge_u32 %[s], 4\n\ts_cbranch_scc1 4f\n\t" VW(WN) "4:\n\t" VW(WW_)) \
  : [c]"=&s"(c_),[so]"+s"(wso),[ld]"+s"(wld),[t]"=&v"(t_),[s]"=&s"(s_) : [vo]"v"(wvo),[rs]"s"(wrs),[st]"s"(wst),[ad]"v"(C_D*4) : "memory","scc","m0"); wso=RFL(wso); wld=RFL(wld); }while(0)
#define VW(n) "s_waitcnt vmcnt(" STR(n) ")\n\t"

__global__ NOPK __attribute__((amdgpu_num_vgpr(56),amdgpu_num_sgpr(102))) __launch_bounds__(1024,4) void kA(const u16* __restrict__ x,char* ws,const u16* __restrict__ wr,const u16* __restrict__ Dsk,const u16* __restrict__ wg,const u16* __restrict__ wu,
                                              const u16* __restrict__ wd,u16* __restrict__ out,KP kp){
  __shared__ __attribute__((aligned(16))) char smem[LDS_TOTAL];
  lch* sm=(lch*)smem;
  u32x4 xa,xb_,wa,wb,dq[3]; u32 xc,wc;
  asm volatile(
    "s_load_dwordx16 s[64:79], %[ka], 0x0\n\ts_load_dwordx16 s[80:95], %[ka], 0x40\n\t"
    "v_readfirstlane_b32 s60, %[t]\n\ts_and_b32 s61, %[wg], 7\n\ts_lshr_b32 s62, %[wg], 3\n\tv_and_b32 v118, 31, %[t]\n\ts_bfe_u32 s63, s60, 0x20006\n\ts_cmpk_ge_u32 s60, 0x100\n\ts_cbranch_scc1 1f\n\t"
    "s_setprio 3\n\ts_lshl_b32 s56, s61, 5\n\ts_add_u32 s56, s56, s62\n\ts_mulk_i32 s56, 0x2400\n\tv_lshrrev_b32 v119, 5, %[t]\n\tv_mul_u32_u24 v119, 0x480, v119\n\tv_add_u32 v119, s56, v119\n\t"
    "v_lshl_add_u32 v116, v118, 4, v119\n\tv_lshl_add_u32 v117, v118, 2, v119\n\tv_lshlrev_b32 v114, 4, v118\n\tv_lshlrev_b32 v115, 2, v118\n\t"
    "s_and_b32 s57, s62, 3\n\ts_mulk_i32 s57, 0x120\n\ts_lshl_b32 s58, s63, 5\n\ts_add_u32 s57, s57, s58\n\tv_and_b32 v113, 8, %[t]\n\tv_lshl_add_u32 v113, v113, 1, s57\n\tv_min_u32 v112, 0x370, v113\n\t"
    "v_lshlrev_b32 v119, 1, %[t]\n\tv_mov_b32 v118, 0\n\tds_write_b16 v119, v118\n\t"
    "s_waitcnt lgkmcnt(0)\n\t"
    "s_load_dwordx2 s[96:97], s[66:67], 0x0\n\t"
    "global_load_dwordx4 %[wa], v114, s[68:69]\n\tglobal_load_dwordx4 %[wb], v114, s[68:69] offset:512\n\tglobal_load_dword %[wc], v115, s[68:69] offset:1024\n\t"
    "global_load_dwordx4 %[xa], v116, s[64:65] nt\n\tglobal_load_dwordx4 %[xb], v116, s[64:65] offset:512 nt\n\tglobal_load_dword %[xc], v117, s[64:65] offset:1024 nt\n\t"
    "global_load_dwordx4 %[d0], v113, s[70:71]\n\tglobal_load_dwordx4 %[d1], v113, s[70:71] offset:128\n\tglobal_load_dwordx4 %[d2], v112, s[70:71] offset:256\n\ts_barrier\n\t"
    "s_branch 8f\n"
    "1:\n\ts_cmp_ge_u32 s62, " STR(CPF) "\n\ts_cbranch_scc1 6f\n\ts_getpc_b64 s[56:57]\n.Lkpc:\n\ts_and_b32 s58, s62, 3\n\ts_lshl_b32 s58, s58, 13\n\ts_sub_u32 s58, s58, 0x1000\n\tv_lshl_add_u32 v119, %[t], 4, s58\n\tv_min_u32 v119, .Lkend-.Lkpc-16, v119\n\t"
    "global_load_dwordx4 v[120:123], v119, s[56:57]\n\t.subsection 1\n.Lkend:\n\t.subsection 0\n6:\n\t"
    "v_mov_b32 v124, -1\n\tv_mov_b32 v125, -1\n\tv_mov_b32 v126, -1\n\tv_mov_b32 v127, -1\n\tv_lshlrev_b32 v119, 4, %[t]\n\tv_and_b32 v119, 0xff0, v119\n\t"
    "s_lshr_b32 s59, s60, 8\n\ts_cmp_lt_u32 s62, 8\n\ts_cselect_b32 s58, 2, 1\n\ts_cbranch_scc1 2f\n\ts_cmp_eq_u32 s59, 3\n\ts_cbranch_scc1 4f\n"
    "2:\n\ts_sub_u32 s57, s59, 2\n\ts_cmp_eq_u32 s59, 1\n\ts_cselect_b32 s58, s58, s57\n\ts_cmp_lt_u32 s62, 8\n\ts_cselect_b32 s56, 0, 8\n\ts_lshl1_add_u32 s56, s62, s56\n\ts_mulk_i32 s61, 72\n\ts_add_u32 s51, s56, s61\n\ts_sub_u32 s56, 2, s59\n\ts_sub_u32 s57, 16, s62\n\ts_cmp_eq_u32 s59, 3\n\ts_cselect_b32 s50, s57, s56\n\ts_add_u32 s56, s50, s51\n\t"
    "v_mov_b32 v112, 0\n\tv_mov_b32 v113, 0\n\tv_mov_b32 v114, 0\n\tv_mov_b32 v115, 0\n\tv_mov_b32 v116, 0\n\tv_mov_b32 v117, 0\n\ts_setprio 2\n\t"
    "s_lshl_b32 s57, s56, 2\n\ts_add_u32 s57, s57, s63\n\ts_lshl_b32 s57, s57, 6\n\tv_lshl_add_u32 v22, v118, 1, s57\n\ts_mul_i32 s57, s56, %[yst]\n\ts_add_u32 s57, s57, %[wy]\n\tv_add_u32 v23, s57, v119\n\ts_lshl_b32 s56, s56, 1\n\ts_and_b32 s56, s56, -4\n\t"
    "s_waitcnt lgkmcnt(0)\n\t"
    "global_load_short_d16_hi v112, v22, s[80:81] nt\n\tglobal_load_short_d16_hi v113, v22, s[82:83] nt\n\tglobal_load_short_d16_hi v114, v22, s[84:85] nt\n\t"
    "global_load_short_d16_hi v115, v22, s[86:87] nt\n\tglobal_load_short_d16_hi v116, v22, s[88:89] nt\n\tglobal_load_short_d16_hi v117, v22, s[90:91] nt\n\t"
    "s_load_dwordx2 s[96:97], s[66:67], 0x0\n\ts_load_dword s98, s[92:93], s56\n\t"
    "s_barrier\n\t" "global_store_dwordx4 v23, v[124:127], s[66:67] sc1\n\t"
    "s_mul_i32 s52, s58, %[sl]\n\ts_add_u32 s52, s52, %[tb]\n\ts_lshl_b32 s53, s63, 7\n\ts_add_u32 s53, s53, s52\n\tv_lshl_add_u32 v30, v118, 2, s53\n\tv_bfe_u32 v31, %[t], 5, 1\n\ts_movk_i32 s54, 528\n\tv_mul_u32_u24 v29, s54, v31\n\t"
    "v_lshl_add_u32 v2, v29, 3, v30\n\ts_movk_i32 s53, 1056\n\tv_add_u32 v3, s53, v2\n\tv_add_u32 v4, s53, v3\n\tv_add_u32 v5, s53, v4\n\tv_add_u32 v6, 41*528, v2\n\tv_add_u32 v7, s53, v6\n\tv_add_u32 v8, s53, v7\n\tv_add_u32 v9, s53, v8\n\t"
    "v_lshl_add_u32 v10, v29, 2, v30\n\tv_add_u32 v10, 16*528, v10\n\tv_add_u32 v11, s53, v10\n\tv_lshl_add_u32 v12, v29, 1, v30\n\tv_add_u32 v12, 24*528, v12\n\tv_add_u32 v13, 2112, v12\n\tv_add_u32 v14, v29, v30\n\tv_add_u32 v14, 32*528, v14\n\t"
    "s_lshl_b32 s53, s58, 4\n\ts_add_u32 s53, s53, %[ex]\n\tv_mov_b32 v15, s53\n\tv_cvt_f32_ubyte0 v31, v31\n\tv_mul_f32 v16, 0x42800000, v31\n\tv_add_f32 v17, v16, v16\n\tv_mov_b32 v20, 1\n\t"
    "v_and_b32 v29, 15, %[t]\n\tv_and_b32 v30, 48, %[t]\n\tv_mad_u32_u24 v30, v29, s54, v30\n\ts_lshl_b32 s55, s59, 1\n\ts_lshr_b32 s55, 0x48, s55\n\ts_add_u32 s55, s55, s63\n\ts_and_b32 s55, s55, 3\n\ts_mul_i32 s53, s55, 16*528\n\ts_movk_i32 s57, 41*528\n\ts_cmp_eq_u32 s55, 2\n\ts_cselect_b32 s53, s57, s53\n\ts_cselect_b32 s57, 32*528, s57\n\t"
    "s_add_u32 s53, s53, s52\n\ts_add_u32 s57, s57, s52\n\tv_add_u32 v18, s53, v30\n\tv_add_u32 v19, s57, v30\n\t"
    "v_min_u32 v29, 237*16, v119\n\tv_subrev_u32 v30, 204*16, v29\n\tv_add_u32 v29, %[de], v29\n\tv_min_u32 v29, v29, v30\n\ts_sub_u32 s53, s52, 4640\n\tv_add_u32 v29, s53, v29\n\tv_mov_b32 v24, 0\n\tv_mov_b32 v25, 0\n\tv_mov_b32 v26, 0\n\tv_mov_b32 v27, 0\n\tds_write_b128 v29, v[24:27]\n\t"
    "s_cmp_lg_u32 s55, 3\n\ts_cbranch_scc1 9f\n\t"
    "v_bfe_u32 v18, %[t], 1, 5\n\tv_and_b32 v19, 1, %[t]\n\ts_cmp_lt_u32 s62, 8\n\ts_cselect_b32 s53, 2, 1\n\ts_mul_i32 s53, s53, %[sl]\n\tv_mul_u32_u24 v21, s53, v19\n\ts_sub_u32 s55, 2, s59\n\ts_lshl_b32 s55, s55, 7\n\tv_add_u32 v18, s55, v18\n\ts_cmp_lg_u32 s59, 3\n\ts_cbranch_scc1 7f\n\t"
    "v_bfe_u32 v18, %[t], 5, 1\n\tv_lshl_add_u32 v18, v18, 7, v118\n\tv_mov_b32 v19, s50\n\tv_mov_b32 v21, %[sl]\n"
    "7:\n\tv_add_u32 v19, s51, v19\n\tv_lshl_add_u32 v21, v18, 4, v21\n\tv_lshrrev_b32 v24, 3-" STR(TL) ", v19\n\tv_and_b32 v19, 7>>" STR(TL) ", v19\n\tv_lshlrev_b32 v19, 4, v19\n\tv_lshl_add_u32 v19, v24, 7, v19\n\t"
    "v_and_b32 v24, (1<<" STR(TL) ")-1, v18\n\tv_lshl_add_u32 v19, v24, 7-" STR(TL) ", v19\n\tv_lshrrev_b32 v18, " STR(TL) ", v18\n\tv_mul_u32_u24 v18, %[ust], v18\n\tv_add_u32 v18, v18, v19\n\tv_add_u32 v19, 512, v21\n\t"
    "s_branch 9f\n"
    "4:\n\t"
    "v_bfe_u32 v118, %[t], 3, 5\n\tv_lshrrev_b32 v117, 1, v118\n\tv_and_b32 v117, 4, v117\n\tv_and_b32 v116, 2, v118\n\tv_and_b32 v115, 7, %[t]\n\tv_or_b32 v116, v116, v117\n\tv_xor_b32 v115, v115, v116\n\tv_lshlrev_b32 v115, 4, v115\n\t"
    "s_mul_i32 s54, s61, 9\n\ts_sub_u32 s55, s62, 14\n\ts_lshr_b32 s55, s55, 1\n\ts_add_u32 s54, s54, s55\n\ts_lshl_b32 s54, s54, 7\n\t"
    "s_mul_i32 s52, s61, 0xa2000\n\ts_mul_i32 s53, s62, 0x180\n\ts_add_u32 s52, s52, s53\n\ts_cmp_ge_u32 s62, 11\n\ts_cselect_b32 s53, 0x51000-0x480, 0\n\ts_add_u32 s52, s52, s53\n\ts_sub_u32 s52, s52, 0xc00\n\t"
    "s_movk_i32 s55, 0x480\n\ts_cmp_ge_u32 s62, 14\n\ts_cselect_b32 s54, s54, s52\n\ts_cselect_b32 s55, 0x2400, s55\n\tv_mad_u32_u24 v118, v118, s55, v115\n\ts_lshl_b32 s55, s55, 5\n\t"
    "s_lshl_b32 s52, s63, 10\n\ts_add_u32 m0, s52, %[r2]\n\ts_mov_b32 s58, 0x510000\n\ts_mov_b32 s59, 0x20000\n\ts_cmp_ge_u32 s62, 14\n\ts_cselect_b32 s53, " STR(PRE1) "-1, " STR(PRE2) "-1\n\ts_setprio 3\n\t"
    "s_waitcnt lgkmcnt(0)\n\t"
    "s_bitcmp1_b32 %[wg], 3\n\ts_cselect_b64 s[56:57], s[74:75], s[72:73]\n\ts_cmp_ge_u32 s62, 14\n\ts_cselect_b64 s[56:57], s[56:57], s[76:77]\n\ts_cselect_b32 s52, 0x180, -1\n\ts_and_b32 s57, s57, 0xffff\n\ts_add_u32 s60, s56, s54\n\ts_and_b32 s60, s60, 0x380\n\ts_cmp_eq_u32 s60, s52\n\ts_cselect_b32 s53, " STR(PRE3) "-1, s53\n\t"
    "s_load_dwordx2 s[96:97], s[66:67], 0x0\n\ts_barrier\n\t"
    "\n3:\n\t" "buffer_load_dwordx4 v118, s[56:59], s54 offen nt lds\n\ts_add_u32 m0, m0, 0x1000\n\ts_add_u32 s54, s54, s55\n\ts_sub_u32 s53, s53, 1\n\ts_cbranch_scc0 3b\n"
    "9:\n8:"
    : [xa]"=&{v[2:5]}"(xa),[xb]"=&{v[6:9]}"(xb_),[xc]"=&{v30}"(xc),[wa]"=&{v[10:13]}"(wa),[wb]"=&{v[14:17]}"(wb),[wc]"=&{v31}"(wc),[d0]"=&{v[18:21]}"(dq[0]),[d1]"=&{v[22:25]}"(dq[1]),[d2]"=&{v[26:29]}"(dq[2])
    : [ka]"s"(__builtin_amdgcn_kernarg_segment_ptr()),[wg]"s"(BID),[t]"v"(TID),[yst]"i"(YST),[wy]"i"(W_Y),[r2]"i"(O_R2),[sl]"i"(SLSZ),[tb]"i"(O_CH+O_TB),[ex]"i"(O_EXP),[de]"i"(O_DE),[ust]"i"(GST_)
    : "memory","scc","m0","v127","s50","s51","s52","s53","s54","s55","s56","s57","s58","s59","s60","s61","s62","s63","s64","s65","s66","s67","s68","s69","s70","s71","s72","s73","s74","s75","s76","s77","s78","s79",
      "s80","s81","s82","s83","s84","s85","s86","s87","s88","s89","s90","s91","s92","s93","s94","s95","s99");
  int wv=RFL(TID>>6), bid=BID, q4=wv>>2, w4=wv&3;
  int xg=bid&7, lb=bid>>3, tb=32*xg+lb, nch=2+(lb<8);
  u64 ep; u32 ldw;
  {
  }
  #define KILLIN "=v"(xa),"=v"(xb_),"=v"(xc),"=v"(wa),"=v"(wb),"=v"(wc),"=v"(dq[0]),"=v"(dq[1]),"=v"(dq[2])
  if(__builtin_expect(q4==3&&lb>=8,0)){
    int fu=18*xg+lb-14, kq=2*xg+(lb>=11), cgp=lb-8-3*(lb>=11);
  u32 wst= lb>=14? F_*64:D_*64;
  u32 wso=(lb>=14? (fu>>1)*128 : kq*(288*D_*2)+384*cgp)+(lb>=14? PRE1:PRE2)*wst, wld=(u32)(size_t)sm+O_R2+w4*1024+(lb>=14? PRE1:PRE2)*4096; u32x4 wrs=mkrs(lb>=14? ((fu&1)? wu:wg):wd,D_*F_*2);
  GETEP
  #define FLANE(RS) LANE(ln); int g=ln>>4, li=ln&15, qq=li>>2, pq=li&3; u32 wvo=(u32)(8*w4+(ln>>3))*(RS)+(u32)((ln&7)^(((ln>>3)&2)|((w4&1)<<2)))*16u; \
    const lch* tp=sm+O_R2+(8*g+qq)*128+((w4^((qq>>1)|((g&1)<<1)))*32)+pq*8;
  #define WFR(img,ks) trfrag(tp+(img)*CHSZ+(ks)*4096,tp+(img)*CHSZ+(ks)*4096+512)
  u64 tag=ep+1ull; const u32x4 I4={INV,INV,INV,INV};
  if(lb>=14){
    FLANE(F_*2)
    int mt=fu&1;
    if(w4==0) ST(u32x4,sm+O_ONE+ln*16)=u32x4{0x3f803f80u,0x3f803f80u,0x3f803f80u,0x3f803f80u}; else if(ln<8) ST(u32x4,sm+O_ONE+1024+ln*16)=u32x4{0x3f803f80u,0x3f803f80u,0x3f803f80u,0x3f803f80u};
    const lch* xp=sm+(li<8? O_TILE+li*XSTR : O_ONE)+g*16; int of=(mt*288+(fu>>1)*4+w4)*256+(li&7)*32+g*8+(li>>3)*4;
    const lch* mp=sm+O_RC+(li&7)*4;
    asm volatile("" : "+v"(xp),"+v"(of),"+v"(mp));
    rsrc_t rsG=RSRC(ws+W_GU,2*8*F_*2);
    if(w4==0) BST128(I4,rsG,(mt*288+(fu>>1)*4)*256+ln*16,0,SC1);
    bool slow=(((u32)wrs[0]+wso)&0x380u)==0x180u;
    if(slow){ wso-=(PRE1-PRE3)*wst; wld-=(PRE1-PRE3)*4096; FILL(5+PRE1-PRE3,VW(W3)); } else FILLD(5,W1,W1B);
    if(w4==0){ VMW(5); if(ln==0) GST(ws+W_FL+(576+fu)*8,tag); }
    if(slow) FILLD(13-PRE1,W3,W1B); else FILLD(13-PRE1,W1,W1B);
    bf16x8 fB[18];
    VMW(LAST1); gsync(sm,C_P,4);
    #pragma unroll
    for(int ks=0;ks<18-LAST1;ks++) fB[ks]=WFR(0,ks);
    #pragma unroll
    for(int ks=0;ks<18-LAST1;ks++) asm volatile("" : "+v"(fB[ks]));
    DRAIN(); gsync(sm,C_P,8);
    #pragma unroll
    for(int ks=18-LAST1;ks<18;ks++) fB[ks]=WFR(0,ks);
    f32x4 a0={0,0,0,0}; float nm,ri,ce; u32 mdw;
    { bf16x8 xb[18];
      gwait<2>(sm,C_X,1);
      gwait4<64>(C_X,4,xp,xb[0],xb[1],xb[2],xb[3]);
      #pragma unroll
      for(int ks=4;ks<7;ks++) xb[ks]=LD(bf16x8,xp+ks*64);
      SCHED();
      #pragma unroll
      for(int ks=0;ks<17;ks++){
        a0=__builtin_amdgcn_mfma_f32_16x16x32_bf16(fB[ks],xb[ks],a0,0,0,0); SCHED();
        if(2*ks+7<18) xb[2*ks+7]=LD(bf16x8,xp+(2*ks+7)*64); if(2*ks+8<18) xb[2*ks+8]=LD(bf16x8,xp+(2*ks+8)*64);
        if(ks==5) mdw=LD(u32,mp); if(ks==14){ ri=rcpf_(bhi(mdw)); nm=-blo(mdw); PIN(nm); } if(ks==15){ ce=ri*-1.4426950408889634f; PIN(ce); }
        SCHED(); }
      #define RR8 " row_ror:8 row_mask:0xf bank_mask:"
      float v0,v1,s0,s1;
      asm volatile("v_mfma_f32_16x16x32_bf16 v[112:115], %4, %5, %6\n\ts_nop 7\n\tv_fmac_f32_dpp v114, v114, %7" RR8 "0xf\n\tv_fmac_f32_dpp v115, v115, %7" RR8 "0xf\n\tv_fmac_f32_dpp v112, v112, %7" RR8 "0xf\n\tv_fmac_f32_dpp v113, v113, %7" RR8 "0xf\n\t"
        "v_mov_b32_dpp v112, v114" RR8 "0xc\n\tv_mov_b32_dpp v113, v115" RR8 "0xc\n\tv_mul_f32 %2, %9, v112\n\tv_mul_f32 %3, %9, v113\n\tv_mul_f32 %0, %8, v112\n\tv_mul_f32 %1, %8, v113"
        : "=&v"(v0),"=&v"(v1),"=&v"(s0),"=&v"(s1) : "v"(fB[17]),"v"(xb[17]),"v"(a0),"v"(nm),"v"(ri),"v"(ce));
      if(mt==0){ v0*=rcpf_(1.f+__builtin_amdgcn_exp2f(s0)); v1*=rcpf_(1.f+__builtin_amdgcn_exp2f(s1)); }
      __builtin_amdgcn_raw_buffer_store_b32(nv(pk(v0,v1)),rsG,of,0,SC1); garrive(sm,C_H); }
    if(mt==0&&w4==0){
    LANE(l2); ln=l2; li=ln&15; g=ln>>4;
    int c9=(lb-14)>>1, t=RFL((int)LD(u16,sm+O_IDX+xg*2)), c=64*c9+4*li+g; f32x4 pr;
    { u32 u=LD(u16,sm+O_IDX+16+xg*2)^0x8000u, xb=LD(u16,sm+O_TILE+xg*XSTR+c*2), sv=LD(u32,sm+O_RC+xg*4); float ds=b2f(Dsk[c]); u^=(u32)(((int)(u<<16))>>31)&0x7fffu;
      poll1<0>(ws+W_FL+(64*c9+ln)*8,tag); poll1<0>(ws+W_FL+(720+(ln<48? ln:47))*8,tag);
      float xn=(b2f(xb)-blo(sv))*rcpf_(bhi(sv)); pr=f32x4{b2f(xb),xn,xn*ds,sigm_f(b2f(u))}; }
    gwait<2>(sm,C_PR,1);
    { u32x4 rs=mkrs(ws+W_PART+(xg*9+c9)*4096,4096), ry=mkrs(ws+W_Y,D_*YST);
      int yo=c*YST+(t&~1)*2, po=ln*16; u32 a[4], t0,cnt,yb; u16* op=out+t*D_+c;
      #define LD_P "buffer_load_dwordx4 v[112:115], %[of], %[rs], 0 offen sc1\n\tbuffer_load_dwordx4 v[116:119], %[of], %[rs], 0 offen offset:1024 sc1\n\tbuffer_load_dwordx4 v[120:123], %[of], %[rs], 0 offen offset:2048 sc1\n\tbuffer_load_dwordx4 v[124:127], %[of], %[rs], 0 offen offset:3072 sc1\n\t"
      #define LD_YB "buffer_load_dword %[yb], %[yo], %[ry], 0 offen sc1\n\t"
      #define AD4(o,a,b,c,d) "v_add_f32 %[t0], v" #a ", v" #b "\n\tv_add_f32 %[" #o "], v" #c ", v" #d "\n\tv_add_f32 %[" #o "], %[t0], %[" #o "]\n\t"
      u64 ym;
      asm volatile(LD_YB LD_P "s_mov_b32 %[cnt], 0x20000\n1:\n\ts_waitcnt vmcnt(0)\n\tv_cmp_eq_u32 %[ym], -1, %[yb]\n\t" CHK_I0 MX2(116,117) MX2(118,119) MX2(120,121) MX2(122,123) MX2(124,125) MX2(126,127) CHK_IE
        "s_or_b64 exec, vcc, %[ym]\n\ts_cbranch_scc0 9f\n\ts_sleep " STR(QSLP) "\n\ts_mov_b64 exec, %[ym]\n\ts_cbranch_execz 2f\n\t" LD_YB "2:\n\ts_mov_b64 exec, vcc\n\ts_cbranch_execz 3f\n\t" LD_P
        "3:\n\ts_mov_b64 exec, -1\n\ts_sub_u32 %[cnt], %[cnt], 1\n\ts_cbranch_scc0 1b\n9:\n\ts_mov_b64 exec, -1\n\t"
        AD4(a0,112,116,120,124) AD4(a1,113,117,121,125) AD4(a2,114,118,122,126) AD4(a3,115,119,123,127) "s_nop 1"
        : [t0]"=&v"(t0),[cnt]"=&s"(cnt),[a0]"=&v"(a[0]),[a1]"=&v"(a[1]),[a2]"=&v"(a[2]),[a3]"=&v"(a[3]),[yb]"=&v"(yb),[ym]"=&s"(ym) : [of]"v"(po),[rs]"s"(rs),[yo]"v"(yo),[ry]"s"(ry),"v"(op),"v"(pr) : PCLOB);
      auto s0=__builtin_amdgcn_permlane32_swap(a[0],a[2],false,false); auto s1=__builtin_amdgcn_permlane32_swap(a[1],a[3],false,false);
      u32 x0=s0[0],x1=s0[1],y0=s1[0],y1=s1[1]; u32 p0=BC(u32,BC(float,x0)+BC(float,x1)), p1=BC(u32,BC(float,y0)+BC(float,y1));
      auto s2=__builtin_amdgcn_permlane16_swap(p0,p1,false,false); u32 z0=s2[0], z1=s2[1]; float s=BC(float,z0)+BC(float,z1);
      float gq=silu_f(__builtin_fmaf(s,pr[3],pr[1]));
      float sy=((t&1)? bhi(yb):blo(yb))+pr[2];
      float o=__builtin_fmaf(sy,gq,pr[0]);
      *op=(u16)(pk(o,o)&0xffffu);
      if(bid==112&&ln==0) GST(ws+W_EPOCH,tag); }
    }
  } else
  {
    FLANE(D_*2)
    bf16x8 fr[27];
    rsrc_t rsP=RSRC(ws+W_PART+cgp*12288+kq*256+w4*64,8*9*4096-2*12288);
    #pragma unroll
    for(int i=0;i<3;i++) BST128(I4,rsP,li<8? li*(9*4096)+g*16+i*4096 : OOR,0,SC1);
    if(PRE2<9) FILLD(9-PRE2,W2_,W2B);
    wso+=128u-9u*wst;
    FILLD(9,W2_,W2B);
    VMW(9); gsync(sm,C_P,4);
    #pragma unroll
    for(int ks=0;ks<9;ks++) fr[ks]=WFR(0,ks);
    gsync(sm,C_P,8); if(w4==0&&ln==0) GST(ws+W_FL+(720+6*xg+lb-8)*8,tag);
    wso+=128u-9u*wst; wld-=2*CHSZ;
    FILL(9,VW(W2B));
    int fo=W_FL+(576+((9*kq)&~1)+(ln<9? ln:9))*8;
    if(w4==0) asm volatile("global_load_dwordx2 v[112:113], %0, %1 sc1\n\ts_waitcnt vmcnt(10)" :: "v"(fo),"s"(ws) : "memory"); else VMW(9);
    gsync(sm,C_P,12);
    #pragma unroll
    for(int ks=0;ks<9;ks++) fr[9+ks]=WFR(1,ks);
    { int t2=TID; asm volatile("" : "+v"(t2)); ln=t2&63; li=ln&15; g=ln>>4; }
    const lch* hp=sm+O_TILE+(li&7)*64+g*16; int po= li<8? li*(9*4096)+g*16+4096 : OOR; PIN(hp); PIN(po);
    u32x4 rs=mkrs(ws+W_GU,2*8*F_*2);
    int of=((ln>>5)*288+18*kq+2*w4)*256+(ln&31)*16; lch* hd=sm+O_TILE+w4*512+((ln>>1)&7)*64+((ln>>4)&1)*32+(ln&1)*16+(ln>>5)*8; PIN(of); PIN(hd); asm volatile("" : "+s"(rs));
    DRAIN();
    if(w4==0){ u64 bad; asm volatile("v_cmp_ne_u64 %0, %1, v[112:113]" : "=s"(bad) : "s"(tag) : "memory");
      if(bad!=0ull) poll1<0>(ws+fo,tag); }
    gsync(sm,C_P,16);
    { u32 t0,cnt;
      #define LD_G "buffer_load_dwordx4 v[112:115], %[of], %[rs], 0 offen sc1\n\tbuffer_load_dwordx4 v[116:119], %[of], %[rs], 0 offen offset:2048 sc1\n\tbuffer_load_dwordx4 v[120:123], %[of], %[rs], %[so] offen sc1\n\t"
      #define HM1(a,b) "v_lshlrev_b32 %[t0], 16, v" #a "\n\tv_lshlrev_b32 %[t1], 16, v" #b "\n\tv_and_b32 v" #a ", %[hm], v" #a "\n\tv_and_b32 v" #b ", %[hm], v" #b "\n\tv_mul_f32 %[t0], %[t0], %[t1]\n\tv_mul_f32 v" #a ", v" #a ", v" #b "\n\tv_cvt_pk_bf16_f32 v" #a ", %[t0], v" #a "\n\t"
      #define HMUL(a,b,c,d,o) "v_permlane32_swap_b32 v" #a ", v" #c "\n\tv_permlane32_swap_b32 v" #b ", v" #d "\n\t" HM1(a,c) HM1(b,d) "ds_write_b64 %[hd], v[" #a ":" #b "] offset:" #o "\n\t"
      u32 t1;
      asm volatile(LD_G :: [of]"v"(of),[rs]"s"(rs),[so]"s"(w4==0? 4096:0) : "memory");
      fr[18]=WFR(0,0); PIN(fr[18]);
      asm volatile(POLLASM(LD_G,CHK_I0 MX2(116,117) MX2(118,119) MX2(120,121) MX2(122,123) CHK_IE,GSLP)
        HMUL(112,113,114,115,0) HMUL(116,117,118,119,2048) "s_cmp_eq_u32 %[so], 0\n\ts_cbranch_scc1 8f\n\t" HMUL(120,121,122,123,4096) "8:\n\ts_mov_b64 exec, 1\n\tds_add_u32 %[ch], %[one]\n\ts_mov_b64 exec, -1"
        : [t0]"=&v"(t0),[t1]"=&v"(t1),[cnt]"=&s"(cnt) : [of]"v"(of),[rs]"s"(rs),[so]"s"(w4==0? 4096:0),[hd]"v"(hd),[hm]"s"(0xffff0000u),[ch]"v"(C_H*4),[one]"v"(1),"v"(hp),"v"(po) : PCLOB);
      }
    { f32x4 ac[3]={{0,0,0,0},{0,0,0,0},{0,0,0,0}};
      bf16x8 hb[9];
      gwait4<512>(C_H,4,hp,hb[0],hb[1],hb[2],hb[3]);
      #pragma unroll
      for(int ks=0;ks<10;ks++){
        if(ks<9){ ac[0]=__builtin_amdgcn_mfma_f32_16x16x32_bf16(fr[ks],hb[ks],ac[0],0,0,0); ac[1]=__builtin_amdgcn_mfma_f32_16x16x32_bf16(fr[9+ks],hb[ks],ac[1],0,0,0); }
        if(ks>0) ac[2]=__builtin_amdgcn_mfma_f32_16x16x32_bf16(fr[17+ks],hb[ks-1],ac[2],0,0,0);
        SCHED();
        if(ks<4){ fr[19+2*ks]=WFR(0,1+2*ks); fr[20+2*ks]=WFR(0,2+2*ks); } if(ks>=1&&ks<=2){ hb[2*ks+2]=LD(bf16x8,hp+(2*ks+2)*512); hb[2*ks+3]=LD(bf16x8,hp+(2*ks+3)*512); } if(ks==3) hb[8]=LD(bf16x8,hp+8*512);
        SCHED(); }
      rsrc_t r0=RSRC(ws+W_PART+cgp*12288+kq*256+w4*64-4096,8*9*4096), r2=RSRC(ws+W_PART+cgp*12288+kq*256+w4*64+4096,8*9*4096-2*12288);
      BST128(nv4(BC(u32x4,ac[0])),r0,po,0,SC1); BST128(nv4(BC(u32x4,ac[1])),rsP,po,0,SC1); BST128(nv4(BC(u32x4,ac[2])),r2,po,0,SC1); }

  }
  asm volatile("s_endpgm" : KILLIN :: "memory"); }
  {
  if(__builtin_expect(q4!=0,0)){ int grp= q4==1? nch-1 : q4-2;
    chan(sm,grp,(w4+(0x48>>(2*q4)))&3,72*xg+(q4==3? 16+lb : 2*lb+(lb<8? 0:8)+2-q4),(q4+1)>>2,ws,xa,xb_,wa,wb,dq[0]);
    asm volatile("s_endpgm" : KILLIN :: "memory"); }
  u32x4 xq[3]; f32x2 rc; int xr_,xr2_;
  {
    LANE(ln); int g=ln>>4, li=ln&15, qq=li>>2, pq=li&3, j=ln&31, rl=2*w4+(ln>>5);
    lch* tile=sm+O_TILE; u32 Tr=(ln>>1)&16;
    int uo=W_U+(tb>>TL)*GST_+(tb&((1<<TL)-1))*(128>>TL)+(w4*1024<<TL)+(ln>>(3-TL))*128+(ln&(7>>TL))*16, uo1=uo+(4096<<TL), uo2=uo+(8192<<TL); PIN(uo); PIN(uo1); PIN(uo2);
    lch* tw=tile+rl*TSTR2+j*16; lch* tw2=tw+1024-j*12; u32 one=0x3f803f80u; const lch* tp=tile+qq*TSTR2+(w4*64+g*16+pq*4)*2;
    int go= ln<32? W_RG+(ln>>2)*8192+(tb*4+(ln&3))*8 : W_ST+(tb*8+(ln&7))*8; lch* kw=sm+O_RV+rl*2; const lch* kr=sm+(ln<32? O_RV+(ln&3)*4 : O_MD+(ln&7)*4); PIN(kw); PIN(kr);
    int xr=(32*(tb>>2)+8*g+(li&7))*(D_*2)+288*(lb&3)+32*w4+(ln&8)*2, xr2= w4==0? xr+256:OOR;
    rsrc_t rx=RSRC(x,L_*D_*2); u64 gm=((1ull<<(4*NRG))-1ull)|0xff00000000ull; u32 um=~UMASK, km=0x80008000u; asm volatile("" : "+s"(gm),"+s"(um),"+s"(km),"+s"(ws));
    #define D2(a,b,c) __builtin_amdgcn_fdot2_f32_bf16(BC(bf2,a),BC(bf2,b),c,false)
    asm volatile("s_waitcnt vmcnt(6)" : "+v"(wa),"+v"(wb),"+v"(wc),"+v"(Tr),"+v"(tw),"+v"(tw2),"+v"(tp),"+v"(one),"+v"(go),"+v"(xr),"+v"(xr2) :: "memory");
    u32 wq[9]={wa[0],wa[1],wa[2],wa[3],wb[0],wb[1],wb[2],wb[3],wc}; float sw;
    { float a[3];
      #pragma unroll
      for(int i=0;i<9;i++) a[i%3]=D2(wq[i],one,i<3? 0.f:a[i%3]);
      sw=add16x(sum16((a[0]+a[1])+a[2])); }
    GETEP u32 tg=(u32)ep+1u; Tr=((upat(tg,w4)>>Tr)&0xffffu)*0x10001u; u32 tgv=tg;
    float z_[9]={0,0,0,0,0,0,0,0,0};
    asm volatile("s_waitcnt vmcnt(3)" : "+v"(xa),"+v"(xb_),"+v"(xc),"+v"(Tr),"+v"(sw),"+v"(tgv),"+v"(z_[0]),"+v"(z_[1]),"+v"(z_[2]),"+v"(z_[3]),"+v"(z_[4]),"+v"(z_[5]) :: "memory");
    u32 v[9]={xa[0],xa[1],xa[2],xa[3],xb_[0],xb_[1],xb_[2],xb_[3],xc};
    { float xf[18], s1, s2, s3;
      #pragma unroll
      for(int i=0;i<9;i++){ z_[i&1]=D2(v[i],one,z_[i&1]); z_[2+(i&1)]=D2(v[i],v[i],z_[2+(i&1)]); z_[4+(i&1)]=D2(v[i],wq[i],z_[4+(i&1)]); }
      s1=z_[0]+z_[1]; s2=z_[2]+z_[3]; s3=z_[4]+z_[5];
      SCHED();
      #define RSTEP(C) s1+=dppf<C>(s1); s2+=dppf<C>(s2); s3+=dppf<C>(s3); SCHED();
      RSTEP(0xB1) RSTEP(0x4E) RSTEP(0x141) RSTEP(0x140)
      s1=add16x(s1)*(1.f/576.f); s2=add16x(s2)*(1.f/576.f); s3=add16x(s3);
      float mm=s1*s1, var=s2-mm;
      if(__builtin_expect(BALLOT(!(mm<=64.f*var))!=0ull,0)){
        float q=0.f;
        #pragma unroll
        for(int i=0;i<9;i++){ u32 t=v[i]; PIN(t); float d0=blo(t)-s1, d1=bhi(t)-s1; q=__builtin_fmaf(d0,d0,q); q=__builtin_fmaf(d1,d1,q); }
        var=add16x(sum16(q))*(1.f/576.f); }
      #define UNP(i) PIN(v[i]); xf[2*(i)]=blo(v[i]); xf[2*(i)+1]=bhi(v[i]); PIN(xf[2*(i)]); PIN(xf[2*(i)+1]); SCHED();
      u32 vb=pk(var,s1); UNP(0)
      float den=blo(vb)+1e-5f, m=bhi(vb); UNP(1)
      vb=pk(den,den); s3=__builtin_fmaf(-m,sw,s3); UNP(2)
      den=__builtin_amdgcn_sqrtf(bhi(vb)); UNP(3)
      u32 md=pk(m,den); UNP(4)
      float ri=rcpf_(bhi(md)); UNP(5)
      s3*=ri; float rs_=ri*0.0625f; UNP(6)
      u32 u=pk(s3,s3); float cs_=-m*rs_; UNP(7)
      { u32 sg=(u32)((int)u>>31); PIN(sg); u^=sg|km; } UNP(8)
      asm volatile("s_mov_b64 exec, 0x1\n\ts_mov_b32 exec_hi, 1\n\tds_write_b16 %0, %1\n\tv_add_u32 %0, %0, %0\n\tds_write_b32 %0, %2 offset:%3\n\ts_mov_b32 exec_hi, 0\n\tds_add_u32 %4, %5\n\ts_mov_b64 exec, -1" : "+v"(kw) : "v"(u),"v"(md),"i"(O_MD-2*O_RV),"v"(C_I*4),"v"(1) : "memory");
      if(w4==3){ gwait<0>(sm,C_I,4); asm volatile("ds_read_b32 v118, %4\n\tv_mov_b32 v119, %1\n\ts_mov_b64 exec, %3\n\ts_waitcnt lgkmcnt(0)\n\tglobal_store_dwordx2 %0, v[118:119], %2 sc1\n\ts_mov_b64 exec, -1" :: "v"(go),"v"(tgv),"s"(ws),"s"(gm),"v"(kr) : "memory"); }
      SCHED();
      u32 h[9];
      #define UH(i) h[i]=(pkz(__builtin_fmaf(xf[2*(i)],rs_,cs_),__builtin_fmaf(xf[2*(i)+1],rs_,cs_))&um)|Tr;
      UH(0) UH(1) UH(2) UH(3) ST(u32x4,tw)=u32x4{h[0],h[1],h[2],h[3]}; UH(4) UH(5) UH(6) UH(7) ST(u32x4,tw+512)=u32x4{h[4],h[5],h[6],h[7]};
      UH(8) ST(u32,tw2)=h[8]; }
    gsync(sm,C_L,4);
    { u32x4 uq[3];
      #pragma unroll
      for(int z=0;z<3;z++) uq[z]=BC(u32x4,trfrag(tp+z*512,tp+z*512+4*TSTR2));
      asm volatile("global_store_dwordx4 %0, %1, %2 sc1" :: "v"(uo),"v"(uq[0]),"s"(ws) : "memory");
      asm volatile("global_store_dwordx4 %0, %1, %2 sc1" :: "v"(uo1),"v"(uq[1]),"s"(ws) : "memory");
      asm volatile("s_cmp_lg_u32 %3, 0\n\ts_cbranch_scc1 1f\n\tglobal_store_dwordx4 %0, %1, %2 sc1\n1:\n\ts_nop 1" :: "v"(uo2),"v"(uq[2]),"s"(ws),"s"(w4),"v"(uq[0]),"v"(uq[1]) : "memory","scc");
      asm volatile("" : "+v"(dq[0]),"+v"(dq[1]),"+v"(dq[2]));
      xr_=xr; xr2_=xr2;
      #define XQ { rsrc_t rx_=RSRC(x,L_*D_*2); xq[0]=BLD128(rx_,xr_,0,0); xq[1]=BLD128(rx_,xr_+128,0,0); xq[2]=BLD128(rx_,xr2_,0,0); }
    }
    if(lb>=2&&lb<8){
      const u16* wm=(lb&1)? wu:wg; int co=(3-(int)((u64)wm>>7)-xg)&7, r_=576-12*PFN+(((lb>>1)-1)*4+w4)+12*j;
      asm volatile("buffer_load_dword v120, %0, %1, 0 offen" :: "v"(j<PFN&&(ln<32||co==0)? r_*(F_*2)+(9*xg+co)*128+(ln>>5)*1024 : OOR),"s"(mkrs(wm,D_*F_*2)) : "memory"); }
    if(lb>=8&&lb<14) garrive(sm,C_D);
  }
  FP fp;
  { LANE(ln); int g=ln>>4, li=ln&15; u64 tag=ep+1ull;
    const char* sp_=ws+W_ST+(32*(tb>>2)+8*g+(li&7))*8; u64 fs, ff; const char* fp_=ws+W_FL+(144*(lb&3)+16*w4+64*(g<2||w4==0? (g<3? g:2):0)+li)*8;
    if(lb==CTLB||lb>=14){
      u32 M=(u32)tag, rank,key;
      top8(sm,w4,ln,M,ws,rank,key);
      {
        int t0=__builtin_amdgcn_readlane((int)key,__builtin_ctzll(BALLOT(rank==(u32)(2*w4)))), t1=__builtin_amdgcn_readlane((int)key,__builtin_ctzll(BALLOT(rank==(u32)(2*w4+1))));
        int tsel=~((ln>>5)? t1:t0)&2047, j=ln&31; rsrc_t rx=RSRC(x,L_*D_*2);
        u32x4 a=BLD128(rx,tsel*1152+j*16,0,0), b=BLD128(rx,tsel*1152+j*16+512,0,0); u32 c=BLD32(rx,tsel*1152+j*4+1024,0,0);
        const char* rp_=ws+W_ST+tsel*8; u64 f1=GLD(rp_);
        garrive(sm,C_D);
        if(lb==CTLB&&rank<8&&!(ln&1)) GST(ws+W_IDX+xg*1024+w4*128+rank*8,(tag<<16)|(~key&2047u));
        if(w4==0){ if(rank<8) ST(u16,sm+O_IDX+(ln&1)*16+rank*2)=(u16)((ln&1)? key>>16 : ~key&2047u); garrive(sm,C_IX); }
        lch* xw=sm+O_TILE+(2*w4+(ln>>5))*XSTR+j*16;
        if(BALLOT((u32)(f1>>32)!=(u32)tag)!=0ull) f1=poll1<32>(rp_,tag<<32);
        ST(u32x4,xw)=a; ST(u32x4,xw+512)=b; ST(u32,xw+1024-j*12)=c; if(j==0) ST(u32,sm+O_RC+(2*w4+(ln>>5))*4)=(u32)f1;
        garrive(sm,C_X);
      }
    }
    PRIO(0);
    if(lb<14) gwait<8>(sm,C_U,2); else gwait<4>(sm,C_H,4);
    fs=GLD(sp_); ff=GLD(fp_); XQ
    {
      if(BALLOT((u32)(fs>>32)!=(u32)tag)!=0ull) fs=poll1<32>(sp_,tag<<32);
      float ri=rcpf_(bhi((u32)fs)); rc=f32x2{ri,-blo((u32)fs)*ri}; }
    asm volatile("" : "+v"(rc),"+v"(xq[0]),"+v"(xq[1]),"+v"(xq[2]));
    bool ix= w4==3&&lb<14&&lb!=CTLB; u64 f; const char* p=ws+W_IDX+(xg*8+(lb&3))*128+(ln&7)*8; if(ix) f=GLD(p);
    final_pre(fp,w4,xq,dq,rc);
    if(BALLOT(ff!=tag)!=0ull) poll1<0>(fp_,tag);
    if(ix){ if(BALLOT(((f^(tag<<16))>>16)!=0ull)!=0ull) f=poll1<16>(p,tag<<16);
      if(ln<8) ST(u16,sm+O_IDX+ln*2)=(u16)f; garrive(sm,C_IX); }
  }
  final_post(sm,fp,w4,nch,out,ws);
  }
}

extern "C" void kernel_launch(void* const* d_in, const int* in_sizes, int n_in, void* d_out, int out_size, void* d_ws, size_t ws_size, hipStream_t stream) {
  const u16** I=(const u16**)d_in;
  KP kp; kp.Are=I[1]; kp.Aim=I[2]; kp.Bre=I[3]; kp.Bim=I[4]; kp.Cre=I[5]; kp.Cim=I[6]; kp.ldt=I[7]; kp.pad=I[7];
  kA<<<256,1024,0,stream>>>(I[0],(char*)d_ws+((size_t)128<<20),I[10],I[8],I[11],I[12],I[13],(u16*)d_out,kp);
}
```

```cpp
#include <hip/hip_runtime.h>
#include <hip/hip_bf16.h>
#include <stdint.h>
#if defined(__HIP_DEVICE_COMPILE__)
#define NOPK __attribute__((target("no-packed-fp32-ops")))
#else
#define NOPK
#endif
#define DEV __device__ NOPK __forceinline__
typedef unsigned short u16;
typedef unsigned int u32;
typedef unsigned long long u64;
typedef short bf16x8 __attribute__((ext_vector_type(8)));
typedef _Float16 h8 __attribute__((ext_vector_type(8)));
typedef _Float16 h2 __attribute__((ext_vector_type(2)));
typedef float f32x4 __attribute__((ext_vector_type(4)));
typedef float f32x2 __attribute__((ext_vector_type(2)));
typedef u32 u32x4 __attribute__((ext_vector_type(4)));
typedef u32 u32x2 __attribute__((ext_vector_type(2)));
typedef __bf16 bf2 __attribute__((ext_vector_type(2)));
typedef short v4s __attribute__((ext_vector_type(4)));
typedef __amdgpu_buffer_rsrc_t rsrc_t;
#define GAS __attribute__((address_space(1)))
#define LAS __attribute__((address_space(3)))
typedef LAS char lch;
#define L_ 2048
#define D_ 576
#define F_ 4608
#define BC(T,v) __builtin_bit_cast(T,v)
#define TID ((int)__builtin_amdgcn_workitem_id_x())
#define BID ((int)__builtin_amdgcn_workgroup_id_x())
#define RFL(x) __builtin_amdgcn_readfirstlane(x)
#define RLX_AGENT __ATOMIC_RELAXED, __HIP_MEMORY_SCOPE_AGENT
#define LD(T,p) (*(const LAS T*)(p))
#define ST(T,p) (*(LAS T*)(p))
#define SPINCAP (1<<17)
#define DRAIN() asm volatile("s_waitcnt vmcnt(0)" ::: "memory")
#define GLD(p) __hip_atomic_load((const GAS u64*)(p),RLX_AGENT)
#define GST(p,v) __hip_atomic_store((GAS u64*)(p),(u64)(v),RLX_AGENT)
#define BALLOT(c) __builtin_amdgcn_ballot_w64(c)
#define SC1 16
#define LANE(l) int l; { int t2_=TID; asm volatile("" : "+v"(t2_)); l=t2_&63; }
#define WPOL "nt"
#define UE 0
#define NOG2 0
#define SCHED() __builtin_amdgcn_sched_barrier(0)
#define RSRC(p,n) __builtin_amdgcn_make_buffer_rsrc((void*)(p),0,(n),0x00020000)
#define BLD128 __builtin_amdgcn_raw_buffer_load_b128
#define BLD32 __builtin_amdgcn_raw_buffer_load_b32
#define BST128 __builtin_amdgcn_raw_buffer_store_b128
#define PRIO(n) __builtin_amdgcn_s_setprio(n)

DEV float b2f(u32 v){ return BC(float,v<<16); }
DEV float blo(u32 v){ return BC(float,v<<16); }
DEV float bhi(u32 v){ return BC(float,v&0xffff0000u); }
DEV u32 pk(float a,float b){ f32x2 v={a,b}; bf2 r=__builtin_convertvector(v,bf2); return BC(u32,r); }
DEV u32 pkh(float a,float b){ f32x2 v={a,b}; h2 r=__builtin_convertvector(v,h2); return BC(u32,r); }
DEV float rb(float f){ return BC(float,pk(f,f)<<16); }
DEV float rcpf_(float x){ return __builtin_amdgcn_rcpf(x); }
DEV float expf_(float x){
  const float L2E=1.4426950216293335f, L2El=1.9259629911266175e-8f; float h=x*L2E; float l=__builtin_fmaf(x,L2E,-h)+x*L2El;
  float e=__builtin_amdgcn_exp2f(h); return __builtin_fmaf(e*l,0.6931471805599453f,e); }
DEV float sigm_f(float x){ return rcpf_(1.f+__builtin_amdgcn_exp2f(-x*1.4426950408889634f)); }
DEV float silu_f(float x){ return x*sigm_f(x); }
DEV u32 umax_(u32 a,u32 b){ return a>b?a:b; }
DEV u32 umin_(u32 a,u32 b){ return a<b?a:b; }

#define YSH 3
#define PRE1 6
#define PRE3 4
#define PRE2 5
#define W1 5
#define W3 3
#define W2_ 4
#define W1B 20
#define W2B 12
#define LAST1 4
#define RGATE 4
#define UGATE 7
#define R2D 12
#define PFN 32
#define CPF 8
#define NRG 8
#define W_EPOCH 0
#define W_RG 16384
#define W_ST (W_RG+32*8192)
#define W_U (W_ST+32768)
#define UST (D_*16+128)
#define YST (L_*2+128)
#define TL 1
#define GST_ ((D_*16<<TL)+128)
#define W_Y (W_U+256*UST)
#define W_PART (W_Y+D_*YST)
#define W_GU (W_PART+8*9*16*256)
#define W_FL 9216
#define W_IDX 1024
#define CTLB 0
struct KP { const u16 *Are,*Aim,*Bre,*Bim,*Cre,*Cim,*ldt,*pad; };

template<int CTRL> DEV float dppf(float v){ return BC(float,__builtin_amdgcn_update_dpp(0u,BC(u32,v),CTRL,0xf,0xf,false)); }
template<int CTRL> DEV u32 dppu(u32 v){ return (u32)__builtin_amdgcn_update_dpp(0u,v,CTRL,0xf,0xf,false); }
DEV float sum16(float v){ v+=dppf<0xB1>(v); v+=dppf<0x4E>(v); v+=dppf<0x141>(v); v+=dppf<0x140>(v); return v; }
DEV float add16x(float v){ auto r=__builtin_amdgcn_permlane16_swap(BC(u32,v),BC(u32,v),false,false); u32 a=r[0], b=r[1]; return BC(float,a)+BC(float,b); }
DEV u32 wmax(u32 v){
  v=umax_(v,(u32)__builtin_amdgcn_update_dpp(0u,v,0x111,0xf,0xf,false));
  v=umax_(v,(u32)__builtin_amdgcn_update_dpp(0u,v,0x112,0xf,0xf,false));
  v=umax_(v,(u32)__builtin_amdgcn_update_dpp(0u,v,0x114,0xf,0xf,false));
  v=umax_(v,(u32)__builtin_amdgcn_update_dpp(0u,v,0x118,0xf,0xf,false));
  v=umax_(v,(u32)__builtin_amdgcn_update_dpp(0u,v,0x142,0xa,0xf,false));
  v=umax_(v,(u32)__builtin_amdgcn_update_dpp(0u,v,0x143,0xc,0xf,false));
  return (u32)__builtin_amdgcn_readlane((int)v,63); }

DEV void garrive(lch* sm,int c){ u32 ad=(u32)(size_t)sm+c*4, one=1;
  asm volatile("s_mov_b64 exec, 1\n\tds_add_u32 %0, %1\n\ts_mov_b64 exec, -1" :: "v"(ad),"v"(one) : "memory"); }
template<int SLP> DEV void gwait(lch* sm,int c,u32 target){ u32 ad=(u32)(size_t)sm+c*4, tv, cnt, sv; target=RFL(target);
  asm volatile("s_mov_b32 %1, 0x20000\n"
    "1:\n\tds_read_b32 %0, %3\n\ts_waitcnt lgkmcnt(0)\n\tv_readfirstlane_b32 %2, %0\n\ts_cmp_ge_u32 %2, %4\n\ts_cbranch_scc1 2f\n\ts_sleep %5\n\ts_sub_u32 %1, %1, 1\n\ts_cbranch_scc0 1b\n"
    "2:" : "=&v"(tv),"=&s"(cnt),"=&s"(sv) : "v"(ad),"s"(target),"i"(SLP) : "memory","scc"); }
template<int ST_,class T> DEV void gwait4(int c,u32 target,const lch* p,T& a,T& b,T& d,T& e){ u32 tv,cnt,sv;
  asm volatile("s_mov_b32 %1, 0x20000\n1:\n\tds_read_b32 %0, %7\n\tds_read_b128 %3, %8\n\tds_read_b128 %4, %8 offset:%10\n\tds_read_b128 %5, %8 offset:%11\n\tds_read_b128 %6, %8 offset:%12\n\ts_waitcnt lgkmcnt(4)\n\t"
    "v_readfirstlane_b32 %2, %0\n\ts_cmp_ge_u32 %2, %9\n\ts_cbranch_scc1 2f\n\ts_sleep 1\n\ts_sub_u32 %1, %1, 1\n\ts_cbranch_scc0 1b\n2:\n\ts_waitcnt lgkmcnt(0)"
    : "=&v"(tv),"=&s"(cnt),"=&s"(sv),"=&v"(a),"=&v"(b),"=&v"(d),"=&v"(e) : "v"(c*4),"v"(p),"i"(target),"i"(ST_),"i"(2*ST_),"i"(3*ST_) : "memory","scc"); }
template<int D> DEV void tgate(u32 t0){ u32 cnt; asm volatile("s_mov_b32 %0, 100\n1:\n\ts_memrealtime s[98:99]\n\ts_waitcnt lgkmcnt(0)\n\ts_sub_u32 s98, s98, %1\n\ts_cmp_ge_u32 s98, %2\n\ts_cbranch_scc1 2f\n\ts_sleep 1\n\ts_sub_u32 %0, %0, 1\n\ts_cbranch_scc0 1b\n2:" : "=&s"(cnt) : "s"(t0),"i"(D) : "memory","scc"); }
DEV void gsync(lch* sm,int c,u32 target){ garrive(sm,c); gwait<0>(sm,c,target); }
template<int SH> DEV u64 poll1(const char* p,u64 want){ u64 f=GLD(p);
  _Pragma("unroll 1") for(int sp=0;sp<SPINCAP;sp++){ bool bad=((f^want)>>SH)!=0ull; if(BALLOT(bad)==0ull) break; __builtin_amdgcn_s_sleep(1); if(bad) f=GLD(p); }
  __builtin_amdgcn_fence(__ATOMIC_ACQUIRE,"wavefront"); return f; }
#define STR_(x) #x
#define STR(x) STR_(x)
DEV u32x4 mkrs(const void* p,u32 n){ u64 a=(u64)p; return u32x4{(u32)RFL((int)(u32)a),(u32)RFL((int)((u32)(a>>32)&0xffffu)),n,0x00020000u}; }
#define VMW(n) asm volatile("s_waitcnt vmcnt(%0)" :: "i"(n) : "memory")
#define YSLP 1
#define GSLP 1
#define QSLP 1
#define PSLP 3
#define POLLASM(LOADS,CHECK,K) "s_mov_b32 %[cnt], 0x20000\n1:\n\ts_waitcnt vmcnt(0)\n\t" CHECK "s_cbranch_vccz 9f\n\ts_sleep " STR(K) "\n\ts_mov_b64 exec, vcc\n\t" LOADS \
  "s_mov_b64 exec, -1\n\ts_sub_u32 %[cnt], %[cnt], 1\n\ts_cbranch_scc0 1b\n9:\n\t"
#define LD_DUP2 "buffer_load_dwordx4 v[112:115], %[of], %[rs], 0 offen sc1\n\tbuffer_load_dwordx4 v[116:119], %[of], %[rs], %[so] offen sc1\n\t"
#define MX2(a,b) "v_max3_u32 %[t0], %[t0], v" #a ", v" #b "\n\t"
#define CHK_I0 "v_max_u32 %[t0], v112, v113\n\t" MX2(114,115)
#define CHK_IE "v_cmp_eq_u32 vcc, -1, %[t0]\n\t"
#define PCLOB "memory","vcc","scc"
#define UMASK 0x40074007u
DEV u32 upat(u32 tag,int k){ return ((((tag*0xea959c21u)>>(8*k))&255u)*0x04000801u)&UMASK; }
DEV u32 pkz(float a,float b){ return BC(u32,__builtin_amdgcn_cvt_pkrtz(a,b)); }
#define INV 0xffffffffu
DEV u32 nv(u32 v){ return umin_(v,0xfffeffffu); }
DEV u32x4 nv4(u32x4 v){ return u32x4{nv(v[0]),nv(v[1]),nv(v[2]),nv(v[3])}; }
#define OOR 0x40000000

#define CHSZ 36864
#define O_CH 512
#define O_CND 256
#define SLSZ 38080
#define O_R2 (O_CH+2*SLSZ)
#define O_X2 (O_R2+CHSZ)
#define O_TILE (O_X2+CHSZ)
#define XSTR 1168
#define TSTR2 1216
#define O_ONE (O_TILE+9728+16)
#define O_CAND (O_ONE+1168+48)
#define LDS_TOTAL (O_CAND+128)
#define C_G 0
#define C_U 8
#define C_L 4
#define C_D 5
#define C_X 12
#define C_P 13
#define C_H 14
#define C_I 17
#define C_E 18
#define C_IX 19
#define C_PR 21
#define C_RS 22
#define O_IDX 96
#define O_EXP 144
#define O_RMX 192
#define O_RV 128
#define O_MD 400
#define O_RC (O_CAND+64)

struct C2 { float r,i; };
DEV C2 cmul(C2 a,C2 b){ C2 c; c.r=__builtin_fmaf(a.r,b.r,-(a.i*b.i)); c.i=__builtin_fmaf(a.r,b.i,a.i*b.r); return c; }
DEV h8 ldA(const lch* p){ u32x2 a=*(const volatile LAS u32x2*)p, b=*(const volatile LAS u32x2*)(p+8); return BC(h8,(u32x4{a[0],a[1],b[0],b[1]})); }
DEV bf16x8 trfrag(const lch* p0,const lch* p1){
  u32x2 b0=BC(u32x2,__builtin_amdgcn_ds_read_tr16_b64_v4i16((LAS v4s*)p0)), b1=BC(u32x2,__builtin_amdgcn_ds_read_tr16_b64_v4i16((LAS v4s*)p1));
  return BC(bf16x8,(u32x4{b0[0],b0[1],b1[0],b1[1]})); }

#define OPOL SC1
struct FP { float gg[24], bs[24]; };
DEV void final_pre(FP& f,int w4,const u32x4* xq,const u32x4* dq,const f32x2* rc){
  #pragma unroll
  for(int z=0;z<3;z++) if(z<2||w4==0){ float ea=rc[z][0]*-1.4426950408889634f, eb=rc[z][1]*-1.4426950408889634f;
    #pragma unroll
    for(int e=0;e<4;e++){ u32 xw=xq[z][e], dw=dq[z>>1][e]; int i=8*z+2*e; float a=blo(xw), b=bhi(xw);
      float pa=__builtin_fmaf(a,rc[z][0],rc[z][1]), pb=__builtin_fmaf(b,rc[z][0],rc[z][1]);
      float ga=pa*rcpf_(1.f+__builtin_amdgcn_exp2f(__builtin_fmaf(a,ea,eb))), gb=pb*rcpf_(1.f+__builtin_amdgcn_exp2f(__builtin_fmaf(b,ea,eb)));
      f.gg[i]=ga; f.gg[i+1]=gb; f.bs[i]=__builtin_fmaf(pa*blo(dw),ga,a); f.bs[i+1]=__builtin_fmaf(pb*bhi(dw),gb,b);
      asm volatile("" : "+v"(f.gg[i]),"+v"(f.gg[i+1]),"+v"(f.bs[i]),"+v"(f.bs[i+1])); } }
}
#define FTS 544
#define YDA 24
#define YDB 8
#define YDC 10
#define YGS 4
#define NOCE 0
#define XE 0
#define PRS 0
DEV u32 pkmin(u32 a,u32 b){ u32 r; asm("v_pk_min_u16 %0, %1, %2" : "=v"(r) : "v"(a),"v"(b)); return r; }
DEV void final_post(lch* sm,const FP& f,int w4,int nch,u16* __restrict__ out,char* ws,int xr,u32 tk){
  LANE(ln); int bid=BID, xg=bid&7, lb=bid>>3, cg=lb&3, T=8*xg+(lb>>2), ob[3];
  int yo=(128*cg+32*w4+(ln>>2)*2)*YST+(32*T+8*(ln&3))*2, yo3= ln<32? (512+32*(cg&1)+(ln&30))*YST+(32*T+16*(cg>>1)+8*(ln&1))*2 : OOR;
  lch* fb=sm+(NOCE? O_X2+2048:O_CH)+w4*4608; lch* wp=fb+(ln&3)*FTS+(ln&60); lch* wp3=fb+(4+(ln&1)+(ln>>5)*2)*FTS+(ln&30)*2; const lch* tp=fb+(ln>>5)*FTS+(ln&31)*16;
  u32x4 rs=mkrs(ws+W_Y,D_*YST); rsrc_t ro=RSRC(out,L_*D_*2); u32 tt=(u32)(32*T+(ln>>2))*0x10001u;
  asm volatile("" : "+v"(yo),"+v"(yo3),"+v"(wp),"+v"(wp3),"+v"(tp),"+v"(tt),"+s"(rs));
  #define RMASK { gwait<2>(sm,C_IX,1); u32x4 iq=LD(u32x4,sm+O_IDX); \
    _Pragma("unroll") for(int z=0;z<2;z++){ u32 t2=tt+z*0x100010u, m=pkmin(pkmin(iq[0]^t2,iq[1]^t2),pkmin(iq[2]^t2,iq[3]^t2)); ob[z]= umin_(m&0xffffu,m>>16)==0u? OOR : xr+z*(16*D_*2); } \
    ob[2]=((cg&2)? ob[1]:ob[0])+(512+32*(cg&1)-128*cg)*2; asm volatile("" : "+v"(ob[0]),"+v"(ob[1]),"+v"(ob[2])); }
  if(PRS){ RMASK
    if(PRS==1) BST128((u32x4{0,0,0,0}),ro,ln<4? ob[0]:OOR,0,SC1);
    if(PRS==2){ BST128((u32x4{0,0,0,0}),ro,ob[0],0,SC1); BST128((u32x4{0,0,0,0}),ro,ob[1],0,SC1); BST128((u32x4{0,0,0,0}),ro,w4==0? ob[2]:OOR,0,SC1); } }
  if(!NOCE) gwait<YGS>(sm,C_E,4*nch);
  if(lb<8) __builtin_amdgcn_s_sleep(YDA); else if(lb<14) __builtin_amdgcn_s_sleep(YDC); else __builtin_amdgcn_s_sleep(YDB);
  if(lb>=8&&(NOCE||lb<14)) gwait<YGS>(sm,C_H,4);
  PRIO(3);
  { u32 t0,t1,cnt;
    #define LD_Y "buffer_load_dwordx4 v[112:115], %[of], %[rs], 0 offen sc1\n\tbuffer_load_dwordx4 v[116:119], %[of], %[rs], %[s1] offen sc1\n\ts_cmp_lg_u32 %[w], 0\n\ts_cbranch_scc1 5f\n\t" \
      "buffer_load_dwordx4 v[120:123], %[o3], %[rs], 0 offen sc1\n\tbuffer_load_dwordx4 v[124:127], %[o3], %[rs], %[s1] offen sc1\n5:\n\t"
    #define YIN [of]"v"(yo),[rs]"s"(rs),[s1]"s"(YST),[o3]"v"(yo3),[w]"s"(w4)
    asm volatile(LD_Y :: YIN : "memory","scc");
    if(!PRS) RMASK
    #define YW(p,a,b,o) "v_perm_b32 %[t0], v" #b ", v" #a ", %[lo]\n\tv_perm_b32 %[t1], v" #b ", v" #a ", %[hi]\n\tds_write2_b32 %[" #p "], %[t0], %[t1] offset0:" #o " offset1:" #o "+16\n\t"
    asm volatile(POLLASM(LD_Y,CHK_I0 MX2(116,117) MX2(118,119) "s_cmp_lg_u32 %[w], 0\n\ts_cbranch_scc1 6f\n\t" MX2(120,121) MX2(122,123) MX2(124,125) MX2(126,127) "6:\n\t" CHK_IE,YSLP)
      "s_mov_b64 exec, 1\n\tds_add_u32 %[pr], %[one]\n\ts_mov_b64 exec, -1\n\t" YW(wp,112,116,0) YW(wp,113,117,32) YW(wp,114,118,64) YW(wp,115,119,96) "s_cmp_lg_u32 %[w], 0\n\ts_cbranch_scc1 7f\n\t" YW(w3,120,124,0) YW(w3,121,125,32) YW(w3,122,126,64) YW(w3,123,127,96) "7:"
      : [t0]"=&v"(t0),[t1]"=&v"(t1),[cnt]"=&s"(cnt) : YIN,[wp]"v"(wp),[w3]"v"(wp3),[lo]"s"(0x05040100u),[hi]"s"(0x07060302u),[pr]"v"(C_PR*4),[one]"v"(1) : PCLOB); }
  u32x4 r[3];
  #pragma unroll
  for(int z=0;z<3;z++) r[z]=LD(u32x4,tp+z*(2*FTS));
  #pragma unroll
  for(int z=0;z<3;z++) if(z<2||w4==0){ u32x4 ov;
    #pragma unroll
    for(int e=0;e<4;e++){ u32 rw=r[z][e]; int i=8*z+2*e; ov[e]=pk(__builtin_fmaf(blo(rw),f.gg[i],f.bs[i]),__builtin_fmaf(bhi(rw),f.gg[i+1],f.bs[i+1])); }
    BST128(ov,ro,ob[z],0,OPOL); SCHED(); }
}

#define TROW 528
#define O_TB 4640
#define O_DE 34752
#define O_DO 35968
#define O_R2K 37120
#define O_R3K 37632
#define O_DUM 37952
#define O_RED O_TB
#define MH(a,b,c) __builtin_amdgcn_mfma_f32_16x16x32_f16(a,b,c,0,0,0)
#define PIN(x) asm volatile("" : "+v"(x))
DEV h8 ld4w(const lch* p){ return BC(h8,(u32x4{LD(u32,p),LD(u32,p+4),LD(u32,p+8),LD(u32,p+12)})); }
DEV u32 __umul24_(u32 a,u32 b){ u32 r; asm("v_mul_u32_u24 %0, %1, %2" : "=v"(r) : "s"(b),"v"(a)); return r; }
DEV u32x4 band(u32 mask,int g,u32 val){ u32 by=__builtin_amdgcn_ubfe(mask,8*g,8), X=by|(by<<15), k=0x10001u; u32x4 r; asm volatile("" : "+s"(k),"+s"(val));
  #pragma unroll
  for(int d=0;d<4;d++) r[d]=__umul24_((X>>(2*d))&k,val);
  return r; }
DEV h8 zpack(f32x4 e,f32x4 o){ return BC(h8,(u32x4{pkh(e[0],o[0]),pkh(e[1],o[1]),pkh(e[2],o[2]),pkh(e[3],o[3])})); }
struct C3 { float r,i,n; };
DEV C3 c3(C2 a){ C3 c; c.r=a.r; c.i=a.i; PIN(c.i); c.n=-c.i; PIN(c.n); return c; }
DEV C2 cm(C2 a,C3 b){ C2 c; c.r=__builtin_fmaf(a.r,b.r,a.i*b.n); c.i=__builtin_fmaf(a.r,b.i,a.i*b.r); return c; }
DEV u32 ph(C2 a){ return pkh(a.r,a.i); }
#define W2(p,a,b) asm volatile("ds_write2_b32 %0, %1, %2 offset1:132" :: "v"(p),"v"(a),"v"(b) : "memory")
struct CP { const lch *x0,*x1,*bd; lch *p0,*p1,*p2,*wp; u32x4 Be,Bo,AE; int yo; };
template<int ROLE> DEV void lanes(CP& c,lch* cb,int ln){ int m=ln&15, g=ln>>4;
  const lch* ad=cb+((m&1)? O_DO+2*527 : O_DE+2*536)+2*(8*g-m);
  const lch* bd=cb+2*(256+16*m+8*g); lch* dum=cb+O_DUM;
  if(ROLE==0){ int l0=128*(g>>1)+8*m+4*(g&1); c.p0=cb+O_DE+(549-l0)*2; PIN(c.p0); c.p1=c.p0+6; PIN(c.p1); c.p2=c.p0+(O_DO-O_DE-18); c.x0=ad-640; c.bd=bd-64; c.x1=cb+O_R3K+2*(126-2*m+8*g)-192; PIN(c.x1);
    c.Be=band(0x1ffu<<(m+4),g,0x3c00u); PIN(c.Be); }
  if(ROLE==1){ lch* r=cb+O_R2K+(192-64*(g-2)-2*m)*2; c.p0= g<2? dum : r-66; c.p1= ln==0? cb+O_DE+(552-256)*2 : (ln<=32? dum : r); c.p2=cb+O_DE+(544-256-8*ln)*2; c.x0=ad-128-640; c.bd=bd-128-128; PIN(c.p0); PIN(c.p1); PIN(c.p2); }
  if(ROLE==2){ int i0=16*m+4*g; c.p0= m<8? cb+O_R3K+(125-i0)*2 : dum+2; c.p1= ln==0? cb+O_R2K+128 : (ln==8? cb+O_R3K : c.p0+6); c.x0=ad-320-640; c.bd=bd-320-128; PIN(c.p0); PIN(c.p1); }
  if(ROLE==3){ c.x0=cb+O_R2K+2*(188-4*m+8*g)-256; c.bd=cb+2*(248+8*m+8*g);
    c.Be=band(0x1fu<<(m+6),g,0x3c00u); c.Bo=band(0x7u<<(m+11),g,0x3c00u); PIN(c.Be); PIN(c.Bo); }
  PIN(c.x0); PIN(c.bd); }
template<int ROLE> DEV void lanes2(CP& c,lch* cb,int ln){ int m=ln&15, g=ln>>4;
  if(ROLE==0){ c.Bo=band(0x7fu<<(m+5),g,0x3c00u); c.AE=band(3u<<(m+12),g,0xffffu); PIN(c.Bo); PIN(c.AE); }
  c.wp=cb+O_RED+ln*16; c.yo=(g&1)*512+m*32+(g>>1)*16; PIN(c.wp); PIN(c.yo); }
template<int ROLE> DEV void chan2(lch* sm,lch* cb,const lch*& fa,const lch*& fb,int grp,int d,int one_,int emax,u64 tag,char* ws){
  CP c;
  const f32x4 Z4={0,0,0,0}; h8 A[6]; f32x4 acc[8];
  #define LD_U "buffer_load_dwordx4 v[112:115], %[of], %[rs], 0 offen sc1\n\tbuffer_load_dwordx4 v[116:119], %[of], %[rs], %[s1] offen sc1\n\tbuffer_load_dwordx4 v[120:123], %[of], %[rs], %[s2] offen sc1\n\tbuffer_load_dwordx4 v[124:127], %[of], %[rs], %[s3] offen sc1\n\t"
  #define UC(t,a,b,c) "v_bitop3_b32 " t ", v" #a ", v" #b ", %[" #c "] bitop3:0x7e\n\t"
  #define UD(t,a,c) "v_bitop3_b32 " t ", " t ", v" #a ", %[" #c "] bitop3:0xf6\n\t"
  #define CHK_U UC("%[t0]",112,116,c0) UC("%[t1]",113,117,c1) UD("%[t0]",120,c0) UD("%[t1]",121,c1) UD("%[t0]",124,c0) UD("%[t1]",125,c1) UD("%[t0]",114,c2) UD("%[t1]",115,c3) UD("%[t0]",118,c2) UD("%[t1]",119,c3) \
    UD("%[t0]",122,c2) UD("%[t1]",123,c3) UD("%[t0]",126,c2) UD("%[t1]",127,c3) "v_bitop3_b32 %[t0], %[t0], %[t1], %[mk] bitop3:0xa8\n\tv_cmp_ne_u32 vcc, 0, %[t0]\n\t"
  #define UX(a,b,c,d,o) "v_xor_b32 v" #a ", %[e0], v" #a "\n\tv_xor_b32 v" #b ", %[e1], v" #b "\n\tv_xor_b32 v" #c ", %[e2], v" #c "\n\tv_xor_b32 v" #d ", %[e3], v" #d "\n\tds_write_b128 %[up], v[" #a ":" #d "] offset:" #o "\n\t"
  #define UPOLL { u32 t0,t1,cnt; asm volatile(POLLASM(LD_U,CHK_U,PSLP) UX(112,113,114,115,544) UX(116,117,118,119,1056) UX(120,121,122,123,1568) UX(124,125,126,127,2080) \
      : [t0]"=&v"(t0),[t1]"=&v"(t1),[cnt]"=&s"(cnt) : [of]"v"(c.p0),[rs]"s"(rsU),[s1]"s"((32>>TL)*GST_),[s2]"s"((64>>TL)*GST_),[s3]"s"((96>>TL)*GST_),[c0]"s"(c0),[c1]"s"(c1),[c2]"s"(c2),[c3]"s"(c3_), \
        [e0]"s"(c0^0x40004u),[e1]"s"(c1^0x40004u),[e2]"s"(c2^0x40004u),[e3]"s"(c3_^0x40004u),[mk]"s"(UMASK),[up]"v"(c.p1) : PCLOB); garrive(sm,C_U+one_); }
  if(ROLE<3){
    h8 a[8], b[8];
    #pragma unroll
    for(int ks=0;ks<8;ks++){ a[ks]=LD(h8,fa+ks*64); b[ks]=LD(h8,fb+ks*64); }
    SCHED();
    LANE(ln); lanes<ROLE>(c,cb,ln); float fz= ln==32? 0.f : 4.76837158203125e-07f; if(ROLE==1) PIN(fz);
    SCHED();
    f32x4 k=Z4;
    #pragma unroll
    for(int ks=0;ks<8;ks++) k=MH(a[ks],b[ks],k);
    const float f=4.76837158203125e-07f;
    float k0=k[0]*f, k1=k[1]*f, k2=k[2]*f, k3=k[3]*f;
    u32 h32=pkh(k3,k2), h10=pkh(k1,k0);
    if(ROLE!=1){
      ST(u16,c.p0)=(u16)h32; ST(u32,c.p0+2)=pkh(k2,k1); ST(u16,c.p1)=(u16)(h10>>16);
      if(ROLE==0) ST(u32x2,c.p2)=u32x2{h32,h10};
    } else {
      float n0=dppf<0x130>(k[0])*fz, z1= fz==0.f? k0:k1, z2=k[2]*fz, z3=k[3]*fz; u32 h22=pkh(z2,z2);
      ST(u16,c.p0+64)=(u16)h10; ST(u16,c.p0+2)=(u16)(h32>>16); ST(u16,c.p0)=(u16)h32; ST(u16,c.p1)=(u16)(h10>>16);
      u32x4 vo={pkh(n0,z3),h22,pkh(z2,z1),pkh(k0,k0)}, ve={pkh(n0,n0),pkh(z3,z2),h22,pkh(z1,k0)};
      asm volatile("s_mov_b32 exec_hi, 1\n\tds_write_b128 %0, %1\n\tds_write_b128 %0, %2 offset:1200\n\ts_mov_b32 exec_hi, -1" :: "v"(c.p2),"v"(ve),"v"(vo) : "memory"); }
    gsync(sm,C_G+grp,7);
    u32 kv; A[0]=ld4w(c.x0+640); A[1]=ld4w(c.x0+128);
    if(ROLE==0){ A[2]=ld4w(c.x1+192); A[3]=ld4w(c.x1+128); A[4]=ld4w(c.x1+64); A[5]=ld4w(c.x1); kv=LD(u16,cb+O_R2K+128); }
    else { A[2]=ld4w(c.x0+576); A[3]=ld4w(c.x0+64); A[4]=ld4w(c.x0+512); A[5]=ld4w(c.x0); }
    SCHED(); lanes2<ROLE>(c,cb,ln); SCHED();
    if(ROLE==0){ kv|=kv<<16; c.AE=u32x4{c.AE[0]&kv,c.AE[1]&kv,c.AE[2]&kv,c.AE[3]&kv}; PIN(c.AE); }
    PRIO(2);
    gwait<1>(sm,C_U+one_,2-one_);
  } else {
    u32 tg=(u32)tag, c0=upat(tg,0), c1=upat(tg,1), c2=upat(tg,2), c3_=upat(tg,3); u32x4 rsU=mkrs(ws+W_U,256*UST);
    if(!one_) PRIO(2);
    LANE(ln); c.p0=(lch*)fa; c.p1=(lch*)fb;
    bool late= grp==0||BID>=112;
    if(!late) asm volatile(LD_U :: [of]"v"(c.p0),[rs]"s"(rsU),[s1]"s"((32>>TL)*GST_),[s2]"s"((64>>TL)*GST_),[s3]"s"((96>>TL)*GST_) : "memory");
    asm volatile("s_mov_b64 exec, 1\n\tglobal_store_dwordx2 %0, %1, %2 sc1\n\ts_mov_b64 exec, -1" :: "v"(W_FL+d*8),"v"(tag),"s"(ws) : "memory");
    lanes<3>(c,cb,ln); lanes2<3>(c,cb,ln);
    #pragma unroll
    for(int j=2;j<8;j++){ acc[j]=Z4; PIN(acc[j]); }
    if(late){ if(UE==0||BID<112) gwait<2>(sm,C_G,UGATE); if(BID>=112){ gwait<1>(sm,UE? C_RS:C_D,4); if(UE>1) __builtin_amdgcn_s_sleep(UE); } asm volatile(LD_U :: [of]"v"(c.p0),[rs]"s"(rsU),[s1]"s"((32>>TL)*GST_),[s2]"s"((64>>TL)*GST_),[s3]"s"((96>>TL)*GST_) : "memory"); }
    if(!late) UPOLL
    gwait<1>(sm,C_G+grp,7);
    #pragma unroll
    for(int i=0;i<6;i++) A[i]=ldA(c.x0+320-64*i);
    if(late) UPOLL
    PRIO(2);
    gwait<1>(sm,C_U+one_,2-one_);
  }
  const lch* bd=c.bd; h8 Be=BC(h8,c.Be), Bo=BC(h8,c.Bo);
  #pragma unroll
  for(int j=0;j<(ROLE==3? 2:8);j++) acc[j]=Z4;
  #define RW(j) if((j)>=0&&((j)>>1)!=ROLE&&!(ROLE==3&&(j)<2)){ ST(f32x4,c.wp+((j)>>1)*6144+((ROLE-((j)>>1)-1)&3)*2048+((j)&1)*1024)=acc[(j)<0? 0:(j)]; SCHED(); }
  if(ROLE==1||ROLE==2){
    h8 B[8];
    #pragma unroll
    for(int q=0;q<8;q++) B[q]=LD(h8,bd+128+512*q);
    #pragma unroll
    for(int r=0;r<3;r++){
      #pragma unroll
      for(int dl=0;dl<8;dl++){ if(dl<7) acc[dl+1]=MH(A[2*r+1],B[dl],acc[dl+1]); SCHED(); acc[dl]=MH(A[2*r],B[dl],acc[dl]); SCHED();
        if(r<2){ B[dl]=LD(h8,bd+64-64*r+512*dl); SCHED(); } else RW(dl-2) }
      } }
  if(ROLE==0){
    h8 B[8], ue[4];
    #pragma unroll
    for(int kt=0;kt<4;kt++){ ue[kt]=LD(h8,bd+48+512*kt); B[kt]=LD(h8,bd+64+512*kt); }
    f32x4 ce[2],co[2]; h8 Z[2];
    ce[0]=MH(ue[0],Be,Z4); co[0]=MH(B[0],Bo,Z4); ce[1]=MH(ue[1],Be,Z4); co[1]=MH(B[1],Bo,Z4); Z[0]=zpack(ce[0],co[0]);
    #pragma unroll
    for(int kt=0;kt<4;kt++){
      SCHED();
      if(kt+2<4){ ce[kt&1]=MH(ue[kt+2],Be,Z4); co[kt&1]=MH(B[kt+2],Bo,Z4); }
      if(kt+1<4) Z[(kt+1)&1]=zpack(ce[(kt+1)&1],co[(kt+1)&1]);
      #pragma unroll
      for(int rho=0;rho+kt<4;rho++) acc[4+kt+rho]=MH(Z[kt&1],A[2+rho],acc[4+kt+rho]);
      SCHED(); B[4+kt]=LD(h8,bd+64+512*(4+kt)); if(kt==0) A[5]=BC(h8,c.AE); if(kt==1) A[4]=ld4w(c.x0-384); if(kt==2) A[3]=ld4w(c.x0+64); if(kt==3) A[2]=ld4w(c.x0+576); SCHED(); }
    #pragma unroll
    for(int dl=0;dl<8;dl++){ if(dl<4) acc[dl+4]=MH(A[5],B[dl],acc[dl+4]); SCHED(); if(dl<6) acc[dl+2]=MH(A[4],B[dl],acc[dl+2]); SCHED(); if(dl<7) acc[dl+1]=MH(A[1],B[dl],acc[dl+1]); SCHED(); acc[dl]=MH(A[0],B[dl],acc[dl]);
      SCHED(); B[dl]=LD(h8,bd+512*dl); SCHED(); }
    #pragma unroll
    for(int dl=0;dl<8;dl++){ if(dl<7) acc[dl+1]=MH(A[3],B[dl],acc[dl+1]); SCHED(); acc[dl]=MH(A[2],B[dl],acc[dl]); SCHED(); RW(dl-2) } }
  if(ROLE==3){
    h8 U[12];
    #pragma unroll
    for(int kt=0;kt<6;kt++) U[kt]=LD(h8,bd+256*kt);
    f32x4 ce[2],co[2]; h8 Z[2];
    ce[0]=MH(U[0],Be,Z4); co[0]=MH(U[0],Bo,Z4); ce[1]=MH(U[1],Be,Z4); co[1]=MH(U[1],Bo,Z4); Z[0]=zpack(ce[0],co[0]);
    #pragma unroll
    for(int kt=0;kt<12;kt++){
      SCHED();
      if(kt+2<12){ ce[kt&1]=MH(U[kt+2],Be,Z4); co[kt&1]=MH(U[kt+2],Bo,Z4); }
      if(kt+1<12) Z[(kt+1)&1]=zpack(ce[(kt+1)&1],co[(kt+1)&1]);
      #pragma unroll
      for(int rho=-1;rho<5;rho++){ int j=(kt+4+rho)>>1; if(((kt+rho)&1)==0&&j>=2&&j<8) acc[j]=MH(Z[kt&1],A[rho+1],acc[j]); }
      if(kt<6){ SCHED(); U[kt+6]=LD(h8,bd+256*(kt+6)); }
      if(kt>=3&&kt<=9&&(kt&1)){ SCHED(); RW((kt+1)/2) } }
  }
  PRIO(3);
  RW(6) RW(7)
  gsync(sm,C_G+grp,11);
  { const lch* rp=c.wp+ROLE*6144; f32x4 a[3], b[3], o0=acc[2*ROLE], o1=acc[2*ROLE+1];
    #pragma unroll
    for(int q=0;q<(ROLE==0? 2:3);q++){ a[q]=LD(f32x4,rp+q*2048); b[q]=LD(f32x4,rp+q*2048+1024); }
    SCHED();
    float sc=__builtin_amdgcn_ldexpf(1.f,emax-YSH), s0[4], s1[4];
    #pragma unroll
    for(int e=0;e<4;e++){ s0[e]=((o0[e]+a[0][e])+(ROLE==0? a[1][e] : a[1][e]+a[2][e]))*sc; s1[e]=((o1[e]+b[0][e])+(ROLE==0? b[1][e] : b[1][e]+b[2][e]))*sc; }
    auto r0=__builtin_amdgcn_permlane16_swap(nv(pk(s0[0],s0[1])),nv(pk(s1[0],s1[1])),false,false); auto r1=__builtin_amdgcn_permlane16_swap(nv(pk(s0[2],s0[3])),nv(pk(s1[2],s1[3])),false,false);
    BST128(u32x4{r0[0],r1[0],r0[1],r1[1]},RSRC(ws+W_Y+d*YST,4096),c.yo+ROLE*1024,0,SC1); }
  garrive(sm,C_E);

  asm volatile("s_endpgm" : "=v"(fa),"=v"(fb) :: "memory");
}
#define PFR0 520
DEV void pf1(int i,int xg,const u16* wg,const u16* wu){ LANE(ln); u32 q=(u32)(i>>1)*64u+ln, r=(q*7282u)>>16, c=q-9u*r;
  asm volatile("buffer_load_dword v121, %0, %1, 0 offen" :: "v"((PFR0+r)*(F_*2)+(9*xg+c)*128),"s"(mkrs((i&1)? wu:wg,D_*F_*2)) : "memory"); }
#define GETEP asm volatile("s_waitcnt lgkmcnt(0)\n\ts_mov_b64 %0, s[96:97]\n\ts_mov_b32 %1, s98" : "=s"(ep),"=s"(ldw));
DEV void chan(lch* sm,int grp,int w4,int d,int one_,char* ws,u32x4 q0,u32x4 q1,u32x4 q2,u32x4 q3,u32x4 q4){
  lch* cb=sm+O_CH+grp*SLSZ; int role=w4, emax;
  #define LP(v) ((lch*)(size_t)(u32)(v))
  const lch* fa=LP(q4[0]); const lch* fb=LP(q4[1]);
  u64 tag;
  { float a2,bp,br,bi,cr,ci;
    u32 fq=q3[2], fr=q3[3], one=q4[2]; float f64=BC(float,fq), f128=BC(float,fr);
    lch *t8[4]={LP(q0[0]),LP(q0[1]),LP(q0[2]),LP(q0[3])}, *tB[4]={LP(q1[0]),LP(q1[1]),LP(q1[2]),LP(q1[3])}, *t4[2]={LP(q2[0]),LP(q2[1])}, *t2[2]={LP(q2[2]),LP(q2[3])}, *t1=LP(q3[0]), *te=LP(q3[1]);
    if(grp!=0) PRIO(1);
    u64 ep; u32 ldw; GETEP tag=ep+1ull;
    float dt=rb(expf_((d&1)? bhi(ldw):blo(ldw))), ca=dt*1.4426950408889634f, cb_=dt*0.15915494309189535f;
    asm volatile("s_waitcnt vmcnt(1)\n\tv_mul_f32 %2, %8, v114\n\tv_mul_f32 %3, %8, v115\n\tv_mul_f32 %0, %6, v112\n\tv_mul_f32 %1, %7, v113\n\tv_mov_b32 %4, v116\n\tv_mov_b32 %5, v117"
      : "=&v"(a2),"=&v"(bp),"=&v"(br),"=&v"(bi),"=&v"(cr),"=&v"(ci) : "v"(ca),"v"(cb_),"v"(dt) : "memory");
    C2 W; W.r=cr*br-ci*bi; W.i=cr*bi+ci*br;
    u32 mx=BC(u32,__builtin_fmaxf(__builtin_fabsf(W.r),__builtin_fabsf(W.i)));
    #define MXS(C) mx=umax_(mx,dppu<C>(mx));
    float e8=a2*8.f, eB=__builtin_fmaf(a2,f64,14.f), p8=bp*8.f, pB=bp*f64; SCHED();
    MXS(0xB1) p8=__builtin_amdgcn_fractf(p8); pB=__builtin_amdgcn_fractf(pB); e8=__builtin_amdgcn_exp2f(e8); SCHED();
    MXS(0x4E) eB=__builtin_amdgcn_exp2f(eB); float c8=__builtin_amdgcn_cosf(p8); SCHED();
    MXS(0x141) float s8=__builtin_amdgcn_sinf(p8), cB=__builtin_amdgcn_cosf(pB); SCHED();
    float sB=__builtin_amdgcn_sinf(pB); PIN(sB); MXS(0x140) float e1=__builtin_amdgcn_exp2f(a2); PIN(e1); SCHED();
    asm volatile("s_mov_b64 exec, 0x10001\n\tds_max_u32 %0, %1\n\tds_add_u32 %0, %2 offset:4\n\ts_mov_b64 exec, -1" :: "v"(te),"v"(mx),"v"(one) : "memory");
    C3 z8; z8.r=e8*c8; z8.n=e8*s8; z8.i=-z8.n; PIN(z8.i); C2 B; B.r=eB*cB; B.i=-(eB*sB);
    float p1=__builtin_amdgcn_fractf(bp), pE=__builtin_amdgcn_fractf(bp*256.f), pV=__builtin_amdgcn_fractf(bp*f128), eE=a2*256.f, eV=a2*f128, tr[8];
    #pragma unroll
    for(int k=0;k<4;k++){ SCHED(); u32 h0=ph(B); B=cm(B,z8);
      if(k==0){ tr[0]=__builtin_amdgcn_cosf(pV); tr[1]=__builtin_amdgcn_sinf(pV); } if(k==1){ tr[2]=__builtin_amdgcn_exp2f(eV); tr[3]=__builtin_amdgcn_cosf(p1); }
      if(k==2){ tr[4]=__builtin_amdgcn_sinf(p1); tr[5]=__builtin_amdgcn_exp2f(eE); } if(k==3){ tr[6]=__builtin_amdgcn_cosf(pE); tr[7]=__builtin_amdgcn_sinf(pE); }
      PIN(tr[2*k]); PIN(tr[2*k+1]);
      u32 h1=ph(B); if(k<3) B=cm(B,z8); W2(tB[k],h0,h1); }
    asm volatile("ds_read_b64 v[118:119], %0" :: "v"(te) : "memory");
    SCHED();
    C3 z1=c3(C2{e1*tr[3],e1*tr[4]}), E=c3(C2{tr[5]*tr[6],tr[5]*tr[7]}); C2 V[8]; V[0]=cmul(W,C2{tr[2]*tr[0],tr[2]*tr[1]});
    { u32 cnt,n; PIN(V[0].r); PIN(V[0].i); PIN(z1.r); PIN(E.r);
      asm volatile("s_mov_b32 %1, 0x20000\n1:\n\ts_waitcnt lgkmcnt(0)\n\tv_readfirstlane_b32 %2, v119\n\tv_readfirstlane_b32 %0, v118\n\ts_cmp_eq_u32 %2, 8\n\ts_cbranch_scc1 2f\n\tds_read_b64 v[118:119], %3\n\ts_sub_u32 %1, %1, 1\n\ts_cbranch_scc0 1b\n2:"
        : "=&s"(emax),"=&s"(cnt),"=&s"(n) : "v"(te) : "memory","scc");
      emax=RFL(emax); emax=(emax>>23)-126; }
    V[0].r=__builtin_amdgcn_ldexpf(V[0].r,14-emax); V[0].i=__builtin_amdgcn_ldexpf(V[0].i,14-emax);
    C2 Y=cm(V[0],E), Y4; u32 y0=ph(Y);
    V[1]=cm(V[0],z1); W2(t8[0],ph(V[0]),ph(V[1])); Y=cm(Y,E); u32 y1=ph(Y);
    V[2]=cm(V[1],z1); Y=cm(Y,E); u32 y2=ph(Y);
    V[3]=cm(V[2],z1); W2(t8[1],ph(V[2]),ph(V[3])); W2(t4[0],y0,ph(cm(V[2],E)));
    V[4]=cm(V[3],z1); Y=cm(Y,E); ST(u32,t1)=ph(Y);
    V[5]=cm(V[4],z1); W2(t8[2],ph(V[4]),ph(V[5])); Y4=cm(V[4],E); u32 y4=ph(Y4);
    V[6]=cm(V[5],z1); Y=cm(Y,E); ST(u32,t1+2*TROW)=ph(Y);
    V[7]=cm(V[6],z1); W2(t8[3],ph(V[6]),ph(V[7])); Y4=cm(Y4,E); W2(t2[0],y1,ph(Y4));
    W2(t4[1],y4,ph(cm(V[6],E))); Y=cm(Y,E); ST(u32,t1+4*TROW)=ph(Y);
    Y4=cm(Y4,E); W2(t2[1],y2,ph(Y4)); Y=cm(Y,E); ST(u32,t1+6*TROW)=ph(Y);
    Y=cm(Y,E); asm volatile("s_mov_b32 exec_hi, 0\n\tds_write_b32 %0, %1 offset:4224\n\ts_mov_b32 exec_hi, -1" :: "v"(t1),"v"(ph(Y)) : "memory");
  }
  if(one_) PRIO(3);
  DRAIN();
  gsync(sm,C_G+grp,4);
  if(role==0) chan2<0>(sm,cb,fa,fb,grp,d,one_,emax,tag,ws);
  if(role==1) chan2<1>(sm,cb,fa,fb,grp,d,one_,emax,tag,ws);
  if(role==2) chan2<2>(sm,cb,fa,fb,grp,d,one_,emax,tag,ws);
  chan2<3>(sm,cb,fa,fb,grp,d,one_,emax,tag,ws);
}

DEV u32 med3u(u32 a,u32 b,u32 c){ u32 r; asm("v_med3_u32 %0, %1, %2, %3" : "=v"(r) : "v"(a),"v"(b),"v"(c)); return r; }
DEV u32 max3u(u32 a,u32 b,u32 c){ return umax_(umax_(a,b),c); }
#define GTC(r,s) "v_cmp_gt_u32 %[" #s "], v" #r ", %[c]\n\t"
#define GTA(s) "v_addc_co_u32 %[rk], vcc, 0, %[rk], %[" #s "]\n\t"
#define GT4(a,b,c,d) GTC(a,s0) GTC(b,s1) GTC(c,s2) GTC(d,s3) GTA(s0) GTA(s1) GTA(s2) GTA(s3)
#define GTS [s0]"=&s"(q0),[s1]"=&s"(q1),[s2]"=&s"(q2),[s3]"=&s"(q3)
DEV void top8(lch* sm,int w4,int ln,u32 M,char* ws,u32& rank_,u32& key_,u32 tk){
  u32 k[8], rc[8], hm=0xffff0000u; u64 em=0x0001000100010001ull,q0,q1,q2,q3;
  u32x4 rs=mkrs(ws+W_RG+(BID&(NRG-1))*8192,8192); int ro=w4*1024+ln*16;
  #pragma unroll
  for(int e=0;e<8;e++){ rc[e]=(u32)(0xfff-(w4*256+ln*4+(e&3)+(e>>2)*1024)); PIN(rc[e]); }
  lch *aW=sm+O_RMX+(w4*4+(ln>>4))*4, *aC=sm+O_RMX+(ln>>2)*4, *aO=sm+O_RMX+(ln&3)*16, *dC=sm+O_CND+(ln>>1)*4, *dO=sm+O_CND+(ln&1)*64; PIN(aW); PIN(aC); PIN(aO); PIN(dC); PIN(dO); PIN(ro); PIN(ln);
  asm volatile("" : "+s"(hm),"+s"(em),"+s"(rs));
  gwait<2>(sm,C_G,RGATE);
  { u32 cnt;
    #define KL(e,r) "v_lshl_or_b32 %[k" #e "], v" #r ", 16, %[k" #e "]\n\t"
    #define KH(e,r) "v_and_or_b32 %[k" #e "], v" #r ", %[hm], %[k" #e "]\n\t"
    #define CHK_R "v_bitop3_b32 %[t0], v113, v115, %[M] bitop3:0x7e\n\tv_bitop3_b32 %[t1], v117, v119, %[M] bitop3:0x7e\n\tv_or_b32 %[t0], %[t0], %[t1]\n\tv_cmp_ne_u32 vcc, 0, %[t0]\n\t"
    u32 t0,t1;
    asm volatile(LD_DUP2
      "s_sleep " STR(R2D) "\n\tbuffer_load_dwordx4 v[120:123], %[of], %[rs], 0 offen sc1\n\tbuffer_load_dwordx4 v[124:127], %[of], %[rs], %[so] offen sc1\n\ts_mov_b32 %[cnt], 0x20000\n\ts_waitcnt vmcnt(2)\n\t" CHK_R "s_cbranch_vccz 9f\n\t"
      "s_waitcnt vmcnt(0)\n\tv_mov_b64 v[112:113], v[120:121]\n\tv_mov_b64 v[114:115], v[122:123]\n\tv_mov_b64 v[116:117], v[124:125]\n\tv_mov_b64 v[118:119], v[126:127]\n\t"
      POLLASM(LD_DUP2,CHK_R,PSLP)
      KL(0,112) KH(1,112) KL(2,114) KH(3,114) KL(4,116) KH(5,116) KL(6,118) KH(7,118)
      : [k0]"+v"(rc[0]),[k1]"+v"(rc[1]),[k2]"+v"(rc[2]),[k3]"+v"(rc[3]),[k4]"+v"(rc[4]),[k5]"+v"(rc[5]),[k6]"+v"(rc[6]),[k7]"+v"(rc[7]),[cnt]"=&s"(cnt),[t0]"=&v"(t0),[t1]"=&v"(t1) : [of]"v"(ro),[rs]"s"(rs),[so]"s"(4096),[M]"s"(M),[hm]"s"(hm) : PCLOB); }
  if(UE) garrive(sm,C_RS);
  #pragma unroll
  for(int e=0;e<8;e++) k[e]=rc[e];
  u32 h=max3u(k[0],k[1],k[2]), m1=max3u(max3u(k[3],k[4],k[5]),max3u(k[6],k[7],h),h), mx=m1, l=med3u(k[0],k[1],k[2]), t=umin_(umin_(k[0],k[1]),k[2]);
  SCHED();
  #define INS(e) t=med3u(t,l,k[e]); l=med3u(h,l,k[e]); if(e<7) h=umax_(h,k[e]); PIN(t); PIN(l); SCHED();
  mx=umax_(mx,dppu<0xB1>(mx)); INS(3) mx=umax_(mx,dppu<0x4E>(mx)); INS(4) mx=umax_(mx,dppu<0x141>(mx)); INS(5) mx=umax_(mx,dppu<0x140>(mx));
  asm volatile("s_mov_b64 exec, %2\n\tds_write_b32 %0, %1\n\ts_mov_b64 exec, -1\n\tds_read_b32 v116, %3\n\tds_read_b128 v[112:115], %4" :: "v"(aW),"v"(mx),"s"(em),"v"(aC),"v"(aO) : "memory");
  INS(6) INS(7)
  u32 m2=l, m3=t, T;
  { u32 c,rk,cnt;
    asm volatile("s_mov_b32 %[cnt], 0x20000\n1:\n\ts_waitcnt lgkmcnt(0)\n\tv_cmp_eq_u32 vcc, 0, v116\n\ts_cbranch_vccz 2f\n\tds_read_b32 v116, %[aC]\n\tds_read_b128 v[112:115], %[aO]\n\ts_sub_u32 %[cnt], %[cnt], 1\n\ts_cbranch_scc0 1b\n2:\n\t"
      "v_mov_b32 %[c], v116\n\tv_mov_b32 %[rk], 0\n\t" GT4(112,113,114,115) "s_nop 1\n\tv_add_u32_dpp %[rk], %[rk], %[rk] quad_perm:[1,0,3,2] row_mask:0xf bank_mask:0xf\n\ts_nop 1\n\tv_add_u32_dpp %[rk], %[rk], %[rk] quad_perm:[2,3,0,1] row_mask:0xf bank_mask:0xf\n\t"
      "v_cmp_eq_u32 vcc, 7, %[rk]\n\ts_ff1_i32_b64 %[cnt], vcc\n\ts_nop 0\n\tv_readlane_b32 %[T], %[c], %[cnt]\n\ts_nop 1"
      : [c]"=&v"(c),[rk]"=&v"(rk),[cnt]"=&s"(cnt),[T]"=&s"(T),GTS : [aC]"v"(aC),[aO]"v"(aO) : PCLOB); T=RFL(T); }
  { u64 b1=BALLOT(m1>=T), b2=BALLOT(m2>=T), b3=BALLOT(m3>=T); int n1=__builtin_popcountll(b1), n2=n1+__builtin_popcountll(b2);
    lch* cw=sm+O_CND+w4*32;
    if(__builtin_expect(b3==0ull&&n2<=8,1)){
      int p1=(int)__builtin_amdgcn_mbcnt_hi((u32)(b1>>32),__builtin_amdgcn_mbcnt_lo((u32)b1,0u)), pf=n2+ln-p1; bool c1=m1>=T;
      if(__builtin_expect(b2!=0ull,0)){ int p2=(int)__builtin_amdgcn_mbcnt_hi((u32)(b2>>32),__builtin_amdgcn_mbcnt_lo((u32)b2,(u32)n1)); if(m2>=T) ST(u32,cw+p2*4)=m2; }
      int ps= c1? p1:pf; ps= ps<8? ps:32-8*w4; ST(u32,cw+ps*4)= c1? m1:1u;
    } else {
      u32 mine=0;
      _Pragma("unroll 1") for(int c=0;c<8;c++){ u32 lm=umax_(umax_(umax_(k[0],k[1]),umax_(k[2],k[3])),umax_(umax_(k[4],k[5]),umax_(k[6],k[7]))); u32 wm=wmax(lm); if(ln==c) mine=wm;
        #pragma unroll
        for(int e=0;e<8;e++) k[e]=(k[e]==wm)?0u:k[e]; }
      if(ln<8) ST(u32,cw+ln*4)=mine; } }
  { u32 c,rk,cnt;
    asm volatile("s_mov_b32 %[cnt], 0x20000\n\tv_mov_b32 %[rk], 0\n\ts_waitcnt vmcnt(0)\n1:\n\tds_read_b32 %[c], %[dC]\n\tds_read_b128 v[112:115], %[dO]\n\tds_read_b128 v[116:119], %[dO] offset:16\n\tds_read_b128 v[120:123], %[dO] offset:32\n\tds_read_b128 v[124:127], %[dO] offset:48\n\t"
      "s_waitcnt lgkmcnt(4)\n\tv_cmp_eq_u32 vcc, 0, %[c]\n\ts_cbranch_vccz 2f\n\ts_sub_u32 %[cnt], %[cnt], 1\n\ts_cbranch_scc0 1b\n2:\n\t"
      "s_waitcnt lgkmcnt(3)\n\t" GT4(112,113,114,115) "s_waitcnt lgkmcnt(2)\n\t" GT4(116,117,118,119) "s_waitcnt lgkmcnt(1)\n\t" GT4(120,121,122,123) "s_waitcnt lgkmcnt(0)\n\t" GT4(124,125,126,127)
      "s_nop 1\n\tv_add_u32_dpp %[rk], %[rk], %[rk] quad_perm:[1,0,3,2] row_mask:0xf bank_mask:0xf"
      : [c]"=&v"(c),[rk]"=&v"(rk),[cnt]"=&s"(cnt),GTS : [dC]"v"(dC),[dO]"v"(dO) : PCLOB);
    rank_=rk; key_=c; }
}

#define FILL_(n,WAIT) "s_mov_b32 m0, %[ld]\n\ts_mov_b32 %[c], " STR(n) "-1\n3:\n\t" WAIT "buffer_load_dwordx4 %[vo], %[rs], %[so] offen " WPOL " lds\n\ts_add_u32 m0, m0, 0x1000\n\ts_add_u32 %[so], %[so], %[st]\n\t" \
  "s_sub_u32 %[c], %[c], 1\n\ts_cbranch_scc0 3b\n\ts_mov_b32 %[ld], m0\n\t"
#define FILL(n,WAIT) do{ u32 c_; asm volatile(FILL_(n,WAIT) : [c]"=&s"(c_),[so]"+s"(wso),[ld]"+s"(wld) : [vo]"v"(wvo),[rs]"s"(wrs),[st]"s"(wst) : "memory","scc","m0"); }while(0)
#define FILLD(n,WN,WW_) do{ u32 c_,t_,s_; asm volatile(FILL_(n,"ds_read_b32 %[t], %[ad]\n\ts_waitcnt lgkmcnt(0)\n\tv_readfirstlane_b32 %[s], %[t]\n\ts_cmp_ge_u32 %[s], 4\n\ts_cbranch_scc1 4f\n\t" VW(WN) "4:\n\t" VW(WW_)) \
  : [c]"=&s"(c_),[so]"+s"(wso),[ld]"+s"(wld),[t]"=&v"(t_),[s]"=&s"(s_) : [vo]"v"(wvo),[rs]"s"(wrs),[st]"s"(wst),[ad]"v"(C_D*4) : "memory","scc","m0"); wso=RFL(wso); wld=RFL(wld); }while(0)
#define VW(n) "s_waitcnt vmcnt(" STR(n) ")\n\t"

__global__ NOPK __attribute__((amdgpu_num_vgpr(56),amdgpu_num_sgpr(102))) __launch_bounds__(1024,4) void kA(const u16* __restrict__ x,char* ws,const u16* __restrict__ wr,const u16* __restrict__ Dsk,const u16* __restrict__ wg,const u16* __restrict__ wu,
                                              const u16* __restrict__ wd,u16* __restrict__ out,KP kp){
  __shared__ __attribute__((aligned(16))) char smem[LDS_TOTAL];
  lch* sm=(lch*)smem;
  u32x4 xa,xb_,wa,wb,dq[3]; u32 xc,wc;
  asm volatile(
    "s_load_dwordx16 s[64:79], %[ka], 0x0\n\ts_load_dwordx16 s[80:95], %[ka], 0x40\n\t"
    "v_readfirstlane_b32 s60, %[t]\n\ts_and_b32 s61, %[wg], 7\n\ts_lshr_b32 s62, %[wg], 3\n\tv_and_b32 v118, 31, %[t]\n\ts_bfe_u32 s63, s60, 0x20006\n\ts_cmpk_ge_u32 s60, 0x100\n\ts_cbranch_scc1 1f\n\t"
    "s_setprio 3\n\ts_lshl_b32 s56, s61, 5\n\ts_add_u32 s56, s56, s62\n\ts_mulk_i32 s56, 0x2400\n\tv_lshrrev_b32 v119, 5, %[t]\n\tv_mul_u32_u24 v119, 0x480, v119\n\tv_add_u32 v119, s56, v119\n\t"
    "v_lshl_add_u32 v116, v118, 4, v119\n\tv_lshl_add_u32 v117, v118, 2, v119\n\tv_lshlrev_b32 v114, 4, v118\n\tv_lshlrev_b32 v115, 2, v118\n\t"
    "s_and_b32 s57, s62, 3\n\ts_lshl2_add_u32 s57, s57, s63\n\ts_lshl_b32 s57, s57, 6\n\ts_and_b32 s58, s62, 1\n\ts_lshl_b32 s58, s58, 6\n\tv_and_b32 v112, 3, %[t]\n\tv_lshl_add_u32 v113, v112, 4, s57\n\tv_lshl_add_u32 v112, v112, 4, s58\n\t"
    "v_lshlrev_b32 v119, 1, %[t]\n\tv_mov_b32 v118, 0\n\tds_write_b16 v119, v118\n\t"
    "s_waitcnt lgkmcnt(0)\n\t"
    "s_memrealtime s[98:99]\n\ts_load_dwordx2 s[96:97], s[66:67], 0x0\n\t"
    "global_load_dwordx4 %[wa], v114, s[68:69]\n\tglobal_load_dwordx4 %[wb], v114, s[68:69] offset:512\n\tglobal_load_dword %[wc], v115, s[68:69] offset:1024\n\t"
    "global_load_dwordx4 %[xa], v116, s[64:65]\n\tglobal_load_dwordx4 %[xb], v116, s[64:65] offset:512\n\tglobal_load_dword %[xc], v117, s[64:65] offset:1024\n\t"
    "global_load_dwordx4 %[d0], v113, s[70:71]\n\tglobal_load_dwordx4 %[d1], v112, s[70:71] offset:1024\n\ts_barrier\n\t"
    "s_branch 8f\n"
    "1:\n\ts_cmp_ge_u32 s62, " STR(CPF) "\n\ts_cbranch_scc1 6f\n\ts_getpc_b64 s[56:57]\n.Lkpc:\n\ts_and_b32 s58, s62, 3\n\ts_lshl_b32 s58, s58, 13\n\ts_sub_u32 s58, s58, 0x1000\n\tv_lshl_add_u32 v119, %[t], 4, s58\n\tv_min_u32 v119, .Lkend-.Lkpc-16, v119\n\t"
    "global_load_dwordx4 v[120:123], v119, s[56:57]\n\t.subsection 1\n.Lkend:\n\t.subsection 0\n6:\n\t"
    "v_mov_b32 v124, -1\n\tv_mov_b32 v125, -1\n\tv_mov_b32 v126, -1\n\tv_mov_b32 v127, -1\n\tv_lshlrev_b32 v119, 4, %[t]\n\tv_and_b32 v119, 0xff0, v119\n\t"
    "s_lshr_b32 s59, s60, 8\n\ts_cmp_lt_u32 s62, 8\n\ts_cselect_b32 s58, 2, 1\n\ts_cbranch_scc1 2f\n\ts_cmp_eq_u32 s59, 3\n\ts_cbranch_scc1 4f\n"
    "2:\n\ts_sub_u32 s57, s59, 2\n\ts_cmp_eq_u32 s59, 1\n\ts_cselect_b32 s58, s58, s57\n\ts_cmp_lt_u32 s62, 8\n\ts_cselect_b32 s56, 0, 8\n\ts_lshl1_add_u32 s56, s62, s56\n\ts_mulk_i32 s61, 72\n\ts_add_u32 s51, s56, s61\n\ts_sub_u32 s56, 2, s59\n\ts_sub_u32 s57, 16, s62\n\ts_cmp_eq_u32 s59, 3\n\ts_cselect_b32 s50, s57, s56\n\ts_add_u32 s56, s50, s51\n\t"
    "v_mov_b32 v112, 0\n\tv_mov_b32 v113, 0\n\tv_mov_b32 v114, 0\n\tv_mov_b32 v115, 0\n\tv_mov_b32 v116, 0\n\tv_mov_b32 v117, 0\n\ts_setprio 2\n\t"
    "s_lshl_b32 s57, s56, 2\n\ts_add_u32 s57, s57, s63\n\ts_lshl_b32 s57, s57, 6\n\tv_lshl_add_u32 v22, v118, 1, s57\n\ts_mul_i32 s57, s56, %[yst]\n\ts_add_u32 s57, s57, %[wy]\n\tv_add_u32 v23, s57, v119\n\ts_lshl_b32 s56, s56, 1\n\ts_and_b32 s56, s56, -4\n\t"
    "s_waitcnt lgkmcnt(0)\n\t"
    "global_load_short_d16_hi v112, v22, s[80:81] nt\n\tglobal_load_short_d16_hi v113, v22, s[82:83] nt\n\tglobal_load_short_d16_hi v114, v22, s[84:85] nt\n\t"
    "global_load_short_d16_hi v115, v22, s[86:87] nt\n\tglobal_load_short_d16_hi v116, v22, s[88:89] nt\n\tglobal_load_short_d16_hi v117, v22, s[90:91] nt\n\t"
    "s_load_dwordx2 s[96:97], s[66:67], 0x0\n\ts_load_dword s98, s[92:93], s56\n\t"
    "s_barrier\n\t" "global_store_dwordx4 v23, v[124:127], s[66:67] sc1\n\t"
    "s_mul_i32 s52, s58, %[sl]\n\ts_add_u32 s52, s52, %[tb]\n\ts_lshl_b32 s53, s63, 7\n\ts_add_u32 s53, s53, s52\n\tv_lshl_add_u32 v30, v118, 2, s53\n\tv_bfe_u32 v31, %[t], 5, 1\n\ts_movk_i32 s54, 528\n\tv_mul_u32_u24 v29, s54, v31\n\t"
    "v_lshl_add_u32 v2, v29, 3, v30\n\ts_movk_i32 s53, 1056\n\tv_add_u32 v3, s53, v2\n\tv_add_u32 v4, s53, v3\n\tv_add_u32 v5, s53, v4\n\tv_add_u32 v6, 41*528, v2\n\tv_add_u32 v7, s53, v6\n\tv_add_u32 v8, s53, v7\n\tv_add_u32 v9, s53, v8\n\t"
    "v_lshl_add_u32 v10, v29, 2, v30\n\tv_add_u32 v10, 16*528, v10\n\tv_add_u32 v11, s53, v10\n\tv_lshl_add_u32 v12, v29, 1, v30\n\tv_add_u32 v12, 24*528, v12\n\tv_add_u32 v13, 2112, v12\n\tv_add_u32 v14, v29, v30\n\tv_add_u32 v14, 32*528, v14\n\t"
    "s_lshl_b32 s53, s58, 4\n\ts_add_u32 s53, s53, %[ex]\n\tv_mov_b32 v15, s53\n\tv_cvt_f32_ubyte0 v31, v31\n\tv_mul_f32 v16, 0x42800000, v31\n\tv_add_f32 v17, v16, v16\n\tv_mov_b32 v20, 1\n\t"
    "v_and_b32 v29, 15, %[t]\n\tv_and_b32 v30, 48, %[t]\n\tv_mad_u32_u24 v30, v29, s54, v30\n\ts_lshl_b32 s55, s59, 1\n\ts_lshr_b32 s55, 0x48, s55\n\ts_add_u32 s55, s55, s63\n\ts_and_b32 s55, s55, 3\n\ts_mul_i32 s53, s55, 16*528\n\ts_movk_i32 s57, 41*528\n\ts_cmp_eq_u32 s55, 2\n\ts_cselect_b32 s53, s57, s53\n\ts_cselect_b32 s57, 32*528, s57\n\t"
    "s_add_u32 s53, s53, s52\n\ts_add_u32 s57, s57, s52\n\tv_add_u32 v18, s53, v30\n\tv_add_u32 v19, s57, v30\n\t"
    "v_min_u32 v29, 237*16, v119\n\tv_subrev_u32 v30, 204*16, v29\n\tv_add_u32 v29, %[de], v29\n\tv_min_u32 v29, v29, v30\n\ts_sub_u32 s53, s52, 4640\n\tv_add_u32 v29, s53, v29\n\tv_mov_b32 v24, 0\n\tv_mov_b32 v25, 0\n\tv_mov_b32 v26, 0\n\tv_mov_b32 v27, 0\n\tds_write_b128 v29, v[24:27]\n\t"
    "s_cmp_lg_u32 s55, 3\n\ts_cbranch_scc1 9f\n\t"
    "v_bfe_u32 v18, %[t], 1, 5\n\tv_and_b32 v19, 1, %[t]\n\ts_cmp_lt_u32 s62, 8\n\ts_cselect_b32 s53, 2, 1\n\ts_mul_i32 s53, s53, %[sl]\n\tv_mul_u32_u24 v21, s53, v19\n\ts_sub_u32 s55, 2, s59\n\ts_lshl_b32 s55, s55, 7\n\tv_add_u32 v18, s55, v18\n\ts_cmp_lg_u32 s59, 3\n\ts_cbranch_scc1 7f\n\t"
    "v_bfe_u32 v18, %[t], 5, 1\n\tv_lshl_add_u32 v18, v18, 7, v118\n\tv_mov_b32 v19, s50\n\tv_mov_b32 v21, %[sl]\n"
    "7:\n\tv_add_u32 v19, s51, v19\n\tv_lshl_add_u32 v21, v18, 4, v21\n\tv_lshrrev_b32 v24, 3-" STR(TL) ", v19\n\tv_and_b32 v19, 7>>" STR(TL) ", v19\n\tv_lshlrev_b32 v19, 4, v19\n\tv_lshl_add_u32 v19, v24, 7, v19\n\t"
    "v_and_b32 v24, (1<<" STR(TL) ")-1, v18\n\tv_lshl_add_u32 v19, v24, 7-" STR(TL) ", v19\n\tv_lshrrev_b32 v18, " STR(TL) ", v18\n\tv_mul_u32_u24 v18, %[ust], v18\n\tv_add_u32 v18, v18, v19\n\tv_add_u32 v19, 512, v21\n\t"
    "s_branch 9f\n"
    "4:\n\t"
    "v_bfe_u32 v118, %[t], 3, 5\n\tv_lshrrev_b32 v117, 1, v118\n\tv_and_b32 v117, 4, v117\n\tv_and_b32 v116, 2, v118\n\tv_and_b32 v115, 7, %[t]\n\tv_or_b32 v116, v116, v117\n\tv_xor_b32 v115, v115, v116\n\tv_lshlrev_b32 v115, 4, v115\n\t"
    "s_mul_i32 s54, s61, 9\n\ts_sub_u32 s55, s62, 14\n\ts_lshr_b32 s55, s55, 1\n\ts_add_u32 s54, s54, s55\n\ts_lshl_b32 s54, s54, 7\n\t"
    "s_mul_i32 s52, s61, 0xa2000\n\ts_mul_i32 s53, s62, 0x180\n\ts_add_u32 s52, s52, s53\n\ts_cmp_ge_u32 s62, 11\n\ts_cselect_b32 s53, 0x51000-0x480, 0\n\ts_add_u32 s52, s52, s53\n\ts_sub_u32 s52, s52, 0xc00\n\t"
    "s_movk_i32 s55, 0x480\n\ts_cmp_ge_u32 s62, 14\n\ts_cselect_b32 s54, s54, s52\n\ts_cselect_b32 s55, 0x2400, s55\n\tv_mad_u32_u24 v118, v118, s55, v115\n\ts_lshl_b32 s55, s55, 5\n\t"
    "s_lshl_b32 s52, s63, 10\n\ts_add_u32 m0, s52, %[r2]\n\ts_mov_b32 s58, 0x510000\n\ts_mov_b32 s59, 0x20000\n\ts_cmp_ge_u32 s62, 14\n\ts_cselect_b32 s53, " STR(PRE1) "-1, " STR(PRE2) "-1\n\ts_setprio 3\n\t"
    "s_waitcnt lgkmcnt(0)\n\ts_memrealtime s[98:99]\n\t"
    "s_bitcmp1_b32 %[wg], 3\n\ts_cselect_b64 s[56:57], s[74:75], s[72:73]\n\ts_cmp_ge_u32 s62, 14\n\ts_cselect_b64 s[56:57], s[56:57], s[76:77]\n\ts_cselect_b32 s52, 0x180, -1\n\ts_and_b32 s57, s57, 0xffff\n\ts_add_u32 s60, s56, s54\n\ts_and_b32 s60, s60, 0x380\n\ts_cmp_eq_u32 s60, s52\n\ts_cselect_b32 s53, " STR(PRE3) "-1, s53\n\t"
    "s_load_dwordx2 s[96:97], s[66:67], 0x0\n\ts_barrier\n\t"
    "\n3:\n\t" "buffer_load_dwordx4 v118, s[56:59], s54 offen " WPOL " lds\n\ts_add_u32 m0, m0, 0x1000\n\ts_add_u32 s54, s54, s55\n\ts_sub_u32 s53, s53, 1\n\ts_cbranch_scc0 3b\n"
    "9:\n8:"
    : [xa]"=&{v[2:5]}"(xa),[xb]"=&{v[6:9]}"(xb_),[xc]"=&{v30}"(xc),[wa]"=&{v[10:13]}"(wa),[wb]"=&{v[14:17]}"(wb),[wc]"=&{v31}"(wc),[d0]"=&{v[18:21]}"(dq[0]),[d1]"=&{v[22:25]}"(dq[1]),[d2]"=&{v[26:29]}"(dq[2])
    : [ka]"s"(__builtin_amdgcn_kernarg_segment_ptr()),[wg]"s"(BID),[t]"v"(TID),[yst]"i"(YST),[wy]"i"(W_Y),[r2]"i"(O_R2),[sl]"i"(SLSZ),[tb]"i"(O_CH+O_TB),[ex]"i"(O_EXP),[de]"i"(O_DE),[ust]"i"(GST_)
    : "memory","scc","m0","v127","s50","s51","s52","s53","s54","s55","s56","s57","s58","s59","s60","s61","s62","s63","s64","s65","s66","s67","s68","s69","s70","s71","s72","s73","s74","s75","s76","s77","s78","s79",
      "s80","s81","s82","s83","s84","s85","s86","s87","s88","s89","s90","s91","s92","s93","s94","s95","s99");
  int wv=RFL(TID>>6), bid=BID, q4=wv>>2, w4=wv&3;
  int xg=bid&7, lb=bid>>3, tb=32*xg+lb, nch=2+(lb<8);
  u64 ep; u32 ldw;
  {
  }
  #define KILLIN "=v"(xa),"=v"(xb_),"=v"(xc),"=v"(wa),"=v"(wb),"=v"(wc),"=v"(dq[0]),"=v"(dq[1]),"=v"(dq[2])
  if(__builtin_expect(q4==3&&lb>=8,0)){
    int fu=18*xg+lb-14, kq=2*xg+(lb>=11), cgp=lb-8-3*(lb>=11);
  u32 wst= lb>=14? F_*64:D_*64;
  u32 wso=(lb>=14? (fu>>1)*128 : kq*(288*D_*2)+384*cgp)+(lb>=14? PRE1:PRE2)*wst, wld=(u32)(size_t)sm+O_R2+w4*1024+(lb>=14? PRE1:PRE2)*4096; u32x4 wrs=mkrs(lb>=14? ((fu&1)? wu:wg):wd,D_*F_*2);
  GETEP
  #define FLANE(RS) LANE(ln); int g=ln>>4, li=ln&15, qq=li>>2, pq=li&3; u32 wvo=(u32)(8*w4+(ln>>3))*(RS)+(u32)((ln&7)^(((ln>>3)&2)|((w4&1)<<2)))*16u; \
    const lch* tp=sm+O_R2+(8*g+qq)*128+((w4^((qq>>1)|((g&1)<<1)))*32)+pq*8;
  #define WFR(img,ks) trfrag(tp+(img)*CHSZ+(ks)*4096,tp+(img)*CHSZ+(ks)*4096+512)
  u64 tag=ep+1ull; const u32x4 I4={INV,INV,INV,INV};
  if(lb>=14){
    FLANE(F_*2)
    int mt=fu&1;
    if(w4==0) ST(u32x4,sm+O_ONE+ln*16)=u32x4{0x3f803f80u,0x3f803f80u,0x3f803f80u,0x3f803f80u}; else if(ln<8) ST(u32x4,sm+O_ONE+1024+ln*16)=u32x4{0x3f803f80u,0x3f803f80u,0x3f803f80u,0x3f803f80u};
    const lch* xp=sm+(li<8? O_TILE+li*XSTR : O_ONE)+g*16; int of=(mt*288+(fu>>1)*4+w4)*256+(li&7)*32+g*8+(li>>3)*4;
    const lch* mp=sm+O_RC+(li&7)*4;
    asm volatile("" : "+v"(xp),"+v"(of),"+v"(mp));
    rsrc_t rsG=RSRC(ws+W_GU,2*8*F_*2);
    if(w4==0) BST128(I4,rsG,(mt*288+(fu>>1)*4)*256+ln*16,0,SC1);
    bool slow=(((u32)wrs[0]+wso)&0x380u)==0x180u;
    if(slow){ wso-=(PRE1-PRE3)*wst; wld-=(PRE1-PRE3)*4096; FILL(5+PRE1-PRE3,VW(W3)); } else FILLD(5,W1,W1B);
    if(w4==0){ VMW(5); if(ln==0) GST(ws+W_FL+(576+fu)*8,tag); }
    if(slow) FILLD(13-PRE1,W3,W1B); else FILLD(13-PRE1,W1,W1B);
    bf16x8 fB[18];
    VMW(LAST1); gsync(sm,C_P,4);
    #pragma unroll
    for(int ks=0;ks<18-LAST1;ks++) fB[ks]=WFR(0,ks);
    #pragma unroll
    for(int ks=0;ks<18-LAST1;ks++) asm volatile("" : "+v"(fB[ks]));
    DRAIN(); gsync(sm,C_P,8);
    #pragma unroll
    for(int ks=18-LAST1;ks<18;ks++) fB[ks]=WFR(0,ks);
    f32x4 a0={0,0,0,0}; float nm,ri,ce; u32 mdw;
    { bf16x8 xb[18];
      if(!NOG2) gwait<2>(sm,C_X,1);
      gwait4<64>(C_X,4,xp,xb[0],xb[1],xb[2],xb[3]);
      #pragma unroll
      for(int ks=4;ks<7;ks++) xb[ks]=LD(bf16x8,xp+ks*64);
      SCHED();
      #pragma unroll
      for(int ks=0;ks<17;ks++){
        a0=__builtin_amdgcn_mfma_f32_16x16x32_bf16(fB[ks],xb[ks],a0,0,0,0); SCHED();
        if(2*ks+7<18) xb[2*ks+7]=LD(bf16x8,xp+(2*ks+7)*64); if(2*ks+8<18) xb[2*ks+8]=LD(bf16x8,xp+(2*ks+8)*64);
        if(ks==5) mdw=LD(u32,mp); if(ks==14){ ri=rcpf_(bhi(mdw)); nm=-blo(mdw); PIN(nm); } if(ks==15){ ce=ri*-1.4426950408889634f; PIN(ce); }
        SCHED(); }
      #define RR8 " row_ror:8 row_mask:0xf bank_mask:"
      float v0,v1,s0,s1;
      asm volatile("v_mfma_f32_16x16x32_bf16 v[112:115], %4, %5, %6\n\ts_nop 7\n\tv_fmac_f32_dpp v114, v114, %7" RR8 "0xf\n\tv_fmac_f32_dpp v115, v115, %7" RR8 "0xf\n\tv_fmac_f32_dpp v112, v112, %7" RR8 "0xf\n\tv_fmac_f32_dpp v113, v113, %7" RR8 "0xf\n\t"
        "v_mov_b32_dpp v112, v114" RR8 "0xc\n\tv_mov_b32_dpp v113, v115" RR8 "0xc\n\tv_mul_f32 %2, %9, v112\n\tv_mul_f32 %3, %9, v113\n\tv_mul_f32 %0, %8, v112\n\tv_mul_f32 %1, %8, v113"
        : "=&v"(v0),"=&v"(v1),"=&v"(s0),"=&v"(s1) : "v"(fB[17]),"v"(xb[17]),"v"(a0),"v"(nm),"v"(ri),"v"(ce));
      if(mt==0){ v0*=rcpf_(1.f+__builtin_amdgcn_exp2f(s0)); v1*=rcpf_(1.f+__builtin_amdgcn_exp2f(s1)); }
      __builtin_amdgcn_raw_buffer_store_b32(nv(pk(v0,v1)),rsG,of,0,SC1); garrive(sm,C_H); }
    if(mt==0&&w4==0){
    LANE(l2); ln=l2; li=ln&15; g=ln>>4;
    int c9=(lb-14)>>1, t=RFL((int)LD(u16,sm+O_IDX+xg*2)), c=64*c9+4*li+g; f32x4 pr;
    { u32 u=LD(u16,sm+O_IDX+16+xg*2)^0x8000u, xb=LD(u16,sm+O_TILE+xg*XSTR+c*2), sv=LD(u32,sm+O_RC+xg*4); float ds=b2f(Dsk[c]); u^=(u32)(((int)(u<<16))>>31)&0x7fffu;
      poll1<0>(ws+W_FL+(64*c9+ln)*8,tag); poll1<0>(ws+W_FL+(720+(ln<48? ln:47))*8,tag);
      float xn=(b2f(xb)-blo(sv))*rcpf_(bhi(sv)); pr=f32x4{b2f(xb),xn,xn*ds,sigm_f(b2f(u))}; }
    gwait<1>(sm,C_PR,1);
    { u32x4 rs=mkrs(ws+W_PART+(xg*9+c9)*4096,4096), ry=mkrs(ws+W_Y,D_*YST);
      int yo=c*YST+(t&~1)*2, po=ln*16; u32 a[4], t0,cnt,yb; u16* op=out+t*D_+c;
      #define LD_P "buffer_load_dwordx4 v[112:115], %[of], %[rs], 0 offen sc1\n\tbuffer_load_dwordx4 v[116:119], %[of], %[rs], 0 offen offset:1024 sc1\n\tbuffer_load_dwordx4 v[120:123], %[of], %[rs], 0 offen offset:2048 sc1\n\tbuffer_load_dwordx4 v[124:127], %[of], %[rs], 0 offen offset:3072 sc1\n\t"
      #define LD_YB "buffer_load_dword %[yb], %[yo], %[ry], 0 offen sc1\n\t"
      #define AD4(o,a,b,c,d) "v_add_f32 %[t0], v" #a ", v" #b "\n\tv_add_f32 %[" #o "], v" #c ", v" #d "\n\tv_add_f32 %[" #o "], %[t0], %[" #o "]\n\t"
      u64 ym;
      asm volatile(LD_YB LD_P "s_mov_b32 %[cnt], 0x20000\n1:\n\ts_waitcnt vmcnt(0)\n\tv_cmp_eq_u32 %[ym], -1, %[yb]\n\t" CHK_I0 MX2(116,117) MX2(118,119) MX2(120,121) MX2(122,123) MX2(124,125) MX2(126,127) CHK_IE
        "s_or_b64 exec, vcc, %[ym]\n\ts_cbranch_scc0 9f\n\ts_sleep " STR(QSLP) "\n\ts_mov_b64 exec, %[ym]\n\ts_cbranch_execz 2f\n\t" LD_YB "2:\n\ts_mov_b64 exec, vcc\n\ts_cbranch_execz 3f\n\t" LD_P
        "3:\n\ts_mov_b64 exec, -1\n\ts_sub_u32 %[cnt], %[cnt], 1\n\ts_cbranch_scc0 1b\n9:\n\ts_mov_b64 exec, -1\n\t"
        AD4(a0,112,116,120,124) AD4(a1,113,117,121,125) AD4(a2,114,118,122,126) AD4(a3,115,119,123,127) "s_nop 1"
        : [t0]"=&v"(t0),[cnt]"=&s"(cnt),[a0]"=&v"(a[0]),[a1]"=&v"(a[1]),[a2]"=&v"(a[2]),[a3]"=&v"(a[3]),[yb]"=&v"(yb),[ym]"=&s"(ym) : [of]"v"(po),[rs]"s"(rs),[yo]"v"(yo),[ry]"s"(ry),"v"(op),"v"(pr) : PCLOB);
      auto s0=__builtin_amdgcn_permlane32_swap(a[0],a[2],false,false); auto s1=__builtin_amdgcn_permlane32_swap(a[1],a[3],false,false);
      u32 x0=s0[0],x1=s0[1],y0=s1[0],y1=s1[1]; u32 p0=BC(u32,BC(float,x0)+BC(float,x1)), p1=BC(u32,BC(float,y0)+BC(float,y1));
      auto s2=__builtin_amdgcn_permlane16_swap(p0,p1,false,false); u32 z0=s2[0], z1=s2[1]; float s=BC(float,z0)+BC(float,z1);
      float gq=silu_f(__builtin_fmaf(s,pr[3],pr[1]));
      float sy=((t&1)? bhi(yb):blo(yb))+pr[2];
      float o=__builtin_fmaf(sy,gq,pr[0]);
      asm volatile("global_store_short %0, %1, off sc1" :: "v"(op),"v"(pk(o,o)) : "memory");
      if(bid==112&&ln==0) GST(ws+W_EPOCH,tag); }
    }
  } else
  {
    FLANE(D_*2)
    bf16x8 fr[27];
    rsrc_t rsP=RSRC(ws+W_PART+cgp*12288+kq*256+w4*64,8*9*4096-2*12288);
    #pragma unroll
    for(int i=0;i<3;i++) BST128(I4,rsP,li<8? li*(9*4096)+g*16+i*4096 : OOR,0,SC1);
    if(PRE2<9) FILLD(9-PRE2,W2_,W2B);
    wso+=128u-9u*wst;
    FILLD(9,W2_,W2B);
    VMW(9); gsync(sm,C_P,4);
    #pragma unroll
    for(int ks=0;ks<9;ks++) fr[ks]=WFR(0,ks);
    gsync(sm,C_P,8); if(w4==0&&ln==0) GST(ws+W_FL+(720+6*xg+lb-8)*8,tag);
    wso+=128u-9u*wst; wld-=2*CHSZ;
    FILL(9,VW(W2B));
    int fo=W_FL+(576+((9*kq)&~1)+(ln<9? ln:9))*8;
    if(w4==0) asm volatile("global_load_dwordx2 v[112:113], %0, %1 sc1\n\ts_waitcnt vmcnt(10)" :: "v"(fo),"s"(ws) : "memory"); else VMW(9);
    gsync(sm,C_P,12);
    #pragma unroll
    for(int ks=0;ks<9;ks++) fr[9+ks]=WFR(1,ks);
    { int t2=TID; asm volatile("" : "+v"(t2)); ln=t2&63; li=ln&15; g=ln>>4; }
    const lch* hp=sm+O_TILE+(li&7)*64+g*16; int po= li<8? li*(9*4096)+g*16+4096 : OOR; PIN(hp); PIN(po);
    u32x4 rs=mkrs(ws+W_GU,2*8*F_*2);
    int of=((ln>>5)*288+18*kq+2*w4)*256+(ln&31)*16; lch* hd=sm+O_TILE+w4*512+((ln>>1)&7)*64+((ln>>4)&1)*32+(ln&1)*16+(ln>>5)*8; PIN(of); PIN(hd); asm volatile("" : "+s"(rs));
    DRAIN();
    if(w4==0){ u64 bad; asm volatile("v_cmp_ne_u64 %0, %1, v[112:113]" : "=s"(bad) : "s"(tag) : "memory");
      if(bad!=0ull) poll1<0>(ws+fo,tag); }
    gsync(sm,C_P,16);
    { u32 t0,cnt;
      #define LD_G "buffer_load_dwordx4 v[112:115], %[of], %[rs], 0 offen sc1\n\tbuffer_load_dwordx4 v[116:119], %[of], %[rs], 0 offen offset:2048 sc1\n\tbuffer_load_dwordx4 v[120:123], %[of], %[rs], %[so] offen sc1\n\t"
      #define HM1(a,b) "v_lshlrev_b32 %[t0], 16, v" #a "\n\tv_lshlrev_b32 %[t1], 16, v" #b "\n\tv_and_b32 v" #a ", %[hm], v" #a "\n\tv_and_b32 v" #b ", %[hm], v" #b "\n\tv_mul_f32 %[t0], %[t0], %[t1]\n\tv_mul_f32 v" #a ", v" #a ", v" #b "\n\tv_cvt_pk_bf16_f32 v" #a ", %[t0], v" #a "\n\t"
      #define HMUL(a,b,c,d,o) "v_permlane32_swap_b32 v" #a ", v" #c "\n\tv_permlane32_swap_b32 v" #b ", v" #d "\n\t" HM1(a,c) HM1(b,d) "ds_write_b64 %[hd], v[" #a ":" #b "] offset:" #o "\n\t"
      u32 t1;
      asm volatile(LD_G :: [of]"v"(of),[rs]"s"(rs),[so]"s"(w4==0? 4096:0) : "memory");
      fr[18]=WFR(0,0); PIN(fr[18]);
      asm volatile(POLLASM(LD_G,CHK_I0 MX2(116,117) MX2(118,119) MX2(120,121) MX2(122,123) CHK_IE,GSLP)
        HMUL(112,113,114,115,0) HMUL(116,117,118,119,2048) "s_cmp_eq_u32 %[so], 0\n\ts_cbranch_scc1 8f\n\t" HMUL(120,121,122,123,4096) "8:\n\ts_mov_b64 exec, 1\n\tds_add_u32 %[ch], %[one]\n\ts_mov_b64 exec, -1"
        : [t0]"=&v"(t0),[t1]"=&v"(t1),[cnt]"=&s"(cnt) : [of]"v"(of),[rs]"s"(rs),[so]"s"(w4==0? 4096:0),[hd]"v"(hd),[hm]"s"(0xffff0000u),[ch]"v"(C_H*4),[one]"v"(1),"v"(hp),"v"(po) : PCLOB);
      }
    { f32x4 ac[3]={{0,0,0,0},{0,0,0,0},{0,0,0,0}};
      bf16x8 hb[9];
      gwait4<512>(C_H,4,hp,hb[0],hb[1],hb[2],hb[3]);
      #pragma unroll
      for(int ks=0;ks<10;ks++){
        if(ks<9){ ac[0]=__builtin_amdgcn_mfma_f32_16x16x32_bf16(fr[ks],hb[ks],ac[0],0,0,0); ac[1]=__builtin_amdgcn_mfma_f32_16x16x32_bf16(fr[9+ks],hb[ks],ac[1],0,0,0); }
        if(ks>0) ac[2]=__builtin_amdgcn_mfma_f32_16x16x32_bf16(fr[17+ks],hb[ks-1],ac[2],0,0,0);
        SCHED();
        if(ks<4){ fr[19+2*ks]=WFR(0,1+2*ks); fr[20+2*ks]=WFR(0,2+2*ks); } if(ks>=1&&ks<=2){ hb[2*ks+2]=LD(bf16x8,hp+(2*ks+2)*512); hb[2*ks+3]=LD(bf16x8,hp+(2*ks+3)*512); } if(ks==3) hb[8]=LD(bf16x8,hp+8*512);
        SCHED(); }
      rsrc_t r0=RSRC(ws+W_PART+cgp*12288+kq*256+w4*64-4096,8*9*4096), r2=RSRC(ws+W_PART+cgp*12288+kq*256+w4*64+4096,8*9*4096-2*12288);
      BST128(nv4(BC(u32x4,ac[0])),r0,po,0,SC1); BST128(nv4(BC(u32x4,ac[1])),rsP,po,0,SC1); BST128(nv4(BC(u32x4,ac[2])),r2,po,0,SC1); }

  }
  asm volatile("s_endpgm" : KILLIN :: "memory"); }
  {
  if(__builtin_expect(q4!=0,0)){ int grp= q4==1? nch-1 : q4-2;
    chan(sm,grp,(w4+(0x48>>(2*q4)))&3,72*xg+(q4==3? 16+lb : 2*lb+(lb<8? 0:8)+2-q4),(q4+1)>>2,ws,xa,xb_,wa,wb,dq[0]);
    asm volatile("s_endpgm" : KILLIN :: "memory"); }
  u32x4 xq[3]; f32x2 rc[3]; int xr_,xr2_;
  {
    LANE(ln); int g=ln>>4, li=ln&15, qq=li>>2, pq=li&3, j=ln&31, rl=2*w4+(ln>>5);
    lch* tile=sm+O_TILE; u32 Tr=(ln>>1)&16;
    int uo=W_U+(tb>>TL)*GST_+(tb&((1<<TL)-1))*(128>>TL)+(w4*1024<<TL)+(ln>>(3-TL))*128+(ln&(7>>TL))*16, uo1=uo+(4096<<TL), uo2=uo+(8192<<TL); PIN(uo); PIN(uo1); PIN(uo2);
    lch* tw=tile+rl*TSTR2+j*16; lch* tw2=tw+1024-j*12; u32 one=0x3f803f80u; const lch* tp=tile+qq*TSTR2+(w4*64+g*16+pq*4)*2;
    int go= ln<32? W_RG+(ln>>2)*8192+(tb*4+(ln&3))*8 : W_ST+(tb*8+(ln&7))*8; lch* kw=sm+O_RV+rl*2; const lch* kr=sm+(ln<32? O_RV+(ln&3)*4 : O_MD+(ln&7)*4); PIN(kw); PIN(kr);
    int xr=(32*(tb>>2)+(ln>>2))*(D_*2)+256*(lb&3)+64*w4+pq*16, xr2= w4==0? xr+(lb&2)*(8*D_*2)+1024+64*(lb&1)-256*(lb&3):OOR;
    rsrc_t rx=RSRC(x,L_*D_*2); u64 gm=((1ull<<(4*NRG))-1ull)|0xff00000000ull; u32 um=~UMASK, km=0x80008000u; asm volatile("" : "+s"(gm),"+s"(um),"+s"(km),"+s"(ws));
    #define D2(a,b,c) __builtin_amdgcn_fdot2_f32_bf16(BC(bf2,a),BC(bf2,b),c,false)
    asm volatile("s_waitcnt vmcnt(5)" : "+v"(wa),"+v"(wb),"+v"(wc),"+v"(Tr),"+v"(tw),"+v"(tw2),"+v"(tp),"+v"(one),"+v"(go),"+v"(xr),"+v"(xr2) :: "memory");
    u32 wq[9]={wa[0],wa[1],wa[2],wa[3],wb[0],wb[1],wb[2],wb[3],wc}; float sw;
    { float a[3];
      #pragma unroll
      for(int i=0;i<9;i++) a[i%3]=D2(wq[i],one,i<3? 0.f:a[i%3]);
      sw=add16x(sum16((a[0]+a[1])+a[2])); }
    GETEP u32 tg=(u32)ep+1u; Tr=((upat(tg,w4)>>Tr)&0xffffu)*0x10001u; u32 tgv=tg;
    float z_[9]={0,0,0,0,0,0,0,0,0};
    asm volatile("s_waitcnt vmcnt(2)" : "+v"(xa),"+v"(xb_),"+v"(xc),"+v"(Tr),"+v"(sw),"+v"(tgv),"+v"(z_[0]),"+v"(z_[1]),"+v"(z_[2]),"+v"(z_[3]),"+v"(z_[4]),"+v"(z_[5]) :: "memory");
    u32 v[9]={xa[0],xa[1],xa[2],xa[3],xb_[0],xb_[1],xb_[2],xb_[3],xc};
    { float xf[18], s1, s2, s3;
      #pragma unroll
      for(int i=0;i<9;i++){ z_[i&1]=D2(v[i],one,z_[i&1]); z_[2+(i&1)]=D2(v[i],v[i],z_[2+(i&1)]); z_[4+(i&1)]=D2(v[i],wq[i],z_[4+(i&1)]); }
      s1=z_[0]+z_[1]; s2=z_[2]+z_[3]; s3=z_[4]+z_[5];
      SCHED();
      #define RSTEP(C) s1+=dppf<C>(s1); s2+=dppf<C>(s2); s3+=dppf<C>(s3); SCHED();
      RSTEP(0xB1) RSTEP(0x4E) RSTEP(0x141) RSTEP(0x140)
      s1=add16x(s1)*(1.f/576.f); s2=add16x(s2)*(1.f/576.f); s3=add16x(s3);
      float mm=s1*s1, var=s2-mm;
      if(__builtin_expect(BALLOT(!(mm<=64.f*var))!=0ull,0)){
        float q=0.f;
        #pragma unroll
        for(int i=0;i<9;i++){ u32 t=v[i]; PIN(t); float d0=blo(t)-s1, d1=bhi(t)-s1; q=__builtin_fmaf(d0,d0,q); q=__builtin_fmaf(d1,d1,q); }
        var=add16x(sum16(q))*(1.f/576.f); }
      #define UNP(i) PIN(v[i]); xf[2*(i)]=blo(v[i]); xf[2*(i)+1]=bhi(v[i]); PIN(xf[2*(i)]); PIN(xf[2*(i)+1]); SCHED();
      u32 vb=pk(var,s1); UNP(0)
      float den=blo(vb)+1e-5f, m=bhi(vb); UNP(1)
      vb=pk(den,den); s3=__builtin_fmaf(-m,sw,s3); UNP(2)
      den=__builtin_amdgcn_sqrtf(bhi(vb)); UNP(3)
      u32 md=pk(m,den); UNP(4)
      float ri=rcpf_(bhi(md)); UNP(5)
      s3*=ri; float rs_=ri*0.0625f; UNP(6)
      u32 u=pk(s3,s3); float cs_=-m*rs_; UNP(7)
      { u32 sg=(u32)((int)u>>31); PIN(sg); u^=sg|km; } UNP(8)
      asm volatile("s_mov_b64 exec, 0x1\n\ts_mov_b32 exec_hi, 1\n\tds_write_b16 %0, %1\n\tv_add_u32 %0, %0, %0\n\tds_write_b32 %0, %2 offset:%3\n\ts_mov_b32 exec_hi, 0\n\tds_add_u32 %4, %5\n\ts_mov_b64 exec, -1" : "+v"(kw) : "v"(u),"v"(md),"i"(O_MD-2*O_RV),"v"(C_I*4),"v"(1) : "memory");
      if(w4==3){ gwait<0>(sm,C_I,4); asm volatile("ds_read_b32 v118, %4\n\tv_mov_b32 v119, %1\n\ts_mov_b64 exec, %3\n\ts_waitcnt lgkmcnt(0)\n\tglobal_store_dwordx2 %0, v[118:119], %2 sc1\n\ts_mov_b64 exec, -1" :: "v"(go),"v"(tgv),"s"(ws),"s"(gm),"v"(kr) : "memory"); }
      SCHED();
      u32 h[9];
      #define UH(i) h[i]=(pkz(__builtin_fmaf(xf[2*(i)],rs_,cs_),__builtin_fmaf(xf[2*(i)+1],rs_,cs_))&um)|Tr;
      UH(0) UH(1) UH(2) UH(3) ST(u32x4,tw)=u32x4{h[0],h[1],h[2],h[3]}; UH(4) UH(5) UH(6) UH(7) ST(u32x4,tw+512)=u32x4{h[4],h[5],h[6],h[7]};
      UH(8) ST(u32,tw2)=h[8]; }
    gsync(sm,C_L,4);
    { u32x4 uq[3];
      #pragma unroll
      for(int z=0;z<3;z++) uq[z]=BC(u32x4,trfrag(tp+z*512,tp+z*512+4*TSTR2));
      asm volatile("global_store_dwordx4 %0, %1, %2 sc1" :: "v"(uo),"v"(uq[0]),"s"(ws) : "memory");
      asm volatile("global_store_dwordx4 %0, %1, %2 sc1" :: "v"(uo1),"v"(uq[1]),"s"(ws) : "memory");
      asm volatile("s_cmp_lg_u32 %3, 0\n\ts_cbranch_scc1 1f\n\tglobal_store_dwordx4 %0, %1, %2 sc1\n1:\n\ts_nop 1" :: "v"(uo2),"v"(uq[2]),"s"(ws),"s"(w4),"v"(uq[0]),"v"(uq[1]) : "memory","scc");
      asm volatile("" : "+v"(dq[0]),"+v"(dq[1]));
      xr_=xr; xr2_=xr2;
      #define XQ { rsrc_t rx_=RSRC(x,L_*D_*2); xq[0]=BLD128(rx_,xr_,0,0); xq[1]=BLD128(rx_,xr_,16*D_*2,0); xq[2]=BLD128(rx_,xr2_,0,0); }
    }
    if(lb>=2&&lb<8){
      const u16* wm=(lb&1)? wu:wg; int co=(3-(int)((u64)wm>>7)-xg)&7, r_=576-12*PFN+(((lb>>1)-1)*4+w4)+12*j;
      asm volatile("buffer_load_dword v120, %0, %1, 0 offen" :: "v"(j<PFN&&(ln<32||co==0)? r_*(F_*2)+(9*xg+co)*128+(ln>>5)*1024 : OOR),"s"(mkrs(wm,D_*F_*2)) : "memory"); }
    if(lb>=8&&lb<14) garrive(sm,C_D);
  }
  FP fp;
  { LANE(ln); int g=ln>>4, li=ln&15; u64 tag=ep+1ull;
    const char* sp_=ws+W_ST+(32*(tb>>2)+(ln>>2))*8; u64 fs, fs1, ff; const char* fp_=ws+W_FL+((ln>=32&&w4==0? 512+32*(lb&1) : 128*(lb&3)+32*w4)+(ln&31))*8;
    if(lb==CTLB||lb>=14){
      u32 M=(u32)tag, rank,key;
      top8(sm,w4,ln,M,ws,rank,key,ldw);
      {
        int t0=__builtin_amdgcn_readlane((int)key,__builtin_ctzll(BALLOT(rank==(u32)(2*w4)))), t1=__builtin_amdgcn_readlane((int)key,__builtin_ctzll(BALLOT(rank==(u32)(2*w4+1))));
        int tsel=~((ln>>5)? t1:t0)&2047, j=ln&31; rsrc_t rx=RSRC(x,L_*D_*2);
        u32x4 a=BLD128(rx,tsel*1152+j*16,0,0), b=BLD128(rx,tsel*1152+j*16+512,0,0); u32 c=BLD32(rx,tsel*1152+j*4+1024,0,0);
        const char* rp_=ws+W_ST+tsel*8; u64 f1=GLD(rp_);
        garrive(sm,C_D);
        if(lb==CTLB&&rank<8&&!(ln&1)) GST(ws+W_IDX+xg*1024+w4*128+rank*8,(tag<<16)|(~key&2047u));
        if(w4==0){ if(rank<8) ST(u16,sm+O_IDX+(ln&1)*16+rank*2)=(u16)((ln&1)? key>>16 : ~key&2047u); garrive(sm,C_IX); }
        lch* xw=sm+O_TILE+(2*w4+(ln>>5))*XSTR+j*16;
        if(BALLOT((u32)(f1>>32)!=(u32)tag)!=0ull) f1=poll1<32>(rp_,tag<<32);
        ST(u32x4,xw)=a; ST(u32x4,xw+512)=b; ST(u32,xw+1024-j*12)=c; if(j==0) ST(u32,sm+O_RC+(2*w4+(ln>>5))*4)=(u32)f1;
        garrive(sm,C_X);
      }
    }
    PRIO(0);
    if(lb<14) gwait<8>(sm,C_U,2); else if(XE==0) gwait<4>(sm,C_H,4); else if(XE==2) gwait<4>(sm,C_U,2); else __builtin_amdgcn_s_sleep(XE==3? 6:0);
    fs=GLD(sp_); fs1=GLD(sp_+128); ff=GLD(fp_); XQ
    {
      if(BALLOT((u32)(fs>>32)!=(u32)tag||(u32)(fs1>>32)!=(u32)tag)!=0ull){ fs=poll1<32>(sp_,tag<<32); fs1=poll1<32>(sp_+128,tag<<32); }
      float ri=rcpf_(bhi((u32)fs)), rj=rcpf_(bhi((u32)fs1)); rc[0]=f32x2{ri,-blo((u32)fs)*ri}; rc[1]=f32x2{rj,-blo((u32)fs1)*rj}; rc[2]= (lb&2)? rc[1]:rc[0]; }
    asm volatile("" : "+v"(rc[0]),"+v"(rc[1]),"+v"(rc[2]),"+v"(xq[0]),"+v"(xq[1]),"+v"(xq[2]));
    bool ix= w4==3&&lb<14&&lb!=CTLB; u64 f; const char* p=ws+W_IDX+(xg*8+(lb&3))*128+(ln&7)*8; if(ix) f=GLD(p);
    final_pre(fp,w4,xq,dq,rc);
    if(BALLOT(ff!=tag)!=0ull) poll1<0>(fp_,tag);
    if(ix){ if(BALLOT(((f^(tag<<16))>>16)!=0ull)!=0ull) f=poll1<16>(p,tag<<16);
      if(ln<8) ST(u16,sm+O_IDX+ln*2)=(u16)f; garrive(sm,C_IX); }
  }
  final_post(sm,fp,w4,nch,out,ws,xr_,ldw);
  }
}

extern "C" void kernel_launch(void* const* d_in, const int* in_sizes, int n_in, void* d_out, int out_size, void* d_ws, size_t ws_size, hipStream_t stream) {
  const u16** I=(const u16**)d_in;
  KP kp; kp.Are=I[1]; kp.Aim=I[2]; kp.Bre=I[3]; kp.Bim=I[4]; kp.Cre=I[5]; kp.Cim=I[6]; kp.ldt=I[7]; kp.pad=I[7];
  kA<<<256,1024,0,stream>>>(I[0],(char*)d_ws,I[10],I[8],I[11],I[12],I[13],(u16*)d_out,kp);
}
```

```cpp
#define RH 1
#include <hip/hip_runtime.h>
#include <hip/hip_bf16.h>
#include <stdint.h>
#if defined(__HIP_DEVICE_COMPILE__)
#define NOPK __attribute__((target("no-packed-fp32-ops")))
#else
#define NOPK
#endif
#define DEV __device__ NOPK __forceinline__
typedef unsigned short u16;
typedef unsigned int u32;
typedef unsigned long long u64;
typedef short bf16x8 __attribute__((ext_vector_type(8)));
typedef _Float16 h8 __attribute__((ext_vector_type(8)));
typedef _Float16 h2 __attribute__((ext_vector_type(2)));
typedef float f32x4 __attribute__((ext_vector_type(4)));
typedef float f32x2 __attribute__((ext_vector_type(2)));
typedef u32 u32x4 __attribute__((ext_vector_type(4)));
typedef u32 u32x2 __attribute__((ext_vector_type(2)));
typedef __bf16 bf2 __attribute__((ext_vector_type(2)));
typedef short v4s __attribute__((ext_vector_type(4)));
typedef __amdgpu_buffer_rsrc_t rsrc_t;
#define GAS __attribute__((address_space(1)))
#define LAS __attribute__((address_space(3)))
typedef LAS char lch;
#define L_ 2048
#define D_ 576
#define F_ 4608
#define BC(T,v) __builtin_bit_cast(T,v)
#define TID ((int)__builtin_amdgcn_workitem_id_x())
#define BID ((int)__builtin_amdgcn_workgroup_id_x())
#define RFL(x) __builtin_amdgcn_readfirstlane(x)
#define RLX_AGENT __ATOMIC_RELAXED, __HIP_MEMORY_SCOPE_AGENT
#define LD(T,p) (*(const LAS T*)(p))
#define ST(T,p) (*(LAS T*)(p))
#define SPINCAP (1<<17)
#define DRAIN() asm volatile("s_waitcnt vmcnt(0)" ::: "memory")
#define GLD(p) __hip_atomic_load((const GAS u64*)(p),RLX_AGENT)
#define GST(p,v) __hip_atomic_store((GAS u64*)(p),(u64)(v),RLX_AGENT)
#define BALLOT(c) __builtin_amdgcn_ballot_w64(c)
#define SC1 16
#define LANE(l) int l; { int t2_=TID; asm volatile("" : "+v"(t2_)); l=t2_&63; }
#define WPOL "nt"
#define UE 0
#define NOG2 1
#define PPOL "nt"
#define GDL 0
#define RDL 0
#define UDL 0
#define IV 0
#define INVST "global_store_dwordx4 v23, v[124:127], s[66:67] sc1\n\t"
#define PW "1"
#define INVL asm volatile("global_store_dwordx4 v123, v[124:127], %0 sc1" :: "s"(ws) : "memory");
#define HD0 174
#define HD1 148
#define HD3 165
#define HM 100
#define HT 555
#define HR 12
#define XB 1
#define XBW "0"
#define XA 0
#define XC 0
#define FPA 0
#define FPB 0
#define FPC 0
#define HL 650
#define HP 55
#define HQ 40
#define HK 670
#define HS 1
#define H2 600
#define FPE 0
#define SCHED() __builtin_amdgcn_sched_barrier(0)
#define RSRC(p,n) __builtin_amdgcn_make_buffer_rsrc((void*)(p),0,(n),0x00020000)
#define BLD128 __builtin_amdgcn_raw_buffer_load_b128
#define BLD32 __builtin_amdgcn_raw_buffer_load_b32
#define BST128 __builtin_amdgcn_raw_buffer_store_b128
#define PRIO(n) __builtin_amdgcn_s_setprio(n)

DEV float b2f(u32 v){ return BC(float,v<<16); }
DEV float blo(u32 v){ return BC(float,v<<16); }
DEV float bhi(u32 v){ return BC(float,v&0xffff0000u); }
DEV u32 pk(float a,float b){ f32x2 v={a,b}; bf2 r=__builtin_convertvector(v,bf2); return BC(u32,r); }
DEV u32 pkh(float a,float b){ f32x2 v={a,b}; h2 r=__builtin_convertvector(v,h2); return BC(u32,r); }
DEV float rb(float f){ return BC(float,pk(f,f)<<16); }
DEV float rcpf_(float x){ return __builtin_amdgcn_rcpf(x); }
DEV float expf_(float x){
  const float L2E=1.4426950216293335f, L2El=1.9259629911266175e-8f; float h=x*L2E; float l=__builtin_fmaf(x,L2E,-h)+x*L2El;
  float e=__builtin_amdgcn_exp2f(h); return __builtin_fmaf(e*l,0.6931471805599453f,e); }
DEV float sigm_f(float x){ return rcpf_(1.f+__builtin_amdgcn_exp2f(-x*1.4426950408889634f)); }
DEV float silu_f(float x){ return x*sigm_f(x); }
DEV u32 umax_(u32 a,u32 b){ return a>b?a:b; }
DEV u32 umin_(u32 a,u32 b){ return a<b?a:b; }

#define YSH 3
#define PRE1 6
#define PRE3 4
#define PRE2 5
#define W1 5
#define W3 3
#define W2_ 4
#define W1B 20
#define W2B 12
#define LAST1 4
#define RGATE 4
#define UGATE 7
#define R2D 12
#define PFN 32
#define CPF 8
#define NRG 8
#define W_EPOCH 0
#define W_RG 16384
#define W_ST (W_RG+32*8192)
#define W_U (W_ST+32768)
#define UST (D_*16+128)
#define YST (L_*2+128)
#define TL 1
#define GST_ ((D_*16<<TL)+128)
#define W_Y (W_U+256*UST)
#define W_PART (W_Y+D_*YST)
#define W_GU (W_PART+8*9*16*256)
#define W_FL 9216
#define W_HT (W_RG+16*8192)
#define W_HP (W_HT+8192)
#define W_IDX 1024
#define CTLB 0
struct KP { const u16 *Are,*Aim,*Bre,*Bim,*Cre,*Cim,*ldt,*pad; };

template<int CTRL> DEV float dppf(float v){ return BC(float,__builtin_amdgcn_update_dpp(0u,BC(u32,v),CTRL,0xf,0xf,false)); }
template<int CTRL> DEV u32 dppu(u32 v){ return (u32)__builtin_amdgcn_update_dpp(0u,v,CTRL,0xf,0xf,false); }
DEV float sum16(float v){ v+=dppf<0xB1>(v); v+=dppf<0x4E>(v); v+=dppf<0x141>(v); v+=dppf<0x140>(v); return v; }
DEV float add16x(float v){ auto r=__builtin_amdgcn_permlane16_swap(BC(u32,v),BC(u32,v),false,false); u32 a=r[0], b=r[1]; return BC(float,a)+BC(float,b); }
DEV u32 wmax(u32 v){
  v=umax_(v,(u32)__builtin_amdgcn_update_dpp(0u,v,0x111,0xf,0xf,false));
  v=umax_(v,(u32)__builtin_amdgcn_update_dpp(0u,v,0x112,0xf,0xf,false));
  v=umax_(v,(u32)__builtin_amdgcn_update_dpp(0u,v,0x114,0xf,0xf,false));
  v=umax_(v,(u32)__builtin_amdgcn_update_dpp(0u,v,0x118,0xf,0xf,false));
  v=umax_(v,(u32)__builtin_amdgcn_update_dpp(0u,v,0x142,0xa,0xf,false));
  v=umax_(v,(u32)__builtin_amdgcn_update_dpp(0u,v,0x143,0xc,0xf,false));
  return (u32)__builtin_amdgcn_readlane((int)v,63); }

DEV void garrive(lch* sm,int c){ u32 ad=(u32)(size_t)sm+c*4, one=1;
  asm volatile("s_mov_b64 exec, 1\n\tds_add_u32 %0, %1\n\ts_mov_b64 exec, -1" :: "v"(ad),"v"(one) : "memory"); }
template<int SLP> DEV void gwait(lch* sm,int c,u32 target){ u32 ad=(u32)(size_t)sm+c*4, tv, cnt, sv; target=RFL(target);
  asm volatile("s_mov_b32 %1, 0x20000\n"
    "1:\n\tds_read_b32 %0, %3\n\ts_waitcnt lgkmcnt(0)\n\tv_readfirstlane_b32 %2, %0\n\ts_cmp_ge_u32 %2, %4\n\ts_cbranch_scc1 2f\n\ts_sleep %5\n\ts_sub_u32 %1, %1, 1\n\ts_cbranch_scc0 1b\n"
    "2:" : "=&v"(tv),"=&s"(cnt),"=&s"(sv) : "v"(ad),"s"(target),"i"(SLP) : "memory","scc"); }
template<int ST_,class T> DEV void gwait4(int c,u32 target,const lch* p,T& a,T& b,T& d,T& e){ u32 tv,cnt,sv;
  asm volatile("s_mov_b32 %1, 0x20000\n1:\n\tds_read_b32 %0, %7\n\tds_read_b128 %3, %8\n\tds_read_b128 %4, %8 offset:%10\n\tds_read_b128 %5, %8 offset:%11\n\tds_read_b128 %6, %8 offset:%12\n\ts_waitcnt lgkmcnt(4)\n\t"
    "v_readfirstlane_b32 %2, %0\n\ts_cmp_ge_u32 %2, %9\n\ts_cbranch_scc1 2f\n\ts_sleep 1\n\ts_sub_u32 %1, %1, 1\n\ts_cbranch_scc0 1b\n2:\n\ts_waitcnt lgkmcnt(0)"
    : "=&v"(tv),"=&s"(cnt),"=&s"(sv),"=&v"(a),"=&v"(b),"=&v"(d),"=&v"(e) : "v"(c*4),"v"(p),"i"(target),"i"(ST_),"i"(2*ST_),"i"(3*ST_) : "memory","scc"); }
template<int D> DEV void tgate(u32 t0){ u32 cnt; asm volatile("s_mov_b32 %0, 100\n1:\n\ts_memrealtime s[98:99]\n\ts_waitcnt lgkmcnt(0)\n\ts_sub_u32 s98, s98, %1\n\ts_cmp_ge_u32 s98, %2\n\ts_cbranch_scc1 2f\n\ts_sleep 1\n\ts_sub_u32 %0, %0, 1\n\ts_cbranch_scc0 1b\n2:" : "=&s"(cnt) : "s"(t0),"i"(D) : "memory","scc"); }
DEV u32 now_(){ u32 t; asm volatile("s_memrealtime s[98:99]\n\ts_waitcnt lgkmcnt(0)\n\ts_mov_b32 %0, s98" : "=s"(t) :: "memory"); return t; }
DEV void tgate2(u32 t0,u32 d){ u32 cnt; asm volatile("s_mov_b32 %0, 100\n1:\n\ts_memrealtime s[98:99]\n\ts_waitcnt lgkmcnt(0)\n\ts_sub_u32 s98, s98, %1\n\ts_cmp_ge_u32 s98, %2\n\ts_cbranch_scc1 2f\n\ts_sleep 1\n\ts_sub_u32 %0, %0, 1\n\ts_cbranch_scc0 1b\n2:" : "=&s"(cnt) : "s"(t0),"s"(d) : "memory","scc"); }
DEV void gsync(lch* sm,int c,u32 target){ garrive(sm,c); gwait<0>(sm,c,target); }
template<int SH> DEV u64 poll1(const char* p,u64 want){ u64 f=GLD(p);
  _Pragma("unroll 1") for(int sp=0;sp<SPINCAP;sp++){ bool bad=((f^want)>>SH)!=0ull; if(BALLOT(bad)==0ull) break; __builtin_amdgcn_s_sleep(1); if(bad) f=GLD(p); }
  __builtin_amdgcn_fence(__ATOMIC_ACQUIRE,"wavefront"); return f; }
#define STR_(x) #x
#define STR(x) STR_(x)
DEV u32x4 mkrs(const void* p,u32 n){ u64 a=(u64)p; return u32x4{(u32)RFL((int)(u32)a),(u32)RFL((int)((u32)(a>>32)&0xffffu)),n,0x00020000u}; }
#define VMW(n) asm volatile("s_waitcnt vmcnt(%0)" :: "i"(n) : "memory")
#define YSLP 1
#define GSLP 1
#define QSLP 1
#define PSLP 3
#define POLLASM(LOADS,CHECK,K) "s_mov_b32 %[cnt], 0x20000\n1:\n\ts_waitcnt vmcnt(0)\n\t" CHECK "s_cbranch_vccz 9f\n\ts_sleep " STR(K) "\n\ts_mov_b64 exec, vcc\n\t" LOADS \
  "s_mov_b64 exec, -1\n\ts_sub_u32 %[cnt], %[cnt], 1\n\ts_cbranch_scc0 1b\n9:\n\t"
#define LD_DUP2 "buffer_load_dwordx4 v[112:115], %[of], %[rs], 0 offen sc1\n\tbuffer_load_dwordx4 v[116:119], %[of], %[rs], %[so] offen sc1\n\t"
#define MX2(a,b) "v_max3_u32 %[t0], %[t0], v" #a ", v" #b "\n\t"
#define CHK_I0 "v_max_u32 %[t0], v112, v113\n\t" MX2(114,115)
#define CHK_IE "v_cmp_eq_u32 vcc, -1, %[t0]\n\t"
#define PCLOB "memory","vcc","scc"
#define UMASK 0x40074007u
DEV u32 upat(u32 tag,int k){ return ((((tag*0xea959c21u)>>(8*k))&255u)*0x04000801u)&UMASK; }
DEV u32 pkz(float a,float b){ return BC(u32,__builtin_amdgcn_cvt_pkrtz(a,b)); }
#define INV 0xffffffffu
DEV u32 nv(u32 v){ return umin_(v,0xfffeffffu); }
DEV u32x4 nv4(u32x4 v){ return u32x4{nv(v[0]),nv(v[1]),nv(v[2]),nv(v[3])}; }
#define OOR 0x40000000

#define CHSZ 36864
#define O_CH 512
#define O_CND 256
#define SLSZ 38080
#define O_R2 (O_CH+2*SLSZ)
#define O_X2 (O_R2+CHSZ)
#define O_TILE (O_X2+CHSZ)
#define XSTR 1168
#define TSTR2 1216
#define O_ONE (O_TILE+9728+16)
#define O_CAND (O_ONE+1168+48)
#define LDS_TOTAL (O_CAND+128)
#define C_G 0
#define C_U 8
#define C_L 4
#define C_D 5
#define C_X 12
#define C_P 13
#define C_H 14
#define C_I 17
#define C_E 18
#define C_IX 19
#define C_PR 21
#define C_RS 22
#define O_IDX 96
#define O_EXP 144
#define O_RMX 192
#define O_RV 128
#define O_MD 400
#define O_RC (O_CAND+64)

struct C2 { float r,i; };
DEV C2 cmul(C2 a,C2 b){ C2 c; c.r=__builtin_fmaf(a.r,b.r,-(a.i*b.i)); c.i=__builtin_fmaf(a.r,b.i,a.i*b.r); return c; }
DEV h8 ldA(const lch* p){ u32x2 a=*(const volatile LAS u32x2*)p, b=*(const volatile LAS u32x2*)(p+8); return BC(h8,(u32x4{a[0],a[1],b[0],b[1]})); }
DEV bf16x8 trfrag(const lch* p0,const lch* p1){
  u32x2 b0=BC(u32x2,__builtin_amdgcn_ds_read_tr16_b64_v4i16((LAS v4s*)p0)), b1=BC(u32x2,__builtin_amdgcn_ds_read_tr16_b64_v4i16((LAS v4s*)p1));
  return BC(bf16x8,(u32x4{b0[0],b0[1],b1[0],b1[1]})); }

#define OPOL SC1
struct FP { float gg[24], bs[24]; };
template<int Z0,int Z1> DEV void final_pre(FP& f,int w4,const u32x4* xq,const u32x4* dq,const f32x2* rc){
  #pragma unroll
  for(int z=Z0;z<Z1;z++) if(z<2||w4==0){ float ea=rc[z][0]*-1.4426950408889634f, eb=rc[z][1]*-1.4426950408889634f;
    #pragma unroll
    for(int e=0;e<4;e++){ u32 xw=xq[z][e], dw=dq[z>>1][e]; int i=8*z+2*e; float a=blo(xw), b=bhi(xw);
      float pa=__builtin_fmaf(a,rc[z][0],rc[z][1]), pb=__builtin_fmaf(b,rc[z][0],rc[z][1]);
      float ga=pa*rcpf_(1.f+__builtin_amdgcn_exp2f(__builtin_fmaf(a,ea,eb))), gb=pb*rcpf_(1.f+__builtin_amdgcn_exp2f(__builtin_fmaf(b,ea,eb)));
      f.gg[i]=ga; f.gg[i+1]=gb; f.bs[i]=__builtin_fmaf(pa*blo(dw),ga,a); f.bs[i+1]=__builtin_fmaf(pb*bhi(dw),gb,b);
      asm volatile("" : "+v"(f.gg[i]),"+v"(f.gg[i+1]),"+v"(f.bs[i]),"+v"(f.bs[i+1])); } }
}
#define FTS 544
#define YDA 0
#define YDB 0
#define YDC 0
#define YGS 1
#define NOCE 1
#define XE 0
#define PRS 0
DEV u32 pkmin(u32 a,u32 b){ u32 r; asm("v_pk_min_u16 %0, %1, %2" : "=v"(r) : "v"(a),"v"(b)); return r; }
DEV void final_post(lch* sm,const FP& f,int w4,int nch,u16* __restrict__ out,char* ws,int xr,u32 tk,u32 td){
  LANE(ln); int bid=BID, xg=bid&7, lb=bid>>3, cg=lb&3, T=8*xg+(lb>>2), ob[3];
  int yo=(128*cg+32*w4+(ln>>2)*2)*YST+(32*T+8*(ln&3))*2, yo3= ln<32? (512+32*(cg&1)+(ln&30))*YST+(32*T+16*(cg>>1)+8*(ln&1))*2 : OOR;
  lch* fb=sm+(NOCE? O_X2+2048:O_CH)+w4*4608; lch* wp=fb+(ln&3)*FTS+(ln&60); lch* wp3=fb+(4+(ln&1)+(ln>>5)*2)*FTS+(ln&30)*2; const lch* tp=fb+(ln>>5)*FTS+(ln&31)*16;
  u32x4 rs=mkrs(ws+W_Y,D_*YST); rsrc_t ro=RSRC(out,L_*D_*2); u32 tt=(u32)(32*T+(ln>>2))*0x10001u;
  asm volatile("" : "+v"(yo),"+v"(yo3),"+v"(wp),"+v"(wp3),"+v"(tp),"+v"(tt),"+s"(rs));
  #define RMASK { gwait<2>(sm,C_IX,1); u32x4 iq=LD(u32x4,sm+O_IDX); \
    _Pragma("unroll") for(int z=0;z<2;z++){ u32 t2=tt+z*0x100010u, m=pkmin(pkmin(iq[0]^t2,iq[1]^t2),pkmin(iq[2]^t2,iq[3]^t2)); ob[z]= umin_(m&0xffffu,m>>16)==0u? OOR : xr+z*(16*D_*2); } \
    ob[2]=((cg&2)? ob[1]:ob[0])+(512+32*(cg&1)-128*cg)*2; asm volatile("" : "+v"(ob[0]),"+v"(ob[1]),"+v"(ob[2])); }
  if(PRS){ RMASK
    if(PRS==1) BST128((u32x4{0,0,0,0}),ro,ln<4? ob[0]:OOR,0,SC1);
    if(PRS==2){ BST128((u32x4{0,0,0,0}),ro,ob[0],0,SC1); BST128((u32x4{0,0,0,0}),ro,ob[1],0,SC1); BST128((u32x4{0,0,0,0}),ro,w4==0? ob[2]:OOR,0,SC1); } }
  tgate2(tk,td);
  if(!NOCE) gwait<YGS>(sm,C_E,4*nch);
  if(lb<8) __builtin_amdgcn_s_sleep(YDA); else if(lb<14) __builtin_amdgcn_s_sleep(YDC); else __builtin_amdgcn_s_sleep(YDB);
  if(lb>=8&&(NOCE||lb<14)) gwait<YGS>(sm,C_H,4);
  PRIO(3);
  { u32 t0,t1,cnt;
    #define LD_Y "buffer_load_dwordx4 v[112:115], %[of], %[rs], 0 offen sc1\n\tbuffer_load_dwordx4 v[116:119], %[of], %[rs], %[s1] offen sc1\n\ts_cmp_lg_u32 %[w], 0\n\ts_cbranch_scc1 5f\n\t" \
      "buffer_load_dwordx4 v[120:123], %[o3], %[rs], 0 offen sc1\n\tbuffer_load_dwordx4 v[124:127], %[o3], %[rs], %[s1] offen sc1\n5:\n\t"
    #define YIN [of]"v"(yo),[rs]"s"(rs),[s1]"s"(YST),[o3]"v"(yo3),[w]"s"(w4)
    asm volatile(LD_Y :: YIN : "memory","scc");
    if(!PRS) RMASK
    #define YW(p,a,b,o) "v_perm_b32 %[t0], v" #b ", v" #a ", %[lo]\n\tv_perm_b32 %[t1], v" #b ", v" #a ", %[hi]\n\tds_write2_b32 %[" #p "], %[t0], %[t1] offset0:" #o " offset1:" #o "+16\n\t"
    asm volatile(POLLASM(LD_Y,CHK_I0 MX2(116,117) MX2(118,119) "s_cmp_lg_u32 %[w], 0\n\ts_cbranch_scc1 6f\n\t" MX2(120,121) MX2(122,123) MX2(124,125) MX2(126,127) "6:\n\t" CHK_IE,YSLP)
      "s_mov_b64 exec, 1\n\tds_add_u32 %[pr], %[one]\n\ts_mov_b64 exec, -1\n\t" YW(wp,112,116,0) YW(wp,113,117,32) YW(wp,114,118,64) YW(wp,115,119,96) "s_cmp_lg_u32 %[w], 0\n\ts_cbranch_scc1 7f\n\t" YW(w3,120,124,0) YW(w3,121,125,32) YW(w3,122,126,64) YW(w3,123,127,96) "7:"
      : [t0]"=&v"(t0),[t1]"=&v"(t1),[cnt]"=&s"(cnt) : YIN,[wp]"v"(wp),[w3]"v"(wp3),[lo]"s"(0x05040100u),[hi]"s"(0x07060302u),[pr]"v"(C_PR*4),[one]"v"(1) : PCLOB); }
  u32x4 r[3];
  #pragma unroll
  for(int z=0;z<3;z++) r[z]=LD(u32x4,tp+z*(2*FTS));
  #pragma unroll
  for(int z=0;z<3;z++) if(z<2||w4==0){ u32x4 ov;
    #pragma unroll
    for(int e=0;e<4;e++){ u32 rw=r[z][e]; int i=8*z+2*e; ov[e]=pk(__builtin_fmaf(blo(rw),f.gg[i],f.bs[i]),__builtin_fmaf(bhi(rw),f.gg[i+1],f.bs[i+1])); }
    BST128(ov,ro,ob[z],0,OPOL); SCHED(); }
}

#define TROW 528
#define O_TB 4640
#define O_DE 34752
#define O_DO 35968
#define O_R2K 37120
#define O_R3K 37632
#define O_DUM 37952
#define O_RED O_TB
#define MH(a,b,c) __builtin_amdgcn_mfma_f32_16x16x32_f16(a,b,c,0,0,0)
#define PIN(x) asm volatile("" : "+v"(x))
DEV h8 ld4w(const lch* p){ return BC(h8,(u32x4{LD(u32,p),LD(u32,p+4),LD(u32,p+8),LD(u32,p+12)})); }
DEV u32 __umul24_(u32 a,u32 b){ u32 r; asm("v_mul_u32_u24 %0, %1, %2" : "=v"(r) : "s"(b),"v"(a)); return r; }
DEV u32x4 band(u32 mask,int g,u32 val){ u32 by=__builtin_amdgcn_ubfe(mask,8*g,8), X=by|(by<<15), k=0x10001u; u32x4 r; asm volatile("" : "+s"(k),"+s"(val));
  #pragma unroll
  for(int d=0;d<4;d++) r[d]=__umul24_((X>>(2*d))&k,val);
  return r; }
DEV h8 zpack(f32x4 e,f32x4 o){ return BC(h8,(u32x4{pkh(e[0],o[0]),pkh(e[1],o[1]),pkh(e[2],o[2]),pkh(e[3],o[3])})); }
struct C3 { float r,i,n; };
DEV C3 c3(C2 a){ C3 c; c.r=a.r; c.i=a.i; PIN(c.i); c.n=-c.i; PIN(c.n); return c; }
DEV C2 cm(C2 a,C3 b){ C2 c; c.r=__builtin_fmaf(a.r,b.r,a.i*b.n); c.i=__builtin_fmaf(a.r,b.i,a.i*b.r); return c; }
DEV u32 ph(C2 a){ return pkh(a.r,a.i); }
#define W2(p,a,b) asm volatile("ds_write2_b32 %0, %1, %2 offset1:132" :: "v"(p),"v"(a),"v"(b) : "memory")
struct CP { const lch *x0,*x1,*bd; lch *p0,*p1,*p2,*wp; u32x4 Be,Bo,AE; int yo; };
template<int ROLE> DEV void lanes(CP& c,lch* cb,int ln){ int m=ln&15, g=ln>>4;
  const lch* ad=cb+((m&1)? O_DO+2*527 : O_DE+2*536)+2*(8*g-m);
  const lch* bd=cb+2*(256+16*m+8*g); lch* dum=cb+O_DUM;
  if(ROLE==0){ int l0=128*(g>>1)+8*m+4*(g&1); c.p0=cb+O_DE+(549-l0)*2; PIN(c.p0); c.p1=c.p0+6; PIN(c.p1); c.p2=c.p0+(O_DO-O_DE-18); c.x0=ad-640; c.bd=bd-64; c.x1=cb+O_R3K+2*(126-2*m+8*g)-192; PIN(c.x1);
    c.Be=band(0x1ffu<<(m+4),g,0x3c00u); PIN(c.Be); }
  if(ROLE==1){ lch* r=cb+O_R2K+(192-64*(g-2)-2*m)*2; c.p0= g<2? dum : r-66; c.p1= ln==0? cb+O_DE+(552-256)*2 : (ln<=32? dum : r); c.p2=cb+O_DE+(544-256-8*ln)*2; c.x0=ad-128-640; c.bd=bd-128-128; PIN(c.p0); PIN(c.p1); PIN(c.p2); }
  if(ROLE==2){ int i0=16*m+4*g; c.p0= m<8? cb+O_R3K+(125-i0)*2 : dum+2; c.p1= ln==0? cb+O_R2K+128 : (ln==8? cb+O_R3K : c.p0+6); c.x0=ad-320-640; c.bd=bd-320-128; PIN(c.p0); PIN(c.p1); }
  if(ROLE==3){ c.x0=cb+O_R2K+2*(188-4*m+8*g)-256; c.bd=cb+2*(248+8*m+8*g);
    c.Be=band(0x1fu<<(m+6),g,0x3c00u); c.Bo=band(0x7u<<(m+11),g,0x3c00u); PIN(c.Be); PIN(c.Bo); }
  PIN(c.x0); PIN(c.bd); }
template<int ROLE> DEV void lanes2(CP& c,lch* cb,int ln){ int m=ln&15, g=ln>>4;
  if(ROLE==0){ c.Bo=band(0x7fu<<(m+5),g,0x3c00u); c.AE=band(3u<<(m+12),g,0xffffu); PIN(c.Bo); PIN(c.AE); }
  c.wp=cb+O_RED+ln*16; c.yo=(g&1)*512+m*32+(g>>1)*16; PIN(c.wp); PIN(c.yo); }
template<int ROLE> DEV void chan2(lch* sm,lch* cb,const lch*& fa,const lch*& fb,int grp,int d,int one_,int emax,u64 tag,char* ws){
  CP c;
  const f32x4 Z4={0,0,0,0}; h8 A[6]; f32x4 acc[8];
  #define LD_U "buffer_load_dwordx4 v[112:115], %[of], %[rs], 0 offen sc1\n\tbuffer_load_dwordx4 v[116:119], %[of], %[rs], %[s1] offen sc1\n\tbuffer_load_dwordx4 v[120:123], %[of], %[rs], %[s2] offen sc1\n\tbuffer_load_dwordx4 v[124:127], %[of], %[rs], %[s3] offen sc1\n\t"
  #define UC(t,a,b,c) "v_bitop3_b32 " t ", v" #a ", v" #b ", %[" #c "] bitop3:0x7e\n\t"
  #define UD(t,a,c) "v_bitop3_b32 " t ", " t ", v" #a ", %[" #c "] bitop3:0xf6\n\t"
  #define CHK_U UC("%[t0]",112,116,c0) UC("%[t1]",113,117,c1) UD("%[t0]",120,c0) UD("%[t1]",121,c1) UD("%[t0]",124,c0) UD("%[t1]",125,c1) UD("%[t0]",114,c2) UD("%[t1]",115,c3) UD("%[t0]",118,c2) UD("%[t1]",119,c3) \
    UD("%[t0]",122,c2) UD("%[t1]",123,c3) UD("%[t0]",126,c2) UD("%[t1]",127,c3) "v_bitop3_b32 %[t0], %[t0], %[t1], %[mk] bitop3:0xa8\n\tv_cmp_ne_u32 vcc, 0, %[t0]\n\t"
  #define UX(a,b,c,d,o) "v_xor_b32 v" #a ", %[e0], v" #a "\n\tv_xor_b32 v" #b ", %[e1], v" #b "\n\tv_xor_b32 v" #c ", %[e2], v" #c "\n\tv_xor_b32 v" #d ", %[e3], v" #d "\n\tds_write_b128 %[up], v[" #a ":" #d "] offset:" #o "\n\t"
  #define UPOLL { u32 t0,t1,cnt; asm volatile(POLLASM(LD_U,CHK_U,PSLP) UX(112,113,114,115,544) UX(116,117,118,119,1056) UX(120,121,122,123,1568) UX(124,125,126,127,2080) \
      : [t0]"=&v"(t0),[t1]"=&v"(t1),[cnt]"=&s"(cnt) : [of]"v"(c.p0),[rs]"s"(rsU),[s1]"s"((32>>TL)*GST_),[s2]"s"((64>>TL)*GST_),[s3]"s"((96>>TL)*GST_),[c0]"s"(c0),[c1]"s"(c1),[c2]"s"(c2),[c3]"s"(c3_), \
        [e0]"s"(c0^0x40004u),[e1]"s"(c1^0x40004u),[e2]"s"(c2^0x40004u),[e3]"s"(c3_^0x40004u),[mk]"s"(UMASK),[up]"v"(c.p1) : PCLOB); garrive(sm,C_U+one_); }
  if(ROLE<3){
    h8 a[8], b[8];
    #pragma unroll
    for(int ks=0;ks<8;ks++){ a[ks]=LD(h8,fa+ks*64); b[ks]=LD(h8,fb+ks*64); }
    SCHED();
    LANE(ln); lanes<ROLE>(c,cb,ln); float fz= ln==32? 0.f : 4.76837158203125e-07f; if(ROLE==1) PIN(fz);
    SCHED();
    f32x4 k=Z4;
    #pragma unroll
    for(int ks=0;ks<8;ks++) k=MH(a[ks],b[ks],k);
    const float f=4.76837158203125e-07f;
    float k0=k[0]*f, k1=k[1]*f, k2=k[2]*f, k3=k[3]*f;
    u32 h32=pkh(k3,k2), h10=pkh(k1,k0);
    if(ROLE!=1){
      ST(u16,c.p0)=(u16)h32; ST(u32,c.p0+2)=pkh(k2,k1); ST(u16,c.p1)=(u16)(h10>>16);
      if(ROLE==0) ST(u32x2,c.p2)=u32x2{h32,h10};
    } else {
      float n0=dppf<0x130>(k[0])*fz, z1= fz==0.f? k0:k1, z2=k[2]*fz, z3=k[3]*fz; u32 h22=pkh(z2,z2);
      ST(u16,c.p0+64)=(u16)h10; ST(u16,c.p0+2)=(u16)(h32>>16); ST(u16,c.p0)=(u16)h32; ST(u16,c.p1)=(u16)(h10>>16);
      u32x4 vo={pkh(n0,z3),h22,pkh(z2,z1),pkh(k0,k0)}, ve={pkh(n0,n0),pkh(z3,z2),h22,pkh(z1,k0)};
      asm volatile("s_mov_b32 exec_hi, 1\n\tds_write_b128 %0, %1\n\tds_write_b128 %0, %2 offset:1200\n\ts_mov_b32 exec_hi, -1" :: "v"(c.p2),"v"(ve),"v"(vo) : "memory"); }
    gsync(sm,C_G+grp,7);
    u32 kv; A[0]=ld4w(c.x0+640); A[1]=ld4w(c.x0+128);
    if(ROLE==0){ A[2]=ld4w(c.x1+192); A[3]=ld4w(c.x1+128); A[4]=ld4w(c.x1+64); A[5]=ld4w(c.x1); kv=LD(u16,cb+O_R2K+128); }
    else { A[2]=ld4w(c.x0+576); A[3]=ld4w(c.x0+64); A[4]=ld4w(c.x0+512); A[5]=ld4w(c.x0); }
    SCHED(); lanes2<ROLE>(c,cb,ln); SCHED();
    if(ROLE==0){ kv|=kv<<16; c.AE=u32x4{c.AE[0]&kv,c.AE[1]&kv,c.AE[2]&kv,c.AE[3]&kv}; PIN(c.AE); }
    int ha=W_HT+d*8; u32 hd= one_? HD3 : (grp==0? HD0:HD1); if(ROLE==2) asm volatile("v_mov_b32 v113, %2" : "+v"(ha),"+s"(hd) : "s"((u32)tag));
    PRIO(2);
    gwait<1>(sm,C_U+one_,2-one_);
    if(ROLE==2) asm volatile("s_memrealtime s[98:99]\n\ts_waitcnt lgkmcnt(0)\n\ts_add_u32 s98, s98, %1\n\tv_mov_b32 v112, s98\n\ts_mov_b64 exec, 1\n\tglobal_store_dwordx2 %0, v[112:113], %2 sc1\n\ts_mov_b64 exec, -1" :: "v"(ha),"s"(hd),"s"(ws) : "memory","scc");
  } else {
    u32 tg=(u32)tag, c0=upat(tg,0), c1=upat(tg,1), c2=upat(tg,2), c3_=upat(tg,3); u32x4 rsU=mkrs(ws+W_U,256*UST);
    if(!one_) PRIO(2);
    LANE(ln); c.p0=(lch*)fa; c.p1=(lch*)fb;
    bool late= grp==0||BID>=112;
    if(!late&&!one_) __builtin_amdgcn_s_sleep(UDL);
    if(!late) asm volatile(LD_U :: [of]"v"(c.p0),[rs]"s"(rsU),[s1]"s"((32>>TL)*GST_),[s2]"s"((64>>TL)*GST_),[s3]"s"((96>>TL)*GST_) : "memory");
    asm volatile("s_mov_b64 exec, 1\n\tglobal_store_dwordx2 %0, %1, %2 sc1\n\ts_mov_b64 exec, -1" :: "v"(W_FL+d*8),"v"(tag),"s"(ws) : "memory");
    lanes<3>(c,cb,ln); lanes2<3>(c,cb,ln);
    #pragma unroll
    for(int j=2;j<8;j++){ acc[j]=Z4; PIN(acc[j]); }
    if(late){ if(UE==0||BID<112) gwait<2>(sm,C_G,UGATE); if(BID>=112){ gwait<1>(sm,UE? C_RS:C_D,4); if(UE>1) __builtin_amdgcn_s_sleep(UE); } asm volatile(LD_U :: [of]"v"(c.p0),[rs]"s"(rsU),[s1]"s"((32>>TL)*GST_),[s2]"s"((64>>TL)*GST_),[s3]"s"((96>>TL)*GST_) : "memory"); }
    if(!late) UPOLL
    gwait<1>(sm,C_G+grp,7);
    #pragma unroll
    for(int i=0;i<6;i++) A[i]=ldA(c.x0+320-64*i);
    if(late) UPOLL
    PRIO(2);
    gwait<1>(sm,C_U+one_,2-one_);
  }
  const lch* bd=c.bd; h8 Be=BC(h8,c.Be), Bo=BC(h8,c.Bo);
  #pragma unroll
  for(int j=0;j<(ROLE==3? 2:8);j++) acc[j]=Z4;
  #define RW(j) if((j)>=0&&((j)>>1)!=ROLE&&!(ROLE==3&&(j)<2)){ ST(f32x4,c.wp+((j)>>1)*6144+((ROLE-((j)>>1)-1)&3)*2048+((j)&1)*1024)=acc[(j)<0? 0:(j)]; SCHED(); }
  if(ROLE==1||ROLE==2){
    h8 B[8];
    #pragma unroll
    for(int q=0;q<8;q++) B[q]=LD(h8,bd+128+512*q);
    #pragma unroll
    for(int r=0;r<3;r++){
      #pragma unroll
      for(int dl=0;dl<8;dl++){ if(dl<7) acc[dl+1]=MH(A[2*r+1],B[dl],acc[dl+1]); SCHED(); acc[dl]=MH(A[2*r],B[dl],acc[dl]); SCHED();
        if(r<2){ B[dl]=LD(h8,bd+64-64*r+512*dl); SCHED(); } else RW(dl-2) }
      } }
  if(ROLE==0){
    h8 B[8], ue[4];
    #pragma unroll
    for(int kt=0;kt<4;kt++){ ue[kt]=LD(h8,bd+48+512*kt); B[kt]=LD(h8,bd+64+512*kt); }
    f32x4 ce[2],co[2]; h8 Z[2];
    ce[0]=MH(ue[0],Be,Z4); co[0]=MH(B[0],Bo,Z4); ce[1]=MH(ue[1],Be,Z4); co[1]=MH(B[1],Bo,Z4); Z[0]=zpack(ce[0],co[0]);
    #pragma unroll
    for(int kt=0;kt<4;kt++){
      SCHED();
      if(kt+2<4){ ce[kt&1]=MH(ue[kt+2],Be,Z4); co[kt&1]=MH(B[kt+2],Bo,Z4); }
      if(kt+1<4) Z[(kt+1)&1]=zpack(ce[(kt+1)&1],co[(kt+1)&1]);
      #pragma unroll
      for(int rho=0;rho+kt<4;rho++) acc[4+kt+rho]=MH(Z[kt&1],A[2+rho],acc[4+kt+rho]);
      SCHED(); B[4+kt]=LD(h8,bd+64+512*(4+kt)); if(kt==0) A[5]=BC(h8,c.AE); if(kt==1) A[4]=ld4w(c.x0-384); if(kt==2) A[3]=ld4w(c.x0+64); if(kt==3) A[2]=ld4w(c.x0+576); SCHED(); }
    #pragma unroll
    for(int dl=0;dl<8;dl++){ if(dl<4) acc[dl+4]=MH(A[5],B[dl],acc[dl+4]); SCHED(); if(dl<6) acc[dl+2]=MH(A[4],B[dl],acc[dl+2]); SCHED(); if(dl<7) acc[dl+1]=MH(A[1],B[dl],acc[dl+1]); SCHED(); acc[dl]=MH(A[0],B[dl],acc[dl]);
      SCHED(); B[dl]=LD(h8,bd+512*dl); SCHED(); }
    #pragma unroll
    for(int dl=0;dl<8;dl++){ if(dl<7) acc[dl+1]=MH(A[3],B[dl],acc[dl+1]); SCHED(); acc[dl]=MH(A[2],B[dl],acc[dl]); SCHED(); RW(dl-2) } }
  if(ROLE==3){
    h8 U[12];
    #pragma unroll
    for(int kt=0;kt<6;kt++) U[kt]=LD(h8,bd+256*kt);
    f32x4 ce[2],co[2]; h8 Z[2];
    ce[0]=MH(U[0],Be,Z4); co[0]=MH(U[0],Bo,Z4); ce[1]=MH(U[1],Be,Z4); co[1]=MH(U[1],Bo,Z4); Z[0]=zpack(ce[0],co[0]);
    #pragma unroll
    for(int kt=0;kt<12;kt++){
      SCHED();
      if(kt+2<12){ ce[kt&1]=MH(U[kt+2],Be,Z4); co[kt&1]=MH(U[kt+2],Bo,Z4); }
      if(kt+1<12) Z[(kt+1)&1]=zpack(ce[(kt+1)&1],co[(kt+1)&1]);
      #pragma unroll
      for(int rho=-1;rho<5;rho++){ int j=(kt+4+rho)>>1; if(((kt+rho)&1)==0&&j>=2&&j<8) acc[j]=MH(Z[kt&1],A[rho+1],acc[j]); }
      if(kt<6){ SCHED(); U[kt+6]=LD(h8,bd+256*(kt+6)); }
      if(kt>=3&&kt<=9&&(kt&1)){ SCHED(); RW((kt+1)/2) } }
  }
  PRIO(3);
  RW(6) RW(7)
  gsync(sm,C_G+grp,11);
  { const lch* rp=c.wp+ROLE*6144; f32x4 a[3], b[3], o0=acc[2*ROLE], o1=acc[2*ROLE+1];
    #pragma unroll
    for(int q=0;q<(ROLE==0? 2:3);q++){ a[q]=LD(f32x4,rp+q*2048); b[q]=LD(f32x4,rp+q*2048+1024); }
    SCHED();
    float sc=__builtin_amdgcn_ldexpf(1.f,emax-YSH), s0[4], s1[4];
    #pragma unroll
    for(int e=0;e<4;e++){ s0[e]=((o0[e]+a[0][e])+(ROLE==0? a[1][e] : a[1][e]+a[2][e]))*sc; s1[e]=((o1[e]+b[0][e])+(ROLE==0? b[1][e] : b[1][e]+b[2][e]))*sc; }
    auto r0=__builtin_amdgcn_permlane16_swap(nv(pk(s0[0],s0[1])),nv(pk(s1[0],s1[1])),false,false); auto r1=__builtin_amdgcn_permlane16_swap(nv(pk(s0[2],s0[3])),nv(pk(s1[2],s1[3])),false,false);
    BST128(u32x4{r0[0],r1[0],r0[1],r1[1]},RSRC(ws+W_Y+d*YST,4096),c.yo+ROLE*1024,0,SC1); }
  garrive(sm,C_E);

  asm volatile("s_endpgm" : "=v"(fa),"=v"(fb) :: "memory");
}
#define PFR0 520
DEV void pf1(int i,int xg,const u16* wg,const u16* wu){ LANE(ln); u32 q=(u32)(i>>1)*64u+ln, r=(q*7282u)>>16, c=q-9u*r;
  asm volatile("buffer_load_dword v121, %0, %1, 0 offen" :: "v"((PFR0+r)*(F_*2)+(9*xg+c)*128),"s"(mkrs((i&1)? wu:wg,D_*F_*2)) : "memory"); }
#define GETEP asm volatile("s_waitcnt lgkmcnt(0)\n\ts_mov_b64 %0, s[96:97]\n\ts_mov_b32 %1, s98" : "=s"(ep),"=s"(ldw));
DEV void chan(lch* sm,int grp,int w4,int d,int one_,char* ws,u32x4 q0,u32x4 q1,u32x4 q2,u32x4 q3,u32x4 q4){
  lch* cb=sm+O_CH+grp*SLSZ; int role=w4, emax;
  #define LP(v) ((lch*)(size_t)(u32)(v))
  const lch* fa=LP(q4[0]); const lch* fb=LP(q4[1]);
  u64 tag;
  { float a2,bp,br,bi,cr,ci;
    u32 fq=q3[2], fr=q3[3], one=q4[2]; float f64=BC(float,fq), f128=BC(float,fr);
    lch *t8[4]={LP(q0[0]),LP(q0[1]),LP(q0[2]),LP(q0[3])}, *tB[4]={LP(q1[0]),LP(q1[1]),LP(q1[2]),LP(q1[3])}, *t4[2]={LP(q2[0]),LP(q2[1])}, *t2[2]={LP(q2[2]),LP(q2[3])}, *t1=LP(q3[0]), *te=LP(q3[1]);
    if(grp!=0) PRIO(1);
    u64 ep; u32 ldw; GETEP tag=ep+1ull; if(IV==3) INVL
    float dt=rb(expf_((d&1)? bhi(ldw):blo(ldw))), ca=dt*1.4426950408889634f, cb_=dt*0.15915494309189535f;
    asm volatile("s_waitcnt vmcnt(" PW ")\n\tv_mul_f32 %2, %8, v114\n\tv_mul_f32 %3, %8, v115\n\tv_mul_f32 %0, %6, v112\n\tv_mul_f32 %1, %7, v113\n\tv_mov_b32 %4, v116\n\tv_mov_b32 %5, v117"
      : "=&v"(a2),"=&v"(bp),"=&v"(br),"=&v"(bi),"=&v"(cr),"=&v"(ci) : "v"(ca),"v"(cb_),"v"(dt) : "memory");
    if(IV==1) INVL
    C2 W; W.r=cr*br-ci*bi; W.i=cr*bi+ci*br;
    u32 mx=BC(u32,__builtin_fmaxf(__builtin_fabsf(W.r),__builtin_fabsf(W.i)));
    #define MXS(C) mx=umax_(mx,dppu<C>(mx));
    float e8=a2*8.f, eB=__builtin_fmaf(a2,f64,14.f), p8=bp*8.f, pB=bp*f64; SCHED();
    MXS(0xB1) p8=__builtin_amdgcn_fractf(p8); pB=__builtin_amdgcn_fractf(pB); e8=__builtin_amdgcn_exp2f(e8); SCHED();
    MXS(0x4E) eB=__builtin_amdgcn_exp2f(eB); float c8=__builtin_amdgcn_cosf(p8); SCHED();
    MXS(0x141) float s8=__builtin_amdgcn_sinf(p8), cB=__builtin_amdgcn_cosf(pB); SCHED();
    float sB=__builtin_amdgcn_sinf(pB); PIN(sB); MXS(0x140) float e1=__builtin_amdgcn_exp2f(a2); PIN(e1); SCHED();
    asm volatile("s_mov_b64 exec, 0x10001\n\tds_max_u32 %0, %1\n\tds_add_u32 %0, %2 offset:4\n\ts_mov_b64 exec, -1" :: "v"(te),"v"(mx),"v"(one) : "memory");
    C3 z8; z8.r=e8*c8; z8.n=e8*s8; z8.i=-z8.n; PIN(z8.i); C2 B; B.r=eB*cB; B.i=-(eB*sB);
    float p1=__builtin_amdgcn_fractf(bp), pE=__builtin_amdgcn_fractf(bp*256.f), pV=__builtin_amdgcn_fractf(bp*f128), eE=a2*256.f, eV=a2*f128, tr[8];
    #pragma unroll
    for(int k=0;k<4;k++){ SCHED(); u32 h0=ph(B); B=cm(B,z8);
      if(k==0){ tr[0]=__builtin_amdgcn_cosf(pV); tr[1]=__builtin_amdgcn_sinf(pV); } if(k==1){ tr[2]=__builtin_amdgcn_exp2f(eV); tr[3]=__builtin_amdgcn_cosf(p1); }
      if(k==2){ tr[4]=__builtin_amdgcn_sinf(p1); tr[5]=__builtin_amdgcn_exp2f(eE); } if(k==3){ tr[6]=__builtin_amdgcn_cosf(pE); tr[7]=__builtin_amdgcn_sinf(pE); }
      PIN(tr[2*k]); PIN(tr[2*k+1]);
      u32 h1=ph(B); if(k<3) B=cm(B,z8); W2(tB[k],h0,h1); }
    asm volatile("ds_read_b64 v[118:119], %0" :: "v"(te) : "memory");
    if(IV==2) INVL
    SCHED();
    C3 z1=c3(C2{e1*tr[3],e1*tr[4]}), E=c3(C2{tr[5]*tr[6],tr[5]*tr[7]}); C2 V[8]; V[0]=cmul(W,C2{tr[2]*tr[0],tr[2]*tr[1]});
    { u32 cnt,n; PIN(V[0].r); PIN(V[0].i); PIN(z1.r); PIN(E.r);
      asm volatile("s_mov_b32 %1, 0x20000\n1:\n\ts_waitcnt lgkmcnt(0)\n\tv_readfirstlane_b32 %2, v119\n\tv_readfirstlane_b32 %0, v118\n\ts_cmp_eq_u32 %2, 8\n\ts_cbranch_scc1 2f\n\tds_read_b64 v[118:119], %3\n\ts_sub_u32 %1, %1, 1\n\ts_cbranch_scc0 1b\n2:"
        : "=&s"(emax),"=&s"(cnt),"=&s"(n) : "v"(te) : "memory","scc");
      emax=RFL(emax); emax=(emax>>23)-126; }
    V[0].r=__builtin_amdgcn_ldexpf(V[0].r,14-emax); V[0].i=__builtin_amdgcn_ldexpf(V[0].i,14-emax);
    C2 Y=cm(V[0],E), Y4; u32 y0=ph(Y);
    V[1]=cm(V[0],z1); W2(t8[0],ph(V[0]),ph(V[1])); Y=cm(Y,E); u32 y1=ph(Y);
    V[2]=cm(V[1],z1); Y=cm(Y,E); u32 y2=ph(Y);
    V[3]=cm(V[2],z1); W2(t8[1],ph(V[2]),ph(V[3])); W2(t4[0],y0,ph(cm(V[2],E)));
    V[4]=cm(V[3],z1); Y=cm(Y,E); ST(u32,t1)=ph(Y);
    V[5]=cm(V[4],z1); W2(t8[2],ph(V[4]),ph(V[5])); Y4=cm(V[4],E); u32 y4=ph(Y4);
    V[6]=cm(V[5],z1); Y=cm(Y,E); ST(u32,t1+2*TROW)=ph(Y);
    V[7]=cm(V[6],z1); W2(t8[3],ph(V[6]),ph(V[7])); Y4=cm(Y4,E); W2(t2[0],y1,ph(Y4));
    W2(t4[1],y4,ph(cm(V[6],E))); Y=cm(Y,E); ST(u32,t1+4*TROW)=ph(Y);
    Y4=cm(Y4,E); W2(t2[1],y2,ph(Y4)); Y=cm(Y,E); ST(u32,t1+6*TROW)=ph(Y);
    Y=cm(Y,E); asm volatile("s_mov_b32 exec_hi, 0\n\tds_write_b32 %0, %1 offset:4224\n\ts_mov_b32 exec_hi, -1" :: "v"(t1),"v"(ph(Y)) : "memory");
  }
  if(one_) PRIO(3);
  DRAIN();
  gsync(sm,C_G+grp,4);
  if(role==0) chan2<0>(sm,cb,fa,fb,grp,d,one_,emax,tag,ws);
  if(role==1) chan2<1>(sm,cb,fa,fb,grp,d,one_,emax,tag,ws);
  if(role==2) chan2<2>(sm,cb,fa,fb,grp,d,one_,emax,tag,ws);
  chan2<3>(sm,cb,fa,fb,grp,d,one_,emax,tag,ws);
}

struct XR { int so,x0,x2; u32x4 rx,q[4]; };
DEV void xreq(XR& X,char* ws){ asm volatile("global_load_dwordx2 v[108:109], %[so], %[ws] sc1\n\tglobal_load_dwordx2 v[110:111], %[so], %[ws] offset:128 sc1\n\tbuffer_load_dwordx4 %[a], %[x0], %[rx], 0 offen\n\tbuffer_load_dwordx4 %[b], %[x0], %[rx], %[s1] offen\n\tbuffer_load_dwordx4 %[c], %[x2], %[rx], 0 offen"
  : [a]"=&{v[96:99]}"(X.q[0]),[b]"=&{v[100:103]}"(X.q[1]),[c]"=&{v[104:107]}"(X.q[2]),"=&{v[108:111]}"(X.q[3]) : [so]"v"(X.so),[ws]"s"(ws),[x0]"v"(X.x0),[x2]"v"(X.x2),[rx]"s"(X.rx),[s1]"s"(16*D_*2) : "memory"); }
DEV u32 med3u(u32 a,u32 b,u32 c){ u32 r; asm("v_med3_u32 %0, %1, %2, %3" : "=v"(r) : "v"(a),"v"(b),"v"(c)); return r; }
DEV u32 max3u(u32 a,u32 b,u32 c){ return umax_(umax_(a,b),c); }
#define GTC(r,s) "v_cmp_gt_u32 %[" #s "], v" #r ", %[c]\n\t"
#define GTA(s) "v_addc_co_u32 %[rk], vcc, 0, %[rk], %[" #s "]\n\t"
#define GT4(a,b,c,d) GTC(a,s0) GTC(b,s1) GTC(c,s2) GTC(d,s3) GTA(s0) GTA(s1) GTA(s2) GTA(s3)
#define GTS [s0]"=&s"(q0),[s1]"=&s"(q1),[s2]"=&s"(q2),[s3]"=&s"(q3)
DEV void top8(lch* sm,int w4,int ln,u32 M,char* ws,u32& rank_,u32& key_,u32 tk,XR& X){
  u32 k[8], rc[8], hm=0xffff0000u; u64 em=0x0001000100010001ull,q0,q1,q2,q3;
  u32x4 rs=mkrs(ws+W_RG+(BID&(NRG-1))*8192,8192); int ro=w4*1024+ln*16;
  #pragma unroll
  for(int e=0;e<8;e++){ rc[e]=(u32)(0xfff-(w4*256+ln*4+(e&3)+(e>>2)*1024)); PIN(rc[e]); }
  lch *aW=sm+O_RMX+(w4*4+(ln>>4))*4, *aC=sm+O_RMX+(ln>>2)*4, *aO=sm+O_RMX+(ln&3)*16, *dC=sm+O_CND+(ln>>1)*4, *dO=sm+O_CND+(ln&1)*64; PIN(aW); PIN(aC); PIN(aO); PIN(dC); PIN(dO); PIN(ro); PIN(ln);
  asm volatile("" : "+s"(hm),"+s"(em),"+s"(rs));
  gwait<2>(sm,C_G,RGATE); __builtin_amdgcn_s_sleep(RDL);
  { u32 cnt;
    #define KL(e,r) "v_lshl_or_b32 %[k" #e "], v" #r ", 16, %[k" #e "]\n\t"
    #define KH(e,r) "v_and_or_b32 %[k" #e "], v" #r ", %[hm], %[k" #e "]\n\t"
    #define CHK_R "v_bitop3_b32 %[t0], v113, v115, %[M] bitop3:0x7e\n\tv_bitop3_b32 %[t1], v117, v119, %[M] bitop3:0x7e\n\tv_or_b32 %[t0], %[t0], %[t1]\n\tv_cmp_ne_u32 vcc, 0, %[t0]\n\t"
    u32 t0,t1;
    asm volatile(LD_DUP2
      "s_sleep " STR(R2D) "\n\tbuffer_load_dwordx4 v[120:123], %[of], %[rs], 0 offen sc1\n\tbuffer_load_dwordx4 v[124:127], %[of], %[rs], %[so] offen sc1\n\ts_mov_b32 %[cnt], 0x20000\n\ts_waitcnt vmcnt(2)\n\t" CHK_R "s_cbranch_vccz 9f\n\t"
      "s_waitcnt vmcnt(0)\n\tv_mov_b64 v[112:113], v[120:121]\n\tv_mov_b64 v[114:115], v[122:123]\n\tv_mov_b64 v[116:117], v[124:125]\n\tv_mov_b64 v[118:119], v[126:127]\n\t"
      POLLASM(LD_DUP2,CHK_R,PSLP)
      KL(0,112) KH(1,112) KL(2,114) KH(3,114) KL(4,116) KH(5,116) KL(6,118) KH(7,118)
      : [k0]"+v"(rc[0]),[k1]"+v"(rc[1]),[k2]"+v"(rc[2]),[k3]"+v"(rc[3]),[k4]"+v"(rc[4]),[k5]"+v"(rc[5]),[k6]"+v"(rc[6]),[k7]"+v"(rc[7]),[cnt]"=&s"(cnt),[t0]"=&v"(t0),[t1]"=&v"(t1) : [of]"v"(ro),[rs]"s"(rs),[so]"s"(4096),[M]"s"(M),[hm]"s"(hm) : PCLOB); }
  if(UE) garrive(sm,C_RS);
  #pragma unroll
  for(int e=0;e<8;e++) k[e]=rc[e];
  u32 h=max3u(k[0],k[1],k[2]), m1=max3u(max3u(k[3],k[4],k[5]),max3u(k[6],k[7],h),h), mx=m1, l=med3u(k[0],k[1],k[2]), t=umin_(umin_(k[0],k[1]),k[2]);
  SCHED();
  #define INS(e) t=med3u(t,l,k[e]); l=med3u(h,l,k[e]); if(e<7) h=umax_(h,k[e]); PIN(t); PIN(l); SCHED();
  mx=umax_(mx,dppu<0xB1>(mx)); INS(3) mx=umax_(mx,dppu<0x4E>(mx)); INS(4) mx=umax_(mx,dppu<0x141>(mx)); INS(5) mx=umax_(mx,dppu<0x140>(mx));
  asm volatile("s_mov_b64 exec, %2\n\tds_write_b32 %0, %1\n\ts_mov_b64 exec, -1\n\tds_read_b32 v116, %3\n\tds_read_b128 v[112:115], %4" :: "v"(aW),"v"(mx),"s"(em),"v"(aC),"v"(aO) : "memory");
  if(XB==2) xreq(X,ws);
  INS(6) INS(7)
  u32 m2=l, m3=t, T;
  { u32 c,rk,cnt;
    asm volatile("s_mov_b32 %[cnt], 0x20000\n1:\n\ts_waitcnt lgkmcnt(0)\n\tv_cmp_eq_u32 vcc, 0, v116\n\ts_cbranch_vccz 2f\n\tds_read_b32 v116, %[aC]\n\tds_read_b128 v[112:115], %[aO]\n\ts_sub_u32 %[cnt], %[cnt], 1\n\ts_cbranch_scc0 1b\n2:\n\t"
      "v_mov_b32 %[c], v116\n\tv_mov_b32 %[rk], 0\n\t" GT4(112,113,114,115) "s_nop 1\n\tv_add_u32_dpp %[rk], %[rk], %[rk] quad_perm:[1,0,3,2] row_mask:0xf bank_mask:0xf\n\ts_nop 1\n\tv_add_u32_dpp %[rk], %[rk], %[rk] quad_perm:[2,3,0,1] row_mask:0xf bank_mask:0xf\n\t"
      "v_cmp_eq_u32 vcc, 7, %[rk]\n\ts_ff1_i32_b64 %[cnt], vcc\n\ts_nop 0\n\tv_readlane_b32 %[T], %[c], %[cnt]\n\ts_nop 1"
      : [c]"=&v"(c),[rk]"=&v"(rk),[cnt]"=&s"(cnt),[T]"=&s"(T),GTS : [aC]"v"(aC),[aO]"v"(aO) : PCLOB); T=RFL(T); }
  { u64 b1=BALLOT(m1>=T), b2=BALLOT(m2>=T), b3=BALLOT(m3>=T); int n1=__builtin_popcountll(b1), n2=n1+__builtin_popcountll(b2);
    lch* cw=sm+O_CND+w4*32;
    if(__builtin_expect(b3==0ull&&n2<=8,1)){
      int p1=(int)__builtin_amdgcn_mbcnt_hi((u32)(b1>>32),__builtin_amdgcn_mbcnt_lo((u32)b1,0u)), pf=n2+ln-p1; bool c1=m1>=T;
      if(__builtin_expect(b2!=0ull,0)){ int p2=(int)__builtin_amdgcn_mbcnt_hi((u32)(b2>>32),__builtin_amdgcn_mbcnt_lo((u32)b2,(u32)n1)); if(m2>=T) ST(u32,cw+p2*4)=m2; }
      int ps= c1? p1:pf; ps= ps<8? ps:32-8*w4; ST(u32,cw+ps*4)= c1? m1:1u;
    } else {
      u32 mine=0;
      _Pragma("unroll 1") for(int c=0;c<8;c++){ u32 lm=umax_(umax_(umax_(k[0],k[1]),umax_(k[2],k[3])),umax_(umax_(k[4],k[5]),umax_(k[6],k[7]))); u32 wm=wmax(lm); if(ln==c) mine=wm;
        #pragma unroll
        for(int e=0;e<8;e++) k[e]=(k[e]==wm)?0u:k[e]; }
      if(ln<8) ST(u32,cw+ln*4)=mine; } }
  { u32 c,rk,cnt;
    asm volatile("s_mov_b32 %[cnt], 0x20000\n\tv_mov_b32 %[rk], 0\n\ts_waitcnt vmcnt(" XBW ")\n1:\n\tds_read_b32 %[c], %[dC]\n\tds_read_b128 v[112:115], %[dO]\n\tds_read_b128 v[116:119], %[dO] offset:16\n\tds_read_b128 v[120:123], %[dO] offset:32\n\tds_read_b128 v[124:127], %[dO] offset:48\n\t"
      "s_waitcnt lgkmcnt(4)\n\tv_cmp_eq_u32 vcc, 0, %[c]\n\ts_cbranch_vccz 2f\n\ts_sub_u32 %[cnt], %[cnt], 1\n\ts_cbranch_scc0 1b\n2:\n\t"
      "s_waitcnt lgkmcnt(3)\n\t" GT4(112,113,114,115) "s_waitcnt lgkmcnt(2)\n\t" GT4(116,117,118,119) "s_waitcnt lgkmcnt(1)\n\t" GT4(120,121,122,123) "s_waitcnt lgkmcnt(0)\n\t" GT4(124,125,126,127)
      "s_nop 1\n\tv_add_u32_dpp %[rk], %[rk], %[rk] quad_perm:[1,0,3,2] row_mask:0xf bank_mask:0xf"
      : [c]"=&v"(c),[rk]"=&v"(rk),[cnt]"=&s"(cnt),GTS : [dC]"v"(dC),[dO]"v"(dO) : PCLOB);
    rank_=rk; key_=c; }
}

#define FILL_(n,WAIT) "s_mov_b32 m0, %[ld]\n\ts_mov_b32 %[c], " STR(n) "-1\n3:\n\t" WAIT "buffer_load_dwordx4 %[vo], %[rs], %[so] offen " WPOL " lds\n\ts_add_u32 m0, m0, 0x1000\n\ts_add_u32 %[so], %[so], %[st]\n\t" \
  "s_sub_u32 %[c], %[c], 1\n\ts_cbranch_scc0 3b\n\ts_mov_b32 %[ld], m0\n\t"
#define FILL(n,WAIT) do{ u32 c_; asm volatile(FILL_(n,WAIT) : [c]"=&s"(c_),[so]"+s"(wso),[ld]"+s"(wld) : [vo]"v"(wvo),[rs]"s"(wrs),[st]"s"(wst) : "memory","scc","m0"); }while(0)
#define FILLD(n,WN,WW_) do{ u32 c_,t_,s_; asm volatile(FILL_(n,"ds_read_b32 %[t], %[ad]\n\ts_waitcnt lgkmcnt(0)\n\tv_readfirstlane_b32 %[s], %[t]\n\ts_cmp_ge_u32 %[s], 4\n\ts_cbranch_scc1 4f\n\t" VW(WN) "4:\n\t" VW(WW_)) \
  : [c]"=&s"(c_),[so]"+s"(wso),[ld]"+s"(wld),[t]"=&v"(t_),[s]"=&s"(s_) : [vo]"v"(wvo),[rs]"s"(wrs),[st]"s"(wst),[ad]"v"(C_D*4) : "memory","scc","m0"); wso=RFL(wso); wld=RFL(wld); }while(0)
#define VW(n) "s_waitcnt vmcnt(" STR(n) ")\n\t"

__global__ NOPK __attribute__((amdgpu_num_vgpr(56),amdgpu_num_sgpr(102))) __launch_bounds__(1024,4) void kA(const u16* __restrict__ x,char* ws,const u16* __restrict__ wr,const u16* __restrict__ Dsk,const u16* __restrict__ wg,const u16* __restrict__ wu,
                                              const u16* __restrict__ wd,u16* __restrict__ out,KP kp){
  __shared__ __attribute__((aligned(16))) char smem[LDS_TOTAL];
  lch* sm=(lch*)smem;
  u32x4 xa,xb_,wa,wb,dq[3]; u32 xc,wc;
  asm volatile(
    "s_load_dwordx16 s[64:79], %[ka], 0x0\n\ts_load_dwordx16 s[80:95], %[ka], 0x40\n\t"
    "v_readfirstlane_b32 s60, %[t]\n\ts_and_b32 s61, %[wg], 7\n\ts_lshr_b32 s62, %[wg], 3\n\tv_and_b32 v118, 31, %[t]\n\ts_bfe_u32 s63, s60, 0x20006\n\ts_cmpk_ge_u32 s60, 0x100\n\ts_cbranch_scc1 1f\n\t"
    "s_setprio 3\n\ts_lshl_b32 s56, s61, 5\n\ts_add_u32 s56, s56, s62\n\ts_mulk_i32 s56, 0x2400\n\tv_lshrrev_b32 v119, 5, %[t]\n\tv_mul_u32_u24 v119, 0x480, v119\n\tv_add_u32 v119, s56, v119\n\t"
    "v_lshl_add_u32 v116, v118, 4, v119\n\tv_lshl_add_u32 v117, v118, 2, v119\n\tv_lshlrev_b32 v114, 4, v118\n\tv_lshlrev_b32 v115, 2, v118\n\t"
    "s_and_b32 s57, s62, 3\n\ts_lshl2_add_u32 s57, s57, s63\n\ts_lshl_b32 s57, s57, 6\n\ts_and_b32 s58, s62, 1\n\ts_lshl_b32 s58, s58, 6\n\tv_and_b32 v112, 3, %[t]\n\tv_lshl_add_u32 v113, v112, 4, s57\n\tv_lshl_add_u32 v112, v112, 4, s58\n\t"
    "v_lshlrev_b32 v119, 1, %[t]\n\tv_mov_b32 v118, 0\n\tds_write_b16 v119, v118\n\t"
    "s_waitcnt lgkmcnt(0)\n\t"
    "s_memrealtime s[98:99]\n\ts_load_dwordx2 s[96:97], s[66:67], 0x0\n\t"
    "global_load_dwordx4 %[wa], v114, s[68:69]\n\tglobal_load_dwordx4 %[wb], v114, s[68:69] offset:512\n\tglobal_load_dword %[wc], v115, s[68:69] offset:1024\n\t"
    "global_load_dwordx4 %[xa], v116, s[64:65]\n\tglobal_load_dwordx4 %[xb], v116, s[64:65] offset:512\n\tglobal_load_dword %[xc], v117, s[64:65] offset:1024\n\t"
    "global_load_dwordx4 %[d0], v113, s[70:71]\n\tglobal_load_dwordx4 %[d1], v112, s[70:71] offset:1024\n\ts_barrier\n\t"
    "s_branch 8f\n"
    "1:\n\ts_cmp_ge_u32 s62, " STR(CPF) "\n\ts_cbranch_scc1 6f\n\ts_getpc_b64 s[56:57]\n.Lkpc:\n\ts_and_b32 s58, s62, 3\n\ts_lshl_b32 s58, s58, 13\n\ts_sub_u32 s58, s58, 0x1000\n\tv_lshl_add_u32 v119, %[t], 4, s58\n\tv_min_u32 v119, .Lkend-.Lkpc-16, v119\n\t"
    "global_load_dword v120, v119, s[56:57]\n\t.subsection 1\n.Lkend:\n\t.subsection 0\n6:\n\t"
    "v_mov_b32 v124, -1\n\tv_mov_b32 v125, -1\n\tv_mov_b32 v126, -1\n\tv_mov_b32 v127, -1\n\tv_lshlrev_b32 v119, 4, %[t]\n\tv_and_b32 v119, 0xff0, v119\n\t"
    "s_lshr_b32 s59, s60, 8\n\ts_cmp_lt_u32 s62, 8\n\ts_cselect_b32 s58, 2, 1\n\ts_cbranch_scc1 2f\n\ts_cmp_eq_u32 s59, 3\n\ts_cbranch_scc1 4f\n"
    "2:\n\ts_sub_u32 s57, s59, 2\n\ts_cmp_eq_u32 s59, 1\n\ts_cselect_b32 s58, s58, s57\n\ts_cmp_lt_u32 s62, 8\n\ts_cselect_b32 s56, 0, 8\n\ts_lshl1_add_u32 s56, s62, s56\n\ts_mulk_i32 s61, 72\n\ts_add_u32 s51, s56, s61\n\ts_sub_u32 s56, 2, s59\n\ts_sub_u32 s57, 16, s62\n\ts_cmp_eq_u32 s59, 3\n\ts_cselect_b32 s50, s57, s56\n\ts_add_u32 s56, s50, s51\n\t"
    "v_mov_b32 v112, 0\n\tv_mov_b32 v113, 0\n\tv_mov_b32 v114, 0\n\tv_mov_b32 v115, 0\n\tv_mov_b32 v116, 0\n\tv_mov_b32 v117, 0\n\ts_setprio 2\n\t"
    "s_lshl_b32 s57, s56, 2\n\ts_add_u32 s57, s57, s63\n\ts_lshl_b32 s57, s57, 6\n\tv_lshl_add_u32 v22, v118, 1, s57\n\ts_mul_i32 s57, s56, %[yst]\n\ts_add_u32 s57, s57, %[wy]\n\tv_add_u32 v23, s57, v119\n\ts_lshl_b32 s56, s56, 1\n\ts_and_b32 s56, s56, -4\n\t"
    "s_waitcnt lgkmcnt(0)\n\t"
    "global_load_short_d16_hi v112, v22, s[80:81] " PPOL "\n\tglobal_load_short_d16_hi v113, v22, s[82:83] " PPOL "\n\tglobal_load_short_d16_hi v114, v22, s[84:85] " PPOL "\n\t"
    "global_load_short_d16_hi v115, v22, s[86:87] " PPOL "\n\tglobal_load_short_d16_hi v116, v22, s[88:89] " PPOL "\n\tglobal_load_short_d16_hi v117, v22, s[90:91] " PPOL "\n\t"
    "s_load_dwordx2 s[96:97], s[66:67], 0x0\n\ts_load_dword s98, s[92:93], s56\n\t"
    "s_barrier\n\t" INVST
    "s_mul_i32 s52, s58, %[sl]\n\ts_add_u32 s52, s52, %[tb]\n\ts_lshl_b32 s53, s63, 7\n\ts_add_u32 s53, s53, s52\n\tv_lshl_add_u32 v30, v118, 2, s53\n\tv_bfe_u32 v31, %[t], 5, 1\n\ts_movk_i32 s54, 528\n\tv_mul_u32_u24 v29, s54, v31\n\t"
    "v_lshl_add_u32 v2, v29, 3, v30\n\ts_movk_i32 s53, 1056\n\tv_add_u32 v3, s53, v2\n\tv_add_u32 v4, s53, v3\n\tv_add_u32 v5, s53, v4\n\tv_add_u32 v6, 41*528, v2\n\tv_add_u32 v7, s53, v6\n\tv_add_u32 v8, s53, v7\n\tv_add_u32 v9, s53, v8\n\t"
    "v_lshl_add_u32 v10, v29, 2, v30\n\tv_add_u32 v10, 16*528, v10\n\tv_add_u32 v11, s53, v10\n\tv_lshl_add_u32 v12, v29, 1, v30\n\tv_add_u32 v12, 24*528, v12\n\tv_add_u32 v13, 2112, v12\n\tv_add_u32 v14, v29, v30\n\tv_add_u32 v14, 32*528, v14\n\t"
    "s_lshl_b32 s53, s58, 4\n\ts_add_u32 s53, s53, %[ex]\n\tv_mov_b32 v15, s53\n\tv_cvt_f32_ubyte0 v31, v31\n\tv_mul_f32 v16, 0x42800000, v31\n\tv_add_f32 v17, v16, v16\n\tv_mov_b32 v20, 1\n\t"
    "v_and_b32 v29, 15, %[t]\n\tv_and_b32 v30, 48, %[t]\n\tv_mad_u32_u24 v30, v29, s54, v30\n\ts_lshl_b32 s55, s59, 1\n\ts_lshr_b32 s55, 0x48, s55\n\ts_add_u32 s55, s55, s63\n\ts_and_b32 s55, s55, 3\n\ts_mul_i32 s53, s55, 16*528\n\ts_movk_i32 s57, 41*528\n\ts_cmp_eq_u32 s55, 2\n\ts_cselect_b32 s53, s57, s53\n\ts_cselect_b32 s57, 32*528, s57\n\t"
    "s_add_u32 s53, s53, s52\n\ts_add_u32 s57, s57, s52\n\tv_add_u32 v18, s53, v30\n\tv_add_u32 v19, s57, v30\n\t"
    "v_min_u32 v29, 237*16, v119\n\tv_subrev_u32 v30, 204*16, v29\n\tv_add_u32 v29, %[de], v29\n\tv_min_u32 v29, v29, v30\n\ts_sub_u32 s53, s52, 4640\n\tv_add_u32 v29, s53, v29\n\tv_mov_b32 v24, 0\n\tv_mov_b32 v25, 0\n\tv_mov_b32 v26, 0\n\tv_mov_b32 v27, 0\n\tds_write_b128 v29, v[24:27]\n\t"
    "s_cmp_lg_u32 s55, 3\n\ts_cbranch_scc1 9f\n\t"
    "v_bfe_u32 v18, %[t], 1, 5\n\tv_and_b32 v19, 1, %[t]\n\ts_cmp_lt_u32 s62, 8\n\ts_cselect_b32 s53, 2, 1\n\ts_mul_i32 s53, s53, %[sl]\n\tv_mul_u32_u24 v21, s53, v19\n\ts_sub_u32 s55, 2, s59\n\ts_lshl_b32 s55, s55, 7\n\tv_add_u32 v18, s55, v18\n\ts_cmp_lg_u32 s59, 3\n\ts_cbranch_scc1 7f\n\t"
    "v_bfe_u32 v18, %[t], 5, 1\n\tv_lshl_add_u32 v18, v18, 7, v118\n\tv_mov_b32 v19, s50\n\tv_mov_b32 v21, %[sl]\n"
    "7:\n\tv_add_u32 v19, s51, v19\n\tv_lshl_add_u32 v21, v18, 4, v21\n\tv_lshrrev_b32 v24, 3-" STR(TL) ", v19\n\tv_and_b32 v19, 7>>" STR(TL) ", v19\n\tv_lshlrev_b32 v19, 4, v19\n\tv_lshl_add_u32 v19, v24, 7, v19\n\t"
    "v_and_b32 v24, (1<<" STR(TL) ")-1, v18\n\tv_lshl_add_u32 v19, v24, 7-" STR(TL) ", v19\n\tv_lshrrev_b32 v18, " STR(TL) ", v18\n\tv_mul_u32_u24 v18, %[ust], v18\n\tv_add_u32 v18, v18, v19\n\tv_add_u32 v19, 512, v21\n\t"
    "s_branch 9f\n"
    "4:\n\t"
    "v_bfe_u32 v118, %[t], 3, 5\n\tv_lshrrev_b32 v117, 1, v118\n\tv_and_b32 v117, 4, v117\n\tv_and_b32 v116, 2, v118\n\tv_and_b32 v115, 7, %[t]\n\tv_or_b32 v116, v116, v117\n\tv_xor_b32 v115, v115, v116\n\tv_lshlrev_b32 v115, 4, v115\n\t"
    "s_mul_i32 s54, s61, 9\n\ts_sub_u32 s55, s62, 14\n\ts_lshr_b32 s55, s55, 1\n\ts_add_u32 s54, s54, s55\n\ts_lshl_b32 s54, s54, 7\n\t"
    "s_mul_i32 s52, s61, 0xa2000\n\ts_mul_i32 s53, s62, 0x180\n\ts_add_u32 s52, s52, s53\n\ts_cmp_ge_u32 s62, 11\n\ts_cselect_b32 s53, 0x51000-0x480, 0\n\ts_add_u32 s52, s52, s53\n\ts_sub_u32 s52, s52, 0xc00\n\t"
    "s_movk_i32 s55, 0x480\n\ts_cmp_ge_u32 s62, 14\n\ts_cselect_b32 s54, s54, s52\n\ts_cselect_b32 s55, 0x2400, s55\n\tv_mad_u32_u24 v118, v118, s55, v115\n\ts_lshl_b32 s55, s55, 5\n\t"
    "s_lshl_b32 s52, s63, 10\n\ts_add_u32 m0, s52, %[r2]\n\ts_mov_b32 s58, 0x510000\n\ts_mov_b32 s59, 0x20000\n\ts_cmp_ge_u32 s62, 14\n\ts_cselect_b32 s53, " STR(PRE1) "-1, " STR(PRE2) "-1\n\ts_setprio 3\n\t"
    "s_waitcnt lgkmcnt(0)\n\ts_memrealtime s[98:99]\n\t"
    "s_bitcmp1_b32 %[wg], 3\n\ts_cselect_b64 s[56:57], s[74:75], s[72:73]\n\ts_cmp_ge_u32 s62, 14\n\ts_cselect_b64 s[56:57], s[56:57], s[76:77]\n\ts_cselect_b32 s52, 0x180, -1\n\ts_and_b32 s57, s57, 0xffff\n\ts_add_u32 s60, s56, s54\n\ts_and_b32 s60, s60, 0x380\n\ts_cmp_eq_u32 s60, s52\n\ts_cselect_b32 s53, " STR(PRE3) "-1, s53\n\t"
    "s_load_dwordx2 s[96:97], s[66:67], 0x0\n\ts_barrier\n\t"
    "\n3:\n\t" "buffer_load_dwordx4 v118, s[56:59], s54 offen " WPOL " lds\n\ts_add_u32 m0, m0, 0x1000\n\ts_add_u32 s54, s54, s55\n\ts_sub_u32 s53, s53, 1\n\ts_cbranch_scc0 3b\n"
    "9:\n8:"
    : [xa]"=&{v[2:5]}"(xa),[xb]"=&{v[6:9]}"(xb_),[xc]"=&{v30}"(xc),[wa]"=&{v[10:13]}"(wa),[wb]"=&{v[14:17]}"(wb),[wc]"=&{v31}"(wc),[d0]"=&{v[18:21]}"(dq[0]),[d1]"=&{v[22:25]}"(dq[1]),[d2]"=&{v[26:29]}"(dq[2])
    : [ka]"s"(__builtin_amdgcn_kernarg_segment_ptr()),[wg]"s"(BID),[t]"v"(TID),[yst]"i"(YST),[wy]"i"(W_Y),[r2]"i"(O_R2),[sl]"i"(SLSZ),[tb]"i"(O_CH+O_TB),[ex]"i"(O_EXP),[de]"i"(O_DE),[ust]"i"(GST_)
    : "memory","scc","m0","v127","s50","s51","s52","s53","s54","s55","s56","s57","s58","s59","s60","s61","s62","s63","s64","s65","s66","s67","s68","s69","s70","s71","s72","s73","s74","s75","s76","s77","s78","s79",
      "s80","s81","s82","s83","s84","s85","s86","s87","s88","s89","s90","s91","s92","s93","s94","s95","s99");
  int wv=RFL(TID>>6), bid=BID, q4=wv>>2, w4=wv&3;
  int xg=bid&7, lb=bid>>3, tb=32*xg+lb, nch=2+(lb<8);
  u64 ep; u32 ldw;
  {
  }
  #define KILLIN "=v"(xa),"=v"(xb_),"=v"(xc),"=v"(wa),"=v"(wb),"=v"(wc),"=v"(dq[0]),"=v"(dq[1]),"=v"(dq[2])
  if(__builtin_expect(q4==3&&lb>=8,0)){
    int fu=18*xg+lb-14, kq=2*xg+(lb>=11), cgp=lb-8-3*(lb>=11);
  u32 wst= lb>=14? F_*64:D_*64;
  u32 wso=(lb>=14? (fu>>1)*128 : kq*(288*D_*2)+384*cgp)+(lb>=14? PRE1:PRE2)*wst, wld=(u32)(size_t)sm+O_R2+w4*1024+(lb>=14? PRE1:PRE2)*4096; u32x4 wrs=mkrs(lb>=14? ((fu&1)? wu:wg):wd,D_*F_*2);
  GETEP
  #define FLANE(RS) LANE(ln); int g=ln>>4, li=ln&15, qq=li>>2, pq=li&3; u32 wvo=(u32)(8*w4+(ln>>3))*(RS)+(u32)((ln&7)^(((ln>>3)&2)|((w4&1)<<2)))*16u; \
    const lch* tp=sm+O_R2+(8*g+qq)*128+((w4^((qq>>1)|((g&1)<<1)))*32)+pq*8;
  #define WFR(img,ks) trfrag(tp+(img)*CHSZ+(ks)*4096,tp+(img)*CHSZ+(ks)*4096+512)
  u64 tag=ep+1ull; const u32x4 I4={INV,INV,INV,INV};
  if(lb>=14){
    FLANE(F_*2)
    int mt=fu&1;
    if(w4==0) ST(u32x4,sm+O_ONE+ln*16)=u32x4{0x3f803f80u,0x3f803f80u,0x3f803f80u,0x3f803f80u}; else if(ln<8) ST(u32x4,sm+O_ONE+1024+ln*16)=u32x4{0x3f803f80u,0x3f803f80u,0x3f803f80u,0x3f803f80u};
    const lch* xp=sm+(li<8? O_TILE+li*XSTR : O_ONE)+g*16; int of=(mt*288+(fu>>1)*4+w4)*256+(li&7)*32+g*8+(li>>3)*4;
    const lch* mp=sm+O_RC+(li&7)*4;
    asm volatile("" : "+v"(xp),"+v"(of),"+v"(mp));
    rsrc_t rsG=RSRC(ws+W_GU,2*8*F_*2);
    if(w4==0) BST128(I4,rsG,(mt*288+(fu>>1)*4)*256+ln*16,0,SC1);
    bool slow=(((u32)wrs[0]+wso)&0x380u)==0x180u;
    if(slow){ wso-=(PRE1-PRE3)*wst; wld-=(PRE1-PRE3)*4096; FILL(5+PRE1-PRE3,VW(W3)); } else FILLD(5,W1,W1B);
    if(w4==0){ VMW(5); if(ln==0) GST(ws+W_FL+(576+fu)*8,tag); }
    if(slow) FILLD(13-PRE1,W3,W1B); else FILLD(13-PRE1,W1,W1B);
    bf16x8 fB[18];
    VMW(LAST1); gsync(sm,C_P,4);
    #pragma unroll
    for(int ks=0;ks<18-LAST1;ks++) fB[ks]=WFR(0,ks);
    #pragma unroll
    for(int ks=0;ks<18-LAST1;ks++) asm volatile("" : "+v"(fB[ks]));
    DRAIN(); gsync(sm,C_P,8);
    #pragma unroll
    for(int ks=18-LAST1;ks<18;ks++) fB[ks]=WFR(0,ks);
    f32x4 a0={0,0,0,0}; float nm,ri,ce; u32 mdw;
    { bf16x8 xb[18];
      if(!NOG2) gwait<2>(sm,C_X,1);
      gwait4<64>(C_X,4,xp,xb[0],xb[1],xb[2],xb[3]);
      #pragma unroll
      for(int ks=4;ks<7;ks++) xb[ks]=LD(bf16x8,xp+ks*64);
      SCHED();
      #pragma unroll
      for(int ks=0;ks<17;ks++){
        a0=__builtin_amdgcn_mfma_f32_16x16x32_bf16(fB[ks],xb[ks],a0,0,0,0); SCHED();
        if(2*ks+7<18) xb[2*ks+7]=LD(bf16x8,xp+(2*ks+7)*64); if(2*ks+8<18) xb[2*ks+8]=LD(bf16x8,xp+(2*ks+8)*64);
        if(ks==5) mdw=LD(u32,mp); if(ks==14){ ri=rcpf_(bhi(mdw)); nm=-blo(mdw); PIN(nm); } if(ks==15){ ce=ri*-1.4426950408889634f; PIN(ce); }
        SCHED(); }
      #define RR8 " row_ror:8 row_mask:0xf bank_mask:"
      float v0,v1,s0,s1;
      asm volatile("v_mfma_f32_16x16x32_bf16 v[112:115], %4, %5, %6\n\ts_nop 7\n\tv_fmac_f32_dpp v114, v114, %7" RR8 "0xf\n\tv_fmac_f32_dpp v115, v115, %7" RR8 "0xf\n\tv_fmac_f32_dpp v112, v112, %7" RR8 "0xf\n\tv_fmac_f32_dpp v113, v113, %7" RR8 "0xf\n\t"
        "v_mov_b32_dpp v112, v114" RR8 "0xc\n\tv_mov_b32_dpp v113, v115" RR8 "0xc\n\tv_mul_f32 %2, %9, v112\n\tv_mul_f32 %3, %9, v113\n\tv_mul_f32 %0, %8, v112\n\tv_mul_f32 %1, %8, v113"
        : "=&v"(v0),"=&v"(v1),"=&v"(s0),"=&v"(s1) : "v"(fB[17]),"v"(xb[17]),"v"(a0),"v"(nm),"v"(ri),"v"(ce));
      if(mt==0){ v0*=rcpf_(1.f+__builtin_amdgcn_exp2f(s0)); v1*=rcpf_(1.f+__builtin_amdgcn_exp2f(s1)); }
      __builtin_amdgcn_raw_buffer_store_b32(nv(pk(v0,v1)),rsG,of,0,SC1); garrive(sm,C_H); }
    if(mt==0&&w4==0){
    LANE(l2); ln=l2; li=ln&15; g=ln>>4; gwait<1>(sm,C_IX,1);
    int c9=(lb-14)>>1, t=RFL((int)LD(u16,sm+O_IDX+xg*2)), c=64*c9+4*li+g; f32x4 pr;
    { u32 u=LD(u16,sm+O_IDX+16+xg*2)^0x8000u, xb=LD(u16,sm+O_TILE+xg*XSTR+c*2), sv=LD(u32,sm+O_RC+xg*4); float ds=b2f(Dsk[c]); u^=(u32)(((int)(u<<16))>>31)&0x7fffu;
      poll1<0>(ws+W_FL+(64*c9+ln)*8,tag); poll1<0>(ws+W_FL+(720+(ln<48? ln:47))*8,tag);
      float xn=(b2f(xb)-blo(sv))*rcpf_(bhi(sv)); pr=f32x4{b2f(xb),xn,xn*ds,sigm_f(b2f(u))}; }
    { gwait<2>(sm,C_E,4); u32 ti=now_(), mx=0;
      #pragma unroll
      for(int k=0;k<3;k++){ int i=ln+64*k; i= i<144? i:143; int q=(i*3641)>>16; u64 h=GLD(ws+W_HT+(72*q+36+2*(i-18*q))*8); mx=umax_(mx,(u32)(h>>32)==(u32)tag? (u32)h-ldw : ti-ldw+HM); }
      u32 tr=umin_(wmax(mx),ti-ldw+160u); if(tr>HK) tr-=(tr-HK)>>HS; tgate2(ldw,tr+HR); }
    { u32x4 rs=mkrs(ws+W_PART+(xg*9+c9)*4096,4096), ry=mkrs(ws+W_Y,D_*YST);
      int yo=c*YST+(t&~1)*2, po=ln*16; u32 a[4], t0,cnt,yb; u16* op=out+t*D_+c;
      #define LD_P "buffer_load_dwordx4 v[112:115], %[of], %[rs], 0 offen sc1\n\tbuffer_load_dwordx4 v[116:119], %[of], %[rs], 0 offen offset:1024 sc1\n\tbuffer_load_dwordx4 v[120:123], %[of], %[rs], 0 offen offset:2048 sc1\n\tbuffer_load_dwordx4 v[124:127], %[of], %[rs], 0 offen offset:3072 sc1\n\t"
      #define LD_YB "buffer_load_dword %[yb], %[yo], %[ry], 0 offen sc1\n\t"
      #define AD4(o,a,b,c,d) "v_add_f32 %[t0], v" #a ", v" #b "\n\tv_add_f32 %[" #o "], v" #c ", v" #d "\n\tv_add_f32 %[" #o "], %[t0], %[" #o "]\n\t"
      u64 ym;
      asm volatile(LD_YB LD_P "s_mov_b32 %[cnt], 0x20000\n1:\n\ts_waitcnt vmcnt(0)\n\tv_cmp_eq_u32 %[ym], -1, %[yb]\n\t" CHK_I0 MX2(116,117) MX2(118,119) MX2(120,121) MX2(122,123) MX2(124,125) MX2(126,127) CHK_IE
        "s_or_b64 exec, vcc, %[ym]\n\ts_cbranch_scc0 9f\n\ts_sleep " STR(QSLP) "\n\ts_mov_b64 exec, %[ym]\n\ts_cbranch_execz 2f\n\t" LD_YB "2:\n\ts_mov_b64 exec, vcc\n\ts_cbranch_execz 3f\n\t" LD_P
        "3:\n\ts_mov_b64 exec, -1\n\ts_sub_u32 %[cnt], %[cnt], 1\n\ts_cbranch_scc0 1b\n9:\n\ts_mov_b64 exec, -1\n\t"
        AD4(a0,112,116,120,124) AD4(a1,113,117,121,125) AD4(a2,114,118,122,126) AD4(a3,115,119,123,127) "s_nop 1"
        : [t0]"=&v"(t0),[cnt]"=&s"(cnt),[a0]"=&v"(a[0]),[a1]"=&v"(a[1]),[a2]"=&v"(a[2]),[a3]"=&v"(a[3]),[yb]"=&v"(yb),[ym]"=&s"(ym) : [of]"v"(po),[rs]"s"(rs),[yo]"v"(yo),[ry]"s"(ry),"v"(op),"v"(pr) : PCLOB);
      auto s0=__builtin_amdgcn_permlane32_swap(a[0],a[2],false,false); auto s1=__builtin_amdgcn_permlane32_swap(a[1],a[3],false,false);
      u32 x0=s0[0],x1=s0[1],y0=s1[0],y1=s1[1]; u32 p0=BC(u32,BC(float,x0)+BC(float,x1)), p1=BC(u32,BC(float,y0)+BC(float,y1));
      auto s2=__builtin_amdgcn_permlane16_swap(p0,p1,false,false); u32 z0=s2[0], z1=s2[1]; float s=BC(float,z0)+BC(float,z1);
      float gq=silu_f(__builtin_fmaf(s,pr[3],pr[1]));
      float sy=((t&1)? bhi(yb):blo(yb))+pr[2];
      float o=__builtin_fmaf(sy,gq,pr[0]);
      asm volatile("global_store_short %0, %1, off sc1" :: "v"(op),"v"(pk(o,o)) : "memory");
      if(bid==112&&ln==0) GST(ws+W_EPOCH,tag); }
    }
  } else
  {
    FLANE(D_*2)
    bf16x8 fr[27];
    rsrc_t rsP=RSRC(ws+W_PART+cgp*12288+kq*256+w4*64,8*9*4096-2*12288);
    #pragma unroll
    for(int i=0;i<3;i++) BST128(I4,rsP,li<8? li*(9*4096)+g*16+i*4096 : OOR,0,SC1);
    if(PRE2<9) FILLD(9-PRE2,W2_,W2B);
    wso+=128u-9u*wst;
    FILLD(9,W2_,W2B);
    VMW(9); gsync(sm,C_P,4);
    #pragma unroll
    for(int ks=0;ks<9;ks++) fr[ks]=WFR(0,ks);
    gsync(sm,C_P,8); if(w4==0&&ln==0) GST(ws+W_FL+(720+6*xg+lb-8)*8,tag);
    wso+=128u-9u*wst; wld-=2*CHSZ;
    FILL(9,VW(W2B));
    int fo=W_FL+(576+((9*kq)&~1)+(ln<9? ln:9))*8;
    if(w4==0) asm volatile("global_load_dwordx2 v[112:113], %0, %1 sc1\n\ts_waitcnt vmcnt(10)" :: "v"(fo),"s"(ws) : "memory"); else VMW(9);
    gsync(sm,C_P,12);
    #pragma unroll
    for(int ks=0;ks<9;ks++) fr[9+ks]=WFR(1,ks);
    { int t2=TID; asm volatile("" : "+v"(t2)); ln=t2&63; li=ln&15; g=ln>>4; }
    const lch* hp=sm+O_TILE+(li&7)*64+g*16; int po= li<8? li*(9*4096)+g*16+4096 : OOR; PIN(hp); PIN(po);
    u32x4 rs=mkrs(ws+W_GU,2*8*F_*2);
    int of=((ln>>5)*288+18*kq+2*w4)*256+(ln&31)*16; lch* hd=sm+O_TILE+w4*512+((ln>>1)&7)*64+((ln>>4)&1)*32+(ln&1)*16+(ln>>5)*8; PIN(of); PIN(hd); asm volatile("" : "+s"(rs));
    int pha=W_HP+(ln&7)*1536+cgp*512+(kq*4+w4)*8; PIN(pha);
    DRAIN();
    if(w4==0){ u64 bad; asm volatile("v_cmp_ne_u64 %0, %1, v[112:113]" : "=s"(bad) : "s"(tag) : "memory");
      if(bad!=0ull) poll1<0>(ws+fo,tag); }
    gsync(sm,C_P,16);
    { u32 t0,cnt;
      #define LD_G "buffer_load_dwordx4 v[112:115], %[of], %[rs], 0 offen sc1\n\tbuffer_load_dwordx4 v[116:119], %[of], %[rs], 0 offen offset:2048 sc1\n\tbuffer_load_dwordx4 v[120:123], %[of], %[rs], %[so] offen sc1\n\t"
      #define HM1(a,b) "v_lshlrev_b32 %[t0], 16, v" #a "\n\tv_lshlrev_b32 %[t1], 16, v" #b "\n\tv_and_b32 v" #a ", %[hm], v" #a "\n\tv_and_b32 v" #b ", %[hm], v" #b "\n\tv_mul_f32 %[t0], %[t0], %[t1]\n\tv_mul_f32 v" #a ", v" #a ", v" #b "\n\tv_cvt_pk_bf16_f32 v" #a ", %[t0], v" #a "\n\t"
      #define HMUL(a,b,c,d,o) "v_permlane32_swap_b32 v" #a ", v" #c "\n\tv_permlane32_swap_b32 v" #b ", v" #d "\n\t" HM1(a,c) HM1(b,d) "ds_write_b64 %[hd], v[" #a ":" #b "] offset:" #o "\n\t"
      #define PHST
      u32 t1;
      __builtin_amdgcn_s_sleep(GDL);
      asm volatile(LD_G :: [of]"v"(of),[rs]"s"(rs),[so]"s"(w4==0? 4096:0) : "memory");
      fr[18]=WFR(0,0); PIN(fr[18]);
      asm volatile(POLLASM(LD_G,CHK_I0 MX2(116,117) MX2(118,119) MX2(120,121) MX2(122,123) CHK_IE,GSLP) PHST
        HMUL(112,113,114,115,0) HMUL(116,117,118,119,2048) "s_cmp_eq_u32 %[so], 0\n\ts_cbranch_scc1 8f\n\t" HMUL(120,121,122,123,4096) "8:\n\ts_mov_b64 exec, 1\n\tds_add_u32 %[ch], %[one]\n\ts_mov_b64 exec, -1"
        : [t0]"=&v"(t0),[t1]"=&v"(t1),[cnt]"=&s"(cnt) : [of]"v"(of),[rs]"s"(rs),[so]"s"(w4==0? 4096:0),[hd]"v"(hd),[hm]"s"(0xffff0000u),[ch]"v"(C_H*4),[one]"v"(1),[pa]"v"(pha),[ws]"s"(ws),"v"(hp),"v"(po) : PCLOB);
      }
    { f32x4 ac[3]={{0,0,0,0},{0,0,0,0},{0,0,0,0}};
      bf16x8 hb[9];
      gwait4<512>(C_H,4,hp,hb[0],hb[1],hb[2],hb[3]);
      #pragma unroll
      for(int ks=0;ks<10;ks++){
        if(ks<9){ ac[0]=__builtin_amdgcn_mfma_f32_16x16x32_bf16(fr[ks],hb[ks],ac[0],0,0,0); ac[1]=__builtin_amdgcn_mfma_f32_16x16x32_bf16(fr[9+ks],hb[ks],ac[1],0,0,0); }
        if(ks>0) ac[2]=__builtin_amdgcn_mfma_f32_16x16x32_bf16(fr[17+ks],hb[ks-1],ac[2],0,0,0);
        SCHED();
        if(ks<4){ fr[19+2*ks]=WFR(0,1+2*ks); fr[20+2*ks]=WFR(0,2+2*ks); } if(ks>=1&&ks<=2){ hb[2*ks+2]=LD(bf16x8,hp+(2*ks+2)*512); hb[2*ks+3]=LD(bf16x8,hp+(2*ks+3)*512); } if(ks==3) hb[8]=LD(bf16x8,hp+8*512);
        SCHED(); }
      rsrc_t r0=RSRC(ws+W_PART+cgp*12288+kq*256+w4*64-4096,8*9*4096), r2=RSRC(ws+W_PART+cgp*12288+kq*256+w4*64+4096,8*9*4096-2*12288);
      BST128(nv4(BC(u32x4,ac[0])),r0,po,0,SC1); BST128(nv4(BC(u32x4,ac[1])),rsP,po,0,SC1); BST128(nv4(BC(u32x4,ac[2])),r2,po,0,SC1); }

  }
  asm volatile("s_endpgm" : KILLIN :: "memory"); }
  {
  if(__builtin_expect(q4!=0,0)){ int grp= q4==1? nch-1 : q4-2;
    chan(sm,grp,(w4+(0x48>>(2*q4)))&3,72*xg+(q4==3? 16+lb : 2*lb+(lb<8? 0:8)+2-q4),(q4+1)>>2,ws,xa,xb_,wa,wb,dq[0]);
    asm volatile("s_endpgm" : KILLIN :: "memory"); }
  u32x4 xq[3]; f32x2 rc[3]; int xr_,xr2_;
  {
    LANE(ln); int g=ln>>4, li=ln&15, qq=li>>2, pq=li&3, j=ln&31, rl=2*w4+(ln>>5);
    lch* tile=sm+O_TILE; u32 Tr=(ln>>1)&16;
    int uo=W_U+(tb>>TL)*GST_+(tb&((1<<TL)-1))*(128>>TL)+(w4*1024<<TL)+(ln>>(3-TL))*128+(ln&(7>>TL))*16, uo1=uo+(4096<<TL), uo2=uo+(8192<<TL); PIN(uo); PIN(uo1); PIN(uo2);
    lch* tw=tile+rl*TSTR2+j*16; lch* tw2=tw+1024-j*12; u32 one=0x3f803f80u; const lch* tp=tile+qq*TSTR2+(w4*64+g*16+pq*4)*2;
    int go= ln<32? W_RG+(ln>>2)*8192+(tb*4+(ln&3))*8 : W_ST+(tb*8+(ln&7))*8; lch* kw=sm+O_RV+rl*2; const lch* kr=sm+(ln<32? O_RV+(ln&3)*4 : O_MD+(ln&7)*4); PIN(kw); PIN(kr);
    int xr=(32*(tb>>2)+(ln>>2))*(D_*2)+256*(lb&3)+64*w4+pq*16, xr2= w4==0? xr+(lb&2)*(8*D_*2)+1024+64*(lb&1)-256*(lb&3):OOR;
    rsrc_t rx=RSRC(x,L_*D_*2); u64 gm=((1ull<<(4*NRG))-1ull)|0xff00000000ull; u32 um=~UMASK, km=0x80008000u; asm volatile("" : "+s"(gm),"+s"(um),"+s"(km),"+s"(ws));
    #define D2(a,b,c) __builtin_amdgcn_fdot2_f32_bf16(BC(bf2,a),BC(bf2,b),c,false)
#define XW0 "5"
#define XW1 "2"
    asm volatile("s_waitcnt vmcnt(" XW0 ")" : "+v"(wa),"+v"(wb),"+v"(wc),"+v"(Tr),"+v"(tw),"+v"(tw2),"+v"(tp),"+v"(one),"+v"(go),"+v"(xr),"+v"(xr2) :: "memory");
    u32 wq[9]={wa[0],wa[1],wa[2],wa[3],wb[0],wb[1],wb[2],wb[3],wc}; float sw;
    { float a[3];
      #pragma unroll
      for(int i=0;i<9;i++) a[i%3]=D2(wq[i],one,i<3? 0.f:a[i%3]);
      sw=add16x(sum16((a[0]+a[1])+a[2])); }
    GETEP u32 tg=(u32)ep+1u; Tr=((upat(tg,w4)>>Tr)&0xffffu)*0x10001u; u32 tgv=tg;
    float z_[9]={0,0,0,0,0,0,0,0,0};
    asm volatile("s_waitcnt vmcnt(" XW1 ")" : "+v"(xa),"+v"(xb_),"+v"(xc),"+v"(Tr),"+v"(sw),"+v"(tgv),"+v"(z_[0]),"+v"(z_[1]),"+v"(z_[2]),"+v"(z_[3]),"+v"(z_[4]),"+v"(z_[5]) :: "memory");
    u32 v[9]={xa[0],xa[1],xa[2],xa[3],xb_[0],xb_[1],xb_[2],xb_[3],xc};
    { float xf[18], s1, s2, s3;
      #pragma unroll
      for(int i=0;i<9;i++){ z_[i&1]=D2(v[i],one,z_[i&1]); z_[2+(i&1)]=D2(v[i],v[i],z_[2+(i&1)]); z_[4+(i&1)]=D2(v[i],wq[i],z_[4+(i&1)]); }
      s1=z_[0]+z_[1]; s2=z_[2]+z_[3]; s3=z_[4]+z_[5];
      SCHED();
      #define RSTEP(C) s1+=dppf<C>(s1); s2+=dppf<C>(s2); s3+=dppf<C>(s3); SCHED();
      RSTEP(0xB1) RSTEP(0x4E) RSTEP(0x141) RSTEP(0x140)
      s1=add16x(s1)*(1.f/576.f); s2=add16x(s2)*(1.f/576.f); s3=add16x(s3);
      float mm=s1*s1, var=s2-mm;
      if(__builtin_expect(BALLOT(!(mm<=64.f*var))!=0ull,0)){
        float q=0.f;
        #pragma unroll
        for(int i=0;i<9;i++){ u32 t=v[i]; PIN(t); float d0=blo(t)-s1, d1=bhi(t)-s1; q=__builtin_fmaf(d0,d0,q); q=__builtin_fmaf(d1,d1,q); }
        var=add16x(sum16(q))*(1.f/576.f); }
      #define UNP(i) PIN(v[i]); xf[2*(i)]=blo(v[i]); xf[2*(i)+1]=bhi(v[i]); PIN(xf[2*(i)]); PIN(xf[2*(i)+1]); SCHED();
      u32 vb=pk(var,s1); UNP(0)
      float den=blo(vb)+1e-5f, m=bhi(vb); UNP(1)
      vb=pk(den,den); s3=__builtin_fmaf(-m,sw,s3); UNP(2)
      den=__builtin_amdgcn_sqrtf(bhi(vb)); UNP(3)
      u32 md=pk(m,den); UNP(4)
      float ri=rcpf_(bhi(md)); UNP(5)
      s3*=ri; float rs_=ri*0.0625f; UNP(6)
      u32 u=pk(s3,s3); float cs_=-m*rs_; UNP(7)
      { u32 sg=(u32)((int)u>>31); PIN(sg); u^=sg|km; } UNP(8)
      asm volatile("s_mov_b64 exec, 0x1\n\ts_mov_b32 exec_hi, 1\n\tds_write_b16 %0, %1\n\tv_add_u32 %0, %0, %0\n\tds_write_b32 %0, %2 offset:%3\n\ts_mov_b32 exec_hi, 0\n\tds_add_u32 %4, %5\n\ts_mov_b64 exec, -1" : "+v"(kw) : "v"(u),"v"(md),"i"(O_MD-2*O_RV),"v"(C_I*4),"v"(1) : "memory");
      if(w4==3){ gwait<0>(sm,C_I,4); asm volatile("ds_read_b32 v118, %4\n\tv_mov_b32 v119, %1\n\ts_mov_b64 exec, %3\n\ts_waitcnt lgkmcnt(0)\n\tglobal_store_dwordx2 %0, v[118:119], %2 sc1\n\ts_mov_b64 exec, -1" :: "v"(go),"v"(tgv),"s"(ws),"s"(gm),"v"(kr) : "memory"); }
      SCHED();
      u32 h[9];
      #define UH(i) h[i]=(pkz(__builtin_fmaf(xf[2*(i)],rs_,cs_),__builtin_fmaf(xf[2*(i)+1],rs_,cs_))&um)|Tr;
      UH(0) UH(1) UH(2) UH(3) ST(u32x4,tw)=u32x4{h[0],h[1],h[2],h[3]}; UH(4) UH(5) UH(6) UH(7) ST(u32x4,tw+512)=u32x4{h[4],h[5],h[6],h[7]};
      UH(8) ST(u32,tw2)=h[8]; }
    gsync(sm,C_L,4);
    { u32x4 uq[3];
      #pragma unroll
      for(int z=0;z<3;z++) uq[z]=BC(u32x4,trfrag(tp+z*512,tp+z*512+4*TSTR2));
      asm volatile("global_store_dwordx4 %0, %1, %2 sc1" :: "v"(uo),"v"(uq[0]),"s"(ws) : "memory");
      asm volatile("global_store_dwordx4 %0, %1, %2 sc1" :: "v"(uo1),"v"(uq[1]),"s"(ws) : "memory");
      asm volatile("s_cmp_lg_u32 %3, 0\n\ts_cbranch_scc1 1f\n\tglobal_store_dwordx4 %0, %1, %2 sc1\n1:\n\ts_nop 1" :: "v"(uo2),"v"(uq[2]),"s"(ws),"s"(w4),"v"(uq[0]),"v"(uq[1]) : "memory","scc");
      asm volatile("" : "+v"(dq[0]),"+v"(dq[1]));
      xr_=xr; xr2_=xr2;
      #define XQ { rsrc_t rx_=RSRC(x,L_*D_*2); xq[0]=BLD128(rx_,xr_,0,0); xq[1]=BLD128(rx_,xr_,16*D_*2,0); xq[2]=BLD128(rx_,xr2_,0,0); }
    }
    if(lb>=2&&lb<8){
      const u16* wm=(lb&1)? wu:wg; int co=(3-(int)((u64)wm>>7)-xg)&7, r_=576-12*PFN+(((lb>>1)-1)*4+w4)+12*j;
      asm volatile("buffer_load_dword v120, %0, %1, 0 offen" :: "v"(j<PFN&&(ln<32||co==0)? r_*(F_*2)+(9*xg+co)*128+(ln>>5)*1024 : OOR),"s"(mkrs(wm,D_*F_*2)) : "memory"); }
    if(lb>=8&&lb<14) garrive(sm,C_D);
  }
  FP fp; u32 ti,td;
  { LANE(ln); u64 tag=ep+1ull;
    int so_=W_ST+(32*(tb>>2)+(ln>>2))*8, fo_=W_FL+((ln>=32&&w4==0? 512+32*(lb&1) : 128*(lb&3)+32*w4)+(ln&31))*8; u32x4 rx=mkrs(x,L_*D_*2); PIN(so_); PIN(fo_);
    XR X; X.so=so_; X.x0=xr_; X.x2=xr2_; X.rx=rx;
    if(lb==CTLB||lb>=14){
      u32 M=(u32)tag, rank,key; int j=ln&31, j16=j*16, j12=j*12, st0=W_ST; lch* xw=sm+O_TILE+(2*w4+(ln>>5))*XSTR+j*16; lch* rcw=sm+O_RC+(2*w4+(ln>>5))*4; PIN(rcw); PIN(j12); PIN(xw); PIN(j16); asm volatile("" : "+s"(rx),"+s"(st0));
      top8(sm,w4,ln,M,ws,rank,key,ldw,X);
      {
        int t0=__builtin_amdgcn_readlane((int)key,__builtin_ctzll(BALLOT(rank==(u32)(2*w4)))), t1=__builtin_amdgcn_readlane((int)key,__builtin_ctzll(BALLOT(rank==(u32)(2*w4+1))));
        int tsel=~((ln>>5)? t1:t0)&2047, ro=tsel*1152+j16, sr=tsel*8+st0;
        u32x4 a,b; u32 c; u64 f1;
        asm volatile("buffer_load_dwordx4 %[a], %[ro], %[rx], 0 offen\n\tbuffer_load_dwordx4 %[b], %[ro], %[rx], 0 offen offset:512\n\tbuffer_load_dword %[c], %[r2], %[rx], 0 offen offset:1024\n\tglobal_load_dwordx2 %[f], %[sr], %[ws] sc1\n\t"
          "s_mov_b64 exec, 1\n\tds_add_u32 %[cd], %[one]\n\ts_mov_b64 exec, -1\n\ts_waitcnt vmcnt(0)"
          : [a]"=&v"(a),[b]"=&v"(b),[c]"=&v"(c),[f]"=&v"(f1) : [ro]"v"(ro),[r2]"v"(ro-j12),[sr]"v"(sr),[cd]"v"(C_D*4),[one]"v"(1),[rx]"s"(rx),[ws]"s"(ws) : "memory");
        if(__builtin_expect(BALLOT((u32)(f1>>32)!=(u32)tag)!=0ull,0)){ f1=poll1<32>(ws+sr,tag<<32); __builtin_amdgcn_s_waitcnt(0x0f70); }
        ST(u32x4,xw)=a; ST(u32x4,xw+512)=b; ST(u32,xw+1024-j12)=c; ST(u32,rcw)=(u32)f1;
        garrive(sm,C_X);
        if(w4==0){ if(rank<8) ST(u16,sm+O_IDX+(ln&1)*16+rank*2)=(u16)((ln&1)? key>>16 : ~key&2047u); garrive(sm,C_IX); }
        if(lb==CTLB&&rank<8&&!(ln&1)) GST(ws+W_IDX+xg*1024+w4*128+rank*8,(tag<<16)|(~key&2047u));
        PRIO(FPB);
        if(XB!=2){ if(lb>=14&&XB==0) gwait<4>(sm,C_H,4); xreq(X,ws); }
      }
    } else { PRIO(0); if(lb<8? XA:XC){ gwait<4>(sm,C_G,7); __builtin_amdgcn_s_sleep(XC); } else gwait<8>(sm,C_U,2); xreq(X,ws); if(lb<8) PRIO(FPA); else PRIO(FPC); }
    { u64 fs,fs1;
      asm volatile("s_waitcnt vmcnt(0)" : "+{v[96:99]}"(X.q[0]),"+{v[100:103]}"(X.q[1]),"+{v[104:107]}"(X.q[2]),"+{v[108:111]}"(X.q[3]) :: "memory");
      fs=(u64)X.q[3][0]|((u64)X.q[3][1]<<32); fs1=(u64)X.q[3][2]|((u64)X.q[3][3]<<32);
      if(__builtin_expect(BALLOT((u32)(fs>>32)!=(u32)tag||(u32)(fs1>>32)!=(u32)tag)!=0ull,0)){ fs=poll1<32>(ws+so_,tag<<32); fs1=poll1<32>(ws+so_+128,tag<<32); __builtin_amdgcn_s_waitcnt(0x0f70); }
      float ri=rcpf_(bhi((u32)fs)), rj=rcpf_(bhi((u32)fs1)); rc[0]=f32x2{ri,-blo((u32)fs)*ri}; rc[1]=f32x2{rj,-blo((u32)fs1)*rj}; rc[2]= (lb&2)? rc[1]:rc[0];
      #pragma unroll
      for(int z=0;z<3;z++) xq[z]=X.q[z]; }
    bool ix= w4==3&&lb<14&&lb!=CTLB; int po=W_IDX+(xg*8+(lb&3))*128+(ln&7)*8;
    int ho_=fo_+(W_HT-W_FL);
    final_pre<0,1>(fp,w4,xq,dq,rc);
    ti=now_()-ldw;
    asm volatile("global_load_dwordx2 v[112:113], %0, %2 sc1\n\tglobal_load_dwordx2 v[114:115], %1, %2 sc1\n\tv_mov_b32 v117, %5\n\ts_cmp_lt_u32 %4, " STR(HT) "\n\ts_cbranch_scc1 1f\n\tglobal_load_dwordx2 v[116:117], %3, %2 sc1\n1:"
      :: "v"(fo_),"v"(ix? po:fo_),"s"(ws),"v"(ho_),"s"(ti),"s"(~(u32)tag) : "memory","scc");
    final_pre<1,3>(fp,w4,xq,dq,rc); PRIO(FPE);
    { u64 ff,f; u32x2 h;
      asm volatile("s_waitcnt vmcnt(0)\n\tv_mov_b64 %0, v[112:113]\n\tv_mov_b64 %1, v[114:115]\n\tv_mov_b64 %2, v[116:117]" : "=v"(ff),"=v"(f),"=v"(h) :: "memory");
      _Pragma("unroll 1") for(int k=0;k<2;k++){ if(BALLOT(h[1]!=(u32)tag)==0ull||ti>=H2) break;
        tgate2(ldw,ti<HT? HT:H2); ti=now_()-ldw; if(ti>=HL){ h=u32x2{ldw,(u32)tag}; break; }
        asm volatile("global_load_dwordx2 %0, %1, %2 sc1\n\ts_waitcnt vmcnt(0)" : "=&v"(h) : "v"(ho_),"s"(ws) : "memory"); }
      u32 rel= h[1]==(u32)tag? h[0]-ldw : ti+HM; td=umin_(wmax(rel),ti+160u); if(td>HK) td-=(td-HK)>>HS;
      if(__builtin_expect(BALLOT(ff!=tag)!=0ull,0)) poll1<0>(ws+fo_,tag);
      if(ix){ if(BALLOT(((f^(tag<<16))>>16)!=0ull)!=0ull) f=poll1<16>(ws+po,tag<<16);
        if(ln<8) ST(u16,sm+O_IDX+ln*2)=(u16)f; garrive(sm,C_IX); } }
  }
  final_post(sm,fp,w4,nch,out,ws,xr_,ldw,td);
  }
}

extern "C" void kernel_launch(void* const* d_in, const int* in_sizes, int n_in, void* d_out, int out_size, void* d_ws, size_t ws_size, hipStream_t stream) {
  const u16** I=(const u16**)d_in;
  KP kp; kp.Are=I[1]; kp.Aim=I[2]; kp.Bre=I[3]; kp.Bim=I[4]; kp.Cre=I[5]; kp.Cim=I[6]; kp.ldt=I[7]; kp.pad=I[7];
  kA<<<256,1024,0,stream>>>(I[0],(char*)d_ws,I[10],I[8],I[11],I[12],I[13],(u16*)d_out,kp);
}
```

```cpp
#include <hip/hip_runtime.h>
#include <hip/hip_bf16.h>
#include <stdint.h>
#if defined(__HIP_DEVICE_COMPILE__)
#define NOPK __attribute__((target("no-packed-fp32-ops")))
#else
#define NOPK
#endif
#define DEV __device__ NOPK __forceinline__
typedef unsigned short u16;
typedef unsigned int u32;
typedef unsigned long long u64;
typedef short bf16x8 __attribute__((ext_vector_type(8)));
typedef _Float16 h8 __attribute__((ext_vector_type(8)));
typedef _Float16 h2 __attribute__((ext_vector_type(2)));
typedef float f32x4 __attribute__((ext_vector_type(4)));
typedef float f32x2 __attribute__((ext_vector_type(2)));
typedef u32 u32x4 __attribute__((ext_vector_type(4)));
typedef u32 u32x2 __attribute__((ext_vector_type(2)));
typedef __bf16 bf2 __attribute__((ext_vector_type(2)));
typedef short v4s __attribute__((ext_vector_type(4)));
typedef __amdgpu_buffer_rsrc_t rsrc_t;
#define GAS __attribute__((address_space(1)))
#define LAS __attribute__((address_space(3)))
typedef LAS char lch;
#define L_ 2048
#define D_ 576
#define F_ 4608
#define BC(T,v) __builtin_bit_cast(T,v)
#define TID ((int)__builtin_amdgcn_workitem_id_x())
#define BID ((int)__builtin_amdgcn_workgroup_id_x())
#define RFL(x) __builtin_amdgcn_readfirstlane(x)
#define RLX_AGENT __ATOMIC_RELAXED, __HIP_MEMORY_SCOPE_AGENT
#define LD(T,p) (*(const LAS T*)(p))
#define ST(T,p) (*(LAS T*)(p))
#define SPINCAP (1<<17)
#define DRAIN() asm volatile("s_waitcnt vmcnt(0)" ::: "memory")
#define GLD(p) __hip_atomic_load((const GAS u64*)(p),RLX_AGENT)
#define GST(p,v) __hip_atomic_store((GAS u64*)(p),(u64)(v),RLX_AGENT)
#define BALLOT(c) __builtin_amdgcn_ballot_w64(c)
#define SC1 16
#define LANE(l) int l; { int t2_=TID; asm volatile("" : "+v"(t2_)); l=t2_&63; }
#define WPOL "nt"
#define UE 0
#define NOG2 1
#define PPOL "nt"
#define GDL 0
#define RDL 0
#define UDL 0
#define IV 0
#define INVST "global_store_dwordx4 v23, v[124:127], s[66:67] sc1\n\t"
#define PW "1"
#define INVL asm volatile("global_store_dwordx4 v123, v[124:127], %0 sc1" :: "s"(ws) : "memory");
#define HD0 174
#define HD1 148
#define HD3 165
#define HM 100
#define HT 555
#define HR 12
#define XB 1
#define XBW "0"
#define XA 0
#define XC 0
#define FPA 0
#define FPB 0
#define FPC 0
#define HL 650
#define HP 55
#define HQ 40
#define HK 660
#define HS 1
#define H2 600
#define FPE 0
#define AN 470
#define HKR 65
#define ANA 410
#define ANC 540
#define MG 0
#define UW 0
#define W2C 12
#define SCHED() __builtin_amdgcn_sched_barrier(0)
#define RSRC(p,n) __builtin_amdgcn_make_buffer_rsrc((void*)(p),0,(n),0x00020000)
#define BLD128 __builtin_amdgcn_raw_buffer_load_b128
#define BLD32 __builtin_amdgcn_raw_buffer_load_b32
#define BST128 __builtin_amdgcn_raw_buffer_store_b128
#define PRIO(n) __builtin_amdgcn_s_setprio(n)

DEV float b2f(u32 v){ return BC(float,v<<16); }
DEV float blo(u32 v){ return BC(float,v<<16); }
DEV float bhi(u32 v){ return BC(float,v&0xffff0000u); }
DEV u32 pk(float a,float b){ f32x2 v={a,b}; bf2 r=__builtin_convertvector(v,bf2); return BC(u32,r); }
DEV u32 pkh(float a,float b){ f32x2 v={a,b}; h2 r=__builtin_convertvector(v,h2); return BC(u32,r); }
DEV float rb(float f){ return BC(float,pk(f,f)<<16); }
DEV float rcpf_(float x){ return __builtin_amdgcn_rcpf(x); }
DEV float expf_(float x){
  const float L2E=1.4426950216293335f, L2El=1.9259629911266175e-8f; float h=x*L2E; float l=__builtin_fmaf(x,L2E,-h)+x*L2El;
  float e=__builtin_amdgcn_exp2f(h); return __builtin_fmaf(e*l,0.6931471805599453f,e); }
DEV float sigm_f(float x){ return rcpf_(1.f+__builtin_amdgcn_exp2f(-x*1.4426950408889634f)); }
DEV float silu_f(float x){ return x*sigm_f(x); }
DEV u32 umax_(u32 a,u32 b){ return a>b?a:b; }
DEV u32 umin_(u32 a,u32 b){ return a<b?a:b; }

#define YSH 3
#define PRE1 6
#define PRE3 4
#define PRE2 5
#define W1 5
#define W3 3
#define W2_ 4
#define W1B 20
#define W2B 12
#define LAST1 4
#define RGATE 4
#define UGATE 7
#define R2D 12
#define PFN 32
#define CPF 8
#define NRG 8
#define W_EPOCH 0
#define W_RG 16384
#define W_ST (W_RG+32*8192)
#define W_U (W_ST+32768)
#define UST (D_*16+128)
#define YST (L_*2+128)
#define TL 1
#define GST_ ((D_*16<<TL)+128)
#define W_Y (W_U+256*UST)
#define W_PART (W_Y+D_*YST)
#define W_GU (W_PART+8*9*16*256)
#define W_FL 9216
#define W_HT (W_RG+16*8192)
#define W_HP (W_HT+8192)
#define W_IDX 1024
#define CTLB 0
struct KP { const u16 *Are,*Aim,*Bre,*Bim,*Cre,*Cim,*ldt,*pad; };

template<int CTRL> DEV float dppf(float v){ return BC(float,__builtin_amdgcn_update_dpp(0u,BC(u32,v),CTRL,0xf,0xf,false)); }
template<int CTRL> DEV u32 dppu(u32 v){ return (u32)__builtin_amdgcn_update_dpp(0u,v,CTRL,0xf,0xf,false); }
DEV float sum16(float v){ v+=dppf<0xB1>(v); v+=dppf<0x4E>(v); v+=dppf<0x141>(v); v+=dppf<0x140>(v); return v; }
DEV float add16x(float v){ auto r=__builtin_amdgcn_permlane16_swap(BC(u32,v),BC(u32,v),false,false); u32 a=r[0], b=r[1]; return BC(float,a)+BC(float,b); }
DEV u32 wmax(u32 v){
  v=umax_(v,(u32)__builtin_amdgcn_update_dpp(0u,v,0x111,0xf,0xf,false));
  v=umax_(v,(u32)__builtin_amdgcn_update_dpp(0u,v,0x112,0xf,0xf,false));
  v=umax_(v,(u32)__builtin_amdgcn_update_dpp(0u,v,0x114,0xf,0xf,false));
  v=umax_(v,(u32)__builtin_amdgcn_update_dpp(0u,v,0x118,0xf,0xf,false));
  v=umax_(v,(u32)__builtin_amdgcn_update_dpp(0u,v,0x142,0xa,0xf,false));
  v=umax_(v,(u32)__builtin_amdgcn_update_dpp(0u,v,0x143,0xc,0xf,false));
  return (u32)__builtin_amdgcn_readlane((int)v,63); }

DEV void garrive(lch* sm,int c){ u32 ad=(u32)(size_t)sm+c*4, one=1;
  asm volatile("s_mov_b64 exec, 1\n\tds_add_u32 %0, %1\n\ts_mov_b64 exec, -1" :: "v"(ad),"v"(one) : "memory"); }
template<int SLP> DEV void gwait(lch* sm,int c,u32 target){ u32 ad=(u32)(size_t)sm+c*4, tv, cnt, sv; target=RFL(target);
  asm volatile("s_mov_b32 %1, 0x20000\n"
    "1:\n\tds_read_b32 %0, %3\n\ts_waitcnt lgkmcnt(0)\n\tv_readfirstlane_b32 %2, %0\n\ts_cmp_ge_u32 %2, %4\n\ts_cbranch_scc1 2f\n\ts_sleep %5\n\ts_sub_u32 %1, %1, 1\n\ts_cbranch_scc0 1b\n"
    "2:" : "=&v"(tv),"=&s"(cnt),"=&s"(sv) : "v"(ad),"s"(target),"i"(SLP) : "memory","scc"); }
template<int ST_,class T> DEV void gwait4(int c,u32 target,const lch* p,T& a,T& b,T& d,T& e){ u32 tv,cnt,sv;
  asm volatile("s_mov_b32 %1, 0x20000\n1:\n\tds_read_b32 %0, %7\n\tds_read_b128 %3, %8\n\tds_read_b128 %4, %8 offset:%10\n\tds_read_b128 %5, %8 offset:%11\n\tds_read_b128 %6, %8 offset:%12\n\ts_waitcnt lgkmcnt(4)\n\t"
    "v_readfirstlane_b32 %2, %0\n\ts_cmp_ge_u32 %2, %9\n\ts_cbranch_scc1 2f\n\ts_sleep 1\n\ts_sub_u32 %1, %1, 1\n\ts_cbranch_scc0 1b\n2:\n\ts_waitcnt lgkmcnt(0)"
    : "=&v"(tv),"=&s"(cnt),"=&s"(sv),"=&v"(a),"=&v"(b),"=&v"(d),"=&v"(e) : "v"(c*4),"v"(p),"i"(target),"i"(ST_),"i"(2*ST_),"i"(3*ST_) : "memory","scc"); }
template<int D> DEV void tgate(u32 t0){ u32 cnt; asm volatile("s_mov_b32 %0, 100\n1:\n\ts_memrealtime s[98:99]\n\ts_waitcnt lgkmcnt(0)\n\ts_sub_u32 s98, s98, %1\n\ts_cmp_ge_u32 s98, %2\n\ts_cbranch_scc1 2f\n\ts_sleep 1\n\ts_sub_u32 %0, %0, 1\n\ts_cbranch_scc0 1b\n2:" : "=&s"(cnt) : "s"(t0),"i"(D) : "memory","scc"); }
DEV u32 now_(){ u32 t; asm volatile("s_memrealtime s[98:99]\n\ts_waitcnt lgkmcnt(0)\n\ts_mov_b32 %0, s98" : "=s"(t) :: "memory"); return t; }
DEV void tgate2(u32 t0,u32 d){ u32 cnt; asm volatile("s_mov_b32 %0, 100\n1:\n\ts_memrealtime s[98:99]\n\ts_waitcnt lgkmcnt(0)\n\ts_sub_u32 s98, s98, %1\n\ts_cmp_ge_u32 s98, %2\n\ts_cbranch_scc1 2f\n\ts_sleep 1\n\ts_sub_u32 %0, %0, 1\n\ts_cbranch_scc0 1b\n2:" : "=&s"(cnt) : "s"(t0),"s"(d) : "memory","scc"); }
DEV void gsync(lch* sm,int c,u32 target){ garrive(sm,c); gwait<0>(sm,c,target); }
template<int SH> DEV u64 poll1(const char* p,u64 want){ u64 f=GLD(p);
  _Pragma("unroll 1") for(int sp=0;sp<SPINCAP;sp++){ bool bad=((f^want)>>SH)!=0ull; if(BALLOT(bad)==0ull) break; __builtin_amdgcn_s_sleep(1); if(bad) f=GLD(p); }
  __builtin_amdgcn_fence(__ATOMIC_ACQUIRE,"wavefront"); return f; }
#define STR_(x) #x
#define STR(x) STR_(x)
DEV u32x4 mkrs(const void* p,u32 n){ u64 a=(u64)p; return u32x4{(u32)RFL((int)(u32)a),(u32)RFL((int)((u32)(a>>32)&0xffffu)),n,0x00020000u}; }
#define VMW(n) asm volatile("s_waitcnt vmcnt(%0)" :: "i"(n) : "memory")
#define YSLP 1
#define GSLP 1
#define QSLP 1
#define PSLP 3
#define POLLASM(LOADS,CHECK,K) "s_mov_b32 %[cnt], 0x20000\n1:\n\ts_waitcnt vmcnt(0)\n\t" CHECK "s_cbranch_vccz 9f\n\ts_sleep " STR(K) "\n\ts_mov_b64 exec, vcc\n\t" LOADS \
  "s_mov_b64 exec, -1\n\ts_sub_u32 %[cnt], %[cnt], 1\n\ts_cbranch_scc0 1b\n9:\n\t"
#define LD_DUP2 "buffer_load_dwordx4 v[112:115], %[of], %[rs], 0 offen sc1\n\tbuffer_load_dwordx4 v[116:119], %[of], %[rs], %[so] offen sc1\n\t"
#define MX2(a,b) "v_max3_u32 %[t0], %[t0], v" #a ", v" #b "\n\t"
#define CHK_I0 "v_max_u32 %[t0], v112, v113\n\t" MX2(114,115)
#define CHK_IE "v_cmp_eq_u32 vcc, -1, %[t0]\n\t"
#define PCLOB "memory","vcc","scc"
#define UMASK 0x40074007u
DEV u32 upat(u32 tag,int k){ return ((((tag*0xea959c21u)>>(8*k))&255u)*0x04000801u)&UMASK; }
DEV u32 pkz(float a,float b){ return BC(u32,__builtin_amdgcn_cvt_pkrtz(a,b)); }
#define INV 0xffffffffu
DEV u32 nv(u32 v){ return umin_(v,0xfffeffffu); }
DEV u32x4 nv4(u32x4 v){ return u32x4{nv(v[0]),nv(v[1]),nv(v[2]),nv(v[3])}; }
#define OOR 0x40000000

#define CHSZ 36864
#define O_CH 512
#define O_CND 256
#define SLSZ 38080
#define O_R2 (O_CH+2*SLSZ)
#define O_X2 (O_R2+CHSZ)
#define O_TILE (O_X2+CHSZ)
#define XSTR 1168
#define TSTR2 1216
#define O_ONE (O_TILE+9728+16)
#define O_CAND (O_ONE+1168+48)
#define LDS_TOTAL (O_CAND+128)
#define C_G 0
#define C_U 8
#define C_L 4
#define C_D 5
#define C_X 12
#define C_P 13
#define C_H 14
#define C_I 17
#define C_E 18
#define C_IX 19
#define C_PR 21
#define C_RS 22
#define C_W 20
#define O_IDX 96
#define O_EXP 144
#define O_RMX 192
#define O_RV 128
#define O_MD 400
#define O_RC (O_CAND+64)

struct C2 { float r,i; };
DEV C2 cmul(C2 a,C2 b){ C2 c; c.r=__builtin_fmaf(a.r,b.r,-(a.i*b.i)); c.i=__builtin_fmaf(a.r,b.i,a.i*b.r); return c; }
DEV h8 ldA(const lch* p){ u32x2 a=*(const volatile LAS u32x2*)p, b=*(const volatile LAS u32x2*)(p+8); return BC(h8,(u32x4{a[0],a[1],b[0],b[1]})); }
DEV bf16x8 trfrag(const lch* p0,const lch* p1){
  u32x2 b0=BC(u32x2,__builtin_amdgcn_ds_read_tr16_b64_v4i16((LAS v4s*)p0)), b1=BC(u32x2,__builtin_amdgcn_ds_read_tr16_b64_v4i16((LAS v4s*)p1));
  return BC(bf16x8,(u32x4{b0[0],b0[1],b1[0],b1[1]})); }

#define OPOL SC1
struct FP { float gg[24], bs[24]; };
template<int Z0,int Z1> DEV void final_pre(FP& f,int w4,const u32x4* xq,const u32x4* dq,const f32x2* rc){
  #pragma unroll
  for(int z=Z0;z<Z1;z++) if(z<2||w4==0){ float ea=rc[z][0]*-1.4426950408889634f, eb=rc[z][1]*-1.4426950408889634f;
    #pragma unroll
    for(int e=0;e<4;e++){ u32 xw=xq[z][e], dw=dq[z>>1][e]; int i=8*z+2*e; float a=blo(xw), b=bhi(xw);
      float pa=__builtin_fmaf(a,rc[z][0],rc[z][1]), pb=__builtin_fmaf(b,rc[z][0],rc[z][1]);
      float ga=pa*rcpf_(1.f+__builtin_amdgcn_exp2f(__builtin_fmaf(a,ea,eb))), gb=pb*rcpf_(1.f+__builtin_amdgcn_exp2f(__builtin_fmaf(b,ea,eb)));
      f.gg[i]=ga; f.gg[i+1]=gb; f.bs[i]=__builtin_fmaf(pa*blo(dw),ga,a); f.bs[i+1]=__builtin_fmaf(pb*bhi(dw),gb,b);
      asm volatile("" : "+v"(f.gg[i]),"+v"(f.gg[i+1]),"+v"(f.bs[i]),"+v"(f.bs[i+1])); } }
}
#define FTS 544
#define YDA 0
#define YDB 0
#define YDC 0
#define YGS 1
#define NOCE 1
#define XE 0
#define PRS 0
DEV u32 pkmin(u32 a,u32 b){ u32 r; asm("v_pk_min_u16 %0, %1, %2" : "=v"(r) : "v"(a),"v"(b)); return r; }
DEV void final_post(lch* sm,const FP& f,int w4,int nch,u16* __restrict__ out,char* ws,int xr,u32 tk,u32 td){
  LANE(ln); int bid=BID, xg=bid&7, lb=bid>>3, cg=lb&3, T=8*xg+(lb>>2), ob[3];
  int yo=(128*cg+32*w4+(ln>>2)*2)*YST+(32*T+8*(ln&3))*2, yo3= ln<32? (512+32*(cg&1)+(ln&30))*YST+(32*T+16*(cg>>1)+8*(ln&1))*2 : OOR;
  lch* fb=sm+(NOCE? O_X2+2048:O_CH)+w4*4608; lch* wp=fb+(ln&3)*FTS+(ln&60); lch* wp3=fb+(4+(ln&1)+(ln>>5)*2)*FTS+(ln&30)*2; const lch* tp=fb+(ln>>5)*FTS+(ln&31)*16;
  u32x4 rs=mkrs(ws+W_Y,D_*YST); rsrc_t ro=RSRC(out,L_*D_*2); u32 tt=(u32)(32*T+(ln>>2))*0x10001u;
  asm volatile("" : "+v"(yo),"+v"(yo3),"+v"(wp),"+v"(wp3),"+v"(tp),"+v"(tt),"+s"(rs));
  #define RMASK { gwait<2>(sm,C_IX,1); u32x4 iq=LD(u32x4,sm+O_IDX); \
    _Pragma("unroll") for(int z=0;z<2;z++){ u32 t2=tt+z*0x100010u, m=pkmin(pkmin(iq[0]^t2,iq[1]^t2),pkmin(iq[2]^t2,iq[3]^t2)); ob[z]= umin_(m&0xffffu,m>>16)==0u? OOR : xr+z*(16*D_*2); } \
    ob[2]=((cg&2)? ob[1]:ob[0])+(512+32*(cg&1)-128*cg)*2; asm volatile("" : "+v"(ob[0]),"+v"(ob[1]),"+v"(ob[2])); }
  if(PRS){ RMASK
    if(PRS==1) BST128((u32x4{0,0,0,0}),ro,ln<4? ob[0]:OOR,0,SC1);
    if(PRS==2){ BST128((u32x4{0,0,0,0}),ro,ob[0],0,SC1); BST128((u32x4{0,0,0,0}),ro,ob[1],0,SC1); BST128((u32x4{0,0,0,0}),ro,w4==0? ob[2]:OOR,0,SC1); } }
  tgate2(tk,td);
  if(!NOCE) gwait<YGS>(sm,C_E,4*nch);
  if(lb<8) __builtin_amdgcn_s_sleep(YDA); else if(lb<14) __builtin_amdgcn_s_sleep(YDC); else __builtin_amdgcn_s_sleep(YDB);
  if(lb>=8&&(NOCE||lb<14)) gwait<YGS>(sm,C_H,4);
  PRIO(3);
  { u32 t0,t1,cnt;
    #define LD_Y "buffer_load_dwordx4 v[112:115], %[of], %[rs], 0 offen sc1\n\tbuffer_load_dwordx4 v[116:119], %[of], %[rs], %[s1] offen sc1\n\ts_cmp_lg_u32 %[w], 0\n\ts_cbranch_scc1 5f\n\t" \
      "buffer_load_dwordx4 v[120:123], %[o3], %[rs], 0 offen sc1\n\tbuffer_load_dwordx4 v[124:127], %[o3], %[rs], %[s1] offen sc1\n5:\n\t"
    #define YIN [of]"v"(yo),[rs]"s"(rs),[s1]"s"(YST),[o3]"v"(yo3),[w]"s"(w4)
    asm volatile(LD_Y :: YIN : "memory","scc");
    if(!PRS) RMASK
    #define YW(p,a,b,o) "v_perm_b32 %[t0], v" #b ", v" #a ", %[lo]\n\tv_perm_b32 %[t1], v" #b ", v" #a ", %[hi]\n\tds_write2_b32 %[" #p "], %[t0], %[t1] offset0:" #o " offset1:" #o "+16\n\t"
    asm volatile(POLLASM(LD_Y,CHK_I0 MX2(116,117) MX2(118,119) "s_cmp_lg_u32 %[w], 0\n\ts_cbranch_scc1 6f\n\t" MX2(120,121) MX2(122,123) MX2(124,125) MX2(126,127) "6:\n\t" CHK_IE,YSLP)
      "s_mov_b64 exec, 1\n\tds_add_u32 %[pr], %[one]\n\ts_mov_b64 exec, -1\n\t" YW(wp,112,116,0) YW(wp,113,117,32) YW(wp,114,118,64) YW(wp,115,119,96) "s_cmp_lg_u32 %[w], 0\n\ts_cbranch_scc1 7f\n\t" YW(w3,120,124,0) YW(w3,121,125,32) YW(w3,122,126,64) YW(w3,123,127,96) "7:"
      : [t0]"=&v"(t0),[t1]"=&v"(t1),[cnt]"=&s"(cnt) : YIN,[wp]"v"(wp),[w3]"v"(wp3),[lo]"s"(0x05040100u),[hi]"s"(0x07060302u),[pr]"v"(C_PR*4),[one]"v"(1) : PCLOB); }
  u32x4 r[3];
  #pragma unroll
  for(int z=0;z<3;z++) r[z]=LD(u32x4,tp+z*(2*FTS));
  #pragma unroll
  for(int z=0;z<3;z++) if(z<2||w4==0){ u32x4 ov;
    #pragma unroll
    for(int e=0;e<4;e++){ u32 rw=r[z][e]; int i=8*z+2*e; ov[e]=pk(__builtin_fmaf(blo(rw),f.gg[i],f.bs[i]),__builtin_fmaf(bhi(rw),f.gg[i+1],f.bs[i+1])); }
    BST128(ov,ro,ob[z],0,OPOL); SCHED(); }
}

#define TROW 528
#define O_TB 4640
#define O_DE 34752
#define O_DO 35968
#define O_R2K 37120
#define O_R3K 37632
#define O_DUM 37952
#define O_RED O_TB
#define MH(a,b,c) __builtin_amdgcn_mfma_f32_16x16x32_f16(a,b,c,0,0,0)
#define PIN(x) asm volatile("" : "+v"(x))
DEV h8 ld4w(const lch* p){ return BC(h8,(u32x4{LD(u32,p),LD(u32,p+4),LD(u32,p+8),LD(u32,p+12)})); }
DEV u32 __umul24_(u32 a,u32 b){ u32 r; asm("v_mul_u32_u24 %0, %1, %2" : "=v"(r) : "s"(b),"v"(a)); return r; }
DEV u32x4 band(u32 mask,int g,u32 val){ u32 by=__builtin_amdgcn_ubfe(mask,8*g,8), X=by|(by<<15), k=0x10001u; u32x4 r; asm volatile("" : "+s"(k),"+s"(val));
  #pragma unroll
  for(int d=0;d<4;d++) r[d]=__umul24_((X>>(2*d))&k,val);
  return r; }
DEV h8 zpack(f32x4 e,f32x4 o){ return BC(h8,(u32x4{pkh(e[0],o[0]),pkh(e[1],o[1]),pkh(e[2],o[2]),pkh(e[3],o[3])})); }
struct C3 { float r,i,n; };
DEV C3 c3(C2 a){ C3 c; c.r=a.r; c.i=a.i; PIN(c.i); c.n=-c.i; PIN(c.n); return c; }
DEV C2 cm(C2 a,C3 b){ C2 c; c.r=__builtin_fmaf(a.r,b.r,a.i*b.n); c.i=__builtin_fmaf(a.r,b.i,a.i*b.r); return c; }
DEV u32 ph(C2 a){ return pkh(a.r,a.i); }
#define W2(p,a,b) asm volatile("ds_write2_b32 %0, %1, %2 offset1:132" :: "v"(p),"v"(a),"v"(b) : "memory")
struct CP { const lch *x0,*x1,*bd; lch *p0,*p1,*p2,*wp; u32x4 Be,Bo,AE; int yo; };
template<int ROLE> DEV void lanes(CP& c,lch* cb,int ln){ int m=ln&15, g=ln>>4;
  const lch* ad=cb+((m&1)? O_DO+2*527 : O_DE+2*536)+2*(8*g-m);
  const lch* bd=cb+2*(256+16*m+8*g); lch* dum=cb+O_DUM;
  if(ROLE==0){ int l0=128*(g>>1)+8*m+4*(g&1); c.p0=cb+O_DE+(549-l0)*2; PIN(c.p0); c.p1=c.p0+6; PIN(c.p1); c.p2=c.p0+(O_DO-O_DE-18); c.x0=ad-640; c.bd=bd-64; c.x1=cb+O_R3K+2*(126-2*m+8*g)-192; PIN(c.x1);
    c.Be=band(0x1ffu<<(m+4),g,0x3c00u); PIN(c.Be); }
  if(ROLE==1){ lch* r=cb+O_R2K+(192-64*(g-2)-2*m)*2; c.p0= g<2? dum : r-66; c.p1= ln==0? cb+O_DE+(552-256)*2 : (ln<=32? dum : r); c.p2=cb+O_DE+(544-256-8*ln)*2; c.x0=ad-128-640; c.bd=bd-128-128; PIN(c.p0); PIN(c.p1); PIN(c.p2); }
  if(ROLE==2){ int i0=16*m+4*g; c.p0= m<8? cb+O_R3K+(125-i0)*2 : dum+2; c.p1= ln==0? cb+O_R2K+128 : (ln==8? cb+O_R3K : c.p0+6); c.x0=ad-320-640; c.bd=bd-320-128; PIN(c.p0); PIN(c.p1); }
  if(ROLE==3){ c.x0=cb+O_R2K+2*(188-4*m+8*g)-256; c.bd=cb+2*(248+8*m+8*g);
    c.Be=band(0x1fu<<(m+6),g,0x3c00u); c.Bo=band(0x7u<<(m+11),g,0x3c00u); PIN(c.Be); PIN(c.Bo); }
  PIN(c.x0); PIN(c.bd); }
template<int ROLE> DEV void lanes2(CP& c,lch* cb,int ln){ int m=ln&15, g=ln>>4;
  if(ROLE==0){ c.Bo=band(0x7fu<<(m+5),g,0x3c00u); c.AE=band(3u<<(m+12),g,0xffffu); PIN(c.Bo); PIN(c.AE); }
  c.wp=cb+O_RED+ln*16; c.yo=(g&1)*512+m*32+(g>>1)*16; PIN(c.wp); PIN(c.yo); }
template<int ROLE> DEV void chan2(lch* sm,lch* cb,const lch*& fa,const lch*& fb,int grp,int d,int one_,int emax,u64 tag,char* ws){
  CP c;
  const f32x4 Z4={0,0,0,0}; h8 A[6]; f32x4 acc[8];
  #define LD_U "buffer_load_dwordx4 v[112:115], %[of], %[rs], 0 offen sc1\n\tbuffer_load_dwordx4 v[116:119], %[of], %[rs], %[s1] offen sc1\n\tbuffer_load_dwordx4 v[120:123], %[of], %[rs], %[s2] offen sc1\n\tbuffer_load_dwordx4 v[124:127], %[of], %[rs], %[s3] offen sc1\n\t"
  #define UC(t,a,b,c) "v_bitop3_b32 " t ", v" #a ", v" #b ", %[" #c "] bitop3:0x7e\n\t"
  #define UD(t,a,c) "v_bitop3_b32 " t ", " t ", v" #a ", %[" #c "] bitop3:0xf6\n\t"
  #define CHK_U UC("%[t0]",112,116,c0) UC("%[t1]",113,117,c1) UD("%[t0]",120,c0) UD("%[t1]",121,c1) UD("%[t0]",124,c0) UD("%[t1]",125,c1) UD("%[t0]",114,c2) UD("%[t1]",115,c3) UD("%[t0]",118,c2) UD("%[t1]",119,c3) \
    UD("%[t0]",122,c2) UD("%[t1]",123,c3) UD("%[t0]",126,c2) UD("%[t1]",127,c3) "v_bitop3_b32 %[t0], %[t0], %[t1], %[mk] bitop3:0xa8\n\tv_cmp_ne_u32 vcc, 0, %[t0]\n\t"
  #define UX(a,b,c,d,o) "v_xor_b32 v" #a ", %[e0], v" #a "\n\tv_xor_b32 v" #b ", %[e1], v" #b "\n\tv_xor_b32 v" #c ", %[e2], v" #c "\n\tv_xor_b32 v" #d ", %[e3], v" #d "\n\tds_write_b128 %[up], v[" #a ":" #d "] offset:" #o "\n\t"
  #define UPOLL { u32 t0,t1,cnt; asm volatile(POLLASM(LD_U,CHK_U,PSLP) UX(112,113,114,115,544) UX(116,117,118,119,1056) UX(120,121,122,123,1568) UX(124,125,126,127,2080) \
      : [t0]"=&v"(t0),[t1]"=&v"(t1),[cnt]"=&s"(cnt) : [of]"v"(c.p0),[rs]"s"(rsU),[s1]"s"((32>>TL)*GST_),[s2]"s"((64>>TL)*GST_),[s3]"s"((96>>TL)*GST_),[c0]"s"(c0),[c1]"s"(c1),[c2]"s"(c2),[c3]"s"(c3_), \
        [e0]"s"(c0^0x40004u),[e1]"s"(c1^0x40004u),[e2]"s"(c2^0x40004u),[e3]"s"(c3_^0x40004u),[mk]"s"(UMASK),[up]"v"(c.p1) : PCLOB); garrive(sm,C_U+one_); }
  if(ROLE<3){
    h8 a[8], b[8];
    #pragma unroll
    for(int ks=0;ks<8;ks++){ a[ks]=LD(h8,fa+ks*64); b[ks]=LD(h8,fb+ks*64); }
    SCHED();
    LANE(ln); lanes<ROLE>(c,cb,ln); float fz= ln==32? 0.f : 4.76837158203125e-07f; if(ROLE==1) PIN(fz);
    SCHED();
    f32x4 k=Z4;
    #pragma unroll
    for(int ks=0;ks<8;ks++) k=MH(a[ks],b[ks],k);
    const float f=4.76837158203125e-07f;
    float k0=k[0]*f, k1=k[1]*f, k2=k[2]*f, k3=k[3]*f;
    u32 h32=pkh(k3,k2), h10=pkh(k1,k0);
    if(ROLE!=1){
      ST(u16,c.p0)=(u16)h32; ST(u32,c.p0+2)=pkh(k2,k1); ST(u16,c.p1)=(u16)(h10>>16);
      if(ROLE==0) ST(u32x2,c.p2)=u32x2{h32,h10};
    } else {
      float n0=dppf<0x130>(k[0])*fz, z1= fz==0.f? k0:k1, z2=k[2]*fz, z3=k[3]*fz; u32 h22=pkh(z2,z2);
      ST(u16,c.p0+64)=(u16)h10; ST(u16,c.p0+2)=(u16)(h32>>16); ST(u16,c.p0)=(u16)h32; ST(u16,c.p1)=(u16)(h10>>16);
      u32x4 vo={pkh(n0,z3),h22,pkh(z2,z1),pkh(k0,k0)}, ve={pkh(n0,n0),pkh(z3,z2),h22,pkh(z1,k0)};
      asm volatile("s_mov_b32 exec_hi, 1\n\tds_write_b128 %0, %1\n\tds_write_b128 %0, %2 offset:1200\n\ts_mov_b32 exec_hi, -1" :: "v"(c.p2),"v"(ve),"v"(vo) : "memory"); }
    gsync(sm,C_G+grp,7);
    u32 kv; A[0]=ld4w(c.x0+640); A[1]=ld4w(c.x0+128);
    if(ROLE==0){ A[2]=ld4w(c.x1+192); A[3]=ld4w(c.x1+128); A[4]=ld4w(c.x1+64); A[5]=ld4w(c.x1); kv=LD(u16,cb+O_R2K+128); }
    else { A[2]=ld4w(c.x0+576); A[3]=ld4w(c.x0+64); A[4]=ld4w(c.x0+512); A[5]=ld4w(c.x0); }
    SCHED(); lanes2<ROLE>(c,cb,ln); SCHED();
    if(ROLE==0){ kv|=kv<<16; c.AE=u32x4{c.AE[0]&kv,c.AE[1]&kv,c.AE[2]&kv,c.AE[3]&kv}; PIN(c.AE); }
    int ha=W_HT+d*8; u32 hd= one_? HD3 : (grp==0? HD0:HD1); if(ROLE==2) asm volatile("v_mov_b32 v113, %2" : "+v"(ha),"+s"(hd) : "s"((u32)tag));
    PRIO(2);
    gwait<1>(sm,C_U+one_,2-one_);
    if(ROLE==2) asm volatile("s_memrealtime s[98:99]\n\ts_waitcnt lgkmcnt(0)\n\ts_add_u32 s98, s98, %1\n\tv_mov_b32 v112, s98\n\ts_mov_b64 exec, 1\n\tglobal_store_dwordx2 %0, v[112:113], %2 sc1\n\ts_mov_b64 exec, -1" :: "v"(ha),"s"(hd),"s"(ws) : "memory","scc");
  } else {
    u32 tg=(u32)tag, c0=upat(tg,0), c1=upat(tg,1), c2=upat(tg,2), c3_=upat(tg,3); u32x4 rsU=mkrs(ws+W_U,256*UST);
    if(!one_) PRIO(2);
    LANE(ln); c.p0=(lch*)fa; c.p1=(lch*)fb;
    bool late= grp==0||BID>=112;
    if(!late&&!one_) __builtin_amdgcn_s_sleep(UDL);
    if(!late) asm volatile(LD_U :: [of]"v"(c.p0),[rs]"s"(rsU),[s1]"s"((32>>TL)*GST_),[s2]"s"((64>>TL)*GST_),[s3]"s"((96>>TL)*GST_) : "memory");
    asm volatile("s_mov_b64 exec, 1\n\tglobal_store_dwordx2 %0, %1, %2 sc1\n\ts_mov_b64 exec, -1" :: "v"(W_FL+d*8),"v"(tag),"s"(ws) : "memory");
    lanes<3>(c,cb,ln); lanes2<3>(c,cb,ln);
    #pragma unroll
    for(int j=2;j<8;j++){ acc[j]=Z4; PIN(acc[j]); }
    if(late){ if(UE==0||BID<112) gwait<2>(sm,C_G,UGATE); if(BID>=112){ gwait<1>(sm,UE? C_RS:C_D,4); if(UW) gwait<1>(sm,C_W,4); if(UE>1) __builtin_amdgcn_s_sleep(UE); } asm volatile(LD_U :: [of]"v"(c.p0),[rs]"s"(rsU),[s1]"s"((32>>TL)*GST_),[s2]"s"((64>>TL)*GST_),[s3]"s"((96>>TL)*GST_) : "memory"); }
    if(!late) UPOLL
    gwait<1>(sm,C_G+grp,7);
    #pragma unroll
    for(int i=0;i<6;i++) A[i]=ldA(c.x0+320-64*i);
    if(late) UPOLL
    PRIO(2);
    gwait<1>(sm,C_U+one_,2-one_);
  }
  const lch* bd=c.bd; h8 Be=BC(h8,c.Be), Bo=BC(h8,c.Bo);
  #pragma unroll
  for(int j=0;j<(ROLE==3? 2:8);j++) acc[j]=Z4;
  #define RW(j) if((j)>=0&&((j)>>1)!=ROLE&&!(ROLE==3&&(j)<2)){ ST(f32x4,c.wp+((j)>>1)*6144+((ROLE-((j)>>1)-1)&3)*2048+((j)&1)*1024)=acc[(j)<0? 0:(j)]; SCHED(); }
  if(ROLE==1||ROLE==2){
    h8 B[8];
    #pragma unroll
    for(int q=0;q<8;q++) B[q]=LD(h8,bd+128+512*q);
    #pragma unroll
    for(int r=0;r<3;r++){
      #pragma unroll
      for(int dl=0;dl<8;dl++){ if(dl<7) acc[dl+1]=MH(A[2*r+1],B[dl],acc[dl+1]); SCHED(); acc[dl]=MH(A[2*r],B[dl],acc[dl]); SCHED();
        if(r<2){ B[dl]=LD(h8,bd+64-64*r+512*dl); SCHED(); } else RW(dl-2) }
      } }
  if(ROLE==0){
    h8 B[8], ue[4];
    #pragma unroll
    for(int kt=0;kt<4;kt++){ ue[kt]=LD(h8,bd+48+512*kt); B[kt]=LD(h8,bd+64+512*kt); }
    f32x4 ce[2],co[2]; h8 Z[2];
    ce[0]=MH(ue[0],Be,Z4); co[0]=MH(B[0],Bo,Z4); ce[1]=MH(ue[1],Be,Z4); co[1]=MH(B[1],Bo,Z4); Z[0]=zpack(ce[0],co[0]);
    #pragma unroll
    for(int kt=0;kt<4;kt++){
      SCHED();
      if(kt+2<4){ ce[kt&1]=MH(ue[kt+2],Be,Z4); co[kt&1]=MH(B[kt+2],Bo,Z4); }
      if(kt+1<4) Z[(kt+1)&1]=zpack(ce[(kt+1)&1],co[(kt+1)&1]);
      #pragma unroll
      for(int rho=0;rho+kt<4;rho++) acc[4+kt+rho]=MH(Z[kt&1],A[2+rho],acc[4+kt+rho]);
      SCHED(); B[4+kt]=LD(h8,bd+64+512*(4+kt)); if(kt==0) A[5]=BC(h8,c.AE); if(kt==1) A[4]=ld4w(c.x0-384); if(kt==2) A[3]=ld4w(c.x0+64); if(kt==3) A[2]=ld4w(c.x0+576); SCHED(); }
    #pragma unroll
    for(int dl=0;dl<8;dl++){ if(dl<4) acc[dl+4]=MH(A[5],B[dl],acc[dl+4]); SCHED(); if(dl<6) acc[dl+2]=MH(A[4],B[dl],acc[dl+2]); SCHED(); if(dl<7) acc[dl+1]=MH(A[1],B[dl],acc[dl+1]); SCHED(); acc[dl]=MH(A[0],B[dl],acc[dl]);
      SCHED(); B[dl]=LD(h8,bd+512*dl); SCHED(); }
    #pragma unroll
    for(int dl=0;dl<8;dl++){ if(dl<7) acc[dl+1]=MH(A[3],B[dl],acc[dl+1]); SCHED(); acc[dl]=MH(A[2],B[dl],acc[dl]); SCHED(); RW(dl-2) } }
  if(ROLE==3){
    h8 U[12];
    #pragma unroll
    for(int kt=0;kt<6;kt++) U[kt]=LD(h8,bd+256*kt);
    f32x4 ce[2],co[2]; h8 Z[2];
    ce[0]=MH(U[0],Be,Z4); co[0]=MH(U[0],Bo,Z4); ce[1]=MH(U[1],Be,Z4); co[1]=MH(U[1],Bo,Z4); Z[0]=zpack(ce[0],co[0]);
    #pragma unroll
    for(int kt=0;kt<12;kt++){
      SCHED();
      if(kt+2<12){ ce[kt&1]=MH(U[kt+2],Be,Z4); co[kt&1]=MH(U[kt+2],Bo,Z4); }
      if(kt+1<12) Z[(kt+1)&1]=zpack(ce[(kt+1)&1],co[(kt+1)&1]);
      #pragma unroll
      for(int rho=-1;rho<5;rho++){ int j=(kt+4+rho)>>1; if(((kt+rho)&1)==0&&j>=2&&j<8) acc[j]=MH(Z[kt&1],A[rho+1],acc[j]); }
      if(kt<6){ SCHED(); U[kt+6]=LD(h8,bd+256*(kt+6)); }
      if(kt>=3&&kt<=9&&(kt&1)){ SCHED(); RW((kt+1)/2) } }
  }
  PRIO(3);
  RW(6) RW(7)
  gsync(sm,C_G+grp,11);
  { const lch* rp=c.wp+ROLE*6144; f32x4 a[3], b[3], o0=acc[2*ROLE], o1=acc[2*ROLE+1];
    #pragma unroll
    for(int q=0;q<(ROLE==0? 2:3);q++){ a[q]=LD(f32x4,rp+q*2048); b[q]=LD(f32x4,rp+q*2048+1024); }
    SCHED();
    float sc=__builtin_amdgcn_ldexpf(1.f,emax-YSH), s0[4], s1[4];
    #pragma unroll
    for(int e=0;e<4;e++){ s0[e]=((o0[e]+a[0][e])+(ROLE==0? a[1][e] : a[1][e]+a[2][e]))*sc; s1[e]=((o1[e]+b[0][e])+(ROLE==0? b[1][e] : b[1][e]+b[2][e]))*sc; }
    auto r0=__builtin_amdgcn_permlane16_swap(nv(pk(s0[0],s0[1])),nv(pk(s1[0],s1[1])),false,false); auto r1=__builtin_amdgcn_permlane16_swap(nv(pk(s0[2],s0[3])),nv(pk(s1[2],s1[3])),false,false);
    BST128(u32x4{r0[0],r1[0],r0[1],r1[1]},RSRC(ws+W_Y+d*YST,4096),c.yo+ROLE*1024,0,SC1); }
  garrive(sm,C_E);

  asm volatile("s_endpgm" : "=v"(fa),"=v"(fb) :: "memory");
}
#define PFR0 520
DEV void pf1(int i,int xg,const u16* wg,const u16* wu){ LANE(ln); u32 q=(u32)(i>>1)*64u+ln, r=(q*7282u)>>16, c=q-9u*r;
  asm volatile("buffer_load_dword v121, %0, %1, 0 offen" :: "v"((PFR0+r)*(F_*2)+(9*xg+c)*128),"s"(mkrs((i&1)? wu:wg,D_*F_*2)) : "memory"); }
DEV void pf2(int e,int xg,const u16* wd){ LANE(ln); u32 q=e*64+ln, r=(q*21846u)>>16, c=q-3u*r;
  asm volatile("buffer_load_dword v123, %0, %1, 0 offen" :: "v"((576*xg+288*(r>>8)+32+(r&255))*(D_*2)+384*c+256),"s"(mkrs(wd,D_*F_*2)) : "memory"); }
#define GETEP asm volatile("s_waitcnt lgkmcnt(0)\n\ts_mov_b64 %0, s[96:97]\n\ts_mov_b32 %1, s98" : "=s"(ep),"=s"(ldw));
DEV void chan(lch* sm,int grp,int w4,int d,int one_,char* ws,u32x4 q0,u32x4 q1,u32x4 q2,u32x4 q3,u32x4 q4){
  lch* cb=sm+O_CH+grp*SLSZ; int role=w4, emax;
  #define LP(v) ((lch*)(size_t)(u32)(v))
  const lch* fa=LP(q4[0]); const lch* fb=LP(q4[1]);
  u64 tag;
  { float a2,bp,br,bi,cr,ci;
    u32 fq=q3[2], fr=q3[3], one=q4[2]; float f64=BC(float,fq), f128=BC(float,fr);
    lch *t8[4]={LP(q0[0]),LP(q0[1]),LP(q0[2]),LP(q0[3])}, *tB[4]={LP(q1[0]),LP(q1[1]),LP(q1[2]),LP(q1[3])}, *t4[2]={LP(q2[0]),LP(q2[1])}, *t2[2]={LP(q2[2]),LP(q2[3])}, *t1=LP(q3[0]), *te=LP(q3[1]);
    if(grp!=0) PRIO(1);
    u64 ep; u32 ldw; GETEP tag=ep+1ull; if(IV==3) INVL
    float dt=rb(expf_((d&1)? bhi(ldw):blo(ldw))), ca=dt*1.4426950408889634f, cb_=dt*0.15915494309189535f;
    asm volatile("s_waitcnt vmcnt(" PW ")\n\tv_mul_f32 %2, %8, v114\n\tv_mul_f32 %3, %8, v115\n\tv_mul_f32 %0, %6, v112\n\tv_mul_f32 %1, %7, v113\n\tv_mov_b32 %4, v116\n\tv_mov_b32 %5, v117"
      : "=&v"(a2),"=&v"(bp),"=&v"(br),"=&v"(bi),"=&v"(cr),"=&v"(ci) : "v"(ca),"v"(cb_),"v"(dt) : "memory");
    if(IV==1) INVL
    C2 W; W.r=cr*br-ci*bi; W.i=cr*bi+ci*br;
    u32 mx=BC(u32,__builtin_fmaxf(__builtin_fabsf(W.r),__builtin_fabsf(W.i)));
    #define MXS(C) mx=umax_(mx,dppu<C>(mx));
    float e8=a2*8.f, eB=__builtin_fmaf(a2,f64,14.f), p8=bp*8.f, pB=bp*f64; SCHED();
    MXS(0xB1) p8=__builtin_amdgcn_fractf(p8); pB=__builtin_amdgcn_fractf(pB); e8=__builtin_amdgcn_exp2f(e8); SCHED();
    MXS(0x4E) eB=__builtin_amdgcn_exp2f(eB); float c8=__builtin_amdgcn_cosf(p8); SCHED();
    MXS(0x141) float s8=__builtin_amdgcn_sinf(p8), cB=__builtin_amdgcn_cosf(pB); SCHED();
    float sB=__builtin_amdgcn_sinf(pB); PIN(sB); MXS(0x140) float e1=__builtin_amdgcn_exp2f(a2); PIN(e1); SCHED();
    asm volatile("s_mov_b64 exec, 0x10001\n\tds_max_u32 %0, %1\n\tds_add_u32 %0, %2 offset:4\n\ts_mov_b64 exec, -1" :: "v"(te),"v"(mx),"v"(one) : "memory");
    C3 z8; z8.r=e8*c8; z8.n=e8*s8; z8.i=-z8.n; PIN(z8.i); C2 B; B.r=eB*cB; B.i=-(eB*sB);
    float p1=__builtin_amdgcn_fractf(bp), pE=__builtin_amdgcn_fractf(bp*256.f), pV=__builtin_amdgcn_fractf(bp*f128), eE=a2*256.f, eV=a2*f128, tr[8];
    #pragma unroll
    for(int k=0;k<4;k++){ SCHED(); u32 h0=ph(B); B=cm(B,z8);
      if(k==0){ tr[0]=__builtin_amdgcn_cosf(pV); tr[1]=__builtin_amdgcn_sinf(pV); } if(k==1){ tr[2]=__builtin_amdgcn_exp2f(eV); tr[3]=__builtin_amdgcn_cosf(p1); }
      if(k==2){ tr[4]=__builtin_amdgcn_sinf(p1); tr[5]=__builtin_amdgcn_exp2f(eE); } if(k==3){ tr[6]=__builtin_amdgcn_cosf(pE); tr[7]=__builtin_amdgcn_sinf(pE); }
      PIN(tr[2*k]); PIN(tr[2*k+1]);
      u32 h1=ph(B); if(k<3) B=cm(B,z8); W2(tB[k],h0,h1); }
    asm volatile("ds_read_b64 v[118:119], %0" :: "v"(te) : "memory");
    if(IV==2) INVL
    SCHED();
    C3 z1=c3(C2{e1*tr[3],e1*tr[4]}), E=c3(C2{tr[5]*tr[6],tr[5]*tr[7]}); C2 V[8]; V[0]=cmul(W,C2{tr[2]*tr[0],tr[2]*tr[1]});
    { u32 cnt,n; PIN(V[0].r); PIN(V[0].i); PIN(z1.r); PIN(E.r);
      asm volatile("s_mov_b32 %1, 0x20000\n1:\n\ts_waitcnt lgkmcnt(0)\n\tv_readfirstlane_b32 %2, v119\n\tv_readfirstlane_b32 %0, v118\n\ts_cmp_eq_u32 %2, 8\n\ts_cbranch_scc1 2f\n\tds_read_b64 v[118:119], %3\n\ts_sub_u32 %1, %1, 1\n\ts_cbranch_scc0 1b\n2:"
        : "=&s"(emax),"=&s"(cnt),"=&s"(n) : "v"(te) : "memory","scc");
      emax=RFL(emax); emax=(emax>>23)-126; }
    V[0].r=__builtin_amdgcn_ldexpf(V[0].r,14-emax); V[0].i=__builtin_amdgcn_ldexpf(V[0].i,14-emax);
    C2 Y=cm(V[0],E), Y4; u32 y0=ph(Y);
    V[1]=cm(V[0],z1); W2(t8[0],ph(V[0]),ph(V[1])); Y=cm(Y,E); u32 y1=ph(Y);
    V[2]=cm(V[1],z1); Y=cm(Y,E); u32 y2=ph(Y);
    V[3]=cm(V[2],z1); W2(t8[1],ph(V[2]),ph(V[3])); W2(t4[0],y0,ph(cm(V[2],E)));
    V[4]=cm(V[3],z1); Y=cm(Y,E); ST(u32,t1)=ph(Y);
    V[5]=cm(V[4],z1); W2(t8[2],ph(V[4]),ph(V[5])); Y4=cm(V[4],E); u32 y4=ph(Y4);
    V[6]=cm(V[5],z1); Y=cm(Y,E); ST(u32,t1+2*TROW)=ph(Y);
    V[7]=cm(V[6],z1); W2(t8[3],ph(V[6]),ph(V[7])); Y4=cm(Y4,E); W2(t2[0],y1,ph(Y4));
    W2(t4[1],y4,ph(cm(V[6],E))); Y=cm(Y,E); ST(u32,t1+4*TROW)=ph(Y);
    Y4=cm(Y4,E); W2(t2[1],y2,ph(Y4)); Y=cm(Y,E); ST(u32,t1+6*TROW)=ph(Y);
    Y=cm(Y,E); asm volatile("s_mov_b32 exec_hi, 0\n\tds_write_b32 %0, %1 offset:4224\n\ts_mov_b32 exec_hi, -1" :: "v"(t1),"v"(ph(Y)) : "memory");
  }
  if(one_) PRIO(3);
  DRAIN();
  gsync(sm,C_G+grp,4);
  if(role==0) chan2<0>(sm,cb,fa,fb,grp,d,one_,emax,tag,ws);
  if(role==1) chan2<1>(sm,cb,fa,fb,grp,d,one_,emax,tag,ws);
  if(role==2) chan2<2>(sm,cb,fa,fb,grp,d,one_,emax,tag,ws);
  chan2<3>(sm,cb,fa,fb,grp,d,one_,emax,tag,ws);
}

struct XR { int so,x0,x2; u32x4 rx,q[4]; };
DEV void xreq(XR& X,char* ws){ asm volatile("global_load_dwordx2 v[108:109], %[so], %[ws] sc1\n\tglobal_load_dwordx2 v[110:111], %[so], %[ws] offset:128 sc1\n\tbuffer_load_dwordx4 %[a], %[x0], %[rx], 0 offen\n\tbuffer_load_dwordx4 %[b], %[x0], %[rx], %[s1] offen\n\tbuffer_load_dwordx4 %[c], %[x2], %[rx], 0 offen"
  : [a]"=&{v[96:99]}"(X.q[0]),[b]"=&{v[100:103]}"(X.q[1]),[c]"=&{v[104:107]}"(X.q[2]),"=&{v[108:111]}"(X.q[3]) : [so]"v"(X.so),[ws]"s"(ws),[x0]"v"(X.x0),[x2]"v"(X.x2),[rx]"s"(X.rx),[s1]"s"(16*D_*2) : "memory"); }
DEV u32 med3u(u32 a,u32 b,u32 c){ u32 r; asm("v_med3_u32 %0, %1, %2, %3" : "=v"(r) : "v"(a),"v"(b),"v"(c)); return r; }
DEV u32 max3u(u32 a,u32 b,u32 c){ return umax_(umax_(a,b),c); }
#define GTC(r,s) "v_cmp_gt_u32 %[" #s "], v" #r ", %[c]\n\t"
#define GTA(s) "v_addc_co_u32 %[rk], vcc, 0, %[rk], %[" #s "]\n\t"
#define GT4(a,b,c,d) GTC(a,s0) GTC(b,s1) GTC(c,s2) GTC(d,s3) GTA(s0) GTA(s1) GTA(s2) GTA(s3)
#define GTS [s0]"=&s"(q0),[s1]"=&s"(q1),[s2]"=&s"(q2),[s3]"=&s"(q3)
DEV void top8(lch* sm,int w4,int ln,u32 M,char* ws,u32& rank_,u32& key_,u32 tk,XR& X){
  u32 k[8], rc[8], hm=0xffff0000u; u64 em=0x0001000100010001ull,q0,q1,q2,q3;
  u32x4 rs=mkrs(ws+W_RG+(BID&(NRG-1))*8192,8192); int ro=w4*1024+ln*16;
  #pragma unroll
  for(int e=0;e<8;e++){ rc[e]=(u32)(0xfff-(w4*256+ln*4+(e&3)+(e>>2)*1024)); PIN(rc[e]); }
  lch *aW=sm+O_RMX+(w4*4+(ln>>4))*4, *aC=sm+O_RMX+(ln>>2)*4, *aO=sm+O_RMX+(ln&3)*16, *dC=sm+O_CND+(ln>>1)*4, *dO=sm+O_CND+(ln&1)*64; PIN(aW); PIN(aC); PIN(aO); PIN(dC); PIN(dO); PIN(ro); PIN(ln);
  asm volatile("" : "+s"(hm),"+s"(em),"+s"(rs));
  gwait<2>(sm,C_G,RGATE); __builtin_amdgcn_s_sleep(RDL);
  { u32 cnt;
    #define KL(e,r) "v_lshl_or_b32 %[k" #e "], v" #r ", 16, %[k" #e "]\n\t"
    #define KH(e,r) "v_and_or_b32 %[k" #e "], v" #r ", %[hm], %[k" #e "]\n\t"
    #define CHK_R "v_bitop3_b32 %[t0], v113, v115, %[M] bitop3:0x7e\n\tv_bitop3_b32 %[t1], v117, v119, %[M] bitop3:0x7e\n\tv_or_b32 %[t0], %[t0], %[t1]\n\tv_cmp_ne_u32 vcc, 0, %[t0]\n\t"
    u32 t0,t1;
    asm volatile(LD_DUP2
      "s_sleep " STR(R2D) "\n\tbuffer_load_dwordx4 v[120:123], %[of], %[rs], 0 offen sc1\n\tbuffer_load_dwordx4 v[124:127], %[of], %[rs], %[so] offen sc1\n\ts_mov_b32 %[cnt], 0x20000\n\ts_waitcnt vmcnt(2)\n\t" CHK_R "s_cbranch_vccz 9f\n\t"
      "s_waitcnt vmcnt(0)\n\tv_mov_b64 v[112:113], v[120:121]\n\tv_mov_b64 v[114:115], v[122:123]\n\tv_mov_b64 v[116:117], v[124:125]\n\tv_mov_b64 v[118:119], v[126:127]\n\t"
      POLLASM(LD_DUP2,CHK_R,PSLP)
      KL(0,112) KH(1,112) KL(2,114) KH(3,114) KL(4,116) KH(5,116) KL(6,118) KH(7,118)
      : [k0]"+v"(rc[0]),[k1]"+v"(rc[1]),[k2]"+v"(rc[2]),[k3]"+v"(rc[3]),[k4]"+v"(rc[4]),[k5]"+v"(rc[5]),[k6]"+v"(rc[6]),[k7]"+v"(rc[7]),[cnt]"=&s"(cnt),[t0]"=&v"(t0),[t1]"=&v"(t1) : [of]"v"(ro),[rs]"s"(rs),[so]"s"(4096),[M]"s"(M),[hm]"s"(hm) : PCLOB); }
  if(UE) garrive(sm,C_RS);
  #pragma unroll
  for(int e=0;e<8;e++) k[e]=rc[e];
  u32 h=max3u(k[0],k[1],k[2]), m1=max3u(max3u(k[3],k[4],k[5]),max3u(k[6],k[7],h),h), mx=m1, l=med3u(k[0],k[1],k[2]), t=umin_(umin_(k[0],k[1]),k[2]);
  SCHED();
  #define INS(e) t=med3u(t,l,k[e]); l=med3u(h,l,k[e]); if(e<7) h=umax_(h,k[e]); PIN(t); PIN(l); SCHED();
  mx=umax_(mx,dppu<0xB1>(mx)); INS(3) mx=umax_(mx,dppu<0x4E>(mx)); INS(4) mx=umax_(mx,dppu<0x141>(mx)); INS(5) mx=umax_(mx,dppu<0x140>(mx));
  asm volatile("s_mov_b64 exec, %2\n\tds_write_b32 %0, %1\n\ts_mov_b64 exec, -1\n\tds_read_b32 v116, %3\n\tds_read_b128 v[112:115], %4" :: "v"(aW),"v"(mx),"s"(em),"v"(aC),"v"(aO) : "memory");
  if(XB==2) xreq(X,ws);
  INS(6) INS(7)
  u32 m2=l, m3=t, T;
  { u32 c,rk,cnt;
    asm volatile("s_mov_b32 %[cnt], 0x20000\n1:\n\ts_waitcnt lgkmcnt(0)\n\tv_cmp_eq_u32 vcc, 0, v116\n\ts_cbranch_vccz 2f\n\tds_read_b32 v116, %[aC]\n\tds_read_b128 v[112:115], %[aO]\n\ts_sub_u32 %[cnt], %[cnt], 1\n\ts_cbranch_scc0 1b\n2:\n\t"
      "v_mov_b32 %[c], v116\n\tv_mov_b32 %[rk], 0\n\t" GT4(112,113,114,115) "s_nop 1\n\tv_add_u32_dpp %[rk], %[rk], %[rk] quad_perm:[1,0,3,2] row_mask:0xf bank_mask:0xf\n\ts_nop 1\n\tv_add_u32_dpp %[rk], %[rk], %[rk] quad_perm:[2,3,0,1] row_mask:0xf bank_mask:0xf\n\t"
      "v_cmp_eq_u32 vcc, 7, %[rk]\n\ts_ff1_i32_b64 %[cnt], vcc\n\ts_nop 0\n\tv_readlane_b32 %[T], %[c], %[cnt]\n\ts_nop 1"
      : [c]"=&v"(c),[rk]"=&v"(rk),[cnt]"=&s"(cnt),[T]"=&s"(T),GTS : [aC]"v"(aC),[aO]"v"(aO) : PCLOB); T=RFL(T); }
  { u64 b1=BALLOT(m1>=T), b2=BALLOT(m2>=T), b3=BALLOT(m3>=T); int n1=__builtin_popcountll(b1), n2=n1+__builtin_popcountll(b2);
    lch* cw=sm+O_CND+w4*32;
    if(__builtin_expect(b3==0ull&&n2<=8,1)){
      int p1=(int)__builtin_amdgcn_mbcnt_hi((u32)(b1>>32),__builtin_amdgcn_mbcnt_lo((u32)b1,0u)), pf=n2+ln-p1; bool c1=m1>=T;
      if(__builtin_expect(b2!=0ull,0)){ int p2=(int)__builtin_amdgcn_mbcnt_hi((u32)(b2>>32),__builtin_amdgcn_mbcnt_lo((u32)b2,(u32)n1)); if(m2>=T) ST(u32,cw+p2*4)=m2; }
      int ps= c1? p1:pf; ps= ps<8? ps:32-8*w4; ST(u32,cw+ps*4)= c1? m1:1u;
    } else {
      u32 mine=0;
      _Pragma("unroll 1") for(int c=0;c<8;c++){ u32 lm=umax_(umax_(umax_(k[0],k[1]),umax_(k[2],k[3])),umax_(umax_(k[4],k[5]),umax_(k[6],k[7]))); u32 wm=wmax(lm); if(ln==c) mine=wm;
        #pragma unroll
        for(int e=0;e<8;e++) k[e]=(k[e]==wm)?0u:k[e]; }
      if(ln<8) ST(u32,cw+ln*4)=mine; } }
  { u32 c,rk,cnt;
    asm volatile("s_mov_b32 %[cnt], 0x20000\n\tv_mov_b32 %[rk], 0\n\ts_waitcnt vmcnt(" XBW ")\n1:\n\tds_read_b32 %[c], %[dC]\n\tds_read_b128 v[112:115], %[dO]\n\tds_read_b128 v[116:119], %[dO] offset:16\n\tds_read_b128 v[120:123], %[dO] offset:32\n\tds_read_b128 v[124:127], %[dO] offset:48\n\t"
      "s_waitcnt lgkmcnt(4)\n\tv_cmp_eq_u32 vcc, 0, %[c]\n\ts_cbranch_vccz 2f\n\ts_sub_u32 %[cnt], %[cnt], 1\n\ts_cbranch_scc0 1b\n2:\n\t"
      "s_waitcnt lgkmcnt(3)\n\t" GT4(112,113,114,115) "s_waitcnt lgkmcnt(2)\n\t" GT4(116,117,118,119) "s_waitcnt lgkmcnt(1)\n\t" GT4(120,121,122,123) "s_waitcnt lgkmcnt(0)\n\t" GT4(124,125,126,127)
      "s_nop 1\n\tv_add_u32_dpp %[rk], %[rk], %[rk] quad_perm:[1,0,3,2] row_mask:0xf bank_mask:0xf"
      : [c]"=&v"(c),[rk]"=&v"(rk),[cnt]"=&s"(cnt),GTS : [dC]"v"(dC),[dO]"v"(dO) : PCLOB);
    rank_=rk; key_=c; }
}

#define FILL_(n,WAIT) "s_mov_b32 m0, %[ld]\n\ts_mov_b32 %[c], " STR(n) "-1\n3:\n\t" WAIT "buffer_load_dwordx4 %[vo], %[rs], %[so] offen " WPOL " lds\n\ts_add_u32 m0, m0, 0x1000\n\ts_add_u32 %[so], %[so], %[st]\n\t" \
  "s_sub_u32 %[c], %[c], 1\n\ts_cbranch_scc0 3b\n\ts_mov_b32 %[ld], m0\n\t"
#define FILL(n,WAIT) do{ u32 c_; asm volatile(FILL_(n,WAIT) : [c]"=&s"(c_),[so]"+s"(wso),[ld]"+s"(wld) : [vo]"v"(wvo),[rs]"s"(wrs),[st]"s"(wst) : "memory","scc","m0"); }while(0)
#define FILLD(n,WN,WW_) do{ u32 c_,t_,s_; asm volatile(FILL_(n,"ds_read_b32 %[t], %[ad]\n\ts_waitcnt lgkmcnt(0)\n\tv_readfirstlane_b32 %[s], %[t]\n\ts_cmp_ge_u32 %[s], 4\n\ts_cbranch_scc1 4f\n\t" VW(WN) "4:\n\t" VW(WW_)) \
  : [c]"=&s"(c_),[so]"+s"(wso),[ld]"+s"(wld),[t]"=&v"(t_),[s]"=&s"(s_) : [vo]"v"(wvo),[rs]"s"(wrs),[st]"s"(wst),[ad]"v"(C_D*4) : "memory","scc","m0"); wso=RFL(wso); wld=RFL(wld); }while(0)
#define VW(n) "s_waitcnt vmcnt(" STR(n) ")\n\t"

__global__ NOPK __attribute__((amdgpu_num_vgpr(56),amdgpu_num_sgpr(102))) __launch_bounds__(1024,4) void kA(const u16* __restrict__ x,char* ws,const u16* __restrict__ wr,const u16* __restrict__ Dsk,const u16* __restrict__ wg,const u16* __restrict__ wu,
                                              const u16* __restrict__ wd,u16* __restrict__ out,KP kp){
  __shared__ __attribute__((aligned(16))) char smem[LDS_TOTAL];
  lch* sm=(lch*)smem;
  u32x4 xa,xb_,wa,wb,dq[3]; u32 xc,wc;
  asm volatile(
    "s_load_dwordx16 s[64:79], %[ka], 0x0\n\ts_load_dwordx16 s[80:95], %[ka], 0x40\n\t"
    "v_readfirstlane_b32 s60, %[t]\n\ts_and_b32 s61, %[wg], 7\n\ts_lshr_b32 s62, %[wg], 3\n\tv_and_b32 v118, 31, %[t]\n\ts_bfe_u32 s63, s60, 0x20006\n\ts_cmpk_ge_u32 s60, 0x100\n\ts_cbranch_scc1 1f\n\t"
    "s_setprio 3\n\ts_lshl_b32 s56, s61, 5\n\ts_add_u32 s56, s56, s62\n\ts_mulk_i32 s56, 0x2400\n\tv_lshrrev_b32 v119, 5, %[t]\n\tv_mul_u32_u24 v119, 0x480, v119\n\tv_add_u32 v119, s56, v119\n\t"
    "v_lshl_add_u32 v116, v118, 4, v119\n\tv_lshl_add_u32 v117, v118, 2, v119\n\tv_lshlrev_b32 v114, 4, v118\n\tv_lshlrev_b32 v115, 2, v118\n\t"
    "s_and_b32 s57, s62, 3\n\ts_lshl2_add_u32 s57, s57, s63\n\ts_lshl_b32 s57, s57, 6\n\ts_and_b32 s58, s62, 1\n\ts_lshl_b32 s58, s58, 6\n\tv_and_b32 v112, 3, %[t]\n\tv_lshl_add_u32 v113, v112, 4, s57\n\tv_lshl_add_u32 v112, v112, 4, s58\n\t"
    "v_lshlrev_b32 v119, 1, %[t]\n\tv_mov_b32 v118, 0\n\tds_write_b16 v119, v118\n\t"
    "s_waitcnt lgkmcnt(0)\n\t"
    "s_memrealtime s[98:99]\n\ts_load_dwordx2 s[96:97], s[66:67], 0x0\n\t"
    "global_load_dwordx4 %[wa], v114, s[68:69]\n\tglobal_load_dwordx4 %[wb], v114, s[68:69] offset:512\n\tglobal_load_dword %[wc], v115, s[68:69] offset:1024\n\t"
    "global_load_dwordx4 %[xa], v116, s[64:65]\n\tglobal_load_dwordx4 %[xb], v116, s[64:65] offset:512\n\tglobal_load_dword %[xc], v117, s[64:65] offset:1024\n\t"
    "global_load_dwordx4 %[d0], v113, s[70:71]\n\tglobal_load_dwordx4 %[d1], v112, s[70:71] offset:1024\n\ts_barrier\n\t"
    "s_branch 8f\n"
    "1:\n\ts_cmp_ge_u32 s62, " STR(CPF) "\n\ts_cbranch_scc1 6f\n\ts_getpc_b64 s[56:57]\n.Lkpc:\n\ts_and_b32 s58, s62, 3\n\ts_lshl_b32 s58, s58, 13\n\ts_sub_u32 s58, s58, 0x1000\n\tv_lshl_add_u32 v119, %[t], 4, s58\n\tv_min_u32 v119, .Lkend-.Lkpc-16, v119\n\t"
    "global_load_dword v120, v119, s[56:57]\n\t.subsection 1\n.Lkend:\n\t.subsection 0\n6:\n\t"
    "v_mov_b32 v124, -1\n\tv_mov_b32 v125, -1\n\tv_mov_b32 v126, -1\n\tv_mov_b32 v127, -1\n\tv_lshlrev_b32 v119, 4, %[t]\n\tv_and_b32 v119, 0xff0, v119\n\t"
    "s_lshr_b32 s59, s60, 8\n\ts_cmp_lt_u32 s62, 8\n\ts_cselect_b32 s58, 2, 1\n\ts_cbranch_scc1 2f\n\ts_cmp_eq_u32 s59, 3\n\ts_cbranch_scc1 4f\n"
    "2:\n\ts_sub_u32 s57, s59, 2\n\ts_cmp_eq_u32 s59, 1\n\ts_cselect_b32 s58, s58, s57\n\ts_cmp_lt_u32 s62, 8\n\ts_cselect_b32 s56, 0, 8\n\ts_lshl1_add_u32 s56, s62, s56\n\ts_mulk_i32 s61, 72\n\ts_add_u32 s51, s56, s61\n\ts_sub_u32 s56, 2, s59\n\ts_sub_u32 s57, 16, s62\n\ts_cmp_eq_u32 s59, 3\n\ts_cselect_b32 s50, s57, s56\n\ts_add_u32 s56, s50, s51\n\t"
    "v_mov_b32 v112, 0\n\tv_mov_b32 v113, 0\n\tv_mov_b32 v114, 0\n\tv_mov_b32 v115, 0\n\tv_mov_b32 v116, 0\n\tv_mov_b32 v117, 0\n\ts_setprio 2\n\t"
    "s_lshl_b32 s57, s56, 2\n\ts_add_u32 s57, s57, s63\n\ts_lshl_b32 s57, s57, 6\n\tv_lshl_add_u32 v22, v118, 1, s57\n\ts_mul_i32 s57, s56, %[yst]\n\ts_add_u32 s57, s57, %[wy]\n\tv_add_u32 v23, s57, v119\n\ts_lshl_b32 s56, s56, 1\n\ts_and_b32 s56, s56, -4\n\t"
    "s_waitcnt lgkmcnt(0)\n\t"
    "global_load_short_d16_hi v112, v22, s[80:81] " PPOL "\n\tglobal_load_short_d16_hi v113, v22, s[82:83] " PPOL "\n\tglobal_load_short_d16_hi v114, v22, s[84:85] " PPOL "\n\t"
    "global_load_short_d16_hi v115, v22, s[86:87] " PPOL "\n\tglobal_load_short_d16_hi v116, v22, s[88:89] " PPOL "\n\tglobal_load_short_d16_hi v117, v22, s[90:91] " PPOL "\n\t"
    "s_load_dwordx2 s[96:97], s[66:67], 0x0\n\ts_load_dword s98, s[92:93], s56\n\t"
    "s_barrier\n\t" INVST
    "s_mul_i32 s52, s58, %[sl]\n\ts_add_u32 s52, s52, %[tb]\n\ts_lshl_b32 s53, s63, 7\n\ts_add_u32 s53, s53, s52\n\tv_lshl_add_u32 v30, v118, 2, s53\n\tv_bfe_u32 v31, %[t], 5, 1\n\ts_movk_i32 s54, 528\n\tv_mul_u32_u24 v29, s54, v31\n\t"
    "v_lshl_add_u32 v2, v29, 3, v30\n\ts_movk_i32 s53, 1056\n\tv_add_u32 v3, s53, v2\n\tv_add_u32 v4, s53, v3\n\tv_add_u32 v5, s53, v4\n\tv_add_u32 v6, 41*528, v2\n\tv_add_u32 v7, s53, v6\n\tv_add_u32 v8, s53, v7\n\tv_add_u32 v9, s53, v8\n\t"
    "v_lshl_add_u32 v10, v29, 2, v30\n\tv_add_u32 v10, 16*528, v10\n\tv_add_u32 v11, s53, v10\n\tv_lshl_add_u32 v12, v29, 1, v30\n\tv_add_u32 v12, 24*528, v12\n\tv_add_u32 v13, 2112, v12\n\tv_add_u32 v14, v29, v30\n\tv_add_u32 v14, 32*528, v14\n\t"
    "s_lshl_b32 s53, s58, 4\n\ts_add_u32 s53, s53, %[ex]\n\tv_mov_b32 v15, s53\n\tv_cvt_f32_ubyte0 v31, v31\n\tv_mul_f32 v16, 0x42800000, v31\n\tv_add_f32 v17, v16, v16\n\tv_mov_b32 v20, 1\n\t"
    "v_and_b32 v29, 15, %[t]\n\tv_and_b32 v30, 48, %[t]\n\tv_mad_u32_u24 v30, v29, s54, v30\n\ts_lshl_b32 s55, s59, 1\n\ts_lshr_b32 s55, 0x48, s55\n\ts_add_u32 s55, s55, s63\n\ts_and_b32 s55, s55, 3\n\ts_mul_i32 s53, s55, 16*528\n\ts_movk_i32 s57, 41*528\n\ts_cmp_eq_u32 s55, 2\n\ts_cselect_b32 s53, s57, s53\n\ts_cselect_b32 s57, 32*528, s57\n\t"
    "s_add_u32 s53, s53, s52\n\ts_add_u32 s57, s57, s52\n\tv_add_u32 v18, s53, v30\n\tv_add_u32 v19, s57, v30\n\t"
    "v_min_u32 v29, 237*16, v119\n\tv_subrev_u32 v30, 204*16, v29\n\tv_add_u32 v29, %[de], v29\n\tv_min_u32 v29, v29, v30\n\ts_sub_u32 s53, s52, 4640\n\tv_add_u32 v29, s53, v29\n\tv_mov_b32 v24, 0\n\tv_mov_b32 v25, 0\n\tv_mov_b32 v26, 0\n\tv_mov_b32 v27, 0\n\tds_write_b128 v29, v[24:27]\n\t"
    "s_cmp_lg_u32 s55, 3\n\ts_cbranch_scc1 9f\n\t"
    "v_bfe_u32 v18, %[t], 1, 5\n\tv_and_b32 v19, 1, %[t]\n\ts_cmp_lt_u32 s62, 8\n\ts_cselect_b32 s53, 2, 1\n\ts_mul_i32 s53, s53, %[sl]\n\tv_mul_u32_u24 v21, s53, v19\n\ts_sub_u32 s55, 2, s59\n\ts_lshl_b32 s55, s55, 7\n\tv_add_u32 v18, s55, v18\n\ts_cmp_lg_u32 s59, 3\n\ts_cbranch_scc1 7f\n\t"
    "v_bfe_u32 v18, %[t], 5, 1\n\tv_lshl_add_u32 v18, v18, 7, v118\n\tv_mov_b32 v19, s50\n\tv_mov_b32 v21, %[sl]\n"
    "7:\n\tv_add_u32 v19, s51, v19\n\tv_lshl_add_u32 v21, v18, 4, v21\n\tv_lshrrev_b32 v24, 3-" STR(TL) ", v19\n\tv_and_b32 v19, 7>>" STR(TL) ", v19\n\tv_lshlrev_b32 v19, 4, v19\n\tv_lshl_add_u32 v19, v24, 7, v19\n\t"
    "v_and_b32 v24, (1<<" STR(TL) ")-1, v18\n\tv_lshl_add_u32 v19, v24, 7-" STR(TL) ", v19\n\tv_lshrrev_b32 v18, " STR(TL) ", v18\n\tv_mul_u32_u24 v18, %[ust], v18\n\tv_add_u32 v18, v18, v19\n\tv_add_u32 v19, 512, v21\n\t"
    "s_branch 9f\n"
    "4:\n\t"
    "v_bfe_u32 v118, %[t], 3, 5\n\tv_lshrrev_b32 v117, 1, v118\n\tv_and_b32 v117, 4, v117\n\tv_and_b32 v116, 2, v118\n\tv_and_b32 v115, 7, %[t]\n\tv_or_b32 v116, v116, v117\n\tv_xor_b32 v115, v115, v116\n\tv_lshlrev_b32 v115, 4, v115\n\t"
    "s_mul_i32 s54, s61, 9\n\ts_sub_u32 s55, s62, 14\n\ts_lshr_b32 s55, s55, 1\n\ts_add_u32 s54, s54, s55\n\ts_lshl_b32 s54, s54, 7\n\t"
    "s_mul_i32 s52, s61, 0xa2000\n\ts_mul_i32 s53, s62, 0x180\n\ts_add_u32 s52, s52, s53\n\ts_cmp_ge_u32 s62, 11\n\ts_cselect_b32 s53, 0x51000-0x480, 0\n\ts_add_u32 s52, s52, s53\n\ts_sub_u32 s52, s52, 0xc00\n\t"
    "s_movk_i32 s55, 0x480\n\ts_cmp_ge_u32 s62, 14\n\ts_cselect_b32 s54, s54, s52\n\ts_cselect_b32 s55, 0x2400, s55\n\tv_mad_u32_u24 v118, v118, s55, v115\n\ts_lshl_b32 s55, s55, 5\n\t"
    "s_lshl_b32 s52, s63, 10\n\ts_add_u32 m0, s52, %[r2]\n\ts_mov_b32 s58, 0x510000\n\ts_mov_b32 s59, 0x20000\n\ts_cmp_ge_u32 s62, 14\n\ts_cselect_b32 s53, " STR(PRE1) "-1, " STR(PRE2) "-1\n\ts_setprio 3\n\t"
    "s_waitcnt lgkmcnt(0)\n\ts_memrealtime s[98:99]\n\t"
    "s_bitcmp1_b32 %[wg], 3\n\ts_cselect_b64 s[56:57], s[74:75], s[72:73]\n\ts_cmp_ge_u32 s62, 14\n\ts_cselect_b64 s[56:57], s[56:57], s[76:77]\n\ts_cselect_b32 s52, 0x180, -1\n\ts_and_b32 s57, s57, 0xffff\n\ts_add_u32 s60, s56, s54\n\ts_and_b32 s60, s60, 0x380\n\ts_cmp_eq_u32 s60, s52\n\ts_cselect_b32 s53, " STR(PRE3) "-1, s53\n\t"
    "s_load_dwordx2 s[96:97], s[66:67], 0x0\n\ts_barrier\n\t"
    "\n3:\n\t" "buffer_load_dwordx4 v118, s[56:59], s54 offen " WPOL " lds\n\ts_add_u32 m0, m0, 0x1000\n\ts_add_u32 s54, s54, s55\n\ts_sub_u32 s53, s53, 1\n\ts_cbranch_scc0 3b\n"
    "9:\n8:"
    : [xa]"=&{v[2:5]}"(xa),[xb]"=&{v[6:9]}"(xb_),[xc]"=&{v30}"(xc),[wa]"=&{v[10:13]}"(wa),[wb]"=&{v[14:17]}"(wb),[wc]"=&{v31}"(wc),[d0]"=&{v[18:21]}"(dq[0]),[d1]"=&{v[22:25]}"(dq[1]),[d2]"=&{v[26:29]}"(dq[2])
    : [ka]"s"(__builtin_amdgcn_kernarg_segment_ptr()),[wg]"s"(BID),[t]"v"(TID),[yst]"i"(YST),[wy]"i"(W_Y),[r2]"i"(O_R2),[sl]"i"(SLSZ),[tb]"i"(O_CH+O_TB),[ex]"i"(O_EXP),[de]"i"(O_DE),[ust]"i"(GST_)
    : "memory","scc","m0","v127","s50","s51","s52","s53","s54","s55","s56","s57","s58","s59","s60","s61","s62","s63","s64","s65","s66","s67","s68","s69","s70","s71","s72","s73","s74","s75","s76","s77","s78","s79",
      "s80","s81","s82","s83","s84","s85","s86","s87","s88","s89","s90","s91","s92","s93","s94","s95","s99");
  int wv=RFL(TID>>6), bid=BID, q4=wv>>2, w4=wv&3;
  int xg=bid&7, lb=bid>>3, tb=32*xg+lb, nch=2+(lb<8);
  u64 ep; u32 ldw;
  {
  }
  #define KILLIN "=v"(xa),"=v"(xb_),"=v"(xc),"=v"(wa),"=v"(wb),"=v"(wc),"=v"(dq[0]),"=v"(dq[1]),"=v"(dq[2])
  if(__builtin_expect(q4==3&&lb>=8,0)){
    int fu=18*xg+lb-14, kq=2*xg+(lb>=11), cgp=lb-8-3*(lb>=11);
  u32 wst= lb>=14? F_*64:D_*64;
  u32 wso=(lb>=14? (fu>>1)*128 : kq*(288*D_*2)+384*cgp)+(lb>=14? PRE1:PRE2)*wst, wld=(u32)(size_t)sm+O_R2+w4*1024+(lb>=14? PRE1:PRE2)*4096; u32x4 wrs=mkrs(lb>=14? ((fu&1)? wu:wg):wd,D_*F_*2);
  GETEP
  #define FLANE(RS) LANE(ln); int g=ln>>4, li=ln&15, qq=li>>2, pq=li&3; u32 wvo=(u32)(8*w4+(ln>>3))*(RS)+(u32)((ln&7)^(((ln>>3)&2)|((w4&1)<<2)))*16u; \
    const lch* tp=sm+O_R2+(8*g+qq)*128+((w4^((qq>>1)|((g&1)<<1)))*32)+pq*8;
  #define WFR(img,ks) trfrag(tp+(img)*CHSZ+(ks)*4096,tp+(img)*CHSZ+(ks)*4096+512)
  u64 tag=ep+1ull; const u32x4 I4={INV,INV,INV,INV};
  if(lb>=14){
    FLANE(F_*2)
    int mt=fu&1;
    if(w4==0) ST(u32x4,sm+O_ONE+ln*16)=u32x4{0x3f803f80u,0x3f803f80u,0x3f803f80u,0x3f803f80u}; else if(ln<8) ST(u32x4,sm+O_ONE+1024+ln*16)=u32x4{0x3f803f80u,0x3f803f80u,0x3f803f80u,0x3f803f80u};
    const lch* xp=sm+(li<8? O_TILE+li*XSTR : O_ONE)+g*16; int of=(mt*288+(fu>>1)*4+w4)*256+(li&7)*32+g*8+(li>>3)*4;
    const lch* mp=sm+O_RC+(li&7)*4;
    asm volatile("" : "+v"(xp),"+v"(of),"+v"(mp));
    rsrc_t rsG=RSRC(ws+W_GU,2*8*F_*2);
    if(w4==0) BST128(I4,rsG,(mt*288+(fu>>1)*4)*256+ln*16,0,SC1);
    bool slow=(((u32)wrs[0]+wso)&0x380u)==0x180u;
    if(slow){ wso-=(PRE1-PRE3)*wst; wld-=(PRE1-PRE3)*4096; FILL(5+PRE1-PRE3,VW(W3)); } else FILLD(5,W1,W1B);
    if(w4==0){ VMW(5); if(ln==0) GST(ws+W_FL+(576+fu)*8,tag); }
    if(slow) FILLD(13-PRE1,W3,W1B); else FILLD(13-PRE1,W1,W1B);
    if(UW) garrive(sm,C_W);
    bf16x8 fB[18];
    VMW(LAST1); gsync(sm,C_P,4);
    #pragma unroll
    for(int ks=0;ks<18-LAST1;ks++) fB[ks]=WFR(0,ks);
    #pragma unroll
    for(int ks=0;ks<18-LAST1;ks++) asm volatile("" : "+v"(fB[ks]));
    DRAIN(); gsync(sm,C_P,8);
    #pragma unroll
    for(int ks=18-LAST1;ks<18;ks++) fB[ks]=WFR(0,ks);
    f32x4 a0={0,0,0,0}; float nm,ri,ce; u32 mdw;
    { bf16x8 xb[18];
      if(!NOG2) gwait<2>(sm,C_X,1);
      gwait4<64>(C_X,4,xp,xb[0],xb[1],xb[2],xb[3]);
      #pragma unroll
      for(int ks=4;ks<7;ks++) xb[ks]=LD(bf16x8,xp+ks*64);
      SCHED();
      #pragma unroll
      for(int ks=0;ks<17;ks++){
        a0=__builtin_amdgcn_mfma_f32_16x16x32_bf16(fB[ks],xb[ks],a0,0,0,0); SCHED();
        if(2*ks+7<18) xb[2*ks+7]=LD(bf16x8,xp+(2*ks+7)*64); if(2*ks+8<18) xb[2*ks+8]=LD(bf16x8,xp+(2*ks+8)*64);
        if(ks==5) mdw=LD(u32,mp); if(ks==14){ ri=rcpf_(bhi(mdw)); nm=-blo(mdw); PIN(nm); } if(ks==15){ ce=ri*-1.4426950408889634f; PIN(ce); }
        SCHED(); }
      #define RR8 " row_ror:8 row_mask:0xf bank_mask:"
      float v0,v1,s0,s1;
      asm volatile("v_mfma_f32_16x16x32_bf16 v[112:115], %4, %5, %6\n\ts_nop 7\n\tv_fmac_f32_dpp v114, v114, %7" RR8 "0xf\n\tv_fmac_f32_dpp v115, v115, %7" RR8 "0xf\n\tv_fmac_f32_dpp v112, v112, %7" RR8 "0xf\n\tv_fmac_f32_dpp v113, v113, %7" RR8 "0xf\n\t"
        "v_mov_b32_dpp v112, v114" RR8 "0xc\n\tv_mov_b32_dpp v113, v115" RR8 "0xc\n\tv_mul_f32 %2, %9, v112\n\tv_mul_f32 %3, %9, v113\n\tv_mul_f32 %0, %8, v112\n\tv_mul_f32 %1, %8, v113"
        : "=&v"(v0),"=&v"(v1),"=&v"(s0),"=&v"(s1) : "v"(fB[17]),"v"(xb[17]),"v"(a0),"v"(nm),"v"(ri),"v"(ce));
      if(mt==0){ v0*=rcpf_(1.f+__builtin_amdgcn_exp2f(s0)); v1*=rcpf_(1.f+__builtin_amdgcn_exp2f(s1)); }
      __builtin_amdgcn_raw_buffer_store_b32(nv(pk(v0,v1)),rsG,of,0,SC1); garrive(sm,C_H); }
    if(mt==0&&w4==0){
    LANE(l2); ln=l2; li=ln&15; g=ln>>4; gwait<1>(sm,C_IX,1);
    int c9=(lb-14)>>1, t=RFL((int)LD(u16,sm+O_IDX+xg*2)), c=64*c9+4*li+g; f32x4 pr;
    { u32 u=LD(u16,sm+O_IDX+16+xg*2)^0x8000u, xb=LD(u16,sm+O_TILE+xg*XSTR+c*2), sv=LD(u32,sm+O_RC+xg*4); float ds=b2f(Dsk[c]); u^=(u32)(((int)(u<<16))>>31)&0x7fffu;
      poll1<0>(ws+W_FL+(64*c9+ln)*8,tag); poll1<0>(ws+W_FL+(720+(ln<48? ln:47))*8,tag);
      float xn=(b2f(xb)-blo(sv))*rcpf_(bhi(sv)); pr=f32x4{b2f(xb),xn,xn*ds,sigm_f(b2f(u))}; }
    { gwait<2>(sm,C_E,4); u32 ti=now_(), mx=0;
      #pragma unroll
      for(int k=0;k<3;k++){ int i=ln+64*k; i= i<144? i:143; int q=(i*3641)>>16; u64 h=GLD(ws+W_HT+(72*q+36+2*(i-18*q))*8); mx=umax_(mx,(u32)(h>>32)==(u32)tag? (u32)h-ldw : ti-ldw+HM); }
      u32 tr=umin_(wmax(mx),ti-ldw+160u); u32 hk= AN? ti-ldw+HKR : HK; if(tr>hk) tr-=(tr-hk)>>HS; tgate2(ldw,tr+HR); }
    { u32x4 rs=mkrs(ws+W_PART+(xg*9+c9)*4096,4096), ry=mkrs(ws+W_Y,D_*YST);
      int yo=c*YST+(t&~1)*2, po=ln*16; u32 a[4], t0,cnt,yb; u16* op=out+t*D_+c;
      #define LD_P "buffer_load_dwordx4 v[112:115], %[of], %[rs], 0 offen sc1\n\tbuffer_load_dwordx4 v[116:119], %[of], %[rs], 0 offen offset:1024 sc1\n\tbuffer_load_dwordx4 v[120:123], %[of], %[rs], 0 offen offset:2048 sc1\n\tbuffer_load_dwordx4 v[124:127], %[of], %[rs], 0 offen offset:3072 sc1\n\t"
      #define LD_YB "buffer_load_dword %[yb], %[yo], %[ry], 0 offen sc1\n\t"
      #define AD4(o,a,b,c,d) "v_add_f32 %[t0], v" #a ", v" #b "\n\tv_add_f32 %[" #o "], v" #c ", v" #d "\n\tv_add_f32 %[" #o "], %[t0], %[" #o "]\n\t"
      u64 ym;
      asm volatile(LD_YB LD_P "s_mov_b32 %[cnt], 0x20000\n1:\n\ts_waitcnt vmcnt(0)\n\tv_cmp_eq_u32 %[ym], -1, %[yb]\n\t" CHK_I0 MX2(116,117) MX2(118,119) MX2(120,121) MX2(122,123) MX2(124,125) MX2(126,127) CHK_IE
        "s_or_b64 exec, vcc, %[ym]\n\ts_cbranch_scc0 9f\n\ts_sleep " STR(QSLP) "\n\ts_mov_b64 exec, %[ym]\n\ts_cbranch_execz 2f\n\t" LD_YB "2:\n\ts_mov_b64 exec, vcc\n\ts_cbranch_execz 3f\n\t" LD_P
        "3:\n\ts_mov_b64 exec, -1\n\ts_sub_u32 %[cnt], %[cnt], 1\n\ts_cbranch_scc0 1b\n9:\n\ts_mov_b64 exec, -1\n\t"
        AD4(a0,112,116,120,124) AD4(a1,113,117,121,125) AD4(a2,114,118,122,126) AD4(a3,115,119,123,127) "s_nop 1"
        : [t0]"=&v"(t0),[cnt]"=&s"(cnt),[a0]"=&v"(a[0]),[a1]"=&v"(a[1]),[a2]"=&v"(a[2]),[a3]"=&v"(a[3]),[yb]"=&v"(yb),[ym]"=&s"(ym) : [of]"v"(po),[rs]"s"(rs),[yo]"v"(yo),[ry]"s"(ry),"v"(op),"v"(pr) : PCLOB);
      auto s0=__builtin_amdgcn_permlane32_swap(a[0],a[2],false,false); auto s1=__builtin_amdgcn_permlane32_swap(a[1],a[3],false,false);
      u32 x0=s0[0],x1=s0[1],y0=s1[0],y1=s1[1]; u32 p0=BC(u32,BC(float,x0)+BC(float,x1)), p1=BC(u32,BC(float,y0)+BC(float,y1));
      auto s2=__builtin_amdgcn_permlane16_swap(p0,p1,false,false); u32 z0=s2[0], z1=s2[1]; float s=BC(float,z0)+BC(float,z1);
      float gq=silu_f(__builtin_fmaf(s,pr[3],pr[1]));
      float sy=((t&1)? bhi(yb):blo(yb))+pr[2];
      float o=__builtin_fmaf(sy,gq,pr[0]);
      asm volatile("global_store_short %0, %1, off sc1" :: "v"(op),"v"(pk(o,o)) : "memory");
      if(bid==112&&ln==0) GST(ws+W_EPOCH,tag); }
    }
  } else
  {
    FLANE(D_*2)
    bf16x8 fr[27];
    rsrc_t rsP=RSRC(ws+W_PART+cgp*12288+kq*256+w4*64,8*9*4096-2*12288);
    #pragma unroll
    for(int i=0;i<3;i++) BST128(I4,rsP,li<8? li*(9*4096)+g*16+i*4096 : OOR,0,SC1);
    if(PRE2<9) FILLD(9-PRE2,W2_,W2B);
    wso+=128u-9u*wst;
    FILLD(9,W2_,W2B);
    VMW(9); gsync(sm,C_P,4);
    #pragma unroll
    for(int ks=0;ks<9;ks++) fr[ks]=WFR(0,ks);
    gsync(sm,C_P,8); if(w4==0&&ln==0) GST(ws+W_FL+(720+6*xg+lb-8)*8,tag);
    wso+=128u-9u*wst; wld-=2*CHSZ;
    FILL(9,VW(W2C));
    int fo=W_FL+(576+((9*kq)&~1)+(ln<9? ln:9))*8;
    if(w4==0) asm volatile("global_load_dwordx2 v[112:113], %0, %1 sc1\n\ts_waitcnt vmcnt(10)" :: "v"(fo),"s"(ws) : "memory"); else VMW(9);
    gsync(sm,C_P,12);
    #pragma unroll
    for(int ks=0;ks<9;ks++) fr[9+ks]=WFR(1,ks);
    { int t2=TID; asm volatile("" : "+v"(t2)); ln=t2&63; li=ln&15; g=ln>>4; }
    const lch* hp=sm+O_TILE+(li&7)*64+g*16; int po= li<8? li*(9*4096)+g*16+4096 : OOR; PIN(hp); PIN(po);
    u32x4 rs=mkrs(ws+W_GU,2*8*F_*2);
    int of=((ln>>5)*288+18*kq+2*w4)*256+(ln&31)*16; lch* hd=sm+O_TILE+w4*512+((ln>>1)&7)*64+((ln>>4)&1)*32+(ln&1)*16+(ln>>5)*8; PIN(of); PIN(hd); asm volatile("" : "+s"(rs));
    int pha=W_HP+(ln&7)*1536+cgp*512+(kq*4+w4)*8; PIN(pha);
    DRAIN();
    if(w4==0){ u64 bad; asm volatile("v_cmp_ne_u64 %0, %1, v[112:113]" : "=s"(bad) : "s"(tag) : "memory");
      if(bad!=0ull) poll1<0>(ws+fo,tag); }
    gsync(sm,C_P,16);
    { u32 t0,cnt;
      #define LD_G "buffer_load_dwordx4 v[112:115], %[of], %[rs], 0 offen sc1\n\tbuffer_load_dwordx4 v[116:119], %[of], %[rs], 0 offen offset:2048 sc1\n\tbuffer_load_dwordx4 v[120:123], %[of], %[rs], %[so] offen sc1\n\t"
      #define HM1(a,b) "v_lshlrev_b32 %[t0], 16, v" #a "\n\tv_lshlrev_b32 %[t1], 16, v" #b "\n\tv_and_b32 v" #a ", %[hm], v" #a "\n\tv_and_b32 v" #b ", %[hm], v" #b "\n\tv_mul_f32 %[t0], %[t0], %[t1]\n\tv_mul_f32 v" #a ", v" #a ", v" #b "\n\tv_cvt_pk_bf16_f32 v" #a ", %[t0], v" #a "\n\t"
      #define HMUL(a,b,c,d,o) "v_permlane32_swap_b32 v" #a ", v" #c "\n\tv_permlane32_swap_b32 v" #b ", v" #d "\n\t" HM1(a,c) HM1(b,d) "ds_write_b64 %[hd], v[" #a ":" #b "] offset:" #o "\n\t"
      #define PHST
      u32 t1;
      __builtin_amdgcn_s_sleep(GDL);
      asm volatile(LD_G :: [of]"v"(of),[rs]"s"(rs),[so]"s"(w4==0? 4096:0) : "memory");
      fr[18]=WFR(0,0); PIN(fr[18]);
      asm volatile(POLLASM(LD_G,CHK_I0 MX2(116,117) MX2(118,119) MX2(120,121) MX2(122,123) CHK_IE,GSLP) PHST
        HMUL(112,113,114,115,0) HMUL(116,117,118,119,2048) "s_cmp_eq_u32 %[so], 0\n\ts_cbranch_scc1 8f\n\t" HMUL(120,121,122,123,4096) "8:\n\ts_mov_b64 exec, 1\n\tds_add_u32 %[ch], %[one]\n\ts_mov_b64 exec, -1"
        : [t0]"=&v"(t0),[t1]"=&v"(t1),[cnt]"=&s"(cnt) : [of]"v"(of),[rs]"s"(rs),[so]"s"(w4==0? 4096:0),[hd]"v"(hd),[hm]"s"(0xffff0000u),[ch]"v"(C_H*4),[one]"v"(1),[pa]"v"(pha),[ws]"s"(ws),"v"(hp),"v"(po) : PCLOB);
      }
    { f32x4 ac[3]={{0,0,0,0},{0,0,0,0},{0,0,0,0}};
      bf16x8 hb[9];
      gwait4<512>(C_H,4,hp,hb[0],hb[1],hb[2],hb[3]);
      #pragma unroll
      for(int ks=0;ks<10;ks++){
        if(ks<9){ ac[0]=__builtin_amdgcn_mfma_f32_16x16x32_bf16(fr[ks],hb[ks],ac[0],0,0,0); ac[1]=__builtin_amdgcn_mfma_f32_16x16x32_bf16(fr[9+ks],hb[ks],ac[1],0,0,0); }
        if(ks>0) ac[2]=__builtin_amdgcn_mfma_f32_16x16x32_bf16(fr[17+ks],hb[ks-1],ac[2],0,0,0);
        SCHED();
        if(ks<4){ fr[19+2*ks]=WFR(0,1+2*ks); fr[20+2*ks]=WFR(0,2+2*ks); } if(ks>=1&&ks<=2){ hb[2*ks+2]=LD(bf16x8,hp+(2*ks+2)*512); hb[2*ks+3]=LD(bf16x8,hp+(2*ks+3)*512); } if(ks==3) hb[8]=LD(bf16x8,hp+8*512);
        SCHED(); }
      rsrc_t r0=RSRC(ws+W_PART+cgp*12288+kq*256+w4*64-4096,8*9*4096), r2=RSRC(ws+W_PART+cgp*12288+kq*256+w4*64+4096,8*9*4096-2*12288);
      BST128(nv4(BC(u32x4,ac[0])),r0,po,0,SC1); BST128(nv4(BC(u32x4,ac[1])),rsP,po,0,SC1); BST128(nv4(BC(u32x4,ac[2])),r2,po,0,SC1); }

  }
  asm volatile("s_endpgm" : KILLIN :: "memory"); }
  {
  if(__builtin_expect(q4!=0,0)){ int grp= q4==1? nch-1 : q4-2;
    chan(sm,grp,(w4+(0x48>>(2*q4)))&3,72*xg+(q4==3? 16+lb : 2*lb+(lb<8? 0:8)+2-q4),(q4+1)>>2,ws,xa,xb_,wa,wb,dq[0]);
    asm volatile("s_endpgm" : KILLIN :: "memory"); }
  u32x4 xq[3]; f32x2 rc[3]; int xr_,xr2_;
  {
    LANE(ln); int g=ln>>4, li=ln&15, qq=li>>2, pq=li&3, j=ln&31, rl=2*w4+(ln>>5);
    lch* tile=sm+O_TILE; u32 Tr=(ln>>1)&16;
    int uo=W_U+(tb>>TL)*GST_+(tb&((1<<TL)-1))*(128>>TL)+(w4*1024<<TL)+(ln>>(3-TL))*128+(ln&(7>>TL))*16, uo1=uo+(4096<<TL), uo2=uo+(8192<<TL); PIN(uo); PIN(uo1); PIN(uo2);
    lch* tw=tile+rl*TSTR2+j*16; lch* tw2=tw+1024-j*12; u32 one=0x3f803f80u; const lch* tp=tile+qq*TSTR2+(w4*64+g*16+pq*4)*2;
    int go= ln<32? W_RG+(ln>>2)*8192+(tb*4+(ln&3))*8 : W_ST+(tb*8+(ln&7))*8; lch* kw=sm+O_RV+rl*2; const lch* kr=sm+(ln<32? O_RV+(ln&3)*4 : O_MD+(ln&7)*4); PIN(kw); PIN(kr);
    int xr=(32*(tb>>2)+(ln>>2))*(D_*2)+256*(lb&3)+64*w4+pq*16, xr2= w4==0? xr+(lb&2)*(8*D_*2)+1024+64*(lb&1)-256*(lb&3):OOR;
    rsrc_t rx=RSRC(x,L_*D_*2); u64 gm=((1ull<<(4*NRG))-1ull)|0xff00000000ull; u32 um=~UMASK, km=0x80008000u; asm volatile("" : "+s"(gm),"+s"(um),"+s"(km),"+s"(ws));
    #define D2(a,b,c) __builtin_amdgcn_fdot2_f32_bf16(BC(bf2,a),BC(bf2,b),c,false)
#define XW0 "5"
#define XW1 "2"
    asm volatile("s_waitcnt vmcnt(" XW0 ")" : "+v"(wa),"+v"(wb),"+v"(wc),"+v"(Tr),"+v"(tw),"+v"(tw2),"+v"(tp),"+v"(one),"+v"(go),"+v"(xr),"+v"(xr2) :: "memory");
    u32 wq[9]={wa[0],wa[1],wa[2],wa[3],wb[0],wb[1],wb[2],wb[3],wc}; float sw;
    { float a[3];
      #pragma unroll
      for(int i=0;i<9;i++) a[i%3]=D2(wq[i],one,i<3? 0.f:a[i%3]);
      sw=add16x(sum16((a[0]+a[1])+a[2])); }
    GETEP u32 tg=(u32)ep+1u; Tr=((upat(tg,w4)>>Tr)&0xffffu)*0x10001u; u32 tgv=tg;
    float z_[9]={0,0,0,0,0,0,0,0,0};
    asm volatile("s_waitcnt vmcnt(" XW1 ")" : "+v"(xa),"+v"(xb_),"+v"(xc),"+v"(Tr),"+v"(sw),"+v"(tgv),"+v"(z_[0]),"+v"(z_[1]),"+v"(z_[2]),"+v"(z_[3]),"+v"(z_[4]),"+v"(z_[5]) :: "memory");
    u32 v[9]={xa[0],xa[1],xa[2],xa[3],xb_[0],xb_[1],xb_[2],xb_[3],xc};
    { float xf[18], s1, s2, s3;
      #pragma unroll
      for(int i=0;i<9;i++){ z_[i&1]=D2(v[i],one,z_[i&1]); z_[2+(i&1)]=D2(v[i],v[i],z_[2+(i&1)]); z_[4+(i&1)]=D2(v[i],wq[i],z_[4+(i&1)]); }
      s1=z_[0]+z_[1]; s2=z_[2]+z_[3]; s3=z_[4]+z_[5];
      SCHED();
      #define RSTEP(C) s1+=dppf<C>(s1); s2+=dppf<C>(s2); s3+=dppf<C>(s3); SCHED();
      RSTEP(0xB1) RSTEP(0x4E) RSTEP(0x141) RSTEP(0x140)
      s1=add16x(s1)*(1.f/576.f); s2=add16x(s2)*(1.f/576.f); s3=add16x(s3);
      float mm=s1*s1, var=s2-mm;
      if(__builtin_expect(BALLOT(!(mm<=64.f*var))!=0ull,0)){
        float q=0.f;
        #pragma unroll
        for(int i=0;i<9;i++){ u32 t=v[i]; PIN(t); float d0=blo(t)-s1, d1=bhi(t)-s1; q=__builtin_fmaf(d0,d0,q); q=__builtin_fmaf(d1,d1,q); }
        var=add16x(sum16(q))*(1.f/576.f); }
      #define UNP(i) PIN(v[i]); xf[2*(i)]=blo(v[i]); xf[2*(i)+1]=bhi(v[i]); PIN(xf[2*(i)]); PIN(xf[2*(i)+1]); SCHED();
      u32 vb=pk(var,s1); UNP(0)
      float den=blo(vb)+1e-5f, m=bhi(vb); UNP(1)
      vb=pk(den,den); s3=__builtin_fmaf(-m,sw,s3); UNP(2)
      den=__builtin_amdgcn_sqrtf(bhi(vb)); UNP(3)
      u32 md=pk(m,den); UNP(4)
      float ri=rcpf_(bhi(md)); UNP(5)
      s3*=ri; float rs_=ri*0.0625f; UNP(6)
      u32 u=pk(s3,s3); float cs_=-m*rs_; UNP(7)
      { u32 sg=(u32)((int)u>>31); PIN(sg); u^=sg|km; } UNP(8)
      asm volatile("s_mov_b64 exec, 0x1\n\ts_mov_b32 exec_hi, 1\n\tds_write_b16 %0, %1\n\tv_add_u32 %0, %0, %0\n\tds_write_b32 %0, %2 offset:%3\n\ts_mov_b32 exec_hi, 0\n\tds_add_u32 %4, %5\n\ts_mov_b64 exec, -1" : "+v"(kw) : "v"(u),"v"(md),"i"(O_MD-2*O_RV),"v"(C_I*4),"v"(1) : "memory");
      if(w4==3){ gwait<0>(sm,C_I,4); asm volatile("ds_read_b32 v118, %4\n\tv_mov_b32 v119, %1\n\ts_mov_b64 exec, %3\n\ts_waitcnt lgkmcnt(0)\n\tglobal_store_dwordx2 %0, v[118:119], %2 sc1\n\ts_mov_b64 exec, -1" :: "v"(go),"v"(tgv),"s"(ws),"s"(gm),"v"(kr) : "memory"); }
      SCHED();
      u32 h[9];
      #define UH(i) h[i]=(pkz(__builtin_fmaf(xf[2*(i)],rs_,cs_),__builtin_fmaf(xf[2*(i)+1],rs_,cs_))&um)|Tr;
      UH(0) UH(1) UH(2) UH(3) ST(u32x4,tw)=u32x4{h[0],h[1],h[2],h[3]}; UH(4) UH(5) UH(6) UH(7) ST(u32x4,tw+512)=u32x4{h[4],h[5],h[6],h[7]};
      UH(8) ST(u32,tw2)=h[8]; }
    gsync(sm,C_L,4);
    { u32x4 uq[3];
      #pragma unroll
      for(int z=0;z<3;z++) uq[z]=BC(u32x4,trfrag(tp+z*512,tp+z*512+4*TSTR2));
      asm volatile("global_store_dwordx4 %0, %1, %2 sc1" :: "v"(uo),"v"(uq[0]),"s"(ws) : "memory");
      asm volatile("global_store_dwordx4 %0, %1, %2 sc1" :: "v"(uo1),"v"(uq[1]),"s"(ws) : "memory");
      asm volatile("s_cmp_lg_u32 %3, 0\n\ts_cbranch_scc1 1f\n\tglobal_store_dwordx4 %0, %1, %2 sc1\n1:\n\ts_nop 1" :: "v"(uo2),"v"(uq[2]),"s"(ws),"s"(w4),"v"(uq[0]),"v"(uq[1]) : "memory","scc");
      asm volatile("" : "+v"(dq[0]),"+v"(dq[1]));
      xr_=xr; xr2_=xr2;
      #define XQ { rsrc_t rx_=RSRC(x,L_*D_*2); xq[0]=BLD128(rx_,xr_,0,0); xq[1]=BLD128(rx_,xr_,16*D_*2,0); xq[2]=BLD128(rx_,xr2_,0,0); }
    }
    if(lb>=2&&lb<8){
      const u16* wm=(lb&1)? wu:wg; int co=(3-(int)((u64)wm>>7)-xg)&7, r_=576-12*PFN+(((lb>>1)-1)*4+w4)+12*j;
      asm volatile("buffer_load_dword v120, %0, %1, 0 offen" :: "v"(j<PFN&&(ln<32||co==0)? r_*(F_*2)+(9*xg+co)*128+(ln>>5)*1024 : OOR),"s"(mkrs(wm,D_*F_*2)) : "memory"); }
    if(lb>=8&&lb<14) garrive(sm,C_D);
  }
  FP fp; u32 ti,td;
  { LANE(ln); u64 tag=ep+1ull;
    int so_=W_ST+(32*(tb>>2)+(ln>>2))*8, fo_=W_FL+((ln>=32&&w4==0? 512+32*(lb&1) : 128*(lb&3)+32*w4)+(ln&31))*8; u32x4 rx=mkrs(x,L_*D_*2); PIN(so_); PIN(fo_);
    XR X; X.so=so_; X.x0=xr_; X.x2=xr2_; X.rx=rx;
    if(lb==CTLB||lb>=14){
      u32 M=(u32)tag, rank,key; int j=ln&31, j16=j*16, j12=j*12, st0=W_ST; lch* xw=sm+O_TILE+(2*w4+(ln>>5))*XSTR+j*16; lch* rcw=sm+O_RC+(2*w4+(ln>>5))*4; PIN(rcw); PIN(j12); PIN(xw); PIN(j16); asm volatile("" : "+s"(rx),"+s"(st0));
      top8(sm,w4,ln,M,ws,rank,key,ldw,X);
      {
        int t0=__builtin_amdgcn_readlane((int)key,__builtin_ctzll(BALLOT(rank==(u32)(2*w4)))), t1=__builtin_amdgcn_readlane((int)key,__builtin_ctzll(BALLOT(rank==(u32)(2*w4+1))));
        int tsel=~((ln>>5)? t1:t0)&2047, ro=tsel*1152+j16, sr=tsel*8+st0;
        u32x4 a,b; u32 c; u64 f1;
        asm volatile("buffer_load_dwordx4 %[a], %[ro], %[rx], 0 offen\n\tbuffer_load_dwordx4 %[b], %[ro], %[rx], 0 offen offset:512\n\tbuffer_load_dword %[c], %[r2], %[rx], 0 offen offset:1024\n\tglobal_load_dwordx2 %[f], %[sr], %[ws] sc1\n\t"
          "s_mov_b64 exec, 1\n\tds_add_u32 %[cd], %[one]\n\ts_mov_b64 exec, -1\n\ts_waitcnt vmcnt(0)"
          : [a]"=&v"(a),[b]"=&v"(b),[c]"=&v"(c),[f]"=&v"(f1) : [ro]"v"(ro),[r2]"v"(ro-j12),[sr]"v"(sr),[cd]"v"(C_D*4),[one]"v"(1),[rx]"s"(rx),[ws]"s"(ws) : "memory");
        if(__builtin_expect(BALLOT((u32)(f1>>32)!=(u32)tag)!=0ull,0)){ f1=poll1<32>(ws+sr,tag<<32); __builtin_amdgcn_s_waitcnt(0x0f70); }
        ST(u32x4,xw)=a; ST(u32x4,xw+512)=b; ST(u32,xw+1024-j12)=c; ST(u32,rcw)=(u32)f1;
        garrive(sm,C_X);
        if(w4==0){ if(rank<8) ST(u16,sm+O_IDX+(ln&1)*16+rank*2)=(u16)((ln&1)? key>>16 : ~key&2047u); garrive(sm,C_IX); }
        if(lb==CTLB&&rank<8&&!(ln&1)) GST(ws+W_IDX+xg*1024+w4*128+rank*8,(tag<<16)|(~key&2047u));
        PRIO(FPB);
        if(XB!=2){ if(lb>=14&&XB==0) gwait<4>(sm,C_H,4); xreq(X,ws); }
      }
    } else { PRIO(0); if(lb<8? XA:XC){ gwait<4>(sm,C_G,7); __builtin_amdgcn_s_sleep(XC); } else gwait<8>(sm,C_U,2);
      xreq(X,ws); if(lb<8) PRIO(FPA); else PRIO(FPC); }
    if(AN){ u32 e=now_()-ldw, nm= lb<8&&lb!=CTLB? ANA : (lb>=8&&lb<14? ANC:AN); if(e>nm) ldw+=e-nm+MG; }
    { u64 fs,fs1;
      asm volatile("s_waitcnt vmcnt(0)" : "+{v[96:99]}"(X.q[0]),"+{v[100:103]}"(X.q[1]),"+{v[104:107]}"(X.q[2]),"+{v[108:111]}"(X.q[3]) :: "memory");
      fs=(u64)X.q[3][0]|((u64)X.q[3][1]<<32); fs1=(u64)X.q[3][2]|((u64)X.q[3][3]<<32);
      if(__builtin_expect(BALLOT((u32)(fs>>32)!=(u32)tag||(u32)(fs1>>32)!=(u32)tag)!=0ull,0)){ fs=poll1<32>(ws+so_,tag<<32); fs1=poll1<32>(ws+so_+128,tag<<32); __builtin_amdgcn_s_waitcnt(0x0f70); }
      float ri=rcpf_(bhi((u32)fs)), rj=rcpf_(bhi((u32)fs1)); rc[0]=f32x2{ri,-blo((u32)fs)*ri}; rc[1]=f32x2{rj,-blo((u32)fs1)*rj}; rc[2]= (lb&2)? rc[1]:rc[0];
      #pragma unroll
      for(int z=0;z<3;z++) xq[z]=X.q[z]; }
    bool ix= w4==3&&lb<14&&lb!=CTLB; int po=W_IDX+(xg*8+(lb&3))*128+(ln&7)*8;
    int ho_=fo_+(W_HT-W_FL);
    final_pre<0,1>(fp,w4,xq,dq,rc);
    ti=now_()-ldw;
    asm volatile("global_load_dwordx2 v[112:113], %0, %2 sc1\n\tglobal_load_dwordx2 v[114:115], %1, %2 sc1\n\tv_mov_b32 v117, %5\n\ts_cmp_lt_u32 %4, " STR(HT) "\n\ts_cbranch_scc1 1f\n\tglobal_load_dwordx2 v[116:117], %3, %2 sc1\n1:"
      :: "v"(fo_),"v"(ix? po:fo_),"s"(ws),"v"(ho_),"s"(ti),"s"(~(u32)tag) : "memory","scc");
    final_pre<1,3>(fp,w4,xq,dq,rc); PRIO(FPE);
    { u64 ff,f; u32x2 h;
      asm volatile("s_waitcnt vmcnt(0)\n\tv_mov_b64 %0, v[112:113]\n\tv_mov_b64 %1, v[114:115]\n\tv_mov_b64 %2, v[116:117]" : "=v"(ff),"=v"(f),"=v"(h) :: "memory");
      _Pragma("unroll 1") for(int k=0;k<2;k++){ if(BALLOT(h[1]!=(u32)tag)==0ull||ti>=H2) break;
        tgate2(ldw,ti<HT? HT:H2); ti=now_()-ldw; if(ti>=HL){ h=u32x2{ldw,(u32)tag}; break; }
        asm volatile("global_load_dwordx2 %0, %1, %2 sc1\n\ts_waitcnt vmcnt(0)" : "=&v"(h) : "v"(ho_),"s"(ws) : "memory"); }
      u32 rel= h[1]==(u32)tag? h[0]-ldw : ti+HM; td=umin_(wmax(rel),ti+160u); if(td>HK) td-=(td-HK)>>HS;
      if(__builtin_expect(BALLOT(ff!=tag)!=0ull,0)) poll1<0>(ws+fo_,tag);
      if(ix){ if(BALLOT(((f^(tag<<16))>>16)!=0ull)!=0ull) f=poll1<16>(ws+po,tag<<16);
        if(ln<8) ST(u16,sm+O_IDX+ln*2)=(u16)f; garrive(sm,C_IX); } }
  }
  final_post(sm,fp,w4,nch,out,ws,xr_,ldw,td);
  }
}

extern "C" void kernel_launch(void* const* d_in, const int* in_sizes, int n_in, void* d_out, int out_size, void* d_ws, size_t ws_size, hipStream_t stream) {
  const u16** I=(const u16**)d_in;
  KP kp; kp.Are=I[1]; kp.Aim=I[2]; kp.Bre=I[3]; kp.Bim=I[4]; kp.Cre=I[5]; kp.Cim=I[6]; kp.ldt=I[7]; kp.pad=I[7];
  kA<<<256,1024,0,stream>>>(I[0],(char*)d_ws,I[10],I[8],I[11],I[12],I[13],(u16*)d_out,kp);
}
```

```cpp
#include <hip/hip_runtime.h>
#include <hip/hip_bf16.h>
#include <stdint.h>
#if defined(__HIP_DEVICE_COMPILE__)
#define NOPK __attribute__((target("no-packed-fp32-ops")))
#else
#define NOPK
#endif
#define DEV __device__ NOPK __forceinline__
typedef unsigned short u16;
typedef unsigned int u32;
typedef unsigned long long u64;
typedef short bf16x8 __attribute__((ext_vector_type(8)));
typedef _Float16 h8 __attribute__((ext_vector_type(8)));
typedef _Float16 h2 __attribute__((ext_vector_type(2)));
typedef float f32x4 __attribute__((ext_vector_type(4)));
typedef float f32x2 __attribute__((ext_vector_type(2)));
typedef u32 u32x4 __attribute__((ext_vector_type(4)));
typedef u32 u32x2 __attribute__((ext_vector_type(2)));
typedef u32 u32x8 __attribute__((ext_vector_type(8)));
typedef __bf16 bf2 __attribute__((ext_vector_type(2)));
typedef short v4s __attribute__((ext_vector_type(4)));
typedef __amdgpu_buffer_rsrc_t rsrc_t;
#define GAS __attribute__((address_space(1)))
#define LAS __attribute__((address_space(3)))
typedef LAS char lch;
#define L_ 2048
#define D_ 576
#define F_ 4608
#define BC(T,v) __builtin_bit_cast(T,v)
#define TID ((int)__builtin_amdgcn_workitem_id_x())
#define BID ((int)__builtin_amdgcn_workgroup_id_x())
#define RFL(x) __builtin_amdgcn_readfirstlane(x)
#define RLX_AGENT __ATOMIC_RELAXED, __HIP_MEMORY_SCOPE_AGENT
#define LD(T,p) (*(const LAS T*)(p))
#define ST(T,p) (*(LAS T*)(p))
#define SPINCAP (1<<17)
#define DRAIN() asm volatile("s_waitcnt vmcnt(0)" ::: "memory")
#define GLD(p) __hip_atomic_load((const GAS u64*)(p),RLX_AGENT)
#define GST(p,v) __hip_atomic_store((GAS u64*)(p),(u64)(v),RLX_AGENT)
#define BALLOT(c) __builtin_amdgcn_ballot_w64(c)
#define SC1 16
#define LANE(l) int l; { int t2_=TID; asm volatile("" : "+v"(t2_)); l=t2_&63; }
#define XB 1
#define XBW "0"
#define UE 0
#define UN 4
#define SCHED() __builtin_amdgcn_sched_barrier(0)
#define RSRC(p,n) __builtin_amdgcn_make_buffer_rsrc((void*)(p),0,(n),0x00020000)
#define BLD128 __builtin_amdgcn_raw_buffer_load_b128
#define BLD32 __builtin_amdgcn_raw_buffer_load_b32
#define BST128 __builtin_amdgcn_raw_buffer_store_b128
#define PRIO(n) __builtin_amdgcn_s_setprio(n)

DEV float b2f(u32 v){ return BC(float,v<<16); }
DEV float blo(u32 v){ return BC(float,v<<16); }
DEV float bhi(u32 v){ return BC(float,v&0xffff0000u); }
DEV u32 pk(float a,float b){ f32x2 v={a,b}; bf2 r=__builtin_convertvector(v,bf2); return BC(u32,r); }
DEV u32 pkh(float a,float b){ f32x2 v={a,b}; h2 r=__builtin_convertvector(v,h2); return BC(u32,r); }
DEV float rb(float f){ return BC(float,pk(f,f)<<16); }
DEV float rcpf_(float x){ return __builtin_amdgcn_rcpf(x); }
DEV float expf_(float x){
  const float L2E=1.4426950216293335f, L2El=1.9259629911266175e-8f; float h=x*L2E; float l=__builtin_fmaf(x,L2E,-h)+x*L2El;
  float e=__builtin_amdgcn_exp2f(h); return __builtin_fmaf(e*l,0.6931471805599453f,e); }
DEV float sigm_f(float x){ return rcpf_(1.f+__builtin_amdgcn_exp2f(-x*1.4426950408889634f)); }
DEV float silu_f(float x){ return x*sigm_f(x); }
DEV u32 umax_(u32 a,u32 b){ return a>b?a:b; }
DEV u32 umin_(u32 a,u32 b){ return a<b?a:b; }

#define YSH 3
#define PRE1 6
#define PRE3 4
#define PRE2 5
#define W1 5
#define W3 3
#define W2_ 4
#define W1B 20
#define W2B 12
#define LAST1 4
#define RGATE 4
#define UGATE 7
#define R2D 12
#define PFN 32
#define CPF 8
#define HD0 174
#define HD1 148
#define HD3 165
#define HM 100
#define HT 555
#define H2 600
#define HL 650
#define HK 660
#define HKR 65
#define HR 12
#define ANA 410
#define ANB 470
#define ANC 540
#define NRG 8
#define W_EPOCH 0
#define W_RG 16384
#define W_ST (W_RG+32*8192)
#define W_U (W_ST+32768)
#define UST (D_*16+128)
#define YST (L_*2+128)
#define TL 1
#define GST_ ((D_*16<<TL)+128)
#define W_Y (W_U+256*UST)
#define W_PART (W_Y+D_*YST)
#define W_GU (W_PART+8*9*16*256)
#define W_FL 9216
#define W_HT (W_RG+16*8192)
#define W_IDX 1024
#define CTLB 0
struct KP { const u16 *Are,*Aim,*Bre,*Bim,*Cre,*Cim,*ldt,*pad; };

template<int CTRL> DEV float dppf(float v){ return BC(float,__builtin_amdgcn_update_dpp(0u,BC(u32,v),CTRL,0xf,0xf,false)); }
template<int CTRL> DEV u32 dppu(u32 v){ return (u32)__builtin_amdgcn_update_dpp(0u,v,CTRL,0xf,0xf,false); }
DEV float sum16(float v){ v+=dppf<0xB1>(v); v+=dppf<0x4E>(v); v+=dppf<0x141>(v); v+=dppf<0x140>(v); return v; }
DEV float add16x(float v){ auto r=__builtin_amdgcn_permlane16_swap(BC(u32,v),BC(u32,v),false,false); u32 a=r[0], b=r[1]; return BC(float,a)+BC(float,b); }
DEV u32 wmax(u32 v){
  v=umax_(v,(u32)__builtin_amdgcn_update_dpp(0u,v,0x111,0xf,0xf,false));
  v=umax_(v,(u32)__builtin_amdgcn_update_dpp(0u,v,0x112,0xf,0xf,false));
  v=umax_(v,(u32)__builtin_amdgcn_update_dpp(0u,v,0x114,0xf,0xf,false));
  v=umax_(v,(u32)__builtin_amdgcn_update_dpp(0u,v,0x118,0xf,0xf,false));
  v=umax_(v,(u32)__builtin_amdgcn_update_dpp(0u,v,0x142,0xa,0xf,false));
  v=umax_(v,(u32)__builtin_amdgcn_update_dpp(0u,v,0x143,0xc,0xf,false));
  return (u32)__builtin_amdgcn_readlane((int)v,63); }

DEV void garrive(lch* sm,int c){ u32 ad=(u32)(size_t)sm+c*4, one=1;
  asm volatile("s_mov_b64 exec, 1\n\tds_add_u32 %0, %1\n\ts_mov_b64 exec, -1" :: "v"(ad),"v"(one) : "memory"); }
template<int SLP> DEV void gwait(lch* sm,int c,u32 target){ u32 ad=(u32)(size_t)sm+c*4, tv, cnt, sv; target=RFL(target);
  asm volatile("s_mov_b32 %1, 0x20000\n"
    "1:\n\tds_read_b32 %0, %3\n\ts_waitcnt lgkmcnt(0)\n\tv_readfirstlane_b32 %2, %0\n\ts_cmp_ge_u32 %2, %4\n\ts_cbranch_scc1 2f\n\ts_sleep %5\n\ts_sub_u32 %1, %1, 1\n\ts_cbranch_scc0 1b\n"
    "2:" : "=&v"(tv),"=&s"(cnt),"=&s"(sv) : "v"(ad),"s"(target),"i"(SLP) : "memory","scc"); }
DEV u32 now_(){ u32 t; asm volatile("s_memrealtime s[98:99]\n\ts_waitcnt lgkmcnt(0)\n\ts_mov_b32 %0, s98" : "=s"(t) :: "memory"); return t; }
DEV void tgate2(u32 t0,u32 d){ u32 cnt; asm volatile("s_mov_b32 %0, 100\n1:\n\ts_memrealtime s[98:99]\n\ts_waitcnt lgkmcnt(0)\n\ts_sub_u32 s98, s98, %1\n\ts_cmp_ge_u32 s98, %2\n\ts_cbranch_scc1 2f\n\ts_sleep 1\n\ts_sub_u32 %0, %0, 1\n\ts_cbranch_scc0 1b\n2:" : "=&s"(cnt) : "s"(t0),"s"(d) : "memory","scc"); }
DEV void gsync(lch* sm,int c,u32 target){ garrive(sm,c); gwait<0>(sm,c,target); }
template<int SH> DEV u64 poll1(const char* p,u64 want){ u64 f=GLD(p);
  _Pragma("unroll 1") for(int sp=0;sp<SPINCAP;sp++){ bool bad=((f^want)>>SH)!=0ull; if(BALLOT(bad)==0ull) break; __builtin_amdgcn_s_sleep(1); if(bad) f=GLD(p); }
  __builtin_amdgcn_fence(__ATOMIC_ACQUIRE,"wavefront"); return f; }
#define STR_(x) #x
#define STR(x) STR_(x)
DEV u32x4 mkrs(const void* p,u32 n){ u64 a=(u64)p; return u32x4{(u32)RFL((int)(u32)a),(u32)RFL((int)((u32)(a>>32)&0xffffu)),n,0x00020000u}; }
#define VMW(n) asm volatile("s_waitcnt vmcnt(%0)" :: "i"(n) : "memory")
#define YSLP 1
#define GSLP 1
#define QSLP 1
#define PSLP 3
#define POLLASM(LOADS,CHECK,K) "s_mov_b32 %[cnt], 0x20000\n1:\n\ts_waitcnt vmcnt(0)\n\t" CHECK "s_cbranch_vccz 9f\n\ts_sleep " STR(K) "\n\ts_mov_b64 exec, vcc\n\t" LOADS \
  "s_mov_b64 exec, -1\n\ts_sub_u32 %[cnt], %[cnt], 1\n\ts_cbranch_scc0 1b\n9:\n\t"
#define LD_DUP2 "buffer_load_dwordx4 v[112:115], %[of], %[rs], 0 offen sc1\n\tbuffer_load_dwordx4 v[116:119], %[of], %[rs], %[so] offen sc1\n\t"
#define MX2(a,b) "v_max3_u32 %[t0], %[t0], v" #a ", v" #b "\n\t"
#define CHK_I0 "v_max_u32 %[t0], v112, v113\n\t" MX2(114,115)
#define CHK_IE "v_cmp_eq_u32 vcc, -1, %[t0]\n\t"
#define PCLOB "memory","vcc","scc"
#define UMASK 0x40074007u
DEV u32 upat(u32 tag,int k){ return ((((tag*0xea959c21u)>>(8*k))&255u)*0x04000801u)&UMASK; }
DEV u32 pkz(float a,float b){ return BC(u32,__builtin_amdgcn_cvt_pkrtz(a,b)); }
#define INV 0xffffffffu
DEV u32 nv(u32 v){ return umin_(v,0xfffeffffu); }
DEV u32x4 nv4(u32x4 v){ return u32x4{nv(v[0]),nv(v[1]),nv(v[2]),nv(v[3])}; }
#define OOR 0x40000000

#define CHSZ 36864
#define O_CH 512
#define O_CND 256
#define SLSZ 38080
#define O_R2 (O_CH+2*SLSZ)
#define O_X2 (O_R2+CHSZ)
#define O_TILE (O_X2+CHSZ)
#define XSTR 1216
#define TSTR2 1216
#define O_ONE (O_TILE+9728+16)
#define O_CAND (O_ONE+1168+48)
#define LDS_TOTAL (O_CAND+128)
#define C_G 0
#define C_U 8
#define C_L 4
#define C_D 5
#define C_X 12
#define C_P 13
#define C_H 14
#define C_I 17
#define C_E 18
#define C_IX 19
#define C_RS 22
#define O_IDX 96
#define O_EXP 144
#define O_RMX 192
#define O_RV 128
#define O_MD 400
#define O_RC (O_CAND+64)

struct C2 { float r,i; };
DEV C2 cmul(C2 a,C2 b){ C2 c; c.r=__builtin_fmaf(a.r,b.r,-(a.i*b.i)); c.i=__builtin_fmaf(a.r,b.i,a.i*b.r); return c; }
DEV h8 ldA(const lch* p){ u32x2 a=*(const volatile LAS u32x2*)p, b=*(const volatile LAS u32x2*)(p+8); return BC(h8,(u32x4{a[0],a[1],b[0],b[1]})); }
DEV bf16x8 trfrag(const lch* p0,const lch* p1){
  u32x2 b0=BC(u32x2,__builtin_amdgcn_ds_read_tr16_b64_v4i16((LAS v4s*)p0)), b1=BC(u32x2,__builtin_amdgcn_ds_read_tr16_b64_v4i16((LAS v4s*)p1));
  return BC(bf16x8,(u32x4{b0[0],b0[1],b1[0],b1[1]})); }

struct FP { float gg[24], bs[24]; };
template<int Z0,int Z1> DEV void final_pre(FP& f,int w4,const u32x4* xq,const u32x4* dq,const f32x2* rc){
  #pragma unroll
  for(int z=Z0;z<Z1;z++) if(z<2||w4==0){ float ea=rc[z][0]*-1.4426950408889634f, eb=rc[z][1]*-1.4426950408889634f;
    #pragma unroll
    for(int e=0;e<4;e++){ u32 xw=xq[z][e], dw=dq[z>>1][e]; int i=8*z+2*e; float a=blo(xw), b=bhi(xw);
      float pa=__builtin_fmaf(a,rc[z][0],rc[z][1]), pb=__builtin_fmaf(b,rc[z][0],rc[z][1]);
      float ga=pa*rcpf_(1.f+__builtin_amdgcn_exp2f(__builtin_fmaf(a,ea,eb))), gb=pb*rcpf_(1.f+__builtin_amdgcn_exp2f(__builtin_fmaf(b,ea,eb)));
      f.gg[i]=ga; f.gg[i+1]=gb; f.bs[i]=__builtin_fmaf(pa*blo(dw),ga,a); f.bs[i+1]=__builtin_fmaf(pb*bhi(dw),gb,b);
      asm volatile("" : "+v"(f.gg[i]),"+v"(f.gg[i+1]),"+v"(f.bs[i]),"+v"(f.bs[i+1])); } }
}
#define FTS 544
DEV u32 pkmin(u32 a,u32 b){ u32 r; asm("v_pk_min_u16 %0, %1, %2" : "=v"(r) : "v"(a),"v"(b)); return r; }
DEV void final_post(lch* sm,const FP& f,int w4,u16* __restrict__ out,char* ws,int xr,u32 tk,u32 td){
  LANE(ln); int bid=BID, xg=bid&7, lb=bid>>3, cg=lb&3, T=8*xg+(lb>>2), ob[3];
  int yo=(128*cg+32*w4+(ln>>2)*2)*YST+(32*T+8*(ln&3))*2, yo3= ln<32? (512+32*(cg&1)+(ln&30))*YST+(32*T+16*(cg>>1)+8*(ln&1))*2 : OOR;
  lch* fb=sm+O_X2+2048+w4*4608; lch* wp=fb+(ln&3)*FTS+(ln&60); lch* wp3=fb+(4+(ln&1)+(ln>>5)*2)*FTS+(ln&30)*2; const lch* tp=fb+(ln>>5)*FTS+(ln&31)*16;
  u32x4 rs=mkrs(ws+W_Y,D_*YST); rsrc_t ro=RSRC(out,L_*D_*2); u32 tt=(u32)(32*T+(ln>>2))*0x10001u;
  asm volatile("" : "+v"(yo),"+v"(yo3),"+v"(wp),"+v"(wp3),"+v"(tp),"+v"(tt),"+s"(rs));
  #define RMASK { gwait<2>(sm,C_IX,1); u32x4 iq=LD(u32x4,sm+O_IDX); \
    _Pragma("unroll") for(int z=0;z<2;z++){ u32 t2=tt+z*0x100010u, m=pkmin(pkmin(iq[0]^t2,iq[1]^t2),pkmin(iq[2]^t2,iq[3]^t2)); ob[z]= umin_(m&0xffffu,m>>16)==0u? OOR : xr+z*(16*D_*2); } \
    ob[2]=((cg&2)? ob[1]:ob[0])+(512+32*(cg&1)-128*cg)*2; asm volatile("" : "+v"(ob[0]),"+v"(ob[1]),"+v"(ob[2])); }
  tgate2(tk,td);
  if(lb>=8) gwait<1>(sm,C_H,4);
  PRIO(3);
  { u32 t0,t1,cnt;
    #define LD_Y "buffer_load_dwordx4 v[112:115], %[of], %[rs], 0 offen sc1\n\tbuffer_load_dwordx4 v[116:119], %[of], %[rs], %[s1] offen sc1\n\ts_cmp_lg_u32 %[w], 0\n\ts_cbranch_scc1 5f\n\t" \
      "buffer_load_dwordx4 v[120:123], %[o3], %[rs], 0 offen sc1\n\tbuffer_load_dwordx4 v[124:127], %[o3], %[rs], %[s1] offen sc1\n5:\n\t"
    #define YIN [of]"v"(yo),[rs]"s"(rs),[s1]"s"(YST),[o3]"v"(yo3),[w]"s"(w4)
    asm volatile(LD_Y :: YIN : "memory","scc");
    RMASK
    #define YW(p,a,b,o) "v_perm_b32 %[t0], v" #b ", v" #a ", %[lo]\n\tv_perm_b32 %[t1], v" #b ", v" #a ", %[hi]\n\tds_write2_b32 %[" #p "], %[t0], %[t1] offset0:" #o " offset1:" #o "+16\n\t"
    asm volatile(POLLASM(LD_Y,CHK_I0 MX2(116,117) MX2(118,119) "s_cmp_lg_u32 %[w], 0\n\ts_cbranch_scc1 6f\n\t" MX2(120,121) MX2(122,123) MX2(124,125) MX2(126,127) "6:\n\t" CHK_IE,YSLP)
      YW(wp,112,116,0) YW(wp,113,117,32) YW(wp,114,118,64) YW(wp,115,119,96) "s_cmp_lg_u32 %[w], 0\n\ts_cbranch_scc1 7f\n\t" YW(w3,120,124,0) YW(w3,121,125,32) YW(w3,122,126,64) YW(w3,123,127,96) "7:"
      : [t0]"=&v"(t0),[t1]"=&v"(t1),[cnt]"=&s"(cnt) : YIN,[wp]"v"(wp),[w3]"v"(wp3),[lo]"s"(0x05040100u),[hi]"s"(0x07060302u) : PCLOB); }
  u32x4 r[3];
  #pragma unroll
  for(int z=0;z<3;z++) r[z]=LD(u32x4,tp+z*(2*FTS));
  #pragma unroll
  for(int z=0;z<3;z++) if(z<2||w4==0){ u32x4 ov;
    #pragma unroll
    for(int e=0;e<4;e++){ u32 rw=r[z][e]; int i=8*z+2*e; ov[e]=pk(__builtin_fmaf(blo(rw),f.gg[i],f.bs[i]),__builtin_fmaf(bhi(rw),f.gg[i+1],f.bs[i+1])); }
    BST128(ov,ro,ob[z],0,SC1); SCHED(); }
}

#define TROW 528
#define O_TB 4640
#define O_DE 34752
#define O_DO 35968
#define O_R2K 37120
#define O_R3K 37632
#define O_DUM 37952
#define O_RED O_TB
#define MH(a,b,c) __builtin_amdgcn_mfma_f32_16x16x32_f16(a,b,c,0,0,0)
#define PIN(x) asm volatile("" : "+v"(x))
DEV h8 ld4w(const lch* p){ return BC(h8,(u32x4{LD(u32,p),LD(u32,p+4),LD(u32,p+8),LD(u32,p+12)})); }
DEV u32 __umul24_(u32 a,u32 b){ u32 r; asm("v_mul_u32_u24 %0, %1, %2" : "=v"(r) : "s"(b),"v"(a)); return r; }
DEV u32x4 band(u32 mask,int g,u32 val){ u32 by=__builtin_amdgcn_ubfe(mask,8*g,8), X=by|(by<<15), k=0x10001u; u32x4 r; asm volatile("" : "+s"(k),"+s"(val));
  #pragma unroll
  for(int d=0;d<4;d++) r[d]=__umul24_((X>>(2*d))&k,val);
  return r; }
DEV h8 zpack(f32x4 e,f32x4 o){ return BC(h8,(u32x4{pkh(e[0],o[0]),pkh(e[1],o[1]),pkh(e[2],o[2]),pkh(e[3],o[3])})); }
struct C3 { float r,i,n; };
DEV C3 c3(C2 a){ C3 c; c.r=a.r; c.i=a.i; PIN(c.i); c.n=-c.i; PIN(c.n); return c; }
DEV C2 cm(C2 a,C3 b){ C2 c; c.r=__builtin_fmaf(a.r,b.r,a.i*b.n); c.i=__builtin_fmaf(a.r,b.i,a.i*b.r); return c; }
DEV u32 ph(C2 a){ return pkh(a.r,a.i); }
#define W2(p,a,b) asm volatile("ds_write2_b32 %0, %1, %2 offset1:132" :: "v"(p),"v"(a),"v"(b) : "memory")
struct CP { const lch *x0,*x1,*bd; lch *p0,*p1,*p2,*wp; u32x4 Be,Bo,AE; int yo; };
template<int ROLE> DEV void lanes(CP& c,lch* cb,int ln){ int m=ln&15, g=ln>>4;
  const lch* ad=cb+((m&1)? O_DO+2*527 : O_DE+2*536)+2*(8*g-m);
  const lch* bd=cb+2*(256+16*m+8*g); lch* dum=cb+O_DUM;
  if(ROLE==0){ int l0=128*(g>>1)+8*m+4*(g&1); c.p0=cb+O_DE+(549-l0)*2; PIN(c.p0); c.p1=c.p0+6; PIN(c.p1); c.p2=c.p0+(O_DO-O_DE-18); c.x0=ad-640; c.bd=bd-64; c.x1=cb+O_R3K+2*(126-2*m+8*g)-192; PIN(c.x1);
    c.Be=band(0x1ffu<<(m+4),g,0x3c00u); PIN(c.Be); }
  if(ROLE==1){ lch* r=cb+O_R2K+(192-64*(g-2)-2*m)*2; c.p0= g<2? dum : r-66; c.p1= ln==0? cb+O_DE+(552-256)*2 : (ln<=32? dum : r); c.p2=cb+O_DE+(544-256-8*ln)*2; c.x0=ad-128-640; c.bd=bd-128-128; PIN(c.p0); PIN(c.p1); PIN(c.p2); }
  if(ROLE==2){ int i0=16*m+4*g; c.p0= m<8? cb+O_R3K+(125-i0)*2 : dum+2; c.p1= ln==0? cb+O_R2K+128 : (ln==8? cb+O_R3K : c.p0+6); c.x0=ad-320-640; c.bd=bd-320-128; PIN(c.p0); PIN(c.p1); }
  if(ROLE==3){ c.x0=cb+O_R2K+2*(188-4*m+8*g)-256; c.bd=cb+2*(248+8*m+8*g);
    c.Be=band(0x1fu<<(m+6),g,0x3c00u); c.Bo=band(0x7u<<(m+11),g,0x3c00u); PIN(c.Be); PIN(c.Bo); }
  PIN(c.x0); PIN(c.bd); }
template<int ROLE> DEV void lanes2(CP& c,lch* cb,int ln){ int m=ln&15, g=ln>>4;
  if(ROLE==0){ c.Bo=band(0x7fu<<(m+5),g,0x3c00u); c.AE=band(3u<<(m+12),g,0xffffu); PIN(c.Bo); PIN(c.AE); }
  c.wp=cb+O_RED+ln*16; c.yo=(g&1)*512+m*32+(g>>1)*16; PIN(c.wp); PIN(c.yo); }
template<int ROLE> DEV void chan2(lch* sm,lch* cb,const lch*& fa,const lch*& fb,int grp,int d,int one_,int emax,u64 tag,char* ws){
  CP c;
  const f32x4 Z4={0,0,0,0}; h8 A[6]; f32x4 acc[8];
  #define LD_U "buffer_load_dwordx4 v[112:115], %[of], %[rs], 0 offen sc1\n\tbuffer_load_dwordx4 v[116:119], %[of], %[rs], %[s1] offen sc1\n\tbuffer_load_dwordx4 v[120:123], %[of], %[rs], %[s2] offen sc1\n\tbuffer_load_dwordx4 v[124:127], %[of], %[rs], %[s3] offen sc1\n\t"
  #define UC(t,a,b,c) "v_bitop3_b32 " t ", v" #a ", v" #b ", %[" #c "] bitop3:0x7e\n\t"
  #define UD(t,a,c) "v_bitop3_b32 " t ", " t ", v" #a ", %[" #c "] bitop3:0xf6\n\t"
  #define CHK_U UC("%[t0]",112,116,c0) UC("%[t1]",113,117,c1) UD("%[t0]",120,c0) UD("%[t1]",121,c1) UD("%[t0]",124,c0) UD("%[t1]",125,c1) UD("%[t0]",114,c2) UD("%[t1]",115,c3) UD("%[t0]",118,c2) UD("%[t1]",119,c3) \
    UD("%[t0]",122,c2) UD("%[t1]",123,c3) UD("%[t0]",126,c2) UD("%[t1]",127,c3) "v_bitop3_b32 %[t0], %[t0], %[t1], %[mk] bitop3:0xa8\n\tv_cmp_ne_u32 vcc, 0, %[t0]\n\t"
  #define UX(a,b,c,d,o) "v_xor_b32 v" #a ", %[e0], v" #a "\n\tv_xor_b32 v" #b ", %[e1], v" #b "\n\tv_xor_b32 v" #c ", %[e2], v" #c "\n\tv_xor_b32 v" #d ", %[e3], v" #d "\n\tds_write_b128 %[up], v[" #a ":" #d "] offset:" #o "\n\t"
  #define UPOLL { u32 t0,t1,cnt; asm volatile(POLLASM(LD_U,CHK_U,PSLP) UX(112,113,114,115,544) UX(116,117,118,119,1056) UX(120,121,122,123,1568) UX(124,125,126,127,2080) \
      : [t0]"=&v"(t0),[t1]"=&v"(t1),[cnt]"=&s"(cnt) : [of]"v"(c.p0),[rs]"s"(rsU),[s1]"s"((32>>TL)*GST_),[s2]"s"((64>>TL)*GST_),[s3]"s"((96>>TL)*GST_),[c0]"s"(c0),[c1]"s"(c1),[c2]"s"(c2),[c3]"s"(c3_), \
        [e0]"s"(c0^0x40004u),[e1]"s"(c1^0x40004u),[e2]"s"(c2^0x40004u),[e3]"s"(c3_^0x40004u),[mk]"s"(UMASK),[up]"v"(c.p1) : PCLOB); garrive(sm,C_U+one_); }
  if(ROLE<3){
    h8 a[8], b[8];
    #pragma unroll
    for(int ks=0;ks<8;ks++){ a[ks]=LD(h8,fa+ks*64); b[ks]=LD(h8,fb+ks*64); }
    SCHED();
    LANE(ln); lanes<ROLE>(c,cb,ln); float fz= ln==32? 0.f : 4.76837158203125e-07f; if(ROLE==1) PIN(fz);
    SCHED();
    f32x4 k=Z4;
    #pragma unroll
    for(int ks=0;ks<8;ks++) k=MH(a[ks],b[ks],k);
    const float f=4.76837158203125e-07f;
    float k0=k[0]*f, k1=k[1]*f, k2=k[2]*f, k3=k[3]*f;
    u32 h32=pkh(k3,k2), h10=pkh(k1,k0);
    if(ROLE!=1){
      ST(u16,c.p0)=(u16)h32; ST(u32,c.p0+2)=pkh(k2,k1); ST(u16,c.p1)=(u16)(h10>>16);
      if(ROLE==0) ST(u32x2,c.p2)=u32x2{h32,h10};
    } else {
      float n0=dppf<0x130>(k[0])*fz, z1= fz==0.f? k0:k1, z2=k[2]*fz, z3=k[3]*fz; u32 h22=pkh(z2,z2);
      ST(u16,c.p0+64)=(u16)h10; ST(u16,c.p0+2)=(u16)(h32>>16); ST(u16,c.p0)=(u16)h32; ST(u16,c.p1)=(u16)(h10>>16);
      u32x4 vo={pkh(n0,z3),h22,pkh(z2,z1),pkh(k0,k0)}, ve={pkh(n0,n0),pkh(z3,z2),h22,pkh(z1,k0)};
      asm volatile("s_mov_b32 exec_hi, 1\n\tds_write_b128 %0, %1\n\tds_write_b128 %0, %2 offset:1200\n\ts_mov_b32 exec_hi, -1" :: "v"(c.p2),"v"(ve),"v"(vo) : "memory"); }
    gsync(sm,C_G+grp,7);
    u32 kv; A[0]=ld4w(c.x0+640); A[1]=ld4w(c.x0+128);
    if(ROLE==0){ A[2]=ld4w(c.x1+192); A[3]=ld4w(c.x1+128); A[4]=ld4w(c.x1+64); A[5]=ld4w(c.x1); kv=LD(u16,cb+O_R2K+128); }
    else { A[2]=ld4w(c.x0+576); A[3]=ld4w(c.x0+64); A[4]=ld4w(c.x0+512); A[5]=ld4w(c.x0); }
    SCHED(); lanes2<ROLE>(c,cb,ln); SCHED();
    if(ROLE==0){ kv|=kv<<16; c.AE=u32x4{c.AE[0]&kv,c.AE[1]&kv,c.AE[2]&kv,c.AE[3]&kv}; PIN(c.AE); }
    int ha=W_HT+d*8; u32 hd= one_? HD3 : (grp==0? HD0:HD1); if(ROLE==2) asm volatile("v_mov_b32 v113, %2" : "+v"(ha),"+v"(hd) : "s"((u32)tag));
    PRIO(2);
    if(BID>=112) gwait<4>(sm,UE? C_D:C_X,4);
    gwait<1>(sm,C_U+one_,2-one_);
    if(ROLE==2) asm volatile("s_memrealtime s[98:99]\n\ts_waitcnt lgkmcnt(0)\n\tv_add_u32 v112, s98, %1\n\ts_mov_b64 exec, 1\n\tglobal_store_dwordx2 %0, v[112:113], %2 sc1\n\ts_mov_b64 exec, -1" :: "v"(ha),"v"(hd),"s"(ws) : "memory");
  } else {
    u32 tg=(u32)tag, c0=upat(tg,0), c1=upat(tg,1), c2=upat(tg,2), c3_=upat(tg,3); u32x4 rsU=mkrs(ws+W_U,256*UST);
    if(!one_) PRIO(2);
    LANE(ln); c.p0=(lch*)fa; c.p1=(lch*)fb;
    bool late= grp==0||BID>=112;
    if(!late) asm volatile(LD_U :: [of]"v"(c.p0),[rs]"s"(rsU),[s1]"s"((32>>TL)*GST_),[s2]"s"((64>>TL)*GST_),[s3]"s"((96>>TL)*GST_) : "memory");
    asm volatile("s_mov_b64 exec, 1\n\tglobal_store_dwordx2 %0, %1, %2 sc1\n\ts_mov_b64 exec, -1" :: "v"(W_FL+d*8),"v"(tag),"s"(ws) : "memory");
    lanes<3>(c,cb,ln); lanes2<3>(c,cb,ln);
    #pragma unroll
    for(int j=2;j<8;j++){ acc[j]=Z4; PIN(acc[j]); }
    if(late){ if(UE==0||BID<112) gwait<2>(sm,C_G,UGATE); if(BID>=112){ gwait<1>(sm,UE? C_RS:C_D,UN); if(UE>1) __builtin_amdgcn_s_sleep(UE); } asm volatile(LD_U :: [of]"v"(c.p0),[rs]"s"(rsU),[s1]"s"((32>>TL)*GST_),[s2]"s"((64>>TL)*GST_),[s3]"s"((96>>TL)*GST_) : "memory"); }
    if(!late) UPOLL
    gwait<1>(sm,C_G+grp,7);
    #pragma unroll
    for(int i=0;i<6;i++) A[i]=ldA(c.x0+320-64*i);
    if(late) UPOLL
    PRIO(2);
    gwait<1>(sm,C_U+one_,2-one_);
  }
  const lch* bd=c.bd; h8 Be=BC(h8,c.Be), Bo=BC(h8,c.Bo);
  #pragma unroll
  for(int j=0;j<(ROLE==3? 2:8);j++) acc[j]=Z4;
  #define RW(j) if((j)>=0&&((j)>>1)!=ROLE&&!(ROLE==3&&(j)<2)){ ST(f32x4,c.wp+((j)>>1)*6144+((ROLE-((j)>>1)-1)&3)*2048+((j)&1)*1024)=acc[(j)<0? 0:(j)]; SCHED(); }
  if(ROLE==1||ROLE==2){
    h8 B[8];
    #pragma unroll
    for(int q=0;q<8;q++) B[q]=LD(h8,bd+128+512*q);
    #pragma unroll
    for(int r=0;r<3;r++){
      #pragma unroll
      for(int dl=0;dl<8;dl++){ if(dl<7) acc[dl+1]=MH(A[2*r+1],B[dl],acc[dl+1]); SCHED(); acc[dl]=MH(A[2*r],B[dl],acc[dl]); SCHED();
        if(r<2){ B[dl]=LD(h8,bd+64-64*r+512*dl); SCHED(); } else RW(dl-2) }
      } }
  if(ROLE==0){
    h8 B[8], ue[4];
    #pragma unroll
    for(int kt=0;kt<4;kt++){ ue[kt]=LD(h8,bd+48+512*kt); B[kt]=LD(h8,bd+64+512*kt); }
    f32x4 ce[2],co[2]; h8 Z[2];
    ce[0]=MH(ue[0],Be,Z4); co[0]=MH(B[0],Bo,Z4); ce[1]=MH(ue[1],Be,Z4); co[1]=MH(B[1],Bo,Z4); Z[0]=zpack(ce[0],co[0]);
    #pragma unroll
    for(int kt=0;kt<4;kt++){
      SCHED();
      if(kt+2<4){ ce[kt&1]=MH(ue[kt+2],Be,Z4); co[kt&1]=MH(B[kt+2],Bo,Z4); }
      if(kt+1<4) Z[(kt+1)&1]=zpack(ce[(kt+1)&1],co[(kt+1)&1]);
      #pragma unroll
      for(int rho=0;rho+kt<4;rho++) acc[4+kt+rho]=MH(Z[kt&1],A[2+rho],acc[4+kt+rho]);
      SCHED(); B[4+kt]=LD(h8,bd+64+512*(4+kt)); if(kt==0) A[5]=BC(h8,c.AE); if(kt==1) A[4]=ld4w(c.x0-384); if(kt==2) A[3]=ld4w(c.x0+64); if(kt==3) A[2]=ld4w(c.x0+576); SCHED(); }
    #pragma unroll
    for(int dl=0;dl<8;dl++){ if(dl<4) acc[dl+4]=MH(A[5],B[dl],acc[dl+4]); SCHED(); if(dl<6) acc[dl+2]=MH(A[4],B[dl],acc[dl+2]); SCHED(); if(dl<7) acc[dl+1]=MH(A[1],B[dl],acc[dl+1]); SCHED(); acc[dl]=MH(A[0],B[dl],acc[dl]);
      SCHED(); B[dl]=LD(h8,bd+512*dl); SCHED(); }
    #pragma unroll
    for(int dl=0;dl<8;dl++){ if(dl<7) acc[dl+1]=MH(A[3],B[dl],acc[dl+1]); SCHED(); acc[dl]=MH(A[2],B[dl],acc[dl]); SCHED(); RW(dl-2) } }
  if(ROLE==3){
    h8 U[12];
    #pragma unroll
    for(int kt=0;kt<6;kt++) U[kt]=LD(h8,bd+256*kt);
    f32x4 ce[2],co[2]; h8 Z[2];
    ce[0]=MH(U[0],Be,Z4); co[0]=MH(U[0],Bo,Z4); ce[1]=MH(U[1],Be,Z4); co[1]=MH(U[1],Bo,Z4); Z[0]=zpack(ce[0],co[0]);
    #pragma unroll
    for(int kt=0;kt<12;kt++){
      SCHED();
      if(kt+2<12){ ce[kt&1]=MH(U[kt+2],Be,Z4); co[kt&1]=MH(U[kt+2],Bo,Z4); }
      if(kt+1<12) Z[(kt+1)&1]=zpack(ce[(kt+1)&1],co[(kt+1)&1]);
      #pragma unroll
      for(int rho=-1;rho<5;rho++){ int j=(kt+4+rho)>>1; if(((kt+rho)&1)==0&&j>=2&&j<8) acc[j]=MH(Z[kt&1],A[rho+1],acc[j]); }
      if(kt<6){ SCHED(); U[kt+6]=LD(h8,bd+256*(kt+6)); }
      if(kt>=3&&kt<=9&&(kt&1)){ SCHED(); RW((kt+1)/2) } }
  }
  PRIO(3);
  RW(6) RW(7)
  gsync(sm,C_G+grp,11);
  { const lch* rp=c.wp+ROLE*6144; f32x4 a[3], b[3], o0=acc[2*ROLE], o1=acc[2*ROLE+1];
    #pragma unroll
    for(int q=0;q<(ROLE==0? 2:3);q++){ a[q]=LD(f32x4,rp+q*2048); b[q]=LD(f32x4,rp+q*2048+1024); }
    SCHED();
    float sc=__builtin_amdgcn_ldexpf(1.f,emax-YSH), s0[4], s1[4];
    #pragma unroll
    for(int e=0;e<4;e++){ s0[e]=((o0[e]+a[0][e])+(ROLE==0? a[1][e] : a[1][e]+a[2][e]))*sc; s1[e]=((o1[e]+b[0][e])+(ROLE==0? b[1][e] : b[1][e]+b[2][e]))*sc; }
    auto r0=__builtin_amdgcn_permlane16_swap(nv(pk(s0[0],s0[1])),nv(pk(s1[0],s1[1])),false,false); auto r1=__builtin_amdgcn_permlane16_swap(nv(pk(s0[2],s0[3])),nv(pk(s1[2],s1[3])),false,false);
    BST128(u32x4{r0[0],r1[0],r0[1],r1[1]},RSRC(ws+W_Y+d*YST,4096),c.yo+ROLE*1024,0,SC1); }
  garrive(sm,C_E);

  asm volatile("s_endpgm" : "=v"(fa),"=v"(fb) :: "memory");
}
#define GETEP asm volatile("s_waitcnt lgkmcnt(0)\n\ts_mov_b64 %0, s[96:97]\n\ts_mov_b32 %1, s98" : "=s"(ep),"=s"(ldw));
DEV void chan(lch* sm,int grp,int w4,int d,int one_,char* ws,u32x4 q0,u32x4 q1,u32x4 q2,u32x4 q3,u32x4 q4){
  lch* cb=sm+O_CH+grp*SLSZ; int role=w4, emax;
  #define LP(v) ((lch*)(size_t)(u32)(v))
  const lch* fa=LP(q4[0]); const lch* fb=LP(q4[1]);
  u64 tag;
  { float a2,bp,br,bi,cr,ci;
    u32 fq=q3[2], fr=q3[3], one=q4[2]; float f64=BC(float,fq), f128=BC(float,fr);
    lch *t8[4]={LP(q0[0]),LP(q0[1]),LP(q0[2]),LP(q0[3])}, *tB[4]={LP(q1[0]),LP(q1[1]),LP(q1[2]),LP(q1[3])}, *t4[2]={LP(q2[0]),LP(q2[1])}, *t2[2]={LP(q2[2]),LP(q2[3])}, *t1=LP(q3[0]), *te=LP(q3[1]);
    if(grp!=0) PRIO(1);
    u64 ep; u32 ldw; GETEP tag=ep+1ull;
    float dt=rb(expf_((d&1)? bhi(ldw):blo(ldw))), ca=dt*1.4426950408889634f, cb_=dt*0.15915494309189535f;
    asm volatile("s_waitcnt vmcnt(1)\n\tv_mul_f32 %2, %8, v114\n\tv_mul_f32 %3, %8, v115\n\tv_mul_f32 %0, %6, v112\n\tv_mul_f32 %1, %7, v113\n\tv_mov_b32 %4, v116\n\tv_mov_b32 %5, v117"
      : "=&v"(a2),"=&v"(bp),"=&v"(br),"=&v"(bi),"=&v"(cr),"=&v"(ci) : "v"(ca),"v"(cb_),"v"(dt) : "memory");
    C2 W; W.r=cr*br-ci*bi; W.i=cr*bi+ci*br;
    u32 mx=BC(u32,__builtin_fmaxf(__builtin_fabsf(W.r),__builtin_fabsf(W.i)));
    #define MXS(C) mx=umax_(mx,dppu<C>(mx));
    float e8=a2*8.f, eB=__builtin_fmaf(a2,f64,14.f), p8=bp*8.f, pB=bp*f64; SCHED();
    MXS(0xB1) p8=__builtin_amdgcn_fractf(p8); pB=__builtin_amdgcn_fractf(pB); e8=__builtin_amdgcn_exp2f(e8); SCHED();
    MXS(0x4E) eB=__builtin_amdgcn_exp2f(eB); float c8=__builtin_amdgcn_cosf(p8); SCHED();
    MXS(0x141) float s8=__builtin_amdgcn_sinf(p8), cB=__builtin_amdgcn_cosf(pB); SCHED();
    float sB=__builtin_amdgcn_sinf(pB); PIN(sB); MXS(0x140) float e1=__builtin_amdgcn_exp2f(a2); PIN(e1); SCHED();
    asm volatile("s_mov_b64 exec, 0x10001\n\tds_max_u32 %0, %1\n\tds_add_u32 %0, %2 offset:4\n\ts_mov_b64 exec, -1" :: "v"(te),"v"(mx),"v"(one) : "memory");
    C3 z8; z8.r=e8*c8; z8.n=e8*s8; z8.i=-z8.n; PIN(z8.i); C2 B; B.r=eB*cB; B.i=-(eB*sB);
    float p1=__builtin_amdgcn_fractf(bp), pE=__builtin_amdgcn_fractf(bp*256.f), pV=__builtin_amdgcn_fractf(bp*f128), eE=a2*256.f, eV=a2*f128, tr[8];
    #pragma unroll
    for(int k=0;k<4;k++){ SCHED(); u32 h0=ph(B); B=cm(B,z8);
      if(k==0){ tr[0]=__builtin_amdgcn_cosf(pV); tr[1]=__builtin_amdgcn_sinf(pV); } if(k==1){ tr[2]=__builtin_amdgcn_exp2f(eV); tr[3]=__builtin_amdgcn_cosf(p1); }
      if(k==2){ tr[4]=__builtin_amdgcn_sinf(p1); tr[5]=__builtin_amdgcn_exp2f(eE); } if(k==3){ tr[6]=__builtin_amdgcn_cosf(pE); tr[7]=__builtin_amdgcn_sinf(pE); }
      PIN(tr[2*k]); PIN(tr[2*k+1]);
      u32 h1=ph(B); if(k<3) B=cm(B,z8); W2(tB[k],h0,h1); }
    asm volatile("ds_read_b64 v[118:119], %0" :: "v"(te) : "memory");
    SCHED();
    C3 z1=c3(C2{e1*tr[3],e1*tr[4]}), E=c3(C2{tr[5]*tr[6],tr[5]*tr[7]}); C2 V[8]; V[0]=cmul(W,C2{tr[2]*tr[0],tr[2]*tr[1]});
    { u32 cnt,n; PIN(V[0].r); PIN(V[0].i); PIN(z1.r); PIN(E.r);
      asm volatile("s_mov_b32 %1, 0x20000\n1:\n\ts_waitcnt lgkmcnt(0)\n\tv_readfirstlane_b32 %2, v119\n\tv_readfirstlane_b32 %0, v118\n\ts_cmp_eq_u32 %2, 8\n\ts_cbranch_scc1 2f\n\tds_read_b64 v[118:119], %3\n\ts_sub_u32 %1, %1, 1\n\ts_cbranch_scc0 1b\n2:"
        : "=&s"(emax),"=&s"(cnt),"=&s"(n) : "v"(te) : "memory","scc");
      emax=RFL(emax); emax=(emax>>23)-126; }
    V[0].r=__builtin_amdgcn_ldexpf(V[0].r,14-emax); V[0].i=__builtin_amdgcn_ldexpf(V[0].i,14-emax);
    C2 Y=cm(V[0],E), Y4; u32 y0=ph(Y);
    V[1]=cm(V[0],z1); W2(t8[0],ph(V[0]),ph(V[1])); Y=cm(Y,E); u32 y1=ph(Y);
    V[2]=cm(V[1],z1); Y=cm(Y,E); u32 y2=ph(Y);
    V[3]=cm(V[2],z1); W2(t8[1],ph(V[2]),ph(V[3])); W2(t4[0],y0,ph(cm(V[2],E)));
    V[4]=cm(V[3],z1); Y=cm(Y,E); ST(u32,t1)=ph(Y);
    V[5]=cm(V[4],z1); W2(t8[2],ph(V[4]),ph(V[5])); Y4=cm(V[4],E); u32 y4=ph(Y4);
    V[6]=cm(V[5],z1); Y=cm(Y,E); ST(u32,t1+2*TROW)=ph(Y);
    V[7]=cm(V[6],z1); W2(t8[3],ph(V[6]),ph(V[7])); Y4=cm(Y4,E); W2(t2[0],y1,ph(Y4));
    W2(t4[1],y4,ph(cm(V[6],E))); Y=cm(Y,E); ST(u32,t1+4*TROW)=ph(Y);
    Y4=cm(Y4,E); W2(t2[1],y2,ph(Y4)); Y=cm(Y,E); ST(u32,t1+6*TROW)=ph(Y);
    Y=cm(Y,E); asm volatile("s_mov_b32 exec_hi, 0\n\tds_write_b32 %0, %1 offset:4224\n\ts_mov_b32 exec_hi, -1" :: "v"(t1),"v"(ph(Y)) : "memory");
  }
  if(one_) PRIO(3);
  DRAIN();
  gsync(sm,C_G+grp,4);
  if(role==0) chan2<0>(sm,cb,fa,fb,grp,d,one_,emax,tag,ws);
  if(role==1) chan2<1>(sm,cb,fa,fb,grp,d,one_,emax,tag,ws);
  if(role==2) chan2<2>(sm,cb,fa,fb,grp,d,one_,emax,tag,ws);
  chan2<3>(sm,cb,fa,fb,grp,d,one_,emax,tag,ws);
}

struct XR { int so,x0,x2; u32x4 rx,q[4]; };
DEV void xreq(XR& X,char* ws){ asm volatile("global_load_dwordx2 v[108:109], %[so], %[ws] sc1\n\tglobal_load_dwordx2 v[110:111], %[so], %[ws] offset:128 sc1\n\tbuffer_load_dwordx4 %[a], %[x0], %[rx], 0 offen\n\tbuffer_load_dwordx4 %[b], %[x0], %[rx], %[s1] offen\n\tbuffer_load_dwordx4 %[c], %[x2], %[rx], 0 offen"
  : [a]"=&{v[96:99]}"(X.q[0]),[b]"=&{v[100:103]}"(X.q[1]),[c]"=&{v[104:107]}"(X.q[2]),"=&{v[108:111]}"(X.q[3]) : [so]"v"(X.so),[ws]"s"(ws),[x0]"v"(X.x0),[x2]"v"(X.x2),[rx]"s"(X.rx),[s1]"s"(16*D_*2) : "memory"); }
DEV u32 med3u(u32 a,u32 b,u32 c){ u32 r; asm("v_med3_u32 %0, %1, %2, %3" : "=v"(r) : "v"(a),"v"(b),"v"(c)); return r; }
DEV u32 max3u(u32 a,u32 b,u32 c){ return umax_(umax_(a,b),c); }
#define GTC(r,s) "v_cmp_gt_u32 %[" #s "], v" #r ", %[c]\n\t"
#define GTA(s) "v_addc_co_u32 %[rk], vcc, 0, %[rk], %[" #s "]\n\t"
#define GT4(a,b,c,d) GTC(a,s0) GTC(b,s1) GTC(c,s2) GTC(d,s3) GTA(s0) GTA(s1) GTA(s2) GTA(s3)
#define GTS [s0]"=&s"(q0),[s1]"=&s"(q1),[s2]"=&s"(q2),[s3]"=&s"(q3)
DEV void top8(lch* sm,int w4,int ln,u32 M,char* ws,u32& rank_,u32& key_,XR& X){
  u32 k[8], rc[8], hm=0xffff0000u; u64 em=0x0001000100010001ull,q0,q1,q2,q3;
  u32x4 rs=mkrs(ws+W_RG+(BID&(NRG-1))*8192,8192); int ro=w4*1024+ln*16;
  #pragma unroll
  for(int e=0;e<8;e++){ rc[e]=(u32)(0xfff-(w4*256+ln*4+(e&3)+(e>>2)*1024)); PIN(rc[e]); }
  lch *aW=sm+O_RMX+(w4*4+(ln>>4))*4, *aC=sm+O_RMX+(ln>>2)*4, *aO=sm+O_RMX+(ln&3)*16, *dC=sm+O_CND+(ln>>1)*4, *dO=sm+O_CND+(ln&1)*64; PIN(aW); PIN(aC); PIN(aO); PIN(dC); PIN(dO); PIN(ro); PIN(ln);
  asm volatile("" : "+s"(hm),"+s"(em),"+s"(rs));
  gwait<2>(sm,C_G,RGATE);
  { u32 cnt;
    #define KL(e,r) "v_lshl_or_b32 %[k" #e "], v" #r ", 16, %[k" #e "]\n\t"
    #define KH(e,r) "v_and_or_b32 %[k" #e "], v" #r ", %[hm], %[k" #e "]\n\t"
    #define CHK_R "v_bitop3_b32 %[t0], v113, v115, %[M] bitop3:0x7e\n\tv_bitop3_b32 %[t1], v117, v119, %[M] bitop3:0x7e\n\tv_or_b32 %[t0], %[t0], %[t1]\n\tv_cmp_ne_u32 vcc, 0, %[t0]\n\t"
    u32 t0,t1;
    asm volatile(LD_DUP2
      "s_sleep " STR(R2D) "\n\tbuffer_load_dwordx4 v[120:123], %[of], %[rs], 0 offen sc1\n\tbuffer_load_dwordx4 v[124:127], %[of], %[rs], %[so] offen sc1\n\ts_mov_b32 %[cnt], 0x20000\n\ts_waitcnt vmcnt(2)\n\t" CHK_R "s_cbranch_vccz 9f\n\t"
      "s_waitcnt vmcnt(0)\n\tv_mov_b64 v[112:113], v[120:121]\n\tv_mov_b64 v[114:115], v[122:123]\n\tv_mov_b64 v[116:117], v[124:125]\n\tv_mov_b64 v[118:119], v[126:127]\n\t"
      POLLASM(LD_DUP2,CHK_R,PSLP)
      KL(0,112) KH(1,112) KL(2,114) KH(3,114) KL(4,116) KH(5,116) KL(6,118) KH(7,118)
      : [k0]"+v"(rc[0]),[k1]"+v"(rc[1]),[k2]"+v"(rc[2]),[k3]"+v"(rc[3]),[k4]"+v"(rc[4]),[k5]"+v"(rc[5]),[k6]"+v"(rc[6]),[k7]"+v"(rc[7]),[cnt]"=&s"(cnt),[t0]"=&v"(t0),[t1]"=&v"(t1) : [of]"v"(ro),[rs]"s"(rs),[so]"s"(4096),[M]"s"(M),[hm]"s"(hm) : PCLOB); }
  if(UE) garrive(sm,C_RS);
  #pragma unroll
  for(int e=0;e<8;e++) k[e]=rc[e];
  u32 h=max3u(k[0],k[1],k[2]), m1=max3u(max3u(k[3],k[4],k[5]),max3u(k[6],k[7],h),h), mx=m1, l=med3u(k[0],k[1],k[2]), t=umin_(umin_(k[0],k[1]),k[2]);
  SCHED();
  #define INS(e) t=med3u(t,l,k[e]); l=med3u(h,l,k[e]); if(e<7) h=umax_(h,k[e]); PIN(t); PIN(l); SCHED();
  mx=umax_(mx,dppu<0xB1>(mx)); INS(3) mx=umax_(mx,dppu<0x4E>(mx)); INS(4) mx=umax_(mx,dppu<0x141>(mx)); INS(5) mx=umax_(mx,dppu<0x140>(mx));
  asm volatile("s_mov_b64 exec, %2\n\tds_write_b32 %0, %1\n\ts_mov_b64 exec, -1\n\tds_read_b32 v116, %3\n\tds_read_b128 v[112:115], %4" :: "v"(aW),"v"(mx),"s"(em),"v"(aC),"v"(aO) : "memory");
  if(XB==2) xreq(X,ws);
  INS(6) INS(7)
  u32 m2=l, m3=t, T;
  { u32 c,rk,cnt;
    asm volatile("s_mov_b32 %[cnt], 0x20000\n1:\n\ts_waitcnt lgkmcnt(0)\n\tv_cmp_eq_u32 vcc, 0, v116\n\ts_cbranch_vccz 2f\n\tds_read_b32 v116, %[aC]\n\tds_read_b128 v[112:115], %[aO]\n\ts_sub_u32 %[cnt], %[cnt], 1\n\ts_cbranch_scc0 1b\n2:\n\t"
      "v_mov_b32 %[c], v116\n\tv_mov_b32 %[rk], 0\n\t" GT4(112,113,114,115) "s_nop 1\n\tv_add_u32_dpp %[rk], %[rk], %[rk] quad_perm:[1,0,3,2] row_mask:0xf bank_mask:0xf\n\ts_nop 1\n\tv_add_u32_dpp %[rk], %[rk], %[rk] quad_perm:[2,3,0,1] row_mask:0xf bank_mask:0xf\n\t"
      "v_cmp_eq_u32 vcc, 7, %[rk]\n\ts_ff1_i32_b64 %[cnt], vcc\n\ts_nop 0\n\tv_readlane_b32 %[T], %[c], %[cnt]\n\ts_nop 1"
      : [c]"=&v"(c),[rk]"=&v"(rk),[cnt]"=&s"(cnt),[T]"=&s"(T),GTS : [aC]"v"(aC),[aO]"v"(aO) : PCLOB); T=RFL(T); }
  { u64 b1=BALLOT(m1>=T), b2=BALLOT(m2>=T), b3=BALLOT(m3>=T); int n1=__builtin_popcountll(b1), n2=n1+__builtin_popcountll(b2);
    lch* cw=sm+O_CND+w4*32;
    if(__builtin_expect(b3==0ull&&n2<=8,1)){
      int p1=(int)__builtin_amdgcn_mbcnt_hi((u32)(b1>>32),__builtin_amdgcn_mbcnt_lo((u32)b1,0u)), pf=n2+ln-p1; bool c1=m1>=T;
      if(__builtin_expect(b2!=0ull,0)){ int p2=(int)__builtin_amdgcn_mbcnt_hi((u32)(b2>>32),__builtin_amdgcn_mbcnt_lo((u32)b2,(u32)n1)); if(m2>=T) ST(u32,cw+p2*4)=m2; }
      int ps= c1? p1:pf; ps= ps<8? ps:32-8*w4; ST(u32,cw+ps*4)= c1? m1:1u;
    } else {
      u32 mine=0;
      _Pragma("unroll 1") for(int c=0;c<8;c++){ u32 lm=umax_(umax_(umax_(k[0],k[1]),umax_(k[2],k[3])),umax_(umax_(k[4],k[5]),umax_(k[6],k[7]))); u32 wm=wmax(lm); if(ln==c) mine=wm;
        #pragma unroll
        for(int e=0;e<8;e++) k[e]=(k[e]==wm)?0u:k[e]; }
      if(ln<8) ST(u32,cw+ln*4)=mine; } }
  { u32 c,rk,cnt;
    asm volatile("s_mov_b32 %[cnt], 0x20000\n\tv_mov_b32 %[rk], 0\n\ts_waitcnt vmcnt(" XBW ")\n1:\n\tds_read_b32 %[c], %[dC]\n\tds_read_b128 v[112:115], %[dO]\n\tds_read_b128 v[116:119], %[dO] offset:16\n\tds_read_b128 v[120:123], %[dO] offset:32\n\tds_read_b128 v[124:127], %[dO] offset:48\n\t"
      "s_waitcnt lgkmcnt(4)\n\tv_cmp_eq_u32 vcc, 0, %[c]\n\ts_cbranch_vccz 2f\n\ts_sub_u32 %[cnt], %[cnt], 1\n\ts_cbranch_scc0 1b\n2:\n\t"
      "s_waitcnt lgkmcnt(3)\n\t" GT4(112,113,114,115) "s_waitcnt lgkmcnt(2)\n\t" GT4(116,117,118,119) "s_waitcnt lgkmcnt(1)\n\t" GT4(120,121,122,123) "s_waitcnt lgkmcnt(0)\n\t" GT4(124,125,126,127)
      "s_nop 1\n\tv_add_u32_dpp %[rk], %[rk], %[rk] quad_perm:[1,0,3,2] row_mask:0xf bank_mask:0xf"
      : [c]"=&v"(c),[rk]"=&v"(rk),[cnt]"=&s"(cnt),GTS : [dC]"v"(dC),[dO]"v"(dO) : PCLOB);
    rank_=rk; key_=c; }
}

#define FILL_(n,WAIT) "s_mov_b32 m0, %[ld]\n\ts_mov_b32 %[c], " STR(n) "-1\n3:\n\t" WAIT "buffer_load_dwordx4 %[vo], %[rs], %[so] offen nt lds\n\ts_add_u32 m0, m0, 0x1000\n\ts_add_u32 %[so], %[so], %[st]\n\t" \
  "s_sub_u32 %[c], %[c], 1\n\ts_cbranch_scc0 3b\n\ts_mov_b32 %[ld], m0\n\t"
#define FILL(n,WAIT) do{ u32 c_; asm volatile(FILL_(n,WAIT) : [c]"=&s"(c_),[so]"+s"(wso),[ld]"+s"(wld) : [vo]"v"(wvo),[rs]"s"(wrs),[st]"s"(wst) : "memory","scc","m0"); }while(0)
#define FILLD(n,WN,WW_) do{ u32 c_,t_,s_; asm volatile(FILL_(n,"ds_read_b32 %[t], %[ad]\n\ts_waitcnt lgkmcnt(0)\n\tv_readfirstlane_b32 %[s], %[t]\n\ts_cmp_ge_u32 %[s], 4\n\ts_cbranch_scc1 4f\n\t" VW(WN) "4:\n\t" VW(WW_)) \
  : [c]"=&s"(c_),[so]"+s"(wso),[ld]"+s"(wld),[t]"=&v"(t_),[s]"=&s"(s_) : [vo]"v"(wvo),[rs]"s"(wrs),[st]"s"(wst),[ad]"v"(C_D*4) : "memory","scc","m0"); wso=RFL(wso); wld=RFL(wld); }while(0)
#define VW(n) "s_waitcnt vmcnt(" STR(n) ")\n\t"

__global__ NOPK __attribute__((amdgpu_num_vgpr(56),amdgpu_num_sgpr(102))) __launch_bounds__(1024,4) void kA(const u16* __restrict__ x,char* ws,const u16* __restrict__ wr,const u16* __restrict__ Dsk,const u16* __restrict__ wg,const u16* __restrict__ wu,
                                              const u16* __restrict__ wd,u16* __restrict__ out,KP kp){
  __shared__ __attribute__((aligned(16))) char smem[LDS_TOTAL];
  lch* sm=(lch*)smem;
  u32x4 xa,xb_,wa,wb,dq[3]; u32 xc,wc;
  asm volatile(
    "s_load_dwordx16 s[64:79], %[ka], 0x0\n\ts_load_dwordx16 s[80:95], %[ka], 0x40\n\t"
    "v_readfirstlane_b32 s60, %[t]\n\ts_and_b32 s61, %[wg], 7\n\ts_lshr_b32 s62, %[wg], 3\n\tv_and_b32 v118, 31, %[t]\n\ts_bfe_u32 s63, s60, 0x20006\n\ts_cmpk_ge_u32 s60, 0x100\n\ts_cbranch_scc1 1f\n\t"
    "s_setprio 3\n\ts_lshl_b32 s56, s61, 5\n\ts_add_u32 s56, s56, s62\n\ts_mulk_i32 s56, 0x2400\n\tv_lshrrev_b32 v119, 5, %[t]\n\tv_mul_u32_u24 v119, 0x480, v119\n\tv_add_u32 v119, s56, v119\n\t"
    "v_lshl_add_u32 v116, v118, 4, v119\n\tv_lshl_add_u32 v117, v118, 2, v119\n\tv_lshlrev_b32 v114, 4, v118\n\tv_lshlrev_b32 v115, 2, v118\n\t"
    "s_and_b32 s57, s62, 3\n\ts_lshl2_add_u32 s57, s57, s63\n\ts_lshl_b32 s57, s57, 6\n\ts_and_b32 s58, s62, 1\n\ts_lshl_b32 s58, s58, 6\n\tv_and_b32 v112, 3, %[t]\n\tv_lshl_add_u32 v113, v112, 4, s57\n\tv_lshl_add_u32 v112, v112, 4, s58\n\t"
    "v_lshlrev_b32 v119, 1, %[t]\n\tv_mov_b32 v118, 0\n\tds_write_b16 v119, v118\n\t"
    "s_waitcnt lgkmcnt(0)\n\t"
    "s_memrealtime s[98:99]\n\ts_load_dwordx2 s[96:97], s[66:67], 0x0\n\t"
    "global_load_dwordx4 %[wa], v114, s[68:69]\n\tglobal_load_dwordx4 %[wb], v114, s[68:69] offset:512\n\tglobal_load_dword %[wc], v115, s[68:69] offset:1024\n\t"
    "global_load_dwordx4 %[xa], v116, s[64:65]\n\tglobal_load_dwordx4 %[xb], v116, s[64:65] offset:512\n\tglobal_load_dword %[xc], v117, s[64:65] offset:1024\n\t"
    "global_load_dwordx4 %[d0], v113, s[70:71]\n\tglobal_load_dwordx4 %[d1], v112, s[70:71] offset:1024\n\ts_barrier\n\t"
    "s_branch 8f\n"
    "1:\n\ts_cmp_ge_u32 s62, " STR(CPF) "\n\ts_cbranch_scc1 6f\n\ts_getpc_b64 s[56:57]\n.Lkpc:\n\ts_and_b32 s58, s62, 3\n\ts_lshl_b32 s58, s58, 13\n\ts_sub_u32 s58, s58, 0x1000\n\tv_lshl_add_u32 v119, %[t], 4, s58\n\tv_min_u32 v119, .Lkend-.Lkpc-16, v119\n\t"
    "global_load_dword v120, v119, s[56:57]\n\t.subsection 1\n.Lkend:\n\t.subsection 0\n6:\n\t"
    "v_mov_b32 v124, -1\n\tv_mov_b32 v125, -1\n\tv_mov_b32 v126, -1\n\tv_mov_b32 v127, -1\n\tv_lshlrev_b32 v119, 4, %[t]\n\tv_and_b32 v119, 0xff0, v119\n\t"
    "s_lshr_b32 s59, s60, 8\n\ts_cmp_lt_u32 s62, 8\n\ts_cselect_b32 s58, 2, 1\n\ts_cbranch_scc1 2f\n\ts_cmp_eq_u32 s59, 3\n\ts_cbranch_scc1 4f\n"
    "2:\n\ts_sub_u32 s57, s59, 2\n\ts_cmp_eq_u32 s59, 1\n\ts_cselect_b32 s58, s58, s57\n\ts_cmp_lt_u32 s62, 8\n\ts_cselect_b32 s56, 0, 8\n\ts_lshl1_add_u32 s56, s62, s56\n\ts_mulk_i32 s61, 72\n\ts_add_u32 s51, s56, s61\n\ts_sub_u32 s56, 2, s59\n\ts_sub_u32 s57, 16, s62\n\ts_cmp_eq_u32 s59, 3\n\ts_cselect_b32 s50, s57, s56\n\ts_add_u32 s56, s50, s51\n\t"
    "v_mov_b32 v112, 0\n\tv_mov_b32 v113, 0\n\tv_mov_b32 v114, 0\n\tv_mov_b32 v115, 0\n\tv_mov_b32 v116, 0\n\tv_mov_b32 v117, 0\n\ts_setprio 2\n\t"
    "s_lshl_b32 s57, s56, 2\n\ts_add_u32 s57, s57, s63\n\ts_lshl_b32 s57, s57, 6\n\tv_lshl_add_u32 v22, v118, 1, s57\n\ts_mul_i32 s57, s56, %[yst]\n\ts_add_u32 s57, s57, %[wy]\n\tv_add_u32 v23, s57, v119\n\ts_lshl_b32 s56, s56, 1\n\ts_and_b32 s56, s56, -4\n\t"
    "s_waitcnt lgkmcnt(0)\n\t"
    "global_load_short_d16_hi v112, v22, s[80:81] nt\n\tglobal_load_short_d16_hi v113, v22, s[82:83] nt\n\tglobal_load_short_d16_hi v114, v22, s[84:85] nt\n\t"
    "global_load_short_d16_hi v115, v22, s[86:87] nt\n\tglobal_load_short_d16_hi v116, v22, s[88:89] nt\n\tglobal_load_short_d16_hi v117, v22, s[90:91] nt\n\t"
    "s_load_dwordx2 s[96:97], s[66:67], 0x0\n\ts_load_dword s98, s[92:93], s56\n\t"
    "s_barrier\n\tglobal_store_dwordx4 v23, v[124:127], s[66:67] sc1\n\t"
    "s_mul_i32 s52, s58, %[sl]\n\ts_add_u32 s52, s52, %[tb]\n\ts_lshl_b32 s53, s63, 7\n\ts_add_u32 s53, s53, s52\n\tv_lshl_add_u32 v30, v118, 2, s53\n\tv_bfe_u32 v31, %[t], 5, 1\n\ts_movk_i32 s54, 528\n\tv_mul_u32_u24 v29, s54, v31\n\t"
    "v_lshl_add_u32 v2, v29, 3, v30\n\ts_movk_i32 s53, 1056\n\tv_add_u32 v3, s53, v2\n\tv_add_u32 v4, s53, v3\n\tv_add_u32 v5, s53, v4\n\tv_add_u32 v6, 41*528, v2\n\tv_add_u32 v7, s53, v6\n\tv_add_u32 v8, s53, v7\n\tv_add_u32 v9, s53, v8\n\t"
    "v_lshl_add_u32 v10, v29, 2, v30\n\tv_add_u32 v10, 16*528, v10\n\tv_add_u32 v11, s53, v10\n\tv_lshl_add_u32 v12, v29, 1, v30\n\tv_add_u32 v12, 24*528, v12\n\tv_add_u32 v13, 2112, v12\n\tv_add_u32 v14, v29, v30\n\tv_add_u32 v14, 32*528, v14\n\t"
    "s_lshl_b32 s53, s58, 4\n\ts_add_u32 s53, s53, %[ex]\n\tv_mov_b32 v15, s53\n\tv_cvt_f32_ubyte0 v31, v31\n\tv_mul_f32 v16, 0x42800000, v31\n\tv_add_f32 v17, v16, v16\n\tv_mov_b32 v20, 1\n\t"
    "v_and_b32 v29, 15, %[t]\n\tv_and_b32 v30, 48, %[t]\n\tv_mad_u32_u24 v30, v29, s54, v30\n\ts_lshl_b32 s55, s59, 1\n\ts_lshr_b32 s55, 0x48, s55\n\ts_add_u32 s55, s55, s63\n\ts_and_b32 s55, s55, 3\n\ts_mul_i32 s53, s55, 16*528\n\ts_movk_i32 s57, 41*528\n\ts_cmp_eq_u32 s55, 2\n\ts_cselect_b32 s53, s57, s53\n\ts_cselect_b32 s57, 32*528, s57\n\t"
    "s_add_u32 s53, s53, s52\n\ts_add_u32 s57, s57, s52\n\tv_add_u32 v18, s53, v30\n\tv_add_u32 v19, s57, v30\n\t"
    "v_min_u32 v29, 237*16, v119\n\tv_subrev_u32 v30, 204*16, v29\n\tv_add_u32 v29, %[de], v29\n\tv_min_u32 v29, v29, v30\n\ts_sub_u32 s53, s52, 4640\n\tv_add_u32 v29, s53, v29\n\tv_mov_b32 v24, 0\n\tv_mov_b32 v25, 0\n\tv_mov_b32 v26, 0\n\tv_mov_b32 v27, 0\n\tds_write_b128 v29, v[24:27]\n\t"
    "s_cmp_lg_u32 s55, 3\n\ts_cbranch_scc1 9f\n\t"
    "v_bfe_u32 v18, %[t], 1, 5\n\tv_and_b32 v19, 1, %[t]\n\ts_cmp_lt_u32 s62, 8\n\ts_cselect_b32 s53, 2, 1\n\ts_mul_i32 s53, s53, %[sl]\n\tv_mul_u32_u24 v21, s53, v19\n\ts_sub_u32 s55, 2, s59\n\ts_lshl_b32 s55, s55, 7\n\tv_add_u32 v18, s55, v18\n\ts_cmp_lg_u32 s59, 3\n\ts_cbranch_scc1 7f\n\t"
    "v_bfe_u32 v18, %[t], 5, 1\n\tv_lshl_add_u32 v18, v18, 7, v118\n\tv_mov_b32 v19, s50\n\tv_mov_b32 v21, %[sl]\n"
    "7:\n\tv_add_u32 v19, s51, v19\n\tv_lshl_add_u32 v21, v18, 4, v21\n\tv_lshrrev_b32 v24, 3-" STR(TL) ", v19\n\tv_and_b32 v19, 7>>" STR(TL) ", v19\n\tv_lshlrev_b32 v19, 4, v19\n\tv_lshl_add_u32 v19, v24, 7, v19\n\t"
    "v_and_b32 v24, (1<<" STR(TL) ")-1, v18\n\tv_lshl_add_u32 v19, v24, 7-" STR(TL) ", v19\n\tv_lshrrev_b32 v18, " STR(TL) ", v18\n\tv_mul_u32_u24 v18, %[ust], v18\n\tv_add_u32 v18, v18, v19\n\tv_add_u32 v19, 512, v21\n\t"
    "s_branch 9f\n"
    "4:\n\t"
    "v_bfe_u32 v118, %[t], 3, 5\n\tv_lshrrev_b32 v117, 1, v118\n\tv_and_b32 v116, 6, v117\n\tv_and_b32 v115, 7, %[t]\n\tv_xor_b32 v115, v115, v116\n\tv_lshlrev_b32 v115, 4, v115\n\t"
    "s_mul_i32 s54, s61, 9\n\ts_sub_u32 s55, s62, 14\n\ts_lshr_b32 s55, s55, 1\n\ts_add_u32 s54, s54, s55\n\ts_lshl_b32 s54, s54, 7\n\t"
    "s_mul_i32 s52, s61, 0xa2000\n\ts_mul_i32 s53, s62, 0x180\n\ts_add_u32 s52, s52, s53\n\ts_cmp_ge_u32 s62, 11\n\ts_cselect_b32 s53, 0x51000-0x480, 0\n\ts_add_u32 s52, s52, s53\n\ts_sub_u32 s52, s52, 0xc00\n\t"
    "s_movk_i32 s55, 0x480\n\ts_cmp_ge_u32 s62, 14\n\ts_cselect_b32 s54, s54, s52\n\ts_cselect_b32 s55, 0x2400, s55\n\tv_mad_u32_u24 v118, v118, s55, v115\n\ts_lshl_b32 s55, s55, 5\n\t"
    "s_lshl_b32 s52, s63, 10\n\ts_add_u32 m0, s52, %[r2]\n\ts_mov_b32 s58, 0x510000\n\ts_mov_b32 s59, 0x20000\n\ts_cmp_ge_u32 s62, 14\n\ts_cselect_b32 s53, " STR(PRE1) "-1, " STR(PRE2) "-1\n\ts_setprio 3\n\t"
    "s_waitcnt lgkmcnt(0)\n\ts_memrealtime s[98:99]\n\t"
    "s_bitcmp1_b32 %[wg], 3\n\ts_cselect_b64 s[56:57], s[74:75], s[72:73]\n\ts_cmp_ge_u32 s62, 14\n\ts_cselect_b64 s[56:57], s[56:57], s[76:77]\n\ts_cselect_b32 s52, 0x180, -1\n\ts_and_b32 s57, s57, 0xffff\n\ts_add_u32 s60, s56, s54\n\ts_and_b32 s60, s60, 0x380\n\ts_cmp_eq_u32 s60, s52\n\ts_cselect_b32 s53, " STR(PRE3) "-1, s53\n\t"
    "s_load_dwordx2 s[96:97], s[66:67], 0x0\n\ts_barrier\n\t"
    "\n3:\n\t" "buffer_load_dwordx4 v118, s[56:59], s54 offen nt lds\n\ts_add_u32 m0, m0, 0x1000\n\ts_add_u32 s54, s54, s55\n\ts_sub_u32 s53, s53, 1\n\ts_cbranch_scc0 3b\n"
    "9:\n8:"
    : [xa]"=&{v[2:5]}"(xa),[xb]"=&{v[6:9]}"(xb_),[xc]"=&{v30}"(xc),[wa]"=&{v[10:13]}"(wa),[wb]"=&{v[14:17]}"(wb),[wc]"=&{v31}"(wc),[d0]"=&{v[18:21]}"(dq[0]),[d1]"=&{v[22:25]}"(dq[1]),[d2]"=&{v[26:29]}"(dq[2])
    : [ka]"s"(__builtin_amdgcn_kernarg_segment_ptr()),[wg]"s"(BID),[t]"v"(TID),[yst]"i"(YST),[wy]"i"(W_Y),[r2]"i"(O_R2),[sl]"i"(SLSZ),[tb]"i"(O_CH+O_TB),[ex]"i"(O_EXP),[de]"i"(O_DE),[ust]"i"(GST_)
    : "memory","scc","m0","v127","s50","s51","s52","s53","s54","s55","s56","s57","s58","s59","s60","s61","s62","s63","s64","s65","s66","s67","s68","s69","s70","s71","s72","s73","s74","s75","s76","s77","s78","s79",
      "s80","s81","s82","s83","s84","s85","s86","s87","s88","s89","s90","s91","s92","s93","s94","s95","s99");
  int wv=RFL(TID>>6), bid=BID, q4=wv>>2, w4=wv&3;
  int xg=bid&7, lb=bid>>3, tb=32*xg+lb, nch=2+(lb<8);
  u64 ep; u32 ldw;
  {
  }
  #define KILLIN "=v"(xa),"=v"(xb_),"=v"(xc),"=v"(wa),"=v"(wb),"=v"(wc),"=v"(dq[0]),"=v"(dq[1]),"=v"(dq[2])
  if(__builtin_expect(q4==3&&lb>=8,0)){
    int fu=18*xg+lb-14, kq=2*xg+(lb>=11), cgp=lb-8-3*(lb>=11);
  u32 wst= lb>=14? F_*64:D_*64;
  u32 wso=(lb>=14? (fu>>1)*128 : kq*(288*D_*2)+384*cgp)+(lb>=14? PRE1:PRE2)*wst, wld=(u32)(size_t)sm+O_R2+w4*1024+(lb>=14? PRE1:PRE2)*4096; u32x4 wrs=mkrs(lb>=14? ((fu&1)? wu:wg):wd,D_*F_*2);
  GETEP
  #define FLANE(RS) LANE(ln); int g=ln>>4, li=ln&15, qq=li>>2, pq=li&3; u32 wvo=(u32)(8*w4+(ln>>3))*(RS)+(u32)((ln&7)^(((ln>>4)&2)|((w4&1)<<2)))*16u; \
    const lch* tp=sm+O_R2+(16*(g>>1)+8*(qq>>1)+4*(g&1)+(qq&1))*128+((w4^((g&1)|(qq&2)))*32)+pq*8;
  #define WFR(img,ks) BC(u32x4,trfrag(tp+(img)*CHSZ+(ks)*4096,tp+(img)*CHSZ+(ks)*4096+256))
  #define WF2(img,s) __builtin_shufflevector(WFR(img,2*(s)),WFR(img,2*(s)+1),0,1,2,3,4,5,6,7)
  f32x4 ac[3]={{0,0,0,0},{0,0,0,0},{0,0,0,0}};
  #define AC0 "+{v[100:103]}"(ac[0])
  #define AC1 "+{v[104:107]}"(ac[1])
  #define AC2 "+{v[108:111]}"(ac[2])
  #define SMF(i,a,b) asm volatile("v_smfmac_f32_16x16x64_bf16 %0, %1, %2, %3" : AC##i : "v"(a),"v"(b),"v"(ix) : "memory"); SCHED();
  #define SMR(i,r,b) asm volatile("v_smfmac_f32_16x16x64_bf16 %0, v[" #r ":" #r "+3], %1, %2" : AC##i : "v"(b),"v"(ix) : "memory"); SCHED();
  #define GW4(c,p) { u32 tv,cnt,sv; asm volatile("s_mov_b32 %1, 0x20000\n1:\n\tds_read_b32 %0, %3\n\tds_read_b128 v[112:115], %4\n\tds_read_b128 v[116:119], %4 offset:128\n\tds_read_b128 v[120:123], %4 offset:256\n\tds_read_b128 v[124:127], %4 offset:384\n\ts_waitcnt lgkmcnt(4)\n\t" \
    "v_readfirstlane_b32 %2, %0\n\ts_cmp_ge_u32 %2, 4\n\ts_cbranch_scc1 2f\n\ts_sleep 1\n\ts_sub_u32 %1, %1, 1\n\ts_cbranch_scc0 1b\n2:" : "=&v"(tv),"=&s"(cnt),"=&s"(sv) : "v"((c)*4),"v"(p) : "memory","scc"); }
  #define LGW(n) asm volatile("s_waitcnt lgkmcnt(" #n ")" ::: "memory");
  u64 tag=ep+1ull; const u32x4 I4={INV,INV,INV,INV};
  if(lb>=14){
    FLANE(F_*2)
    int mt=fu&1;
    const lch* xp=sm+O_TILE+(li&7)*XSTR+g*32+(li>>3)*16; int of=(mt*288+(fu>>1)*4+w4)*256+ln*4; u32 ix= (li&8)? 0xeeeeu:0x4444u;
    const lch* mp=sm+O_RC+(g&1)*16+(g>>1)*8;
    asm volatile("" : "+v"(xp),"+v"(of),"+v"(mp),"+v"(ix));
    rsrc_t rsG=RSRC(ws+W_GU,2*8*F_*2);
    if(w4==0) BST128(I4,rsG,(mt*288+(fu>>1)*4)*256+ln*16,0,SC1);
    bool slow=(((u32)wrs[0]+wso)&0x380u)==0x180u;
    if(slow){ wso-=(PRE1-PRE3)*wst; wld-=(PRE1-PRE3)*4096; FILL(5+PRE1-PRE3,VW(W3)); } else FILLD(5,W1,W1B);
    if(w4==0){ VMW(5); if(ln==0) GST(ws+W_FL+(576+fu)*8,tag); }
    if(slow) FILLD(13-PRE1,W3,W1B); else FILLD(13-PRE1,W1,W1B);
    u32x8 fB[9]; u32x4 one4={0x3f803f80u,0x3f803f80u,0x3f803f80u,0x3f803f80u}; const lch* tq=tp+65536; asm volatile("" : AC0,AC1,"+v"(one4),"+v"(tq));
    VMW(4); gsync(sm,C_P,4);
    #pragma unroll
    for(int s=0;s<7;s++) fB[s]=WF2(0,s);
    #pragma unroll
    for(int s=0;s<7;s++){ SMF(1,one4,fB[s]) }
    DRAIN(); gsync(sm,C_P,8);
    fB[7]=WF2(0,7); { const lch* tp=tq; fB[8]=WF2(0,0); } SMF(1,one4,fB[7]) SMF(1,one4,fB[8])
    { u32x4 xb[5]; u32x2 md;
      GW4(C_X,xp)
      md=LD(u32x2,mp);
      #pragma unroll
      for(int s=0;s<5;s++) xb[s]=LD(u32x4,xp+512+s*128);
      SCHED(); LGW(6)
      SMR(0,112,fB[0])
      float S; asm volatile("v_permlane32_swap_b32 v104, v105\n\tv_add_f32 %0, v104, v105" : "=v"(S),AC1); SCHED();
      SMR(0,116,fB[1]) SMR(0,120,fB[2])
      float ra=rcpf_(bhi(md[0])), rb_=rcpf_(bhi(md[1])); PIN(ra); PIN(rb_); SCHED();
      SMR(0,124,fB[3])
      float na=-blo(md[0])*S, nb=-blo(md[1])*S; PIN(na); PIN(nb); SCHED();
      SMF(0,xb[0],fB[4])
      float ea=ra*-1.4426950408889634f, eb=rb_*-1.4426950408889634f; PIN(ea); PIN(eb); SCHED();
      SMF(0,xb[1],fB[5]) SMF(0,xb[2],fB[6]) SMF(0,xb[3],fB[7]) SMF(0,xb[4],fB[8])
      float v0,v1,s0,s1;
      asm volatile("s_nop 7\n\tv_permlane32_swap_b32 v100, v102\n\tv_permlane32_swap_b32 v101, v103\n\tv_add_f32 v100, %5, v100\n\tv_add_f32 v101, %6, v101\n\tv_add_f32 v100, v100, v102\n\tv_add_f32 v101, v101, v103\n\t"
        "v_mul_f32 %2, %9, v100\n\tv_mul_f32 %3, %10, v101\n\tv_mul_f32 %0, %7, v100\n\tv_mul_f32 %1, %8, v101" : "=&v"(v0),"=&v"(v1),"=&v"(s0),"=&v"(s1),AC0 : "v"(na),"v"(nb),"v"(ra),"v"(rb_),"v"(ea),"v"(eb));
      if(mt==0){ v0*=rcpf_(1.f+__builtin_amdgcn_exp2f(s0)); v1*=rcpf_(1.f+__builtin_amdgcn_exp2f(s1)); }
      __builtin_amdgcn_raw_buffer_store_b32(nv(pk(v0,v1)),rsG,of,0,SC1); garrive(sm,C_H); }
    if(mt==0&&w4==0){
    LANE(l2); ln=l2; li=ln&15; g=ln>>4; gwait<1>(sm,C_IX,1);
    int c9=(lb-14)>>1, t=RFL((int)LD(u16,sm+O_IDX+xg*2)), c=64*c9+4*li+g; f32x4 pr;
    { u32 u=LD(u16,sm+O_IDX+16+xg*2)^0x8000u, xb=LD(u16,sm+O_TILE+xg*XSTR+c*2), sv=LD(u32,sm+O_RC+xg*4); float ds=b2f(Dsk[c]); u^=(u32)(((int)(u<<16))>>31)&0x7fffu;
      poll1<0>(ws+W_FL+(64*c9+ln)*8,tag); poll1<0>(ws+W_FL+(720+(ln<48? ln:47))*8,tag);
      float xn=(b2f(xb)-blo(sv))*rcpf_(bhi(sv)); pr=f32x4{b2f(xb),xn,xn*ds,sigm_f(b2f(u))}; }
    { gwait<2>(sm,C_E,4); u32 ti=now_(), mx=0;
      #pragma unroll
      for(int k=0;k<3;k++){ int i=ln+64*k; i= i<144? i:143; int q=(i*3641)>>16; u64 h=GLD(ws+W_HT+(72*q+36+2*(i-18*q))*8); mx=umax_(mx,(u32)(h>>32)==(u32)tag? (u32)h-ldw : ti-ldw+HM); }
      u32 tr=umin_(wmax(mx),ti-ldw+160u); u32 hk=ti-ldw+HKR; if(tr>hk) tr-=(tr-hk)>>1; tgate2(ldw,tr+HR); }
    { u32x4 rs=mkrs(ws+W_PART+(xg*9+c9)*4096,4096), ry=mkrs(ws+W_Y,D_*YST);
      int yo=c*YST+(t&~1)*2, po=ln*16; u32 a[4], t0,cnt,yb; u16* op=out+t*D_+c;
      #define LD_P "buffer_load_dwordx4 v[112:115], %[of], %[rs], 0 offen sc1\n\tbuffer_load_dwordx4 v[116:119], %[of], %[rs], 0 offen offset:1024 sc1\n\tbuffer_load_dwordx4 v[120:123], %[of], %[rs], 0 offen offset:2048 sc1\n\tbuffer_load_dwordx4 v[124:127], %[of], %[rs], 0 offen offset:3072 sc1\n\t"
      #define LD_YB "buffer_load_dword %[yb], %[yo], %[ry], 0 offen sc1\n\t"
      #define AD4(o,a,b,c,d) "v_add_f32 %[t0], v" #a ", v" #b "\n\tv_add_f32 %[" #o "], v" #c ", v" #d "\n\tv_add_f32 %[" #o "], %[t0], %[" #o "]\n\t"
      u64 ym;
      asm volatile(LD_YB LD_P "s_mov_b32 %[cnt], 0x20000\n1:\n\ts_waitcnt vmcnt(0)\n\tv_cmp_eq_u32 %[ym], -1, %[yb]\n\t" CHK_I0 MX2(116,117) MX2(118,119) MX2(120,121) MX2(122,123) MX2(124,125) MX2(126,127) CHK_IE
        "s_or_b64 exec, vcc, %[ym]\n\ts_cbranch_scc0 9f\n\ts_sleep " STR(QSLP) "\n\ts_mov_b64 exec, %[ym]\n\ts_cbranch_execz 2f\n\t" LD_YB "2:\n\ts_mov_b64 exec, vcc\n\ts_cbranch_execz 3f\n\t" LD_P
        "3:\n\ts_mov_b64 exec, -1\n\ts_sub_u32 %[cnt], %[cnt], 1\n\ts_cbranch_scc0 1b\n9:\n\ts_mov_b64 exec, -1\n\t"
        AD4(a0,112,116,120,124) AD4(a1,113,117,121,125) AD4(a2,114,118,122,126) AD4(a3,115,119,123,127) "s_nop 1"
        : [t0]"=&v"(t0),[cnt]"=&s"(cnt),[a0]"=&v"(a[0]),[a1]"=&v"(a[1]),[a2]"=&v"(a[2]),[a3]"=&v"(a[3]),[yb]"=&v"(yb),[ym]"=&s"(ym) : [of]"v"(po),[rs]"s"(rs),[yo]"v"(yo),[ry]"s"(ry),"v"(op),"v"(pr) : PCLOB);
      auto s0=__builtin_amdgcn_permlane32_swap(a[0],a[2],false,false); auto s1=__builtin_amdgcn_permlane32_swap(a[1],a[3],false,false);
      u32 x0=s0[0],x1=s0[1],y0=s1[0],y1=s1[1]; u32 p0=BC(u32,BC(float,x0)+BC(float,x1)), p1=BC(u32,BC(float,y0)+BC(float,y1));
      auto s2=__builtin_amdgcn_permlane16_swap(p0,p1,false,false); u32 z0=s2[0], z1=s2[1]; float s=BC(float,z0)+BC(float,z1);
      float gq=silu_f(__builtin_fmaf(s,pr[3],pr[1]));
      float sy=((t&1)? bhi(yb):blo(yb))+pr[2];
      float o=__builtin_fmaf(sy,gq,pr[0]);
      asm volatile("global_store_short %0, %1, off sc1" :: "v"(op),"v"(pk(o,o)) : "memory");
      if(bid==112&&ln==0) GST(ws+W_EPOCH,tag); }
    }
  } else
  {
    FLANE(D_*2)
    u32x8 fr[12]; u32x4 fl[3];
    rsrc_t rsP=RSRC(ws+W_PART+cgp*12288+kq*256+w4*64,8*9*4096-2*12288);
    #pragma unroll
    for(int i=0;i<3;i++) BST128(I4,rsP,li<8? li*(9*4096)+g*16+i*4096 : OOR,0,SC1);
    if(PRE2<9) FILLD(9-PRE2,W2_,W2B);
    wso+=128u-9u*wst;
    FILLD(9,W2_,W2B);
    VMW(9); gsync(sm,C_P,4);
    #pragma unroll
    for(int s=0;s<4;s++) fr[s]=WF2(0,s);
    fl[0]=WFR(0,8);
    gsync(sm,C_P,8); if(w4==0&&ln==0) GST(ws+W_FL+(720+6*xg+lb-8)*8,tag);
    wso+=128u-9u*wst; wld-=2*CHSZ;
    FILL(9,VW(W2B));
    int fo=W_FL+(576+((9*kq)&~1)+(ln<9? ln:9))*8;
    if(w4==0) asm volatile("global_load_dwordx2 v[112:113], %0, %1 sc1\n\ts_waitcnt vmcnt(10)" :: "v"(fo),"s"(ws) : "memory"); else VMW(9);
    gsync(sm,C_P,12);
    #pragma unroll
    for(int s=0;s<4;s++) fr[4+s]=WF2(1,s);
    fl[1]=WFR(1,8);
    { int t2=TID; asm volatile("" : "+v"(t2)); ln=t2&63; li=ln&15; g=ln>>4; }
    const lch* hp=sm+O_TILE+(li&7)*576+g*32+(li>>3)*16; int po=(4*(g&1)+2*(g>>1))*(9*4096)+li*4; u32 ix= (li&8)? 0xeeeeu:0x4444u; PIN(hp); PIN(po); PIN(ix);
    const lch* hq=hp+512-(g>>1)*32-(g&1)*24; PIN(hq);
    u32x4 rs=mkrs(ws+W_GU,2*8*F_*2);
    int of=((ln>>5)*288+18*kq+2*w4)*256+(ln&31)*16; lch* hd=sm+O_TILE+((ln&4)+((ln>>2)&2))*576+w4*64+(ln&16)*2+(ln&3)*8+(ln>>5)*4; PIN(of); PIN(hd); asm volatile("" : "+s"(rs));
    u32x4 rp=mkrs(ws+W_PART+cgp*12288+kq*256+w4*64,8*9*4096-2*12288); int po1=po+9*4096; u32 nvm=0xfffeffffu; asm volatile("" : AC0,AC1,AC2,"+v"(po1),"+s"(rp),"+s"(nvm));
    DRAIN();
    if(w4==0){ u64 bad; asm volatile("v_cmp_ne_u64 %0, %1, v[112:113]" : "=s"(bad) : "s"(tag) : "memory");
      if(bad!=0ull) poll1<0>(ws+fo,tag); }
    gsync(sm,C_P,16);
    { u32 t0,cnt;
      #define LD_G "buffer_load_dwordx4 v[112:115], %[of], %[rs], 0 offen sc1\n\tbuffer_load_dwordx4 v[116:119], %[of], %[rs], 0 offen offset:2048 sc1\n\tbuffer_load_dwordx4 v[120:123], %[of], %[rs], %[so] offen sc1\n\t"
      #define HM1(t,a,b) "v_lshlrev_b32 " t ", 16, v" #a "\n\tv_lshlrev_b32 %[t1], 16, v" #b "\n\tv_and_b32 v" #a ", %[hm], v" #a "\n\tv_and_b32 v" #b ", %[hm], v" #b "\n\tv_mul_f32 " t ", " t ", %[t1]\n\tv_mul_f32 v" #a ", v" #a ", v" #b "\n\t"
      #define HMUL(a,b,c,d,o) "v_permlane32_swap_b32 v" #a ", v" #c "\n\tv_permlane32_swap_b32 v" #b ", v" #d "\n\t" HM1("%[t0]",a,c) HM1("v" #c,b,d) "v_cvt_pk_bf16_f32 %[t0], %[t0], v" #c "\n\tv_cvt_pk_bf16_f32 v" #a ", v" #a ", v" #b "\n\tds_write2_b32 %[hd], %[t0], v" #a " offset0:" #o " offset1:" #o "+144\n\t"
      u32 t1;
      asm volatile(LD_G :: [of]"v"(of),[rs]"s"(rs),[so]"s"(w4==0? 4096:0) : "memory");
      fr[8]=WF2(0,0); PIN(fr[8]); fl[2]=WFR(0,8); PIN(fl[2]);
      asm volatile(POLLASM(LD_G,CHK_I0 MX2(116,117) MX2(118,119) MX2(120,121) MX2(122,123) CHK_IE,GSLP)
        HMUL(112,113,114,115,0) HMUL(116,117,118,119,64) "s_cmp_eq_u32 %[so], 0\n\ts_cbranch_scc1 8f\n\tv_add_u32 %[hd], 512, %[hd]\n\t" HMUL(120,121,122,123,0) "8:\n\ts_mov_b64 exec, 1\n\tds_add_u32 %[ch], %[one]\n\ts_mov_b64 exec, -1"
        : [t0]"=&v"(t0),[t1]"=&v"(t1),[cnt]"=&s"(cnt),[hd]"+v"(hd) : [of]"v"(of),[rs]"s"(rs),[so]"s"(w4==0? 4096:0),[hm]"s"(0xffff0000u),[ch]"v"(C_H*4),[one]"v"(1),"v"(hp),"v"(po) : PCLOB);
      }
    {
      GW4(C_H,hp)
      u32x2 h8=LD(u32x2,hq); SCHED(); LGW(1)
      #define SM3(i,b) asm volatile("v_smfmac_f32_16x16x32_bf16 %0, %1, %2, %3" : AC##i : "v"(h8),"v"(b),"v"(ix) : "memory"); SCHED();
      #define PST(a,b,c,d,o) "v_permlane32_swap_b32 v" #a ", v" #c "\n\tv_permlane32_swap_b32 v" #b ", v" #d "\n\tv_add_f32 v" #a ", v" #a ", v" #c "\n\tv_add_f32 v" #b ", v" #b ", v" #d "\n\tv_min_u32 v" #a ", %[nv], v" #a "\n\tv_min_u32 v" #b ", %[nv], v" #b "\n\t" \
        "buffer_store_dword v" #a ", %[p0], %[rp], " o " offen sc1\n\tbuffer_store_dword v" #b ", %[p1], %[rp], " o " offen sc1\n\t"
      SMR(0,112,fr[0]) fr[9]=WF2(0,1); SCHED(); SMR(1,112,fr[4]) fr[10]=WF2(0,2); SCHED(); SMR(2,112,fr[8]) fr[11]=WF2(0,3); SCHED();
      SMR(0,116,fr[1]) SMR(1,116,fr[5]) SMR(0,120,fr[2]) SM3(2,fl[2]) SMR(0,124,fr[3]) SMR(1,120,fr[6]) SM3(0,fl[0]) SMR(1,124,fr[7]) SMR(2,116,fr[9]) SM3(1,fl[1]) SMR(2,120,fr[10]) SMR(2,124,fr[11])
      asm volatile("s_nop 2\n\t" PST(100,101,102,103,"0") PST(104,105,106,107,"%[o1]") PST(108,109,110,111,"%[o2]") : AC0,AC1,AC2 : [p0]"v"(po),[p1]"v"(po1),[rp]"s"(rp),[nv]"s"(nvm),[o1]"s"(4096),[o2]"s"(8192) : "memory"); }

  }
  asm volatile("s_endpgm" : KILLIN :: "memory"); }
  {
  if(__builtin_expect(q4!=0,0)){ int grp= q4==1? nch-1 : q4-2;
    chan(sm,grp,(w4+(0x48>>(2*q4)))&3,72*xg+(q4==3? 16+lb : 2*lb+(lb<8? 0:8)+2-q4),(q4+1)>>2,ws,xa,xb_,wa,wb,dq[0]);
    asm volatile("s_endpgm" : KILLIN :: "memory"); }
  u32x4 xq[3]; f32x2 rc[3]; int xr_,xr2_;
  {
    LANE(ln); int g=ln>>4, li=ln&15, qq=li>>2, pq=li&3, j=ln&31, rl=2*w4+(ln>>5);
    lch* tile=sm+O_TILE; u32 Tr=(ln>>1)&16;
    int uo=W_U+(tb>>TL)*GST_+(tb&((1<<TL)-1))*(128>>TL)+(w4*1024<<TL)+(ln>>(3-TL))*128+(ln&(7>>TL))*16, uo1=uo+(4096<<TL), uo2=uo+(8192<<TL); PIN(uo); PIN(uo1); PIN(uo2);
    lch* tw=tile+rl*TSTR2+j*16; lch* tw2=tw+1024-j*12; u32 one=0x3f803f80u; const lch* tp=tile+qq*TSTR2+(w4*64+g*16+pq*4)*2;
    int go= ln<32? W_RG+(ln>>2)*8192+(tb*4+(ln&3))*8 : W_ST+(tb*8+(ln&7))*8; lch* kw=sm+O_RV+rl*2; const lch* kr=sm+(ln<32? O_RV+(ln&3)*4 : O_MD+(ln&7)*4); PIN(kw); PIN(kr);
    int xr=(32*(tb>>2)+(ln>>2))*(D_*2)+256*(lb&3)+64*w4+pq*16, xr2= w4==0? xr+(lb&2)*(8*D_*2)+1024+64*(lb&1)-256*(lb&3):OOR;
    rsrc_t rx=RSRC(x,L_*D_*2); u64 gm=((1ull<<(4*NRG))-1ull)|0xff00000000ull; u32 um=~UMASK, km=0x80008000u; asm volatile("" : "+s"(gm),"+s"(um),"+s"(km),"+s"(ws));
    #define D2(a,b,c) __builtin_amdgcn_fdot2_f32_bf16(BC(bf2,a),BC(bf2,b),c,false)
    asm volatile("s_waitcnt vmcnt(5)" : "+v"(wa),"+v"(wb),"+v"(wc),"+v"(Tr),"+v"(tw),"+v"(tw2),"+v"(tp),"+v"(one),"+v"(go),"+v"(xr),"+v"(xr2) :: "memory");
    u32 wq[9]={wa[0],wa[1],wa[2],wa[3],wb[0],wb[1],wb[2],wb[3],wc}; float sw;
    { float a[3];
      #pragma unroll
      for(int i=0;i<9;i++) a[i%3]=D2(wq[i],one,i<3? 0.f:a[i%3]);
      sw=add16x(sum16((a[0]+a[1])+a[2])); }
    GETEP u32 tg=(u32)ep+1u; Tr=((upat(tg,w4)>>Tr)&0xffffu)*0x10001u; u32 tgv=tg;
    float z_[9]={0,0,0,0,0,0,0,0,0};
    asm volatile("s_waitcnt vmcnt(2)" : "+v"(xa),"+v"(xb_),"+v"(xc),"+v"(Tr),"+v"(sw),"+v"(tgv),"+v"(z_[0]),"+v"(z_[1]),"+v"(z_[2]),"+v"(z_[3]),"+v"(z_[4]),"+v"(z_[5]) :: "memory");
    u32 v[9]={xa[0],xa[1],xa[2],xa[3],xb_[0],xb_[1],xb_[2],xb_[3],xc};
    { float xf[18], s1, s2, s3;
      #pragma unroll
      for(int i=0;i<9;i++){ z_[i&1]=D2(v[i],one,z_[i&1]); z_[2+(i&1)]=D2(v[i],v[i],z_[2+(i&1)]); z_[4+(i&1)]=D2(v[i],wq[i],z_[4+(i&1)]); }
      s1=z_[0]+z_[1]; s2=z_[2]+z_[3]; s3=z_[4]+z_[5];
      SCHED();
      #define RSTEP(C) s1+=dppf<C>(s1); s2+=dppf<C>(s2); s3+=dppf<C>(s3); SCHED();
      RSTEP(0xB1) RSTEP(0x4E) RSTEP(0x141) RSTEP(0x140)
      s1=add16x(s1)*(1.f/576.f); s2=add16x(s2)*(1.f/576.f); s3=add16x(s3);
      float mm=s1*s1, var=s2-mm;
      if(__builtin_expect(BALLOT(!(mm<=64.f*var))!=0ull,0)){
        float q=0.f;
        #pragma unroll
        for(int i=0;i<9;i++){ u32 t=v[i]; PIN(t); float d0=blo(t)-s1, d1=bhi(t)-s1; q=__builtin_fmaf(d0,d0,q); q=__builtin_fmaf(d1,d1,q); }
        var=add16x(sum16(q))*(1.f/576.f); }
      #define UNP(i) PIN(v[i]); xf[2*(i)]=blo(v[i]); xf[2*(i)+1]=bhi(v[i]); PIN(xf[2*(i)]); PIN(xf[2*(i)+1]); SCHED();
      u32 vb=pk(var,s1); UNP(0)
      float den=blo(vb)+1e-5f, m=bhi(vb); UNP(1)
      vb=pk(den,den); s3=__builtin_fmaf(-m,sw,s3); UNP(2)
      den=__builtin_amdgcn_sqrtf(bhi(vb)); UNP(3)
      u32 md=pk(m,den); UNP(4)
      float ri=rcpf_(bhi(md)); UNP(5)
      s3*=ri; float rs_=ri*0.0625f; UNP(6)
      u32 u=pk(s3,s3); float cs_=-m*rs_; UNP(7)
      { u32 sg=(u32)((int)u>>31); PIN(sg); u^=sg|km; } UNP(8)
      asm volatile("s_mov_b64 exec, 0x1\n\ts_mov_b32 exec_hi, 1\n\tds_write_b16 %0, %1\n\tv_add_u32 %0, %0, %0\n\tds_write_b32 %0, %2 offset:%3\n\ts_mov_b32 exec_hi, 0\n\tds_add_u32 %4, %5\n\ts_mov_b64 exec, -1" : "+v"(kw) : "v"(u),"v"(md),"i"(O_MD-2*O_RV),"v"(C_I*4),"v"(1) : "memory");
      if(w4==3){ gwait<0>(sm,C_I,4); asm volatile("ds_read_b32 v118, %4\n\tv_mov_b32 v119, %1\n\ts_mov_b64 exec, %3\n\ts_waitcnt lgkmcnt(0)\n\tglobal_store_dwordx2 %0, v[118:119], %2 sc1\n\ts_mov_b64 exec, -1" :: "v"(go),"v"(tgv),"s"(ws),"s"(gm),"v"(kr) : "memory"); }
      SCHED();
      u32 h[9];
      #define UH(i) h[i]=(pkz(__builtin_fmaf(xf[2*(i)],rs_,cs_),__builtin_fmaf(xf[2*(i)+1],rs_,cs_))&um)|Tr;
      UH(0) UH(1) UH(2) UH(3) ST(u32x4,tw)=u32x4{h[0],h[1],h[2],h[3]}; UH(4) UH(5) UH(6) UH(7) ST(u32x4,tw+512)=u32x4{h[4],h[5],h[6],h[7]};
      UH(8) ST(u32,tw2)=h[8]; }
    gsync(sm,C_L,4);
    { u32x4 uq[3];
      #pragma unroll
      for(int z=0;z<3;z++) uq[z]=BC(u32x4,trfrag(tp+z*512,tp+z*512+4*TSTR2));
      asm volatile("global_store_dwordx4 %0, %1, %2 sc1" :: "v"(uo),"v"(uq[0]),"s"(ws) : "memory");
      asm volatile("global_store_dwordx4 %0, %1, %2 sc1" :: "v"(uo1),"v"(uq[1]),"s"(ws) : "memory");
      asm volatile("s_cmp_lg_u32 %3, 0\n\ts_cbranch_scc1 1f\n\tglobal_store_dwordx4 %0, %1, %2 sc1\n1:\n\ts_nop 1" :: "v"(uo2),"v"(uq[2]),"s"(ws),"s"(w4),"v"(uq[0]),"v"(uq[1]) : "memory","scc");
      asm volatile("" : "+v"(dq[0]),"+v"(dq[1]));
      xr_=xr; xr2_=xr2;
    }
    if(lb>=2&&lb<8){
      const u16* wm=(lb&1)? wu:wg; int co=(3-(int)((u64)wm>>7)-xg)&7, r_=576-12*PFN+(((lb>>1)-1)*4+w4)+12*j;
      asm volatile("buffer_load_dword v120, %0, %1, 0 offen" :: "v"(j<PFN&&(ln<32||co==0)? r_*(F_*2)+(9*xg+co)*128+(ln>>5)*1024 : OOR),"s"(mkrs(wm,D_*F_*2)) : "memory"); }
    if(lb>=8&&lb<14) garrive(sm,C_D);
  }
  FP fp; u32 ti,td;
  { LANE(ln); u64 tag=ep+1ull;
    int so_=W_ST+(32*(tb>>2)+(ln>>2))*8, fo_=W_FL+((ln>=32&&w4==0? 512+32*(lb&1) : 128*(lb&3)+32*w4)+(ln&31))*8; u32x4 rx=mkrs(x,L_*D_*2); PIN(so_); PIN(fo_);
    XR X; X.so=so_; X.x0=xr_; X.x2=xr2_; X.rx=rx;
    if(lb==CTLB||lb>=14){
      u32 M=(u32)tag, rank,key; int j=ln&31, j16=j*16, j12=j*12, st0=W_ST; lch* xw=sm+O_TILE+(2*w4+(ln>>5))*XSTR+j*16; lch* rcw=sm+O_RC+(2*w4+(ln>>5))*4; PIN(rcw); PIN(j12); PIN(xw); PIN(j16); asm volatile("" : "+s"(rx),"+s"(st0));
      top8(sm,w4,ln,M,ws,rank,key,X);
      {
        int t0=__builtin_amdgcn_readlane((int)key,__builtin_ctzll(BALLOT(rank==(u32)(2*w4)))), t1=__builtin_amdgcn_readlane((int)key,__builtin_ctzll(BALLOT(rank==(u32)(2*w4+1))));
        int tsel=~((ln>>5)? t1:t0)&2047, ro=tsel*1152+j16, sr=tsel*8+st0;
        u32x4 a,b; u32 c; u64 f1;
        asm volatile("buffer_load_dwordx4 %[a], %[ro], %[rx], 0 offen\n\tbuffer_load_dwordx4 %[b], %[ro], %[rx], 0 offen offset:512\n\tbuffer_load_dword %[c], %[r2], %[rx], 0 offen offset:1024\n\tglobal_load_dwordx2 %[f], %[sr], %[ws] sc1\n\t"
          "s_mov_b64 exec, 1\n\tds_add_u32 %[cd], %[one]\n\ts_mov_b64 exec, -1\n\ts_waitcnt vmcnt(0)"
          : [a]"=&v"(a),[b]"=&v"(b),[c]"=&v"(c),[f]"=&v"(f1) : [ro]"v"(ro),[r2]"v"(ro-j12),[sr]"v"(sr),[cd]"v"(C_D*4),[one]"v"(1),[rx]"s"(rx),[ws]"s"(ws) : "memory");
        if(__builtin_expect(BALLOT((u32)(f1>>32)!=(u32)tag)!=0ull,0)){ f1=poll1<32>(ws+sr,tag<<32); __builtin_amdgcn_s_waitcnt(0x0f70); }
        ST(u32x4,xw)=a; ST(u32x4,xw+512)=b; ST(u32,xw+1024-j12)=c; ST(u32,rcw)=(u32)f1;
        garrive(sm,C_X);
        if(w4==0){ if(rank<8) ST(u16,sm+O_IDX+(ln&1)*16+rank*2)=(u16)((ln&1)? key>>16 : ~key&2047u); garrive(sm,C_IX); }
        if(lb==CTLB&&rank<8&&!(ln&1)) GST(ws+W_IDX+xg*1024+w4*128+rank*8,(tag<<16)|(~key&2047u));
      }
      PRIO(0); if(XB!=2) xreq(X,ws);
    } else { PRIO(0); gwait<8>(sm,C_U,2); xreq(X,ws); }
    { u32 e=now_()-ldw, nm= lb<8&&lb!=CTLB? ANA : (lb>=8&&lb<14? ANC:ANB); if(e>nm) ldw+=e-nm; }
    { u64 fs,fs1;
      asm volatile("s_waitcnt vmcnt(0)" : "+{v[96:99]}"(X.q[0]),"+{v[100:103]}"(X.q[1]),"+{v[104:107]}"(X.q[2]),"+{v[108:111]}"(X.q[3]) :: "memory");
      fs=(u64)X.q[3][0]|((u64)X.q[3][1]<<32); fs1=(u64)X.q[3][2]|((u64)X.q[3][3]<<32);
      if(__builtin_expect(BALLOT((u32)(fs>>32)!=(u32)tag||(u32)(fs1>>32)!=(u32)tag)!=0ull,0)){ fs=poll1<32>(ws+so_,tag<<32); fs1=poll1<32>(ws+so_+128,tag<<32); __builtin_amdgcn_s_waitcnt(0x0f70); }
      float ri=rcpf_(bhi((u32)fs)), rj=rcpf_(bhi((u32)fs1)); rc[0]=f32x2{ri,-blo((u32)fs)*ri}; rc[1]=f32x2{rj,-blo((u32)fs1)*rj}; rc[2]= (lb&2)? rc[1]:rc[0];
      #pragma unroll
      for(int z=0;z<3;z++) xq[z]=X.q[z]; }
    bool ix= w4==3&&lb<14&&lb!=CTLB; int po=W_IDX+(xg*8+(lb&3))*128+(ln&7)*8;
    int ho_=fo_+(W_HT-W_FL);
    final_pre<0,1>(fp,w4,xq,dq,rc);
    ti=now_()-ldw;
    asm volatile("global_load_dwordx2 v[112:113], %0, %2 sc1\n\tglobal_load_dwordx2 v[114:115], %1, %2 sc1\n\tv_mov_b32 v117, %5\n\ts_cmp_lt_u32 %4, " STR(HT) "\n\ts_cbranch_scc1 1f\n\tglobal_load_dwordx2 v[116:117], %3, %2 sc1\n1:"
      :: "v"(fo_),"v"(ix? po:fo_),"s"(ws),"v"(ho_),"s"(ti),"s"(~(u32)tag) : "memory","scc");
    final_pre<1,3>(fp,w4,xq,dq,rc);
    { u64 ff,f; u32x2 h;
      asm volatile("s_waitcnt vmcnt(0)\n\tv_mov_b64 %0, v[112:113]\n\tv_mov_b64 %1, v[114:115]\n\tv_mov_b64 %2, v[116:117]" : "=v"(ff),"=v"(f),"=v"(h) :: "memory");
      _Pragma("unroll 1") for(int k=0;k<2;k++){ if(BALLOT(h[1]!=(u32)tag)==0ull||ti>=H2) break;
        tgate2(ldw,ti<HT? HT:H2); ti=now_()-ldw; if(ti>=HL){ h=u32x2{ldw,(u32)tag}; break; }
        asm volatile("global_load_dwordx2 %0, %1, %2 sc1\n\ts_waitcnt vmcnt(0)" : "=&v"(h) : "v"(ho_),"s"(ws) : "memory"); }
      u32 rel= h[1]==(u32)tag? h[0]-ldw : ti+HM; td=umin_(wmax(rel),ti+160u); if(td>HK) td-=(td-HK)>>1;
      if(__builtin_expect(BALLOT(ff!=tag)!=0ull,0)) poll1<0>(ws+fo_,tag);
      if(ix){ if(BALLOT(((f^(tag<<16))>>16)!=0ull)!=0ull) f=poll1<16>(ws+po,tag<<16);
        if(ln<8) ST(u16,sm+O_IDX+ln*2)=(u16)f; garrive(sm,C_IX); } }
  }
  final_post(sm,fp,w4,out,ws,xr_,ldw,td);
  }
}

extern "C" void kernel_launch(void* const* d_in, const int* in_sizes, int n_in, void* d_out, int out_size, void* d_ws, size_t ws_size, hipStream_t stream) {
  const u16** I=(const u16**)d_in;
  KP kp; kp.Are=I[1]; kp.Aim=I[2]; kp.Bre=I[3]; kp.Bim=I[4]; kp.Cre=I[5]; kp.Cim=I[6]; kp.ldt=I[7]; kp.pad=I[7];
  kA<<<256,1024,0,stream>>>(I[0],(char*)d_ws,I[10],I[8],I[11],I[12],I[13],(u16*)d_out,kp);
}
```

```cpp
#include <hip/hip_runtime.h>
#include <hip/hip_bf16.h>
#include <stdint.h>
#if defined(__HIP_DEVICE_COMPILE__)
#define NOPK __attribute__((target("no-packed-fp32-ops")))
#else
#define NOPK
#endif
#define DEV __device__ NOPK __forceinline__
typedef unsigned short u16;
typedef unsigned int u32;
typedef unsigned long long u64;
typedef short bf16x8 __attribute__((ext_vector_type(8)));
typedef _Float16 h8 __attribute__((ext_vector_type(8)));
typedef _Float16 h2 __attribute__((ext_vector_type(2)));
typedef float f32x4 __attribute__((ext_vector_type(4)));
typedef float f32x2 __attribute__((ext_vector_type(2)));
typedef u32 u32x4 __attribute__((ext_vector_type(4)));
typedef u32 u32x2 __attribute__((ext_vector_type(2)));
typedef u32 u32x8 __attribute__((ext_vector_type(8)));
typedef __bf16 bf2 __attribute__((ext_vector_type(2)));
typedef short v4s __attribute__((ext_vector_type(4)));
typedef __amdgpu_buffer_rsrc_t rsrc_t;
#define GAS __attribute__((address_space(1)))
#define LAS __attribute__((address_space(3)))
typedef LAS char lch;
#define L_ 2048
#define D_ 576
#define F_ 4608
#define BC(T,v) __builtin_bit_cast(T,v)
#define TID ((int)__builtin_amdgcn_workitem_id_x())
#define BID ((int)__builtin_amdgcn_workgroup_id_x())
#define RFL(x) __builtin_amdgcn_readfirstlane(x)
#define RLX_AGENT __ATOMIC_RELAXED, __HIP_MEMORY_SCOPE_AGENT
#define LD(T,p) (*(const LAS T*)(p))
#define ST(T,p) (*(LAS T*)(p))
#define SPINCAP (1<<17)
#define DRAIN() asm volatile("s_waitcnt vmcnt(0)" ::: "memory")
#define GLD(p) __hip_atomic_load((const GAS u64*)(p),RLX_AGENT)
#define GST(p,v) __hip_atomic_store((GAS u64*)(p),(u64)(v),RLX_AGENT)
#define BALLOT(c) __builtin_amdgcn_ballot_w64(c)
#define SC1 16
#define LANE(l) int l; { int t2_=TID; asm volatile("" : "+v"(t2_)); l=t2_&63; }
#define XB 1
#define XBW "0"
#define UE 10
#define UN 4
#define LO 0
#define US 0
#define USB 64
#define CL 0
#define PD 0
#define HO 1
#define IV 1
#define INV0
#define INV1 "global_store_dwordx4 v23, v[124:127], s[66:67] sc1\n\t"
#define DF 1
#define DPR 3
#define EPR 0
#define XC 0
#define XG 7
#define XU 2
#define W2C 7
#define AW 0
#define SCHED() __builtin_amdgcn_sched_barrier(0)
#define RSRC(p,n) __builtin_amdgcn_make_buffer_rsrc((void*)(p),0,(n),0x00020000)
#define BLD128 __builtin_amdgcn_raw_buffer_load_b128
#define BLD32 __builtin_amdgcn_raw_buffer_load_b32
#define BST128 __builtin_amdgcn_raw_buffer_store_b128
#define PRIO(n) __builtin_amdgcn_s_setprio(n)

DEV float b2f(u32 v){ return BC(float,v<<16); }
DEV float blo(u32 v){ return BC(float,v<<16); }
DEV float bhi(u32 v){ return BC(float,v&0xffff0000u); }
DEV u32 pk(float a,float b){ f32x2 v={a,b}; bf2 r=__builtin_convertvector(v,bf2); return BC(u32,r); }
DEV u32 pkh(float a,float b){ f32x2 v={a,b}; h2 r=__builtin_convertvector(v,h2); return BC(u32,r); }
DEV float rb(float f){ return BC(float,pk(f,f)<<16); }
DEV float rcpf_(float x){ return __builtin_amdgcn_rcpf(x); }
DEV float expf_(float x){
  const float L2E=1.4426950216293335f, L2El=1.9259629911266175e-8f; float h=x*L2E; float l=__builtin_fmaf(x,L2E,-h)+x*L2El;
  float e=__builtin_amdgcn_exp2f(h); return __builtin_fmaf(e*l,0.6931471805599453f,e); }
DEV float sigm_f(float x){ return rcpf_(1.f+__builtin_amdgcn_exp2f(-x*1.4426950408889634f)); }
DEV float silu_f(float x){ return x*sigm_f(x); }
DEV u32 umax_(u32 a,u32 b){ return a>b?a:b; }
DEV u32 umin_(u32 a,u32 b){ return a<b?a:b; }

#define YSH 3
#define PRE1 6
#define PRE3 4
#define PRE2 5
#define W1 5
#define W3 3
#define W2_ 4
#define W1B 20
#define W2B 7
#define LAST1 4
#define RGATE 4
#define UGATE 7
#define R2D 12
#define PFN 32
#define CPF 8
#define HD0 174
#define HD1 148
#define HD3 165
#define HM 100
#define HT 555
#define H2 600
#define HL 650
#define HK 655
#define HKR 65
#define HR 12
#define ANA 410
#define ANB 470
#define ANC 540
#define NRG 8
#define W_EPOCH 0
#define W_RG 16384
#define W_ST (W_RG+32*8192)
#define W_U (W_ST+32768)
#define UST (D_*16+128)
#define YST (L_*2+128)
#define TL 1
#define GST_ ((D_*16<<TL)+128)
#define W_Y (W_U+256*UST)
#define W_PART (W_Y+D_*YST)
#define W_GU (W_PART+8*9*16*256)
#define W_FL 9216
#define W_HT (W_RG+16*8192)
#define W_IDX 1024
#define CTLB 0
struct KP { const u16 *Are,*Aim,*Bre,*Bim,*Cre,*Cim,*ldt,*pad; };

template<int CTRL> DEV float dppf(float v){ return BC(float,__builtin_amdgcn_update_dpp(0u,BC(u32,v),CTRL,0xf,0xf,false)); }
template<int CTRL> DEV u32 dppu(u32 v){ return (u32)__builtin_amdgcn_update_dpp(0u,v,CTRL,0xf,0xf,false); }
DEV float sum16(float v){ v+=dppf<0xB1>(v); v+=dppf<0x4E>(v); v+=dppf<0x141>(v); v+=dppf<0x140>(v); return v; }
DEV float add16x(float v){ auto r=__builtin_amdgcn_permlane16_swap(BC(u32,v),BC(u32,v),false,false); u32 a=r[0], b=r[1]; return BC(float,a)+BC(float,b); }
DEV u32 wmax(u32 v){
  v=umax_(v,(u32)__builtin_amdgcn_update_dpp(0u,v,0x111,0xf,0xf,false));
  v=umax_(v,(u32)__builtin_amdgcn_update_dpp(0u,v,0x112,0xf,0xf,false));
  v=umax_(v,(u32)__builtin_amdgcn_update_dpp(0u,v,0x114,0xf,0xf,false));
  v=umax_(v,(u32)__builtin_amdgcn_update_dpp(0u,v,0x118,0xf,0xf,false));
  v=umax_(v,(u32)__builtin_amdgcn_update_dpp(0u,v,0x142,0xa,0xf,false));
  v=umax_(v,(u32)__builtin_amdgcn_update_dpp(0u,v,0x143,0xc,0xf,false));
  return (u32)__builtin_amdgcn_readlane((int)v,63); }

DEV void garrive(lch* sm,int c){ u32 ad=(u32)(size_t)sm+c*4, one=1;
  asm volatile("s_mov_b64 exec, 1\n\tds_add_u32 %0, %1\n\ts_mov_b64 exec, -1" :: "v"(ad),"v"(one) : "memory"); }
template<int SLP> DEV void gwait(lch* sm,int c,u32 target){ u32 ad=(u32)(size_t)sm+c*4, tv, cnt, sv; target=RFL(target);
  asm volatile("s_mov_b32 %1, 0x20000\n"
    "1:\n\tds_read_b32 %0, %3\n\ts_waitcnt lgkmcnt(0)\n\tv_readfirstlane_b32 %2, %0\n\ts_cmp_ge_u32 %2, %4\n\ts_cbranch_scc1 2f\n\ts_sleep %5\n\ts_sub_u32 %1, %1, 1\n\ts_cbranch_scc0 1b\n"
    "2:" : "=&v"(tv),"=&s"(cnt),"=&s"(sv) : "v"(ad),"s"(target),"i"(SLP) : "memory","scc"); }
DEV u32 now_(){ u32 t; asm volatile("s_memrealtime s[98:99]\n\ts_waitcnt lgkmcnt(0)\n\ts_mov_b32 %0, s98" : "=s"(t) :: "memory"); return t; }
DEV void tgate2(u32 t0,u32 d){ u32 cnt; asm volatile("s_mov_b32 %0, 100\n1:\n\ts_memrealtime s[98:99]\n\ts_waitcnt lgkmcnt(0)\n\ts_sub_u32 s98, s98, %1\n\ts_cmp_ge_u32 s98, %2\n\ts_cbranch_scc1 2f\n\ts_sleep 1\n\ts_sub_u32 %0, %0, 1\n\ts_cbranch_scc0 1b\n2:" : "=&s"(cnt) : "s"(t0),"s"(d) : "memory","scc"); }
DEV void gsync(lch* sm,int c,u32 target){ garrive(sm,c); gwait<0>(sm,c,target); }
template<int SH> DEV u64 poll1(const char* p,u64 want){ u64 f=GLD(p);
  _Pragma("unroll 1") for(int sp=0;sp<SPINCAP;sp++){ bool bad=((f^want)>>SH)!=0ull; if(BALLOT(bad)==0ull) break; __builtin_amdgcn_s_sleep(1); if(bad) f=GLD(p); }
  __builtin_amdgcn_fence(__ATOMIC_ACQUIRE,"wavefront"); return f; }
#define STR_(x) #x
#define STR(x) STR_(x)
DEV u32x4 mkrs(const void* p,u32 n){ u64 a=(u64)p; return u32x4{(u32)RFL((int)(u32)a),(u32)RFL((int)((u32)(a>>32)&0xffffu)),n,0x00020000u}; }
#define VMW(n) asm volatile("s_waitcnt vmcnt(%0)" :: "i"(n) : "memory")
#define YSLP 1
#define GSLP 1
#define QSLP 1
#define PSLP 3
#define POLLASM(LOADS,CHECK,K) "s_mov_b32 %[cnt], 0x20000\n1:\n\ts_waitcnt vmcnt(0)\n\t" CHECK "s_cbranch_vccz 9f\n\ts_sleep " STR(K) "\n\ts_mov_b64 exec, vcc\n\t" LOADS \
  "s_mov_b64 exec, -1\n\ts_sub_u32 %[cnt], %[cnt], 1\n\ts_cbranch_scc0 1b\n9:\n\t"
#define LD_DUP2 "buffer_load_dwordx4 v[112:115], %[of], %[rs], 0 offen sc1\n\tbuffer_load_dwordx4 v[116:119], %[of], %[rs], %[so] offen sc1\n\t"
#define MX2(a,b) "v_max3_u32 %[t0], %[t0], v" #a ", v" #b "\n\t"
#define CHK_I0 "v_max_u32 %[t0], v112, v113\n\t" MX2(114,115)
#define CHK_IE "v_cmp_eq_u32 vcc, -1, %[t0]\n\t"
#define PCLOB "memory","vcc","scc"
#define UMASK 0x40074007u
DEV u32 upat(u32 tag,int k){ return ((((tag*0xea959c21u)>>(8*k))&255u)*0x04000801u)&UMASK; }
DEV u32 pkz(float a,float b){ return BC(u32,__builtin_amdgcn_cvt_pkrtz(a,b)); }
#define INV 0xffffffffu
DEV u32 nv(u32 v){ return umin_(v,0xfffeffffu); }
DEV u32x4 nv4(u32x4 v){ return u32x4{nv(v[0]),nv(v[1]),nv(v[2]),nv(v[3])}; }
#define OOR 0x40000000

#define CHSZ 36864
#define O_CH 512
#define O_CND 256
#define SLSZ 38080
#define O_R2 (O_CH+2*SLSZ)
#define O_X2 (O_R2+CHSZ)
#define O_TILE (O_X2+CHSZ)
#define XSTR 1216
#define TSTR2 1216
#define O_ONE (O_TILE+9728+16)
#define O_CAND (O_ONE+1168+48)
#define LDS_TOTAL (O_CAND+128)
#define C_G 0
#define C_U 8
#define C_L 4
#define C_D 5
#define C_X 12
#define C_P 13
#define C_H 14
#define C_I 17
#define C_E 18
#define C_IX 19
#define C_RS 22
#define O_IDX 96
#define O_EXP 144
#define O_RMX 192
#define O_RV 128
#define O_MD 400
#define O_RC (O_CAND+64)

struct C2 { float r,i; };
DEV C2 cmul(C2 a,C2 b){ C2 c; c.r=__builtin_fmaf(a.r,b.r,-(a.i*b.i)); c.i=__builtin_fmaf(a.r,b.i,a.i*b.r); return c; }
DEV h8 ldA(const lch* p){ u32x2 a=*(const volatile LAS u32x2*)p, b=*(const volatile LAS u32x2*)(p+8); return BC(h8,(u32x4{a[0],a[1],b[0],b[1]})); }
DEV bf16x8 trfrag(const lch* p0,const lch* p1){
  u32x2 b0=BC(u32x2,__builtin_amdgcn_ds_read_tr16_b64_v4i16((LAS v4s*)p0)), b1=BC(u32x2,__builtin_amdgcn_ds_read_tr16_b64_v4i16((LAS v4s*)p1));
  return BC(bf16x8,(u32x4{b0[0],b0[1],b1[0],b1[1]})); }

struct FP { float gg[24], bs[24]; };
template<int Z0,int Z1> DEV void final_pre(FP& f,int w4,const u32x4* xq,const u32x4* dq,const f32x2* rc){
  #pragma unroll
  for(int z=Z0;z<Z1;z++) if(z<2||w4==0){ float ea=rc[z][0]*-1.4426950408889634f, eb=rc[z][1]*-1.4426950408889634f;
    #pragma unroll
    for(int e=0;e<4;e++){ u32 xw=xq[z][e], dw=dq[z>>1][e]; int i=8*z+2*e; float a=blo(xw), b=bhi(xw);
      float pa=__builtin_fmaf(a,rc[z][0],rc[z][1]), pb=__builtin_fmaf(b,rc[z][0],rc[z][1]);
      float ga=pa*rcpf_(1.f+__builtin_amdgcn_exp2f(__builtin_fmaf(a,ea,eb))), gb=pb*rcpf_(1.f+__builtin_amdgcn_exp2f(__builtin_fmaf(b,ea,eb)));
      f.gg[i]=ga; f.gg[i+1]=gb; f.bs[i]=__builtin_fmaf(pa*blo(dw),ga,a); f.bs[i+1]=__builtin_fmaf(pb*bhi(dw),gb,b);
      asm volatile("" : "+v"(f.gg[i]),"+v"(f.gg[i+1]),"+v"(f.bs[i]),"+v"(f.bs[i+1])); } }
}
#define FTS 544
DEV u32 pkmin(u32 a,u32 b){ u32 r; asm("v_pk_min_u16 %0, %1, %2" : "=v"(r) : "v"(a),"v"(b)); return r; }
struct FA { int yo,yo3; lch *wp,*wp3; const lch* tp; u32 tt; };
DEV void final_addr(FA& a,lch* sm,int w4){ LANE(ln); int bid=BID, xg=bid&7, lb=bid>>3, cg=lb&3, T=8*xg+(lb>>2);
  a.yo=(128*cg+32*w4+(ln>>2)*2)*YST+(32*T+8*(ln&3))*2; a.yo3= ln<32? (512+32*(cg&1)+(ln&30))*YST+(32*T+16*(cg>>1)+8*(ln&1))*2 : OOR;
  lch* fb=sm+O_X2+2048+w4*4608; a.wp=fb+(ln&3)*FTS+(ln&60); a.wp3=fb+(4+(ln&1)+(ln>>5)*2)*FTS+(ln&30)*2; a.tp=fb+(ln>>5)*FTS+(ln&31)*16;
  a.tt=(u32)(32*T+(ln>>2))*0x10001u;
  asm volatile("" : "+v"(a.yo),"+v"(a.yo3),"+v"(a.wp),"+v"(a.wp3),"+v"(a.tp),"+v"(a.tt)); }
DEV void final_post(lch* sm,FP& f,const FA& fa,int w4,u16* __restrict__ out,char* ws,int xr,u32 tk,u32 td,const u32x4* xq,const u32x4* dq,const f32x2* rc){
  int lb=BID>>3, cg=lb&3, ob[3], yo=fa.yo, yo3=fa.yo3; lch *wp=fa.wp, *wp3=fa.wp3; const lch* tp=fa.tp; u32 tt=fa.tt;
  u32x4 rs=mkrs(ws+W_Y,D_*YST); rsrc_t ro=RSRC(out,L_*D_*2);
  #define RMASK { gwait<2>(sm,C_IX,1); u32x4 iq=LD(u32x4,sm+O_IDX); \
    _Pragma("unroll") for(int z=0;z<2;z++){ u32 t2=tt+z*0x100010u, m=pkmin(pkmin(iq[0]^t2,iq[1]^t2),pkmin(iq[2]^t2,iq[3]^t2)); ob[z]= umin_(m&0xffffu,m>>16)==0u? OOR : xr+z*(16*D_*2); } \
    ob[2]=((cg&2)? ob[1]:ob[0])+(512+32*(cg&1)-128*cg)*2; asm volatile("" : "+v"(ob[0]),"+v"(ob[1]),"+v"(ob[2])); }
  tgate2(tk,td);
  if(lb>=8) gwait<1>(sm,C_H,4);
  { u32 t0,t1,cnt;
    #define LD_Y "buffer_load_dwordx4 v[112:115], %[of], %[rs], 0 offen sc1\n\tbuffer_load_dwordx4 v[116:119], %[of], %[rs], %[s1] offen sc1\n\ts_cmp_lg_u32 %[w], 0\n\ts_cbranch_scc1 5f\n\t" \
      "buffer_load_dwordx4 v[120:123], %[o3], %[rs], 0 offen sc1\n\tbuffer_load_dwordx4 v[124:127], %[o3], %[rs], %[s1] offen sc1\n5:\n\t"
    #define YIN [of]"v"(yo),[rs]"s"(rs),[s1]"s"(YST),[o3]"v"(yo3),[w]"s"(w4)
    asm volatile(LD_Y :: YIN : "memory","scc");
    PRIO(DPR); if(DF) final_pre<1,2>(f,w4,xq,dq,rc);
    PRIO(3);
    RMASK
    #define YW(p,a,b,o) "v_perm_b32 %[t0], v" #b ", v" #a ", %[lo]\n\tv_perm_b32 %[t1], v" #b ", v" #a ", %[hi]\n\tds_write2_b32 %[" #p "], %[t0], %[t1] offset0:" #o " offset1:" #o "+16\n\t"
    asm volatile(POLLASM(LD_Y,CHK_I0 MX2(116,117) MX2(118,119) "s_cmp_lg_u32 %[w], 0\n\ts_cbranch_scc1 6f\n\t" MX2(120,121) MX2(122,123) MX2(124,125) MX2(126,127) "6:\n\t" CHK_IE,YSLP)
      YW(wp,112,116,0) YW(wp,113,117,32) YW(wp,114,118,64) YW(wp,115,119,96) "s_cmp_lg_u32 %[w], 0\n\ts_cbranch_scc1 7f\n\t" YW(w3,120,124,0) YW(w3,121,125,32) YW(w3,122,126,64) YW(w3,123,127,96) "7:"
      : [t0]"=&v"(t0),[t1]"=&v"(t1),[cnt]"=&s"(cnt) : YIN,[wp]"v"(wp),[w3]"v"(wp3),[lo]"s"(0x05040100u),[hi]"s"(0x07060302u) : PCLOB); }
  u32x4 r[3];
  #pragma unroll
  for(int z=0;z<3;z++) r[z]=LD(u32x4,tp+z*(2*FTS));
  #pragma unroll
  for(int z=0;z<3;z++) if(z<2||w4==0){ u32x4 ov;
    #pragma unroll
    for(int e=0;e<4;e++){ u32 rw=r[z][e]; int i=8*z+2*e; ov[e]=pk(__builtin_fmaf(blo(rw),f.gg[i],f.bs[i]),__builtin_fmaf(bhi(rw),f.gg[i+1],f.bs[i+1])); }
    BST128(ov,ro,ob[z],0,SC1); SCHED(); }
}

#define TROW 528
#define O_TB 4640
#define O_DE 34752
#define O_DO 35968
#define O_R2K 37120
#define O_R3K 37632
#define O_DUM 37952
#define O_RED O_TB
#define MH(a,b,c) __builtin_amdgcn_mfma_f32_16x16x32_f16(a,b,c,0,0,0)
#define PIN(x) asm volatile("" : "+v"(x))
DEV h8 ld4w(const lch* p){ return BC(h8,(u32x4{LD(u32,p),LD(u32,p+4),LD(u32,p+8),LD(u32,p+12)})); }
DEV u32 __umul24_(u32 a,u32 b){ u32 r; asm("v_mul_u32_u24 %0, %1, %2" : "=v"(r) : "s"(b),"v"(a)); return r; }
DEV u32x4 band(u32 mask,int g,u32 val){ u32 by=__builtin_amdgcn_ubfe(mask,8*g,8), X=by|(by<<15), k=0x10001u; u32x4 r; asm volatile("" : "+s"(k),"+s"(val));
  #pragma unroll
  for(int d=0;d<4;d++) r[d]=__umul24_((X>>(2*d))&k,val);
  return r; }
DEV h8 zpack(f32x4 e,f32x4 o){ return BC(h8,(u32x4{pkh(e[0],o[0]),pkh(e[1],o[1]),pkh(e[2],o[2]),pkh(e[3],o[3])})); }
struct C3 { float r,i,n; };
DEV C3 c3(C2 a){ C3 c; c.r=a.r; c.i=a.i; PIN(c.i); c.n=-c.i; PIN(c.n); return c; }
DEV C2 cm(C2 a,C3 b){ C2 c; c.r=__builtin_fmaf(a.r,b.r,a.i*b.n); c.i=__builtin_fmaf(a.r,b.i,a.i*b.r); return c; }
DEV u32 ph(C2 a){ return pkh(a.r,a.i); }
#define W2(p,a,b) asm volatile("ds_write2_b32 %0, %1, %2 offset1:132" :: "v"(p),"v"(a),"v"(b) : "memory")
struct CP { const lch *x0,*x1,*bd; lch *p0,*p1,*p2,*wp; u32x4 Be,Bo,AE; int yo; };
template<int ROLE> DEV void lanes(CP& c,lch* cb,int ln){ int m=ln&15, g=ln>>4;
  const lch* ad=cb+((m&1)? O_DO+2*527 : O_DE+2*536)+2*(8*g-m);
  const lch* bd=cb+2*(256+16*m+8*g); lch* dum=cb+O_DUM;
  if(ROLE==0){ int l0=128*(g>>1)+8*m+4*(g&1); c.p0=cb+O_DE+(549-l0)*2; PIN(c.p0); c.p1=c.p0+6; PIN(c.p1); c.p2=c.p0+(O_DO-O_DE-18); c.x0=ad-640; c.bd=bd-64; c.x1=cb+O_R3K+2*(126-2*m+8*g)-192; PIN(c.x1);
    c.Be=band(0x1ffu<<(m+4),g,0x3c00u); PIN(c.Be); }
  if(ROLE==1){ lch* r=cb+O_R2K+(192-64*(g-2)-2*m)*2; c.p0= g<2? dum : r-66; c.p1= ln==0? cb+O_DE+(552-256)*2 : (ln<=32? dum : r); c.p2=cb+O_DE+(544-256-8*ln)*2; c.x0=ad-128-640; c.bd=bd-128-128; PIN(c.p0); PIN(c.p1); PIN(c.p2); }
  if(ROLE==2){ int i0=16*m+4*g; c.p0= m<8? cb+O_R3K+(125-i0)*2 : dum+2; c.p1= ln==0? cb+O_R2K+128 : (ln==8? cb+O_R3K : c.p0+6); c.x0=ad-320-640; c.bd=bd-320-128; PIN(c.p0); PIN(c.p1); }
  if(ROLE==3){ c.x0=cb+O_R2K+2*(188-4*m+8*g)-256; c.bd=cb+2*(248+8*m+8*g);
    c.Be=band(0x1fu<<(m+6),g,0x3c00u); c.Bo=band(0x7u<<(m+11),g,0x3c00u); PIN(c.Be); PIN(c.Bo); }
  PIN(c.x0); PIN(c.bd); }
template<int ROLE> DEV void lanes2(CP& c,lch* cb,int ln){ int m=ln&15, g=ln>>4;
  if(ROLE==0){ c.Bo=band(0x7fu<<(m+5),g,0x3c00u); c.AE=band(3u<<(m+12),g,0xffffu); PIN(c.Bo); PIN(c.AE); }
  c.wp=cb+O_RED+ln*16; c.yo=(g&1)*512+m*32+(g>>1)*16; PIN(c.wp); PIN(c.yo); }
template<int ROLE> DEV void chan2(CP& c,lch* sm,lch* cb,const lch*& fa,const lch*& fb,int grp,int d,int one_,int emax,u64 tag,char* ws){
  const f32x4 Z4={0,0,0,0}; h8 A[6]; f32x4 acc[8];
  #define LD_U "buffer_load_dwordx4 v[112:115], %[of], %[rs], 0 offen sc1\n\tbuffer_load_dwordx4 v[116:119], %[of], %[rs], %[s1] offen sc1\n\tbuffer_load_dwordx4 v[120:123], %[of], %[rs], %[s2] offen sc1\n\tbuffer_load_dwordx4 v[124:127], %[of], %[rs], %[s3] offen sc1\n\t"
  #define UC(t,a,b,c) "v_bitop3_b32 " t ", v" #a ", v" #b ", %[" #c "] bitop3:0x7e\n\t"
  #define UD(t,a,c) "v_bitop3_b32 " t ", " t ", v" #a ", %[" #c "] bitop3:0xf6\n\t"
  #define CHK_U UC("%[t0]",112,116,c0) UC("%[t1]",113,117,c1) UD("%[t0]",120,c0) UD("%[t1]",121,c1) UD("%[t0]",124,c0) UD("%[t1]",125,c1) UD("%[t0]",114,c2) UD("%[t1]",115,c3) UD("%[t0]",118,c2) UD("%[t1]",119,c3) \
    UD("%[t0]",122,c2) UD("%[t1]",123,c3) UD("%[t0]",126,c2) UD("%[t1]",127,c3) "v_bitop3_b32 %[t0], %[t0], %[t1], %[mk] bitop3:0xa8\n\tv_cmp_ne_u32 vcc, 0, %[t0]\n\t"
  #define UX(a,b,c,d,o) "v_xor_b32 v" #a ", %[e0], v" #a "\n\tv_xor_b32 v" #b ", %[e1], v" #b "\n\tv_xor_b32 v" #c ", %[e2], v" #c "\n\tv_xor_b32 v" #d ", %[e3], v" #d "\n\tds_write_b128 %[up], v[" #a ":" #d "] offset:" #o "\n\t"
  #define UPOLL { u32 t0,t1,cnt; asm volatile(POLLASM(LD_U,CHK_U,PSLP) UX(112,113,114,115,544) UX(116,117,118,119,1056) UX(120,121,122,123,1568) UX(124,125,126,127,2080) \
      : [t0]"=&v"(t0),[t1]"=&v"(t1),[cnt]"=&s"(cnt) : [of]"v"(c.p0),[rs]"s"(rsU),[s1]"s"((32>>TL)*GST_),[s2]"s"((64>>TL)*GST_),[s3]"s"((96>>TL)*GST_),[c0]"s"(c0),[c1]"s"(c1),[c2]"s"(c2),[c3]"s"(c3_), \
        [e0]"s"(c0^0x40004u),[e1]"s"(c1^0x40004u),[e2]"s"(c2^0x40004u),[e3]"s"(c3_^0x40004u),[mk]"s"(UMASK),[up]"v"(c.p1) : PCLOB); garrive(sm,C_U+one_); }
  if(ROLE<3){
    h8 a[8], b[8];
    #pragma unroll
    for(int ks=0;ks<8;ks++){ a[ks]=LD(h8,fa+ks*64); b[ks]=LD(h8,fb+ks*64); }
    SCHED();
    LANE(ln); if(!HO) lanes<ROLE>(c,cb,ln); float fz= ln==32? 0.f : 4.76837158203125e-07f; if(ROLE==1) PIN(fz);
    SCHED();
    f32x4 k=Z4;
    #pragma unroll
    for(int ks=0;ks<8;ks++) k=MH(a[ks],b[ks],k);
    const float f=4.76837158203125e-07f;
    float k0=k[0]*f, k1=k[1]*f, k2=k[2]*f, k3=k[3]*f;
    u32 h32=pkh(k3,k2), h10=pkh(k1,k0);
    if(ROLE!=1){
      ST(u16,c.p0)=(u16)h32; ST(u32,c.p0+2)=pkh(k2,k1); ST(u16,c.p1)=(u16)(h10>>16);
      if(ROLE==0) ST(u32x2,c.p2)=u32x2{h32,h10};
    } else {
      float n0=dppf<0x130>(k[0])*fz, z1= fz==0.f? k0:k1, z2=k[2]*fz, z3=k[3]*fz; u32 h22=pkh(z2,z2);
      ST(u16,c.p0+64)=(u16)h10; ST(u16,c.p0+2)=(u16)(h32>>16); ST(u16,c.p0)=(u16)h32; ST(u16,c.p1)=(u16)(h10>>16);
      u32x4 vo={pkh(n0,z3),h22,pkh(z2,z1),pkh(k0,k0)}, ve={pkh(n0,n0),pkh(z3,z2),h22,pkh(z1,k0)};
      asm volatile("s_mov_b32 exec_hi, 1\n\tds_write_b128 %0, %1\n\tds_write_b128 %0, %2 offset:1200\n\ts_mov_b32 exec_hi, -1" :: "v"(c.p2),"v"(ve),"v"(vo) : "memory"); }
    gsync(sm,C_G+grp,7);
    u32 kv; A[0]=ld4w(c.x0+640); A[1]=ld4w(c.x0+128);
    if(ROLE==0){ A[2]=ld4w(c.x1+192); A[3]=ld4w(c.x1+128); A[4]=ld4w(c.x1+64); A[5]=ld4w(c.x1); kv=LD(u16,cb+O_R2K+128); }
    else { A[2]=ld4w(c.x0+576); A[3]=ld4w(c.x0+64); A[4]=ld4w(c.x0+512); A[5]=ld4w(c.x0); }
    SCHED(); if(!HO) lanes2<ROLE>(c,cb,ln); SCHED();
    if(ROLE==0){ kv|=kv<<16; c.AE=u32x4{c.AE[0]&kv,c.AE[1]&kv,c.AE[2]&kv,c.AE[3]&kv}; PIN(c.AE); }
    int ha=W_HT+d*8; u32 hd= one_? HD3 : (grp==0? HD0:HD1); if(ROLE==2) asm volatile("v_mov_b32 v113, %2" : "+v"(ha),"+v"(hd) : "s"((u32)tag));
    PRIO(2);
    if(BID>=112) gwait<4>(sm,UE? C_D:C_X,4);
    gwait<1>(sm,C_U+one_,2-one_);
    if(ROLE==2) asm volatile("s_memrealtime s[98:99]\n\ts_waitcnt lgkmcnt(0)\n\tv_add_u32 v112, s98, %1\n\ts_mov_b64 exec, 1\n\tglobal_store_dwordx2 %0, v[112:113], %2 sc1\n\ts_mov_b64 exec, -1" :: "v"(ha),"v"(hd),"s"(ws) : "memory");
  } else {
    u32 tg=(u32)tag, c0=upat(tg,0), c1=upat(tg,1), c2=upat(tg,2), c3_=upat(tg,3); u32x4 rsU=mkrs(ws+W_U,256*UST);
    if(!one_) PRIO(2);
    LANE(ln); c.p0=(lch*)fa; c.p1=(lch*)fb;
    bool late= grp==0||BID>=112;
    if(!late) asm volatile(LD_U :: [of]"v"(c.p0),[rs]"s"(rsU),[s1]"s"((32>>TL)*GST_),[s2]"s"((64>>TL)*GST_),[s3]"s"((96>>TL)*GST_) : "memory");
    asm volatile("s_mov_b64 exec, 1\n\tglobal_store_dwordx2 %0, %1, %2 sc1\n\ts_mov_b64 exec, -1" :: "v"(W_FL+d*8),"v"(tag),"s"(ws) : "memory");
    if(!HO){ lanes<3>(c,cb,ln); lanes2<3>(c,cb,ln); }
    #pragma unroll
    for(int j=2;j<8;j++){ acc[j]=Z4; PIN(acc[j]); }
    if(late){ if(US&&BID<USB) __builtin_amdgcn_s_sleep(US); else if(UE==0||BID<112) gwait<2>(sm,C_G,UGATE); if(BID>=112){ gwait<1>(sm,UE? C_RS:C_D,UN); if(UE>1) __builtin_amdgcn_s_sleep(UE); } asm volatile(LD_U :: [of]"v"(c.p0),[rs]"s"(rsU),[s1]"s"((32>>TL)*GST_),[s2]"s"((64>>TL)*GST_),[s3]"s"((96>>TL)*GST_) : "memory"); }
    if(!late&&!(LO==2||(LO==1&&one_))) UPOLL
    gwait<1>(sm,C_G+grp,7);
    #pragma unroll
    for(int i=0;i<6;i++) A[i]=ldA(c.x0+320-64*i);
    if(late||LO==2||(LO==1&&one_)) UPOLL
    PRIO(2);
    gwait<1>(sm,C_U+one_,2-one_);
  }
  const lch* bd=c.bd; h8 Be=BC(h8,c.Be), Bo=BC(h8,c.Bo);
  #pragma unroll
  for(int j=0;j<(ROLE==3? 2:8);j++) acc[j]=Z4;
  #define RW(j) if((j)>=0&&((j)>>1)!=ROLE&&!(ROLE==3&&(j)<2)){ ST(f32x4,c.wp+((j)>>1)*6144+((ROLE-((j)>>1)-1)&3)*2048+((j)&1)*1024)=acc[(j)<0? 0:(j)]; SCHED(); }
  if(ROLE==1||ROLE==2){
    h8 B[8];
    #pragma unroll
    for(int q=0;q<8;q++) B[q]=LD(h8,bd+128+512*q);
    #pragma unroll
    for(int r=0;r<3;r++){
      #pragma unroll
      for(int dl=0;dl<8;dl++){ if(dl<7) acc[dl+1]=MH(A[2*r+1],B[dl],acc[dl+1]); SCHED(); acc[dl]=MH(A[2*r],B[dl],acc[dl]); SCHED();
        if(r<2){ B[dl]=LD(h8,bd+64-64*r+512*dl); SCHED(); } else RW(dl-2) }
      } }
  if(ROLE==0){
    h8 B[8], ue[4];
    #pragma unroll
    for(int kt=0;kt<4;kt++){ ue[kt]=LD(h8,bd+48+512*kt); B[kt]=LD(h8,bd+64+512*kt); }
    f32x4 ce[2],co[2]; h8 Z[2];
    ce[0]=MH(ue[0],Be,Z4); co[0]=MH(B[0],Bo,Z4); ce[1]=MH(ue[1],Be,Z4); co[1]=MH(B[1],Bo,Z4); Z[0]=zpack(ce[0],co[0]);
    #pragma unroll
    for(int kt=0;kt<4;kt++){
      SCHED();
      if(kt+2<4){ ce[kt&1]=MH(ue[kt+2],Be,Z4); co[kt&1]=MH(B[kt+2],Bo,Z4); }
      if(kt+1<4) Z[(kt+1)&1]=zpack(ce[(kt+1)&1],co[(kt+1)&1]);
      #pragma unroll
      for(int rho=0;rho+kt<4;rho++) acc[4+kt+rho]=MH(Z[kt&1],A[2+rho],acc[4+kt+rho]);
      SCHED(); B[4+kt]=LD(h8,bd+64+512*(4+kt)); if(kt==0) A[5]=BC(h8,c.AE); if(kt==1) A[4]=ld4w(c.x0-384); if(kt==2) A[3]=ld4w(c.x0+64); if(kt==3) A[2]=ld4w(c.x0+576); SCHED(); }
    #pragma unroll
    for(int dl=0;dl<8;dl++){ if(dl<4) acc[dl+4]=MH(A[5],B[dl],acc[dl+4]); SCHED(); if(dl<6) acc[dl+2]=MH(A[4],B[dl],acc[dl+2]); SCHED(); if(dl<7) acc[dl+1]=MH(A[1],B[dl],acc[dl+1]); SCHED(); acc[dl]=MH(A[0],B[dl],acc[dl]);
      SCHED(); B[dl]=LD(h8,bd+512*dl); SCHED(); }
    #pragma unroll
    for(int dl=0;dl<8;dl++){ if(dl<7) acc[dl+1]=MH(A[3],B[dl],acc[dl+1]); SCHED(); acc[dl]=MH(A[2],B[dl],acc[dl]); SCHED(); RW(dl-2) } }
  if(ROLE==3){
    h8 U[12];
    #pragma unroll
    for(int kt=0;kt<6;kt++) U[kt]=LD(h8,bd+256*kt);
    f32x4 ce[2],co[2]; h8 Z[2];
    ce[0]=MH(U[0],Be,Z4); co[0]=MH(U[0],Bo,Z4); ce[1]=MH(U[1],Be,Z4); co[1]=MH(U[1],Bo,Z4); Z[0]=zpack(ce[0],co[0]);
    #pragma unroll
    for(int kt=0;kt<12;kt++){
      SCHED();
      if(kt+2<12){ ce[kt&1]=MH(U[kt+2],Be,Z4); co[kt&1]=MH(U[kt+2],Bo,Z4); }
      if(kt+1<12) Z[(kt+1)&1]=zpack(ce[(kt+1)&1],co[(kt+1)&1]);
      #pragma unroll
      for(int rho=-1;rho<5;rho++){ int j=(kt+4+rho)>>1; if(((kt+rho)&1)==0&&j>=2&&j<8) acc[j]=MH(Z[kt&1],A[rho+1],acc[j]); }
      if(kt<6){ SCHED(); U[kt+6]=LD(h8,bd+256*(kt+6)); }
      if(kt>=3&&kt<=9&&(kt&1)){ SCHED(); RW((kt+1)/2) } }
  }
  PRIO(3);
  RW(6) RW(7)
  gsync(sm,C_G+grp,11);
  { const lch* rp=c.wp+ROLE*6144; f32x4 a[3], b[3], o0=acc[2*ROLE], o1=acc[2*ROLE+1];
    #pragma unroll
    for(int q=0;q<(ROLE==0? 2:3);q++){ a[q]=LD(f32x4,rp+q*2048); b[q]=LD(f32x4,rp+q*2048+1024); }
    SCHED();
    float sc=__builtin_amdgcn_ldexpf(1.f,emax-YSH), s0[4], s1[4];
    #pragma unroll
    for(int e=0;e<4;e++){ s0[e]=((o0[e]+a[0][e])+(ROLE==0? a[1][e] : a[1][e]+a[2][e]))*sc; s1[e]=((o1[e]+b[0][e])+(ROLE==0? b[1][e] : b[1][e]+b[2][e]))*sc; }
    auto r0=__builtin_amdgcn_permlane16_swap(nv(pk(s0[0],s0[1])),nv(pk(s1[0],s1[1])),false,false); auto r1=__builtin_amdgcn_permlane16_swap(nv(pk(s0[2],s0[3])),nv(pk(s1[2],s1[3])),false,false);
    BST128(u32x4{r0[0],r1[0],r0[1],r1[1]},RSRC(ws+W_Y+d*YST,4096),c.yo+ROLE*1024,0,SC1); }
  garrive(sm,C_E);

  asm volatile("s_endpgm" : "=v"(fa),"=v"(fb),"=v"(c.x0),"=v"(c.x1),"=v"(c.bd),"=v"(c.p0),"=v"(c.p1),"=v"(c.p2),"=v"(c.wp),"=v"(c.Be),"=v"(c.Bo),"=v"(c.AE),"=v"(c.yo) :: "memory");
}
DEV void pf2(int e,int xg,const u16* wd){ LANE(ln); u32 q=e*64+ln, r=(q*21846u)>>16, c=q-3u*r;
  asm volatile("buffer_load_dword v123, %0, %1, 0 offen" :: "v"((576*xg+288*(r&1)+287-(r>>1))*(D_*2)+384*c+256),"s"(mkrs(wd,D_*F_*2)) : "memory"); }
#define GETEP asm volatile("s_waitcnt lgkmcnt(0)\n\ts_mov_b64 %0, s[96:97]\n\ts_mov_b32 %1, s98" : "=s"(ep),"=s"(ldw));
DEV void chan(lch* sm,int grp,int w4,int d,int one_,char* ws,u32x4 q0,u32x4 q1,u32x4 q2,u32x4 q3,u32x4 q4){
  lch* cb=sm+O_CH+grp*SLSZ; int role=w4, emax;
  #define LP(v) ((lch*)(size_t)(u32)(v))
  const lch* fa=LP(q4[0]); const lch* fb=LP(q4[1]);
  u64 tag; CP c;
  { float a2,bp,br,bi,cr,ci;
    u32 fq=q3[2], fr=q3[3], one=q4[2]; float f64=BC(float,fq), f128=BC(float,fr);
    lch *t8[4]={LP(q0[0]),LP(q0[1]),LP(q0[2]),LP(q0[3])}, *tB[4]={LP(q1[0]),LP(q1[1]),LP(q1[2]),LP(q1[3])}, *t4[2]={LP(q2[0]),LP(q2[1])}, *t2[2]={LP(q2[2]),LP(q2[3])}, *t1=LP(q3[0]), *te=LP(q3[1]);
    if(HO){ LANE(ln); asm volatile("" : "=v"(c.x1),"=v"(c.p0),"=v"(c.p1),"=v"(c.p2),"=v"(c.Be),"=v"(c.Bo),"=v"(c.AE));
      if(role==0){ lanes<0>(c,cb,ln); lanes2<0>(c,cb,ln); } else { if(role==1) lanes<1>(c,cb,ln); else if(role==2) lanes<2>(c,cb,ln); else lanes<3>(c,cb,ln); lanes2<1>(c,cb,ln); }
      asm volatile("" : "+v"(c.x0),"+v"(c.x1),"+v"(c.bd),"+v"(c.p0),"+v"(c.p1),"+v"(c.p2),"+v"(c.wp),"+v"(c.Be),"+v"(c.Bo),"+v"(c.AE),"+v"(c.yo)); }
    if(IV==2) asm volatile("global_store_dwordx4 v123, v[124:127], %0 sc1" :: "s"(ws) : "memory");
    if(grp!=0) PRIO(1);
    u64 ep; u32 ldw; GETEP tag=ep+1ull;
    float dt=rb(expf_((d&1)? bhi(ldw):blo(ldw))), ca=dt*1.4426950408889634f, cb_=dt*0.15915494309189535f;
    asm volatile("s_waitcnt vmcnt(1)\n\tv_mul_f32 %2, %8, v114\n\tv_mul_f32 %3, %8, v115\n\tv_mul_f32 %0, %6, v112\n\tv_mul_f32 %1, %7, v113\n\tv_mov_b32 %4, v116\n\tv_mov_b32 %5, v117"
      : "=&v"(a2),"=&v"(bp),"=&v"(br),"=&v"(bi),"=&v"(cr),"=&v"(ci) : "v"(ca),"v"(cb_),"v"(dt) : "memory");
    C2 W; W.r=cr*br-ci*bi; W.i=cr*bi+ci*br;
    u32 mx=BC(u32,__builtin_fmaxf(__builtin_fabsf(W.r),__builtin_fabsf(W.i)));
    #define MXS(C) mx=umax_(mx,dppu<C>(mx));
    float e8=a2*8.f, eB=__builtin_fmaf(a2,f64,14.f), p8=bp*8.f, pB=bp*f64; SCHED();
    MXS(0xB1) p8=__builtin_amdgcn_fractf(p8); pB=__builtin_amdgcn_fractf(pB); e8=__builtin_amdgcn_exp2f(e8); SCHED();
    MXS(0x4E) eB=__builtin_amdgcn_exp2f(eB); float c8=__builtin_amdgcn_cosf(p8); SCHED();
    MXS(0x141) float s8=__builtin_amdgcn_sinf(p8), cB=__builtin_amdgcn_cosf(pB); SCHED();
    float sB=__builtin_amdgcn_sinf(pB); PIN(sB); MXS(0x140) float e1=__builtin_amdgcn_exp2f(a2); PIN(e1); SCHED();
    asm volatile("s_mov_b64 exec, 0x10001\n\tds_max_u32 %0, %1\n\tds_add_u32 %0, %2 offset:4\n\ts_mov_b64 exec, -1" :: "v"(te),"v"(mx),"v"(one) : "memory");
    C3 z8; z8.r=e8*c8; z8.n=e8*s8; z8.i=-z8.n; PIN(z8.i); C2 B; B.r=eB*cB; B.i=-(eB*sB);
    float p1=__builtin_amdgcn_fractf(bp), pE=__builtin_amdgcn_fractf(bp*256.f), pV=__builtin_amdgcn_fractf(bp*f128), eE=a2*256.f, eV=a2*f128, tr[8];
    #pragma unroll
    for(int k=0;k<4;k++){ SCHED(); u32 h0=ph(B); B=cm(B,z8);
      if(k==0){ tr[0]=__builtin_amdgcn_cosf(pV); tr[1]=__builtin_amdgcn_sinf(pV); } if(k==1){ tr[2]=__builtin_amdgcn_exp2f(eV); tr[3]=__builtin_amdgcn_cosf(p1); }
      if(k==2){ tr[4]=__builtin_amdgcn_sinf(p1); tr[5]=__builtin_amdgcn_exp2f(eE); } if(k==3){ tr[6]=__builtin_amdgcn_cosf(pE); tr[7]=__builtin_amdgcn_sinf(pE); }
      PIN(tr[2*k]); PIN(tr[2*k+1]);
      u32 h1=ph(B); if(k<3) B=cm(B,z8); W2(tB[k],h0,h1); }
    asm volatile("ds_read_b64 v[118:119], %0" :: "v"(te) : "memory");
    SCHED();
    C3 z1=c3(C2{e1*tr[3],e1*tr[4]}), E=c3(C2{tr[5]*tr[6],tr[5]*tr[7]}); C2 V[8]; V[0]=cmul(W,C2{tr[2]*tr[0],tr[2]*tr[1]});
    { u32 cnt,n; PIN(V[0].r); PIN(V[0].i); PIN(z1.r); PIN(E.r);
      asm volatile("s_mov_b32 %1, 0x20000\n1:\n\ts_waitcnt lgkmcnt(0)\n\tv_readfirstlane_b32 %2, v119\n\tv_readfirstlane_b32 %0, v118\n\ts_cmp_eq_u32 %2, 8\n\ts_cbranch_scc1 2f\n\tds_read_b64 v[118:119], %3\n\ts_sub_u32 %1, %1, 1\n\ts_cbranch_scc0 1b\n2:"
        : "=&s"(emax),"=&s"(cnt),"=&s"(n) : "v"(te) : "memory","scc");
      emax=RFL(emax); emax=(emax>>23)-126; }
    V[0].r=__builtin_amdgcn_ldexpf(V[0].r,14-emax); V[0].i=__builtin_amdgcn_ldexpf(V[0].i,14-emax);
    C2 Y=cm(V[0],E), Y4; u32 y0=ph(Y);
    V[1]=cm(V[0],z1); W2(t8[0],ph(V[0]),ph(V[1])); Y=cm(Y,E); u32 y1=ph(Y);
    V[2]=cm(V[1],z1); Y=cm(Y,E); u32 y2=ph(Y);
    V[3]=cm(V[2],z1); W2(t8[1],ph(V[2]),ph(V[3])); W2(t4[0],y0,ph(cm(V[2],E)));
    V[4]=cm(V[3],z1); Y=cm(Y,E); ST(u32,t1)=ph(Y);
    V[5]=cm(V[4],z1); W2(t8[2],ph(V[4]),ph(V[5])); Y4=cm(V[4],E); u32 y4=ph(Y4);
    V[6]=cm(V[5],z1); Y=cm(Y,E); ST(u32,t1+2*TROW)=ph(Y);
    V[7]=cm(V[6],z1); W2(t8[3],ph(V[6]),ph(V[7])); Y4=cm(Y4,E); W2(t2[0],y1,ph(Y4));
    W2(t4[1],y4,ph(cm(V[6],E))); Y=cm(Y,E); ST(u32,t1+4*TROW)=ph(Y);
    Y4=cm(Y4,E); W2(t2[1],y2,ph(Y4)); Y=cm(Y,E); ST(u32,t1+6*TROW)=ph(Y);
    Y=cm(Y,E); asm volatile("s_mov_b32 exec_hi, 0\n\tds_write_b32 %0, %1 offset:4224\n\ts_mov_b32 exec_hi, -1" :: "v"(t1),"v"(ph(Y)) : "memory");
  }
  if(one_) PRIO(3);
  DRAIN();
  gsync(sm,C_G+grp,4);
  if(role==0) chan2<0>(c,sm,cb,fa,fb,grp,d,one_,emax,tag,ws);
  if(role==1) chan2<1>(c,sm,cb,fa,fb,grp,d,one_,emax,tag,ws);
  if(role==2) chan2<2>(c,sm,cb,fa,fb,grp,d,one_,emax,tag,ws);
  chan2<3>(c,sm,cb,fa,fb,grp,d,one_,emax,tag,ws);
}

struct XR { int so,x0,x2; u32x4 rx,q[4]; };
DEV void xreq(XR& X,char* ws){ asm volatile("global_load_dwordx2 v[108:109], %[so], %[ws] sc1\n\tglobal_load_dwordx2 v[110:111], %[so], %[ws] offset:128 sc1\n\tbuffer_load_dwordx4 %[a], %[x0], %[rx], 0 offen\n\tbuffer_load_dwordx4 %[b], %[x0], %[rx], %[s1] offen\n\tbuffer_load_dwordx4 %[c], %[x2], %[rx], 0 offen"
  : [a]"=&{v[96:99]}"(X.q[0]),[b]"=&{v[100:103]}"(X.q[1]),[c]"=&{v[104:107]}"(X.q[2]),"=&{v[108:111]}"(X.q[3]) : [so]"v"(X.so),[ws]"s"(ws),[x0]"v"(X.x0),[x2]"v"(X.x2),[rx]"s"(X.rx),[s1]"s"(16*D_*2) : "memory"); }
DEV u32 med3u(u32 a,u32 b,u32 c){ u32 r; asm("v_med3_u32 %0, %1, %2, %3" : "=v"(r) : "v"(a),"v"(b),"v"(c)); return r; }
DEV u32 max3u(u32 a,u32 b,u32 c){ return umax_(umax_(a,b),c); }
#define GTC(r,s) "v_cmp_gt_u32 %[" #s "], v" #r ", %[c]\n\t"
#define GTA(s) "v_addc_co_u32 %[rk], vcc, 0, %[rk], %[" #s "]\n\t"
#define GT4(a,b,c,d) GTC(a,s0) GTC(b,s1) GTC(c,s2) GTC(d,s3) GTA(s0) GTA(s1) GTA(s2) GTA(s3)
#define GTS [s0]"=&s"(q0),[s1]"=&s"(q1),[s2]"=&s"(q2),[s3]"=&s"(q3)
DEV void top8(lch* sm,int w4,int ln,u32 M,char* ws,u32& rank_,u32& key_,XR& X){
  u32 k[8], rc[8], hm=0xffff0000u; u64 em=0x0001000100010001ull,q0,q1,q2,q3;
  u32x4 rs=mkrs(ws+W_RG+(BID&(NRG-1))*8192,8192); int ro=w4*1024+ln*16;
  #pragma unroll
  for(int e=0;e<8;e++){ rc[e]=(u32)(0xfff-(w4*256+ln*4+(e&3)+(e>>2)*1024)); PIN(rc[e]); }
  lch *aW=sm+O_RMX+(w4*4+(ln>>4))*4, *aC=sm+O_RMX+(ln>>2)*4, *aO=sm+O_RMX+(ln&3)*16, *dC=sm+O_CND+(ln>>1)*4, *dO=sm+O_CND+(ln&1)*64; PIN(aW); PIN(aC); PIN(aO); PIN(dC); PIN(dO); PIN(ro); PIN(ln);
  asm volatile("" : "+s"(hm),"+s"(em),"+s"(rs));
  gwait<2>(sm,C_G,RGATE);
  { u32 cnt;
    #define KL(e,r) "v_lshl_or_b32 %[k" #e "], v" #r ", 16, %[k" #e "]\n\t"
    #define KH(e,r) "v_and_or_b32 %[k" #e "], v" #r ", %[hm], %[k" #e "]\n\t"
    #define CHK_R "v_bitop3_b32 %[t0], v113, v115, %[M] bitop3:0x7e\n\tv_bitop3_b32 %[t1], v117, v119, %[M] bitop3:0x7e\n\tv_or_b32 %[t0], %[t0], %[t1]\n\tv_cmp_ne_u32 vcc, 0, %[t0]\n\t"
    u32 t0,t1;
    asm volatile(LD_DUP2
      "s_sleep " STR(R2D) "\n\tbuffer_load_dwordx4 v[120:123], %[of], %[rs], 0 offen sc1\n\tbuffer_load_dwordx4 v[124:127], %[of], %[rs], %[so] offen sc1\n\ts_mov_b32 %[cnt], 0x20000\n\ts_waitcnt vmcnt(2)\n\t" CHK_R "s_cbranch_vccz 9f\n\t"
      "s_waitcnt vmcnt(0)\n\tv_mov_b64 v[112:113], v[120:121]\n\tv_mov_b64 v[114:115], v[122:123]\n\tv_mov_b64 v[116:117], v[124:125]\n\tv_mov_b64 v[118:119], v[126:127]\n\t"
      POLLASM(LD_DUP2,CHK_R,PSLP)
      KL(0,112) KH(1,112) KL(2,114) KH(3,114) KL(4,116) KH(5,116) KL(6,118) KH(7,118)
      : [k0]"+v"(rc[0]),[k1]"+v"(rc[1]),[k2]"+v"(rc[2]),[k3]"+v"(rc[3]),[k4]"+v"(rc[4]),[k5]"+v"(rc[5]),[k6]"+v"(rc[6]),[k7]"+v"(rc[7]),[cnt]"=&s"(cnt),[t0]"=&v"(t0),[t1]"=&v"(t1) : [of]"v"(ro),[rs]"s"(rs),[so]"s"(4096),[M]"s"(M),[hm]"s"(hm) : PCLOB); }
  if(UE) garrive(sm,C_RS);
  #pragma unroll
  for(int e=0;e<8;e++) k[e]=rc[e];
  u32 h=max3u(k[0],k[1],k[2]), m1=max3u(max3u(k[3],k[4],k[5]),max3u(k[6],k[7],h),h), mx=m1, l=med3u(k[0],k[1],k[2]), t=umin_(umin_(k[0],k[1]),k[2]);
  SCHED();
  #define INS(e) t=med3u(t,l,k[e]); l=med3u(h,l,k[e]); if(e<7) h=umax_(h,k[e]); PIN(t); PIN(l); SCHED();
  mx=umax_(mx,dppu<0xB1>(mx)); INS(3) mx=umax_(mx,dppu<0x4E>(mx)); INS(4) mx=umax_(mx,dppu<0x141>(mx)); INS(5) mx=umax_(mx,dppu<0x140>(mx));
  asm volatile("s_mov_b64 exec, %2\n\tds_write_b32 %0, %1\n\ts_mov_b64 exec, -1\n\tds_read_b32 v116, %3\n\tds_read_b128 v[112:115], %4" :: "v"(aW),"v"(mx),"s"(em),"v"(aC),"v"(aO) : "memory");
  if(XB==2) xreq(X,ws);
  INS(6) INS(7)
  u32 m2=l, m3=t, T;
  { u32 c,rk,cnt;
    asm volatile("s_mov_b32 %[cnt], 0x20000\n1:\n\ts_waitcnt lgkmcnt(0)\n\tv_cmp_eq_u32 vcc, 0, v116\n\ts_cbranch_vccz 2f\n\tds_read_b32 v116, %[aC]\n\tds_read_b128 v[112:115], %[aO]\n\ts_sub_u32 %[cnt], %[cnt], 1\n\ts_cbranch_scc0 1b\n2:\n\t"
      "v_mov_b32 %[c], v116\n\tv_mov_b32 %[rk], 0\n\t" GT4(112,113,114,115) "s_nop 1\n\tv_add_u32_dpp %[rk], %[rk], %[rk] quad_perm:[1,0,3,2] row_mask:0xf bank_mask:0xf\n\ts_nop 1\n\tv_add_u32_dpp %[rk], %[rk], %[rk] quad_perm:[2,3,0,1] row_mask:0xf bank_mask:0xf\n\t"
      "v_cmp_eq_u32 vcc, 7, %[rk]\n\ts_ff1_i32_b64 %[cnt], vcc\n\ts_nop 0\n\tv_readlane_b32 %[T], %[c], %[cnt]\n\ts_nop 1"
      : [c]"=&v"(c),[rk]"=&v"(rk),[cnt]"=&s"(cnt),[T]"=&s"(T),GTS : [aC]"v"(aC),[aO]"v"(aO) : PCLOB); T=RFL(T); }
  { u64 b1=BALLOT(m1>=T), b2=BALLOT(m2>=T), b3=BALLOT(m3>=T); int n1=__builtin_popcountll(b1), n2=n1+__builtin_popcountll(b2);
    lch* cw=sm+O_CND+w4*32;
    if(__builtin_expect(b3==0ull&&n2<=8,1)){
      int p1=(int)__builtin_amdgcn_mbcnt_hi((u32)(b1>>32),__builtin_amdgcn_mbcnt_lo((u32)b1,0u)), pf=n2+ln-p1; bool c1=m1>=T;
      if(__builtin_expect(b2!=0ull,0)){ int p2=(int)__builtin_amdgcn_mbcnt_hi((u32)(b2>>32),__builtin_amdgcn_mbcnt_lo((u32)b2,(u32)n1)); if(m2>=T) ST(u32,cw+p2*4)=m2; }
      int ps= c1? p1:pf; ps= ps<8? ps:32-8*w4; ST(u32,cw+ps*4)= c1? m1:1u;
    } else {
      u32 mine=0;
      _Pragma("unroll 1") for(int c=0;c<8;c++){ u32 lm=umax_(umax_(umax_(k[0],k[1]),umax_(k[2],k[3])),umax_(umax_(k[4],k[5]),umax_(k[6],k[7]))); u32 wm=wmax(lm); if(ln==c) mine=wm;
        #pragma unroll
        for(int e=0;e<8;e++) k[e]=(k[e]==wm)?0u:k[e]; }
      if(ln<8) ST(u32,cw+ln*4)=mine; } }
  { u32 c,rk,cnt;
    asm volatile("s_mov_b32 %[cnt], 0x20000\n\tv_mov_b32 %[rk], 0\n\ts_waitcnt vmcnt(" XBW ")\n1:\n\tds_read_b32 %[c], %[dC]\n\tds_read_b128 v[112:115], %[dO]\n\tds_read_b128 v[116:119], %[dO] offset:16\n\tds_read_b128 v[120:123], %[dO] offset:32\n\tds_read_b128 v[124:127], %[dO] offset:48\n\t"
      "s_waitcnt lgkmcnt(4)\n\tv_cmp_eq_u32 vcc, 0, %[c]\n\ts_cbranch_vccz 2f\n\ts_sub_u32 %[cnt], %[cnt], 1\n\ts_cbranch_scc0 1b\n2:\n\t"
      "s_waitcnt lgkmcnt(3)\n\t" GT4(112,113,114,115) "s_waitcnt lgkmcnt(2)\n\t" GT4(116,117,118,119) "s_waitcnt lgkmcnt(1)\n\t" GT4(120,121,122,123) "s_waitcnt lgkmcnt(0)\n\t" GT4(124,125,126,127)
      "s_nop 1\n\tv_add_u32_dpp %[rk], %[rk], %[rk] quad_perm:[1,0,3,2] row_mask:0xf bank_mask:0xf"
      : [c]"=&v"(c),[rk]"=&v"(rk),[cnt]"=&s"(cnt),GTS : [dC]"v"(dC),[dO]"v"(dO) : PCLOB);
    rank_=rk; key_=c; }
}

#define FILL_(n,WAIT) "s_mov_b32 m0, %[ld]\n\ts_mov_b32 %[c], " STR(n) "-1\n3:\n\t" WAIT "buffer_load_dwordx4 %[vo], %[rs], %[so] offen nt lds\n\ts_add_u32 m0, m0, 0x1000\n\ts_add_u32 %[so], %[so], %[st]\n\t" \
  "s_sub_u32 %[c], %[c], 1\n\ts_cbranch_scc0 3b\n\ts_mov_b32 %[ld], m0\n\t"
#define FILL(n,WAIT) do{ u32 c_; asm volatile(FILL_(n,WAIT) : [c]"=&s"(c_),[so]"+s"(wso),[ld]"+s"(wld) : [vo]"v"(wvo),[rs]"s"(wrs),[st]"s"(wst) : "memory","scc","m0"); }while(0)
#define FILLD(n,WN,WW_) do{ u32 c_,t_,s_; asm volatile(FILL_(n,"ds_read_b32 %[t], %[ad]\n\ts_waitcnt lgkmcnt(0)\n\tv_readfirstlane_b32 %[s], %[t]\n\ts_cmp_ge_u32 %[s], 4\n\ts_cbranch_scc1 4f\n\t" VW(WN) "4:\n\t" VW(WW_)) \
  : [c]"=&s"(c_),[so]"+s"(wso),[ld]"+s"(wld),[t]"=&v"(t_),[s]"=&s"(s_) : [vo]"v"(wvo),[rs]"s"(wrs),[st]"s"(wst),[ad]"v"(C_D*4) : "memory","scc","m0"); wso=RFL(wso); wld=RFL(wld); }while(0)
#define VW(n) "s_waitcnt vmcnt(" STR(n) ")\n\t"

__global__ NOPK __attribute__((amdgpu_num_vgpr(56),amdgpu_num_sgpr(102))) __launch_bounds__(1024,4) void kA(const u16* x_,char* ws_,const u16* wr_,const u16* Dsk_,const u16* wg_,const u16* wu_,const u16* wd_,u16* out_,KP kp){
  const u16 *x,*Dsk,*wg,*wu,*wd; char* ws; u16* out;
  __shared__ __attribute__((aligned(16))) char smem[LDS_TOTAL];
  lch* sm=(lch*)smem;
  u32x4 xa,xb_,wa,wb,dq[3]; u32 xc,wc;
  #define SARR "v_mov_b32 v121, 80\n\tv_mov_b32 v122, 1\n\ts_mov_b64 exec, 1\n\tds_add_u32 v121, v122\n\ts_mov_b64 exec, -1\n\t"
  asm volatile(
    "v_readfirstlane_b32 s60, %[t]\n\tv_lshlrev_b32 v119, 1, %[t]\n\tv_mov_b32 v118, 0\n\ts_cmpk_ge_u32 s60, 0x100\n\ts_cbranch_scc1 5f\n\tds_write_b16 v119, v118\n\ts_waitcnt lgkmcnt(0)\n5:\n\t"
    "s_load_dwordx16 s[64:79], %[ka], 0x0\n\ts_load_dwordx16 s[80:95], %[ka], 0x40\n\t"
    "s_and_b32 s61, %[wg], 7\n\ts_lshr_b32 s62, %[wg], 3\n\tv_and_b32 v118, 31, %[t]\n\ts_bfe_u32 s63, s60, 0x20006\n\ts_cmpk_ge_u32 s60, 0x100\n\ts_cbranch_scc1 1f\n\t"
    "s_barrier\n\ts_setprio 3\n\ts_lshl_b32 s56, s61, 5\n\ts_add_u32 s56, s56, s62\n\ts_mulk_i32 s56, 0x2400\n\tv_lshrrev_b32 v119, 5, %[t]\n\tv_mul_u32_u24 v119, 0x480, v119\n\tv_add_u32 v119, s56, v119\n\t"
    "v_lshl_add_u32 v116, v118, 4, v119\n\tv_lshl_add_u32 v117, v118, 2, v119\n\tv_lshlrev_b32 v114, 4, v118\n\tv_lshlrev_b32 v115, 2, v118\n\t"
    "s_and_b32 s57, s62, 3\n\ts_lshl2_add_u32 s57, s57, s63\n\ts_lshl_b32 s57, s57, 6\n\ts_and_b32 s58, s62, 1\n\ts_lshl_b32 s58, s58, 6\n\tv_and_b32 v112, 3, %[t]\n\tv_lshl_add_u32 v113, v112, 4, s57\n\tv_lshl_add_u32 v112, v112, 4, s58\n\t"
    "s_waitcnt lgkmcnt(0)\n\t"
    "s_memrealtime s[98:99]\n\ts_load_dwordx2 s[96:97], s[66:67], 0x0\n\t"
    "global_load_dwordx4 %[wa], v114, s[68:69]\n\tglobal_load_dwordx4 %[wb], v114, s[68:69] offset:512\n\tglobal_load_dword %[wc], v115, s[68:69] offset:1024\n\t"
    "global_load_dwordx4 %[xa], v116, s[64:65]\n\tglobal_load_dwordx4 %[xb], v116, s[64:65] offset:512\n\tglobal_load_dword %[xc], v117, s[64:65] offset:1024\n\t"
    "global_load_dwordx4 %[d0], v113, s[70:71]\n\tglobal_load_dwordx4 %[d1], v112, s[70:71] offset:1024\n\t" SARR
    "s_branch 8f\n"
    "1:\n\ts_cmp_ge_u32 s62, " STR(CPF) "\n\ts_cbranch_scc1 6f\n\ts_getpc_b64 s[56:57]\n.Lkpc:\n\ts_and_b32 s58, s62, 3\n\ts_lshl_b32 s58, s58, 13\n\ts_sub_u32 s58, s58, 0x1000\n\tv_lshl_add_u32 v119, %[t], 4, s58\n\tv_min_u32 v119, .Lkend-.Lkpc-16, v119\n\t"
    "global_load_dword v120, v119, s[56:57]\n\t.subsection 1\n.Lkend:\n\t.subsection 0\n6:\n\ts_barrier\n\t"
    "v_mov_b32 v124, -1\n\tv_mov_b32 v125, -1\n\tv_mov_b32 v126, -1\n\tv_mov_b32 v127, -1\n\tv_lshlrev_b32 v119, 4, %[t]\n\tv_and_b32 v119, 0xff0, v119\n\t"
    "s_lshr_b32 s59, s60, 8\n\ts_cmp_lt_u32 s62, 8\n\ts_cselect_b32 s58, 2, 1\n\ts_cbranch_scc1 2f\n\ts_cmp_eq_u32 s59, 3\n\ts_cbranch_scc1 4f\n"
    "2:\n\ts_sub_u32 s57, s59, 2\n\ts_cmp_eq_u32 s59, 1\n\ts_cselect_b32 s58, s58, s57\n\ts_cmp_lt_u32 s62, 8\n\ts_cselect_b32 s56, 0, 8\n\ts_lshl1_add_u32 s56, s62, s56\n\ts_mulk_i32 s61, 72\n\ts_add_u32 s51, s56, s61\n\ts_sub_u32 s56, 2, s59\n\ts_sub_u32 s57, 16, s62\n\ts_cmp_eq_u32 s59, 3\n\ts_cselect_b32 s50, s57, s56\n\ts_add_u32 s56, s50, s51\n\t"
    "v_mov_b32 v112, 0\n\tv_mov_b32 v113, 0\n\tv_mov_b32 v114, 0\n\tv_mov_b32 v115, 0\n\tv_mov_b32 v116, 0\n\tv_mov_b32 v117, 0\n\ts_setprio 2\n\t"
    "s_lshl_b32 s57, s56, 2\n\ts_add_u32 s57, s57, s63\n\ts_lshl_b32 s57, s57, 6\n\tv_lshl_add_u32 v22, v118, 1, s57\n\ts_mul_i32 s57, s56, %[yst]\n\ts_add_u32 s57, s57, %[wy]\n\tv_add_u32 v23, s57, v119\n\ts_lshl_b32 s56, s56, 1\n\ts_and_b32 s56, s56, -4\n\t"
    "s_waitcnt lgkmcnt(0)\n\t"
    "global_load_short_d16_hi v112, v22, s[80:81] nt\n\tglobal_load_short_d16_hi v113, v22, s[82:83] nt\n\tglobal_load_short_d16_hi v114, v22, s[84:85] nt\n\t"
    "global_load_short_d16_hi v115, v22, s[86:87] nt\n\tglobal_load_short_d16_hi v116, v22, s[88:89] nt\n\tglobal_load_short_d16_hi v117, v22, s[90:91] nt\n\t"
    "s_load_dwordx2 s[96:97], s[66:67], 0x0\n\ts_load_dword s98, s[92:93], s56\n\t"
    SARR INV0
    "s_mul_i32 s52, s58, %[sl]\n\ts_add_u32 s52, s52, %[tb]\n\ts_lshl_b32 s53, s63, 7\n\ts_add_u32 s53, s53, s52\n\tv_lshl_add_u32 v30, v118, 2, s53\n\tv_bfe_u32 v31, %[t], 5, 1\n\ts_movk_i32 s54, 528\n\tv_mul_u32_u24 v29, s54, v31\n\t"
    "v_lshl_add_u32 v2, v29, 3, v30\n\ts_movk_i32 s53, 1056\n\tv_add_u32 v3, s53, v2\n\tv_add_u32 v4, s53, v3\n\tv_add_u32 v5, s53, v4\n\tv_add_u32 v6, 41*528, v2\n\tv_add_u32 v7, s53, v6\n\tv_add_u32 v8, s53, v7\n\tv_add_u32 v9, s53, v8\n\t"
    "v_lshl_add_u32 v10, v29, 2, v30\n\tv_add_u32 v10, 16*528, v10\n\tv_add_u32 v11, s53, v10\n\tv_lshl_add_u32 v12, v29, 1, v30\n\tv_add_u32 v12, 24*528, v12\n\tv_add_u32 v13, 2112, v12\n\tv_add_u32 v14, v29, v30\n\tv_add_u32 v14, 32*528, v14\n\t"
    "s_lshl_b32 s53, s58, 4\n\ts_add_u32 s53, s53, %[ex]\n\tv_mov_b32 v15, s53\n\tv_cvt_f32_ubyte0 v31, v31\n\tv_mul_f32 v16, 0x42800000, v31\n\tv_add_f32 v17, v16, v16\n\tv_mov_b32 v20, 1\n\t"
    "v_and_b32 v29, 15, %[t]\n\tv_and_b32 v30, 48, %[t]\n\tv_mad_u32_u24 v30, v29, s54, v30\n\ts_lshl_b32 s55, s59, 1\n\ts_lshr_b32 s55, 0x48, s55\n\ts_add_u32 s55, s55, s63\n\ts_and_b32 s55, s55, 3\n\ts_mul_i32 s53, s55, 16*528\n\ts_movk_i32 s57, 41*528\n\ts_cmp_eq_u32 s55, 2\n\ts_cselect_b32 s53, s57, s53\n\ts_cselect_b32 s57, 32*528, s57\n\t"
    "s_add_u32 s53, s53, s52\n\ts_add_u32 s57, s57, s52\n\tv_add_u32 v18, s53, v30\n\tv_add_u32 v19, s57, v30\n\t"
    "v_min_u32 v29, 237*16, v119\n\tv_subrev_u32 v30, 204*16, v29\n\tv_add_u32 v29, %[de], v29\n\tv_min_u32 v29, v29, v30\n\ts_sub_u32 s53, s52, 4640\n\tv_add_u32 v29, s53, v29\n\tv_mov_b32 v24, 0\n\tv_mov_b32 v25, 0\n\tv_mov_b32 v26, 0\n\tv_mov_b32 v27, 0\n\tds_write_b128 v29, v[24:27]\n\t"
    "s_cmp_lg_u32 s55, 3\n\ts_cbranch_scc1 10f\n\t"
    "v_bfe_u32 v18, %[t], 1, 5\n\tv_and_b32 v19, 1, %[t]\n\ts_cmp_lt_u32 s62, 8\n\ts_cselect_b32 s53, 2, 1\n\ts_mul_i32 s53, s53, %[sl]\n\tv_mul_u32_u24 v21, s53, v19\n\ts_sub_u32 s55, 2, s59\n\ts_lshl_b32 s55, s55, 7\n\tv_add_u32 v18, s55, v18\n\ts_cmp_lg_u32 s59, 3\n\ts_cbranch_scc1 7f\n\t"
    "v_bfe_u32 v18, %[t], 5, 1\n\tv_lshl_add_u32 v18, v18, 7, v118\n\tv_mov_b32 v19, s50\n\tv_mov_b32 v21, %[sl]\n"
    "7:\n\tv_add_u32 v19, s51, v19\n\tv_lshl_add_u32 v21, v18, 4, v21\n\tv_lshrrev_b32 v24, 3-" STR(TL) ", v19\n\tv_and_b32 v19, 7>>" STR(TL) ", v19\n\tv_lshlrev_b32 v19, 4, v19\n\tv_lshl_add_u32 v19, v24, 7, v19\n\t"
    "v_and_b32 v24, (1<<" STR(TL) ")-1, v18\n\tv_lshl_add_u32 v19, v24, 7-" STR(TL) ", v19\n\tv_lshrrev_b32 v18, " STR(TL) ", v18\n\tv_mul_u32_u24 v18, %[ust], v18\n\tv_add_u32 v18, v18, v19\n\tv_add_u32 v19, 512, v21\n10:\n\t"
    INV1 "s_branch 9f\n"
    "4:\n\t"
    "v_bfe_u32 v118, %[t], 3, 5\n\tv_lshrrev_b32 v117, 1, v118\n\tv_and_b32 v116, 6, v117\n\tv_and_b32 v115, 7, %[t]\n\tv_xor_b32 v115, v115, v116\n\tv_lshlrev_b32 v115, 4, v115\n\t"
    "s_mul_i32 s54, s61, 9\n\ts_sub_u32 s55, s62, 14\n\ts_lshr_b32 s55, s55, 1\n\ts_add_u32 s54, s54, s55\n\ts_lshl_b32 s54, s54, 7\n\t"
    "s_mul_i32 s52, s61, 0xa2000\n\ts_mul_i32 s53, s62, 0x180\n\ts_add_u32 s52, s52, s53\n\ts_cmp_ge_u32 s62, 11\n\ts_cselect_b32 s53, 0x51000-0x480, 0\n\ts_add_u32 s52, s52, s53\n\ts_sub_u32 s52, s52, 0xc00\n\t"
    "s_movk_i32 s55, 0x480\n\ts_cmp_ge_u32 s62, 14\n\ts_cselect_b32 s54, s54, s52\n\ts_cselect_b32 s55, 0x2400, s55\n\tv_mad_u32_u24 v118, v118, s55, v115\n\ts_lshl_b32 s55, s55, 5\n\t"
    "s_lshl_b32 s52, s63, 10\n\ts_add_u32 m0, s52, %[r2]\n\ts_mov_b32 s58, 0x510000\n\ts_mov_b32 s59, 0x20000\n\ts_cmp_ge_u32 s62, 14\n\ts_cselect_b32 s53, " STR(PRE1) "-1, " STR(PRE2) "-1\n\ts_setprio 3\n\t"
    "s_waitcnt lgkmcnt(0)\n\ts_memrealtime s[98:99]\n\t"
    "s_bitcmp1_b32 %[wg], 3\n\ts_cselect_b64 s[56:57], s[74:75], s[72:73]\n\ts_cmp_ge_u32 s62, 14\n\ts_cselect_b64 s[56:57], s[56:57], s[76:77]\n\ts_cselect_b32 s52, 0x180, -1\n\ts_and_b32 s57, s57, 0xffff\n\ts_add_u32 s60, s56, s54\n\ts_and_b32 s60, s60, 0x380\n\ts_cmp_eq_u32 s60, s52\n\ts_cselect_b32 s53, " STR(PRE3) "-1, s53\n\t"
    "v_mov_b32 v121, 80\n\ts_movk_i32 s52, 0x400\n12:\n\tds_read_b32 v122, v121\n\ts_waitcnt lgkmcnt(0)\n\tv_readfirstlane_b32 s60, v122\n\ts_cmp_ge_u32 s60, 12\n\ts_cbranch_scc1 13f\n\ts_sleep 1\n\ts_sub_u32 s52, s52, 1\n\ts_cbranch_scc0 12b\n13:\n\t"
    "s_load_dwordx2 s[96:97], s[66:67], 0x0\n\ts_sleep " STR(PD) "\n\t"
    "\n3:\n\t" "buffer_load_dwordx4 v118, s[56:59], s54 offen nt lds\n\ts_add_u32 m0, m0, 0x1000\n\ts_add_u32 s54, s54, s55\n\ts_sub_u32 s53, s53, 1\n\ts_cbranch_scc0 3b\n"
    "9:\n8:"
    : [xa]"=&{v[2:5]}"(xa),[xb]"=&{v[6:9]}"(xb_),[xc]"=&{v30}"(xc),[wa]"=&{v[10:13]}"(wa),[wb]"=&{v[14:17]}"(wb),[wc]"=&{v31}"(wc),[d0]"=&{v[18:21]}"(dq[0]),[d1]"=&{v[22:25]}"(dq[1]),[d2]"=&{v[26:29]}"(dq[2]),
      "=&{s[64:65]}"(x),"=&{s[66:67]}"(ws),"=&{s[70:71]}"(Dsk),"=&{s[72:73]}"(wg),"=&{s[74:75]}"(wu),"=&{s[76:77]}"(wd),"=&{s[78:79]}"(out)
    : [ka]"s"(__builtin_amdgcn_kernarg_segment_ptr()),[wg]"s"(BID),[t]"v"(TID),[yst]"i"(YST),[wy]"i"(W_Y),[r2]"i"(O_R2),[sl]"i"(SLSZ),[tb]"i"(O_CH+O_TB),[ex]"i"(O_EXP),[de]"i"(O_DE),[ust]"i"(GST_)
    : "memory","scc","m0","v127","s50","s51","s52","s53","s54","s55","s56","s57","s58","s59","s60","s61","s62","s63","s68","s69",
      "s80","s81","s82","s83","s84","s85","s86","s87","s88","s89","s90","s91","s92","s93","s94","s95","s99");
  int wv=RFL(TID>>6), bid=BID, q4=wv>>2, w4=wv&3;
  int xg=bid&7, lb=bid>>3, tb=32*xg+lb, nch=2+(lb<8);
  u64 ep; u32 ldw;
  {
  }
  #define KILLIN "=v"(xa),"=v"(xb_),"=v"(xc),"=v"(wa),"=v"(wb),"=v"(wc),"=v"(dq[0]),"=v"(dq[1]),"=v"(dq[2])
  if(__builtin_expect(q4==3&&lb>=8,0)){
    int fu=18*xg+lb-14, kq=2*xg+(lb>=11), cgp=lb-8-3*(lb>=11);
  u32 wst= lb>=14? F_*64:D_*64;
  u32 wso=(lb>=14? (fu>>1)*128 : kq*(288*D_*2)+384*cgp)+(lb>=14? PRE1:PRE2)*wst, wld=(u32)(size_t)sm+O_R2+w4*1024+(lb>=14? PRE1:PRE2)*4096; u32x4 wrs=mkrs(lb>=14? ((fu&1)? wu:wg):wd,D_*F_*2);
  GETEP
  #define FLANE(RS) LANE(ln); int g=ln>>4, li=ln&15, qq=li>>2, pq=li&3; u32 wvo=(u32)(8*w4+(ln>>3))*(RS)+(u32)((ln&7)^(((ln>>4)&2)|((w4&1)<<2)))*16u; \
    const lch* tp=sm+O_R2+(16*(g>>1)+8*(qq>>1)+4*(g&1)+(qq&1))*128+((w4^((g&1)|(qq&2)))*32)+pq*8;
  #define WFR(img,ks) BC(u32x4,trfrag(tp+(img)*CHSZ+(ks)*4096,tp+(img)*CHSZ+(ks)*4096+256))
  #define WF2(img,s) __builtin_shufflevector(WFR(img,2*(s)),WFR(img,2*(s)+1),0,1,2,3,4,5,6,7)
  f32x4 ac[3]={{0,0,0,0},{0,0,0,0},{0,0,0,0}};
  #define AC0 "+{v[100:103]}"(ac[0])
  #define AC1 "+{v[104:107]}"(ac[1])
  #define AC2 "+{v[108:111]}"(ac[2])
  #define SMF(i,a,b) asm volatile("v_smfmac_f32_16x16x64_bf16 %0, %1, %2, %3" : AC##i : "v"(a),"v"(b),"v"(ix) : "memory"); SCHED();
  #define SMR(i,r,b) asm volatile("v_smfmac_f32_16x16x64_bf16 %0, v[" #r ":" #r "+3], %1, %2" : AC##i : "v"(b),"v"(ix) : "memory"); SCHED();
  #define GW4(c,p) { u32 tv,cnt,sv; asm volatile("s_mov_b32 %1, 0x20000\n1:\n\tds_read_b32 %0, %3\n\tds_read_b128 v[112:115], %4\n\tds_read_b128 v[116:119], %4 offset:128\n\tds_read_b128 v[120:123], %4 offset:256\n\tds_read_b128 v[124:127], %4 offset:384\n\ts_waitcnt lgkmcnt(4)\n\t" \
    "v_readfirstlane_b32 %2, %0\n\ts_cmp_ge_u32 %2, 4\n\ts_cbranch_scc1 2f\n\ts_sleep 1\n\ts_sub_u32 %1, %1, 1\n\ts_cbranch_scc0 1b\n2:" : "=&v"(tv),"=&s"(cnt),"=&s"(sv) : "v"((c)*4),"v"(p) : "memory","scc"); }
  #define LGW(n) asm volatile("s_waitcnt lgkmcnt(" #n ")" ::: "memory");
  u64 tag=ep+1ull; const u32x4 I4={INV,INV,INV,INV};
  if(lb>=14){
    FLANE(F_*2)
    int mt=fu&1;
    const lch* xp=sm+O_TILE+(li&7)*XSTR+g*32+(li>>3)*16; int of=(mt*288+(fu>>1)*4+w4)*256+ln*4; u32 ix= (li&8)? 0xeeeeu:0x4444u;
    const lch* mp=sm+O_RC+(g&1)*16+(g>>1)*8;
    asm volatile("" : "+v"(xp),"+v"(of),"+v"(mp),"+v"(ix));
    rsrc_t rsG=RSRC(ws+W_GU,2*8*F_*2);
    if(w4==0) BST128(I4,rsG,(mt*288+(fu>>1)*4)*256+ln*16,0,SC1);
    bool slow=(((u32)wrs[0]+wso)&0x380u)==0x180u;
    if(slow){ wso-=(PRE1-PRE3)*wst; wld-=(PRE1-PRE3)*4096; FILL(5+PRE1-PRE3,VW(W3)); } else FILLD(5,W1,W1B);
    if(w4==0){ VMW(5); if(ln==0) GST(ws+W_FL+(576+fu)*8,tag); }
    if(slow) FILLD(13-PRE1,W3,W1B); else FILLD(13-PRE1,W1,W1B);
    u32x8 fB[9]; u32x4 one4={0x3f803f80u,0x3f803f80u,0x3f803f80u,0x3f803f80u}; const lch* tq=tp+65536; asm volatile("" : AC0,AC1,"+v"(one4),"+v"(tq));
    VMW(4); gsync(sm,C_P,4);
    #pragma unroll
    for(int s=0;s<7;s++) fB[s]=WF2(0,s);
    #pragma unroll
    for(int s=0;s<7;s++){ SMF(1,one4,fB[s]) }
    DRAIN(); gsync(sm,C_P,8);
    fB[7]=WF2(0,7); { const lch* tp=tq; fB[8]=WF2(0,0); } SMF(1,one4,fB[7]) SMF(1,one4,fB[8])
    { u32x4 xb[5]; u32x2 md;
      GW4(C_X,xp)
      md=LD(u32x2,mp);
      #pragma unroll
      for(int s=0;s<5;s++) xb[s]=LD(u32x4,xp+512+s*128);
      SCHED(); LGW(6)
      SMR(0,112,fB[0])
      float S; asm volatile("v_permlane32_swap_b32 v104, v105\n\tv_add_f32 %0, v104, v105" : "=v"(S),AC1); SCHED();
      SMR(0,116,fB[1]) SMR(0,120,fB[2])
      float ra=rcpf_(bhi(md[0])), rb_=rcpf_(bhi(md[1])); PIN(ra); PIN(rb_); SCHED();
      SMR(0,124,fB[3])
      float na=-blo(md[0])*S, nb=-blo(md[1])*S; PIN(na); PIN(nb); SCHED();
      SMF(0,xb[0],fB[4])
      float ea=ra*-1.4426950408889634f, eb=rb_*-1.4426950408889634f; PIN(ea); PIN(eb); SCHED();
      SMF(0,xb[1],fB[5]) SMF(0,xb[2],fB[6]) SMF(0,xb[3],fB[7]) SMF(0,xb[4],fB[8])
      float v0,v1,s0,s1;
      asm volatile("s_nop 7\n\tv_permlane32_swap_b32 v100, v102\n\tv_permlane32_swap_b32 v101, v103\n\tv_add_f32 v100, %5, v100\n\tv_add_f32 v101, %6, v101\n\tv_add_f32 v100, v100, v102\n\tv_add_f32 v101, v101, v103\n\t"
        "v_mul_f32 %2, %9, v100\n\tv_mul_f32 %3, %10, v101\n\tv_mul_f32 %0, %7, v100\n\tv_mul_f32 %1, %8, v101" : "=&v"(v0),"=&v"(v1),"=&v"(s0),"=&v"(s1),AC0 : "v"(na),"v"(nb),"v"(ra),"v"(rb_),"v"(ea),"v"(eb));
      if(mt==0){ v0*=rcpf_(1.f+__builtin_amdgcn_exp2f(s0)); v1*=rcpf_(1.f+__builtin_amdgcn_exp2f(s1)); }
      __builtin_amdgcn_raw_buffer_store_b32(nv(pk(v0,v1)),rsG,of,0,SC1); garrive(sm,C_H); }
    if(bid==112&&w4==1){
      _Pragma("unroll 1") for(int sp=0;sp<SPINCAP;sp++){ u64 bad=0, f[12];
        #pragma unroll
        for(int i=0;i<12;i++) f[i]=GLD(ws+W_FL+(i*64+ln)*8);
        #pragma unroll
        for(int i=0;i<12;i++) bad|=f[i]^tag;
        if(BALLOT(bad!=0ull)==0ull) break; __builtin_amdgcn_s_sleep(8); }
      if(ln==0) GST(ws+W_EPOCH,tag); }
    if(mt==0&&w4==0){
    LANE(l2); ln=l2; li=ln&15; g=ln>>4; gwait<1>(sm,C_IX,1);
    int c9=(lb-14)>>1, t=RFL((int)LD(u16,sm+O_IDX+xg*2)), c=64*c9+4*li+g; f32x4 pr;
    { u32 u=LD(u16,sm+O_IDX+16+xg*2)^0x8000u, xb=LD(u16,sm+O_TILE+xg*XSTR+c*2), sv=LD(u32,sm+O_RC+xg*4); float ds=b2f(((const GAS u16*)Dsk)[c]); u^=(u32)(((int)(u<<16))>>31)&0x7fffu;
      poll1<0>(ws+W_FL+(64*c9+ln)*8,tag); poll1<0>(ws+W_FL+(720+(ln<48? ln:47))*8,tag);
      float xn=(b2f(xb)-blo(sv))*rcpf_(bhi(sv)); pr=f32x4{b2f(xb),xn,xn*ds,sigm_f(b2f(u))}; }
    { gwait<2>(sm,C_E,4); u32 ti=now_(), mx=0;
      #pragma unroll
      for(int k=0;k<3;k++){ int i=ln+64*k; i= i<144? i:143; int q=(i*3641)>>16; u64 h=GLD(ws+W_HT+(72*q+36+2*(i-18*q))*8); mx=umax_(mx,(u32)(h>>32)==(u32)tag? (u32)h-ldw : ti-ldw+HM); }
      u32 tr=umin_(wmax(mx),ti-ldw+160u); u32 hk=ti-ldw+HKR; if(tr>hk) tr-=(tr-hk)>>1; tgate2(ldw,tr+HR); }
    { u32x4 rs=mkrs(ws+W_PART+(xg*9+c9)*4096,4096), ry=mkrs(ws+W_Y,D_*YST);
      int yo=c*YST+(t&~1)*2, po=ln*16; u32 a[4], t0,cnt,yb; u16* op=out+t*D_+c;
      #define LD_P "buffer_load_dwordx4 v[112:115], %[of], %[rs], 0 offen sc1\n\tbuffer_load_dwordx4 v[116:119], %[of], %[rs], 0 offen offset:1024 sc1\n\tbuffer_load_dwordx4 v[120:123], %[of], %[rs], 0 offen offset:2048 sc1\n\tbuffer_load_dwordx4 v[124:127], %[of], %[rs], 0 offen offset:3072 sc1\n\t"
      #define LD_YB "buffer_load_dword %[yb], %[yo], %[ry], 0 offen sc1\n\t"
      #define AD4(o,a,b,c,d) "v_add_f32 %[t0], v" #a ", v" #b "\n\tv_add_f32 %[" #o "], v" #c ", v" #d "\n\tv_add_f32 %[" #o "], %[t0], %[" #o "]\n\t"
      u64 ym;
      asm volatile(LD_YB LD_P "s_mov_b32 %[cnt], 0x20000\n1:\n\ts_waitcnt vmcnt(0)\n\tv_cmp_eq_u32 %[ym], -1, %[yb]\n\t" CHK_I0 MX2(116,117) MX2(118,119) MX2(120,121) MX2(122,123) MX2(124,125) MX2(126,127) CHK_IE
        "s_or_b64 exec, vcc, %[ym]\n\ts_cbranch_scc0 9f\n\ts_sleep " STR(QSLP) "\n\ts_mov_b64 exec, %[ym]\n\ts_cbranch_execz 2f\n\t" LD_YB "2:\n\ts_mov_b64 exec, vcc\n\ts_cbranch_execz 3f\n\t" LD_P
        "3:\n\ts_mov_b64 exec, -1\n\ts_sub_u32 %[cnt], %[cnt], 1\n\ts_cbranch_scc0 1b\n9:\n\ts_mov_b64 exec, -1\n\t"
        AD4(a0,112,116,120,124) AD4(a1,113,117,121,125) AD4(a2,114,118,122,126) AD4(a3,115,119,123,127) "s_nop 1"
        : [t0]"=&v"(t0),[cnt]"=&s"(cnt),[a0]"=&v"(a[0]),[a1]"=&v"(a[1]),[a2]"=&v"(a[2]),[a3]"=&v"(a[3]),[yb]"=&v"(yb),[ym]"=&s"(ym) : [of]"v"(po),[rs]"s"(rs),[yo]"v"(yo),[ry]"s"(ry),"v"(op),"v"(pr) : PCLOB);
      auto s0=__builtin_amdgcn_permlane32_swap(a[0],a[2],false,false); auto s1=__builtin_amdgcn_permlane32_swap(a[1],a[3],false,false);
      u32 x0=s0[0],x1=s0[1],y0=s1[0],y1=s1[1]; u32 p0=BC(u32,BC(float,x0)+BC(float,x1)), p1=BC(u32,BC(float,y0)+BC(float,y1));
      auto s2=__builtin_amdgcn_permlane16_swap(p0,p1,false,false); u32 z0=s2[0], z1=s2[1]; float s=BC(float,z0)+BC(float,z1);
      float gq=silu_f(__builtin_fmaf(s,pr[3],pr[1]));
      float sy=((t&1)? bhi(yb):blo(yb))+pr[2];
      float o=__builtin_fmaf(sy,gq,pr[0]);
      asm volatile("global_store_short %0, %1, off sc1" :: "v"(op),"v"(pk(o,o)) : "memory");
      }
    }
  } else
  {
    FLANE(D_*2)
    u32x8 fr[12]; u32x4 fl[3];
    rsrc_t rsP=RSRC(ws+W_PART+cgp*12288+kq*256+w4*64,8*9*4096-2*12288);
    #pragma unroll
    for(int i=0;i<3;i++) BST128(I4,rsP,li<8? li*(9*4096)+g*16+i*4096 : OOR,0,SC1);
    if(PRE2<9) FILLD(9-PRE2,W2_,W2B);
    wso+=128u-9u*wst;
    FILLD(9,W2_,W2B);
    VMW(9); gsync(sm,C_P,4);
    #pragma unroll
    for(int s=0;s<4;s++) fr[s]=WF2(0,s);
    fl[0]=WFR(0,8);
    gsync(sm,C_P,8); if(w4==0&&ln==0) GST(ws+W_FL+(720+6*xg+lb-8)*8,tag);
    wso+=128u-9u*wst; wld-=2*CHSZ;
    FILL(9,VW(W2C));
    int fo=W_FL+(576+((9*kq)&~1)+(ln<9? ln:9))*8;
    if(w4==0) asm volatile("global_load_dwordx2 v[112:113], %0, %1 sc1\n\ts_waitcnt vmcnt(10)" :: "v"(fo),"s"(ws) : "memory"); else VMW(9);
    gsync(sm,C_P,12);
    #pragma unroll
    for(int s=0;s<4;s++) fr[4+s]=WF2(1,s);
    fl[1]=WFR(1,8);
    { int t2=TID; asm volatile("" : "+v"(t2)); ln=t2&63; li=ln&15; g=ln>>4; }
    const lch* hp=sm+O_TILE+(li&7)*576+g*32+(li>>3)*16; int po=(4*(g&1)+2*(g>>1))*(9*4096)+li*4; u32 ix= (li&8)? 0xeeeeu:0x4444u; PIN(hp); PIN(po); PIN(ix);
    const lch* hq=hp+512-(g>>1)*32-(g&1)*24; PIN(hq);
    u32x4 rs=mkrs(ws+W_GU,2*8*F_*2);
    int of=((ln>>5)*288+18*kq+2*w4)*256+(ln&31)*16; lch* hd=sm+O_TILE+((ln&4)+((ln>>2)&2))*576+w4*64+(ln&16)*2+(ln&3)*8+(ln>>5)*4; PIN(of); PIN(hd); asm volatile("" : "+s"(rs));
    u32x4 rp=mkrs(ws+W_PART+cgp*12288+kq*256+w4*64,8*9*4096-2*12288); int po1=po+9*4096; u32 nvm=0xfffeffffu; asm volatile("" : AC0,AC1,AC2,"+v"(po1),"+s"(rp),"+s"(nvm));
    DRAIN();
    if(w4==0){ u64 bad; asm volatile("v_cmp_ne_u64 %0, %1, v[112:113]" : "=s"(bad) : "s"(tag) : "memory");
      if(bad!=0ull) poll1<0>(ws+fo,tag); }
    gsync(sm,C_P,16);
    { u32 t0,cnt;
      #define LD_G "buffer_load_dwordx4 v[112:115], %[of], %[rs], 0 offen sc1\n\tbuffer_load_dwordx4 v[116:119], %[of], %[rs], 0 offen offset:2048 sc1\n\tbuffer_load_dwordx4 v[120:123], %[of], %[rs], %[so] offen sc1\n\t"
      #define HM1(t,a,b) "v_lshlrev_b32 " t ", 16, v" #a "\n\tv_lshlrev_b32 %[t1], 16, v" #b "\n\tv_and_b32 v" #a ", %[hm], v" #a "\n\tv_and_b32 v" #b ", %[hm], v" #b "\n\tv_mul_f32 " t ", " t ", %[t1]\n\tv_mul_f32 v" #a ", v" #a ", v" #b "\n\t"
      #define HMUL(a,b,c,d,o) "v_permlane32_swap_b32 v" #a ", v" #c "\n\tv_permlane32_swap_b32 v" #b ", v" #d "\n\t" HM1("%[t0]",a,c) HM1("v" #c,b,d) "v_cvt_pk_bf16_f32 %[t0], %[t0], v" #c "\n\tv_cvt_pk_bf16_f32 v" #a ", v" #a ", v" #b "\n\tds_write2_b32 %[hd], %[t0], v" #a " offset0:" #o " offset1:" #o "+144\n\t"
      u32 t1;
      asm volatile(LD_G :: [of]"v"(of),[rs]"s"(rs),[so]"s"(w4==0? 4096:0) : "memory");
      fr[8]=WF2(0,0); PIN(fr[8]); fl[2]=WFR(0,8); PIN(fl[2]);
      asm volatile(POLLASM(LD_G,CHK_I0 MX2(116,117) MX2(118,119) MX2(120,121) MX2(122,123) CHK_IE,GSLP)
        HMUL(112,113,114,115,0) HMUL(116,117,118,119,64) "s_cmp_eq_u32 %[so], 0\n\ts_cbranch_scc1 8f\n\tv_add_u32 %[hd], 512, %[hd]\n\t" HMUL(120,121,122,123,0) "8:\n\ts_mov_b64 exec, 1\n\tds_add_u32 %[ch], %[one]\n\ts_mov_b64 exec, -1"
        : [t0]"=&v"(t0),[t1]"=&v"(t1),[cnt]"=&s"(cnt),[hd]"+v"(hd) : [of]"v"(of),[rs]"s"(rs),[so]"s"(w4==0? 4096:0),[hm]"s"(0xffff0000u),[ch]"v"(C_H*4),[one]"v"(1),"v"(hp),"v"(po) : PCLOB);
      }
    {
      GW4(C_H,hp)
      u32x2 h8=LD(u32x2,hq); SCHED(); LGW(1)
      #define SM3(i,b) asm volatile("v_smfmac_f32_16x16x32_bf16 %0, %1, %2, %3" : AC##i : "v"(h8),"v"(b),"v"(ix) : "memory"); SCHED();
      #define PST(a,b,c,d,o) "v_permlane32_swap_b32 v" #a ", v" #c "\n\tv_permlane32_swap_b32 v" #b ", v" #d "\n\tv_add_f32 v" #a ", v" #a ", v" #c "\n\tv_add_f32 v" #b ", v" #b ", v" #d "\n\tv_min_u32 v" #a ", %[nv], v" #a "\n\tv_min_u32 v" #b ", %[nv], v" #b "\n\t" \
        "buffer_store_dword v" #a ", %[p0], %[rp], " o " offen sc1\n\tbuffer_store_dword v" #b ", %[p1], %[rp], " o " offen sc1\n\t"
      SMR(0,112,fr[0]) fr[9]=WF2(0,1); SCHED(); SMR(1,112,fr[4]) fr[10]=WF2(0,2); SCHED(); SMR(2,112,fr[8]) fr[11]=WF2(0,3); SCHED();
      SMR(0,116,fr[1]) SMR(1,116,fr[5]) SMR(0,120,fr[2]) SM3(2,fl[2]) SMR(0,124,fr[3]) SMR(1,120,fr[6]) SM3(0,fl[0]) SMR(1,124,fr[7]) SMR(2,116,fr[9]) SM3(1,fl[1]) SMR(2,120,fr[10]) SMR(2,124,fr[11])
      asm volatile("s_nop 2\n\t" PST(100,101,102,103,"0") PST(104,105,106,107,"%[o1]") PST(108,109,110,111,"%[o2]") : AC0,AC1,AC2 : [p0]"v"(po),[p1]"v"(po1),[rp]"s"(rp),[nv]"s"(nvm),[o1]"s"(4096),[o2]"s"(8192) : "memory"); }

  }
  asm volatile("s_endpgm" : KILLIN :: "memory"); }
  {
  if(__builtin_expect(q4!=0,0)){ int grp= q4==1? nch-1 : q4-2;
    chan(sm,grp,(w4+(0x48>>(2*q4)))&3,72*xg+(q4==3? 16+lb : 2*lb+(lb<8? 0:8)+2-q4),(q4+1)>>2,ws,xa,xb_,wa,wb,dq[0]);
    asm volatile("s_endpgm" : KILLIN :: "memory"); }
  u32x4 xq[3]; f32x2 rc[3]; int xr_,xr2_;
  {
    LANE(ln); int g=ln>>4, li=ln&15, qq=li>>2, pq=li&3, j=ln&31, rl=2*w4+(ln>>5);
    lch* tile=sm+O_TILE; u32 Tr=(ln>>1)&16;
    int uo=W_U+(tb>>TL)*GST_+(tb&((1<<TL)-1))*(128>>TL)+(w4*1024<<TL)+(ln>>(3-TL))*128+(ln&(7>>TL))*16, uo1=uo+(4096<<TL), uo2=uo+(8192<<TL); PIN(uo); PIN(uo1); PIN(uo2);
    lch* tw=tile+rl*TSTR2+j*16; lch* tw2=tw+1024-j*12; u32 one=0x3f803f80u; const lch* tp=tile+qq*TSTR2+(w4*64+g*16+pq*4)*2;
    int go= ln<32? W_RG+(ln>>2)*8192+(tb*4+(ln&3))*8 : W_ST+(tb*8+(ln&7))*8; lch* kw=sm+O_RV+rl*2; const lch* kr=sm+(ln<32? O_RV+(ln&3)*4 : O_MD+(ln&7)*4); PIN(kw); PIN(kr);
    int xr=(32*(tb>>2)+(ln>>2))*(D_*2)+256*(lb&3)+64*w4+pq*16, xr2= w4==0? xr+(lb&2)*(8*D_*2)+1024+64*(lb&1)-256*(lb&3):OOR;
    rsrc_t rx=RSRC(x,L_*D_*2); u64 gm=((1ull<<(4*NRG))-1ull)|0xff00000000ull; u32 um=~UMASK, km=0x80008000u; asm volatile("" : "+s"(gm),"+s"(um),"+s"(km),"+s"(ws));
    #define D2(a,b,c) __builtin_amdgcn_fdot2_f32_bf16(BC(bf2,a),BC(bf2,b),c,false)
    asm volatile("s_waitcnt vmcnt(5)" : "+v"(wa),"+v"(wb),"+v"(wc),"+v"(Tr),"+v"(tw),"+v"(tw2),"+v"(tp),"+v"(one),"+v"(go),"+v"(xr),"+v"(xr2) :: "memory");
    u32 wq[9]={wa[0],wa[1],wa[2],wa[3],wb[0],wb[1],wb[2],wb[3],wc}; float sw;
    { float a[3];
      #pragma unroll
      for(int i=0;i<9;i++) a[i%3]=D2(wq[i],one,i<3? 0.f:a[i%3]);
      sw=add16x(sum16((a[0]+a[1])+a[2])); }
    GETEP u32 tg=(u32)ep+1u; Tr=((upat(tg,w4)>>Tr)&0xffffu)*0x10001u; u32 tgv=tg;
    float z_[9]={0,0,0,0,0,0,0,0,0};
    asm volatile("s_waitcnt vmcnt(2)" : "+v"(xa),"+v"(xb_),"+v"(xc),"+v"(Tr),"+v"(sw),"+v"(tgv),"+v"(z_[0]),"+v"(z_[1]),"+v"(z_[2]),"+v"(z_[3]),"+v"(z_[4]),"+v"(z_[5]) :: "memory");
    u32 v[9]={xa[0],xa[1],xa[2],xa[3],xb_[0],xb_[1],xb_[2],xb_[3],xc};
    { float xf[18], s1, s2, s3;
      #pragma unroll
      for(int i=0;i<9;i++){ z_[i&1]=D2(v[i],one,z_[i&1]); z_[2+(i&1)]=D2(v[i],v[i],z_[2+(i&1)]); z_[4+(i&1)]=D2(v[i],wq[i],z_[4+(i&1)]); }
      s1=z_[0]+z_[1]; s2=z_[2]+z_[3]; s3=z_[4]+z_[5];
      SCHED();
      #define RSTEP(C) s1+=dppf<C>(s1); s2+=dppf<C>(s2); s3+=dppf<C>(s3); SCHED();
      RSTEP(0xB1) RSTEP(0x4E) RSTEP(0x141) RSTEP(0x140)
      s1=add16x(s1)*(1.f/576.f); s2=add16x(s2)*(1.f/576.f); s3=add16x(s3);
      float mm=s1*s1, var=s2-mm;
      if(__builtin_expect(BALLOT(!(mm<=64.f*var))!=0ull,0)){
        float q=0.f;
        #pragma unroll
        for(int i=0;i<9;i++){ u32 t=v[i]; PIN(t); float d0=blo(t)-s1, d1=bhi(t)-s1; q=__builtin_fmaf(d0,d0,q); q=__builtin_fmaf(d1,d1,q); }
        var=add16x(sum16(q))*(1.f/576.f); }
      #define UNP(i) PIN(v[i]); xf[2*(i)]=blo(v[i]); xf[2*(i)+1]=bhi(v[i]); PIN(xf[2*(i)]); PIN(xf[2*(i)+1]); SCHED();
      u32 vb=pk(var,s1); UNP(0)
      float den=blo(vb)+1e-5f, m=bhi(vb); UNP(1)
      vb=pk(den,den); s3=__builtin_fmaf(-m,sw,s3); UNP(2)
      den=__builtin_amdgcn_sqrtf(bhi(vb)); UNP(3)
      u32 md=pk(m,den); UNP(4)
      float ri=rcpf_(bhi(md)); UNP(5)
      s3*=ri; float rs_=ri*0.0625f; UNP(6)
      u32 u=pk(s3,s3); float cs_=-m*rs_; UNP(7)
      { u32 sg=(u32)((int)u>>31); PIN(sg); u^=sg|km; } UNP(8)
      asm volatile("s_mov_b64 exec, 0x1\n\ts_mov_b32 exec_hi, 1\n\tds_write_b16 %0, %1\n\tv_add_u32 %0, %0, %0\n\tds_write_b32 %0, %2 offset:%3\n\ts_mov_b32 exec_hi, 0\n\tds_add_u32 %4, %5\n\ts_mov_b64 exec, -1" : "+v"(kw) : "v"(u),"v"(md),"i"(O_MD-2*O_RV),"v"(C_I*4),"v"(1) : "memory");
      if(w4==3){ gwait<0>(sm,C_I,4); asm volatile("ds_read_b32 v118, %4\n\tv_mov_b32 v119, %1\n\ts_mov_b64 exec, %3\n\ts_waitcnt lgkmcnt(0)\n\tglobal_store_dwordx2 %0, v[118:119], %2 sc1\n\ts_mov_b64 exec, -1" :: "v"(go),"v"(tgv),"s"(ws),"s"(gm),"v"(kr) : "memory"); }
      SCHED();
      u32 h[9];
      #define UH(i) h[i]=(pkz(__builtin_fmaf(xf[2*(i)],rs_,cs_),__builtin_fmaf(xf[2*(i)+1],rs_,cs_))&um)|Tr;
      UH(0) UH(1) UH(2) UH(3) ST(u32x4,tw)=u32x4{h[0],h[1],h[2],h[3]}; UH(4) UH(5) UH(6) UH(7) ST(u32x4,tw+512)=u32x4{h[4],h[5],h[6],h[7]};
      UH(8) ST(u32,tw2)=h[8]; }
    gsync(sm,C_L,4);
    { u32x4 uq[3];
      #pragma unroll
      for(int z=0;z<3;z++) uq[z]=BC(u32x4,trfrag(tp+z*512,tp+z*512+4*TSTR2));
      asm volatile("global_store_dwordx4 %0, %1, %2 sc1" :: "v"(uo),"v"(uq[0]),"s"(ws) : "memory");
      asm volatile("global_store_dwordx4 %0, %1, %2 sc1" :: "v"(uo1),"v"(uq[1]),"s"(ws) : "memory");
      asm volatile("s_cmp_lg_u32 %3, 0\n\ts_cbranch_scc1 1f\n\tglobal_store_dwordx4 %0, %1, %2 sc1\n1:\n\ts_nop 1" :: "v"(uo2),"v"(uq[2]),"s"(ws),"s"(w4),"v"(uq[0]),"v"(uq[1]) : "memory","scc");
      asm volatile("" : "+v"(dq[0]),"+v"(dq[1]));
      xr_=xr; xr2_=xr2;
    }
    if(lb>=2&&lb<8){
      const u16* wm=(lb&1)? wu:wg; int co=(3-(int)((u64)wm>>7)-xg)&7, r_=576-12*PFN+(((lb>>1)-1)*4+w4)+12*j;
      asm volatile("buffer_load_dword v120, %0, %1, 0 offen" :: "v"(j<PFN&&(ln<32||co==0)? r_*(F_*2)+(9*xg+co)*128+(ln>>5)*1024 : OOR),"s"(mkrs(wm,D_*F_*2)) : "memory"); }
    if(lb>=8&&lb<14) garrive(sm,C_D);
  }
  FP fp; FA fa; u32 ti,td;
  { LANE(ln); u64 tag=ep+1ull;
    int so_=W_ST+(32*(tb>>2)+(ln>>2))*8, fo_=W_FL+((ln>=32&&w4==0? 512+32*(lb&1) : 128*(lb&3)+32*w4)+(ln&31))*8; u32x4 rx=mkrs(x,L_*D_*2); PIN(so_); PIN(fo_);
    XR X; X.so=so_; X.x0=xr_; X.x2=xr2_; X.rx=rx;
    if(lb==CTLB||lb>=14){
      u32 M=(u32)tag, rank,key; int j=ln&31, j16=j*16, j12=j*12, st0=W_ST; lch* xw=sm+O_TILE+(2*w4+(ln>>5))*XSTR+j*16; lch* rcw=sm+O_RC+(2*w4+(ln>>5))*4; PIN(rcw); PIN(j12); PIN(xw); PIN(j16); asm volatile("" : "+s"(rx),"+s"(st0));
      top8(sm,w4,ln,M,ws,rank,key,X);
      {
        int t0=__builtin_amdgcn_readlane((int)key,__builtin_ctzll(BALLOT(rank==(u32)(2*w4)))), t1=__builtin_amdgcn_readlane((int)key,__builtin_ctzll(BALLOT(rank==(u32)(2*w4+1))));
        int tsel=~((ln>>5)? t1:t0)&2047, ro=tsel*1152+j16, sr=tsel*8+st0;
        u32x4 a,b; u32 c; u64 f1;
        asm volatile("buffer_load_dwordx4 %[a], %[ro], %[rx], 0 offen\n\tbuffer_load_dwordx4 %[b], %[ro], %[rx], 0 offen offset:512\n\tbuffer_load_dword %[c], %[r2], %[rx], 0 offen offset:1024\n\tglobal_load_dwordx2 %[f], %[sr], %[ws] sc1\n\t"
          "s_mov_b64 exec, 1\n\tds_add_u32 %[cd], %[one]\n\ts_mov_b64 exec, -1\n\ts_waitcnt vmcnt(0)"
          : [a]"=&v"(a),[b]"=&v"(b),[c]"=&v"(c),[f]"=&v"(f1) : [ro]"v"(ro),[r2]"v"(ro-j12),[sr]"v"(sr),[cd]"v"(C_D*4),[one]"v"(1),[rx]"s"(rx),[ws]"s"(ws) : "memory");
        if(__builtin_expect(BALLOT((u32)(f1>>32)!=(u32)tag)!=0ull,0)){ f1=poll1<32>(ws+sr,tag<<32); __builtin_amdgcn_s_waitcnt(0x0f70); }
        ST(u32x4,xw)=a; ST(u32x4,xw+512)=b; ST(u32,xw+1024-j12)=c; ST(u32,rcw)=(u32)f1;
        garrive(sm,C_X);
        if(w4==0){ if(rank<8) ST(u16,sm+O_IDX+(ln&1)*16+rank*2)=(u16)((ln&1)? key>>16 : ~key&2047u); garrive(sm,C_IX); }
        if(lb==CTLB&&rank<8&&!(ln&1)) GST(ws+W_IDX+xg*1024+w4*128+rank*8,(tag<<16)|(~key&2047u));
      }
      PRIO(0); if(XB!=2) xreq(X,ws);
    } else { PRIO(0); if(XC&&lb>=8){ gwait<4>(sm,C_G,XG); __builtin_amdgcn_s_sleep(XC); xreq(X,ws); gwait<8>(sm,C_U,XU); } else { gwait<8>(sm,C_U,2); xreq(X,ws); if(AW&&lb<8&&w4>=4-AW) pf2(lb-1+7*(3-w4),xg,wd); } }
    { u32 e=now_()-ldw, nm= lb<8&&lb!=CTLB? ANA : (lb>=8&&lb<14? ANC:ANB); if(e>nm) ldw+=e-nm; }
    final_addr(fa,sm,w4);
    bool ix= w4==3&&lb<14&&lb!=CTLB; int po=W_IDX+(xg*8+(lb&3))*128+(ln&7)*8, ho_=fo_+(W_HT-W_FL), pq_= ix? po:fo_; PIN(ho_); PIN(pq_);
    { u64 fs,fs1;
      asm volatile("s_waitcnt vmcnt(0)" : "+{v[96:99]}"(X.q[0]),"+{v[100:103]}"(X.q[1]),"+{v[104:107]}"(X.q[2]),"+{v[108:111]}"(X.q[3]) :: "memory");
      fs=(u64)X.q[3][0]|((u64)X.q[3][1]<<32); fs1=(u64)X.q[3][2]|((u64)X.q[3][3]<<32);
      if(__builtin_expect(BALLOT((u32)(fs>>32)!=(u32)tag||(u32)(fs1>>32)!=(u32)tag)!=0ull,0)){ fs=poll1<32>(ws+so_,tag<<32); fs1=poll1<32>(ws+so_+128,tag<<32); __builtin_amdgcn_s_waitcnt(0x0f70); }
      float ri=rcpf_(bhi((u32)fs)), rj=rcpf_(bhi((u32)fs1)); rc[0]=f32x2{ri,-blo((u32)fs)*ri}; rc[1]=f32x2{rj,-blo((u32)fs1)*rj}; rc[2]= (lb&2)? rc[1]:rc[0];
      #pragma unroll
      for(int z=0;z<3;z++) xq[z]=X.q[z]; }
    if(DF) final_pre<2,3>(fp,w4,xq,dq,rc); else final_pre<0,1>(fp,w4,xq,dq,rc);
    ti=now_()-ldw;
    asm volatile("global_load_dwordx2 v[112:113], %0, %2 sc1\n\tglobal_load_dwordx2 v[114:115], %1, %2 sc1\n\tv_mov_b32 v117, %5\n\ts_cmp_lt_u32 %4, " STR(HT) "\n\ts_cbranch_scc1 1f\n\tglobal_load_dwordx2 v[116:117], %3, %2 sc1\n1:"
      :: "v"(fo_),"v"(pq_),"s"(ws),"v"(ho_),"s"(ti),"s"(~(u32)tag) : "memory","scc");
    if(DF) final_pre<0,1>(fp,w4,xq,dq,rc); else final_pre<1,3>(fp,w4,xq,dq,rc);
    PRIO(EPR);
    { u64 ff,f; u32x2 h;
      asm volatile("s_waitcnt vmcnt(0)\n\tv_mov_b64 %0, v[112:113]\n\tv_mov_b64 %1, v[114:115]\n\tv_mov_b64 %2, v[116:117]" : "=v"(ff),"=v"(f),"=v"(h) :: "memory");
      _Pragma("unroll 1") for(int k=0;k<2;k++){ if(BALLOT(h[1]!=(u32)tag)==0ull||ti>=H2) break;
        tgate2(ldw,ti<HT? HT:H2); ti=now_()-ldw; if(ti>=HL){ h=u32x2{ldw,(u32)tag}; break; }
        asm volatile("global_load_dwordx2 %0, %1, %2 sc1\n\ts_waitcnt vmcnt(0)" : "=&v"(h) : "v"(ho_),"s"(ws) : "memory"); }
      u32 rel= h[1]==(u32)tag? h[0]-ldw : ti+HM; td=umin_(wmax(rel),ti+160u); if(td>HK) td-=(td-HK)>>1;
      if(__builtin_expect(BALLOT(ff!=tag)!=0ull,0)) poll1<0>(ws+fo_,tag);
      if(ix){ if(BALLOT(((f^(tag<<16))>>16)!=0ull)!=0ull) f=poll1<16>(ws+pq_,tag<<16);
        if(ln<8) ST(u16,sm+O_IDX+ln*2)=(u16)f; garrive(sm,C_IX); }
      __builtin_amdgcn_s_waitcnt(0x0f70); }
  }
  final_post(sm,fp,fa,w4,out,ws,xr_,ldw,td,xq,dq,rc);
  }
}

extern "C" void kernel_launch(void* const* d_in, const int* in_sizes, int n_in, void* d_out, int out_size, void* d_ws, size_t ws_size, hipStream_t stream) {
  const u16** I=(const u16**)d_in;
  KP kp; kp.Are=I[1]; kp.Aim=I[2]; kp.Bre=I[3]; kp.Bim=I[4]; kp.Cre=I[5]; kp.Cim=I[6]; kp.ldt=I[7]; kp.pad=I[7];
  kA<<<256,1024,0,stream>>>(I[0],(char*)d_ws,I[10],I[8],I[11],I[12],I[13],(u16*)d_out,kp);
}
```

```cpp
#include <hip/hip_runtime.h>
#include <hip/hip_bf16.h>
#include <stdint.h>
#if defined(__HIP_DEVICE_COMPILE__)
#define NOPK __attribute__((target("no-packed-fp32-ops")))
#else
#define NOPK
#endif
#define DEV __device__ NOPK __forceinline__
typedef unsigned short u16;
typedef unsigned int u32;
typedef unsigned long long u64;
typedef short bf16x8 __attribute__((ext_vector_type(8)));
typedef _Float16 h8 __attribute__((ext_vector_type(8)));
typedef _Float16 h2 __attribute__((ext_vector_type(2)));
typedef float f32x4 __attribute__((ext_vector_type(4)));
typedef float f32x2 __attribute__((ext_vector_type(2)));
typedef u32 u32x4 __attribute__((ext_vector_type(4)));
typedef u32 u32x2 __attribute__((ext_vector_type(2)));
typedef u32 u32x8 __attribute__((ext_vector_type(8)));
typedef __bf16 bf2 __attribute__((ext_vector_type(2)));
typedef short v4s __attribute__((ext_vector_type(4)));
typedef __amdgpu_buffer_rsrc_t rsrc_t;
#define GAS __attribute__((address_space(1)))
#define LAS __attribute__((address_space(3)))
typedef LAS char lch;
#define L_ 2048
#define D_ 576
#define F_ 4608
#define BC(T,v) __builtin_bit_cast(T,v)
#define TID ((int)__builtin_amdgcn_workitem_id_x())
#define BID ((int)__builtin_amdgcn_workgroup_id_x())
#define RFL(x) __builtin_amdgcn_readfirstlane(x)
#define RLX_AGENT __ATOMIC_RELAXED, __HIP_MEMORY_SCOPE_AGENT
#define LD(T,p) (*(const LAS T*)(p))
#define ST(T,p) (*(LAS T*)(p))
#define SPINCAP (1<<17)
#define DRAIN() asm volatile("s_waitcnt vmcnt(0)" ::: "memory")
#define GLD(p) __hip_atomic_load((const GAS u64*)(p),RLX_AGENT)
#define GST(p,v) __hip_atomic_store((GAS u64*)(p),(u64)(v),RLX_AGENT)
#define BALLOT(c) __builtin_amdgcn_ballot_w64(c)
#define SC1 16
#define LANE(l) int l; { int t2_=TID; asm volatile("" : "+v"(t2_)); l=t2_&63; }
#define XB 1
#define XBW "0"
#define UE 10
#define UN 4
#define LO 0
#define US 0
#define USB 64
#define CL 0
#define PD 0
#define HO 1
#define IV 1
#define INV0
#define INV1 "global_store_dwordx4 v23, v[124:127], s[66:67] sc1\n\t"
#define DF 1
#define DPR 3
#define EPR 0
#define XC 0
#define XG 7
#define XU 2
#define W2C 7
#define AW 0
#define KD0
#define KD1
#define KDC
#define S3 9
#define W3B 6
#define RDL 0
#define PM 0
#define PP0
#define PRD
#define LP0 3
#define SN 12
#define LINV 0
#define SCHED() __builtin_amdgcn_sched_barrier(0)
#define RSRC(p,n) __builtin_amdgcn_make_buffer_rsrc((void*)(p),0,(n),0x00020000)
#define BLD128 __builtin_amdgcn_raw_buffer_load_b128
#define BLD32 __builtin_amdgcn_raw_buffer_load_b32
#define BST128 __builtin_amdgcn_raw_buffer_store_b128
#define PRIO(n) __builtin_amdgcn_s_setprio(n)

DEV float b2f(u32 v){ return BC(float,v<<16); }
DEV float blo(u32 v){ return BC(float,v<<16); }
DEV float bhi(u32 v){ return BC(float,v&0xffff0000u); }
DEV u32 pk(float a,float b){ f32x2 v={a,b}; bf2 r=__builtin_convertvector(v,bf2); return BC(u32,r); }
DEV u32 pkh(float a,float b){ f32x2 v={a,b}; h2 r=__builtin_convertvector(v,h2); return BC(u32,r); }
DEV float rb(float f){ return BC(float,pk(f,f)<<16); }
DEV float rcpf_(float x){ return __builtin_amdgcn_rcpf(x); }
DEV float expf_(float x){
  const float L2E=1.4426950216293335f, L2El=1.9259629911266175e-8f; float h=x*L2E; float l=__builtin_fmaf(x,L2E,-h)+x*L2El;
  float e=__builtin_amdgcn_exp2f(h); return __builtin_fmaf(e*l,0.6931471805599453f,e); }
DEV float sigm_f(float x){ return rcpf_(1.f+__builtin_amdgcn_exp2f(-x*1.4426950408889634f)); }
DEV float silu_f(float x){ return x*sigm_f(x); }
DEV u32 umax_(u32 a,u32 b){ return a>b?a:b; }
DEV u32 umin_(u32 a,u32 b){ return a<b?a:b; }

#define YSH 3
#define PRE1 6
#define PRE3 4
#define PRE2 5
#define W1 5
#define W3 3
#define W2_ 4
#define W1B 20
#define W2B 7
#define LAST1 4
#define RGATE 4
#define UGATE 7
#define R2D 12
#define PFN 32
#define CPF 8
#define HD0 174
#define HD1 148
#define HD3 165
#define HM 100
#define HT 555
#define H2 600
#define HL 650
#define HK 655
#define HKR 65
#define HR 12
#define ANA 410
#define ANB 470
#define ANC 540
#define NRG 8
#define W_EPOCH 0
#define W_RG 16384
#define W_ST (W_RG+32*8192)
#define W_U (W_ST+32768)
#define UST (D_*16+128)
#define YST (L_*2+128)
#define TL 1
#define GST_ ((D_*16<<TL)+128)
#define W_Y (W_U+256*UST)
#define W_PART (W_Y+D_*YST)
#define W_GU (W_PART+8*9*16*256)
#define W_FL 9216
#define W_HT (W_RG+16*8192)
#define W_IDX 1024
#define CTLB 0
struct KP { const u16 *Are,*Aim,*Bre,*Bim,*Cre,*Cim,*ldt,*pad; };

template<int CTRL> DEV float dppf(float v){ return BC(float,__builtin_amdgcn_update_dpp(0u,BC(u32,v),CTRL,0xf,0xf,false)); }
template<int CTRL> DEV u32 dppu(u32 v){ return (u32)__builtin_amdgcn_update_dpp(0u,v,CTRL,0xf,0xf,false); }
DEV float sum16(float v){ v+=dppf<0xB1>(v); v+=dppf<0x4E>(v); v+=dppf<0x141>(v); v+=dppf<0x140>(v); return v; }
DEV float add16x(float v){ auto r=__builtin_amdgcn_permlane16_swap(BC(u32,v),BC(u32,v),false,false); u32 a=r[0], b=r[1]; return BC(float,a)+BC(float,b); }
DEV u32 wmax(u32 v){
  v=umax_(v,(u32)__builtin_amdgcn_update_dpp(0u,v,0x111,0xf,0xf,false));
  v=umax_(v,(u32)__builtin_amdgcn_update_dpp(0u,v,0x112,0xf,0xf,false));
  v=umax_(v,(u32)__builtin_amdgcn_update_dpp(0u,v,0x114,0xf,0xf,false));
  v=umax_(v,(u32)__builtin_amdgcn_update_dpp(0u,v,0x118,0xf,0xf,false));
  v=umax_(v,(u32)__builtin_amdgcn_update_dpp(0u,v,0x142,0xa,0xf,false));
  v=umax_(v,(u32)__builtin_amdgcn_update_dpp(0u,v,0x143,0xc,0xf,false));
  return (u32)__builtin_amdgcn_readlane((int)v,63); }

DEV void garrive(lch* sm,int c){ u32 ad=(u32)(size_t)sm+c*4, one=1;
  asm volatile("s_mov_b64 exec, 1\n\tds_add_u32 %0, %1\n\ts_mov_b64 exec, -1" :: "v"(ad),"v"(one) : "memory"); }
template<int SLP> DEV void gwait(lch* sm,int c,u32 target){ u32 ad=(u32)(size_t)sm+c*4, tv, cnt, sv; target=RFL(target);
  asm volatile("s_mov_b32 %1, 0x20000\n"
    "1:\n\tds_read_b32 %0, %3\n\ts_waitcnt lgkmcnt(0)\n\tv_readfirstlane_b32 %2, %0\n\ts_cmp_ge_u32 %2, %4\n\ts_cbranch_scc1 2f\n\ts_sleep %5\n\ts_sub_u32 %1, %1, 1\n\ts_cbranch_scc0 1b\n"
    "2:" : "=&v"(tv),"=&s"(cnt),"=&s"(sv) : "v"(ad),"s"(target),"i"(SLP) : "memory","scc"); }
DEV u32 now_(){ u32 t; asm volatile("s_memrealtime s[98:99]\n\ts_waitcnt lgkmcnt(0)\n\ts_mov_b32 %0, s98" : "=s"(t) :: "memory"); return t; }
DEV void tgate2(u32 t0,u32 d){ u32 cnt; asm volatile("s_mov_b32 %0, 100\n1:\n\ts_memrealtime s[98:99]\n\ts_waitcnt lgkmcnt(0)\n\ts_sub_u32 s98, s98, %1\n\ts_cmp_ge_u32 s98, %2\n\ts_cbranch_scc1 2f\n\ts_sleep 1\n\ts_sub_u32 %0, %0, 1\n\ts_cbranch_scc0 1b\n2:" : "=&s"(cnt) : "s"(t0),"s"(d) : "memory","scc"); }
DEV void gsync(lch* sm,int c,u32 target){ garrive(sm,c); gwait<0>(sm,c,target); }
template<int SH> DEV u64 poll1(const char* p,u64 want){ u64 f=GLD(p);
  _Pragma("unroll 1") for(int sp=0;sp<SPINCAP;sp++){ bool bad=((f^want)>>SH)!=0ull; if(BALLOT(bad)==0ull) break; __builtin_amdgcn_s_sleep(1); if(bad) f=GLD(p); }
  __builtin_amdgcn_fence(__ATOMIC_ACQUIRE,"wavefront"); return f; }
#define STR_(x) #x
#define STR(x) STR_(x)
DEV u32x4 mkrs(const void* p,u32 n){ u64 a=(u64)p; return u32x4{(u32)RFL((int)(u32)a),(u32)RFL((int)((u32)(a>>32)&0xffffu)),n,0x00020000u}; }
#define VMW(n) asm volatile("s_waitcnt vmcnt(%0)" :: "i"(n) : "memory")
#define YSLP 1
#define GSLP 1
#define QSLP 1
#define PSLP 3
#define POLLASM(LOADS,CHECK,K) "s_mov_b32 %[cnt], 0x20000\n1:\n\ts_waitcnt vmcnt(0)\n\t" CHECK "s_cbranch_vccz 9f\n\ts_sleep " STR(K) "\n\ts_mov_b64 exec, vcc\n\t" LOADS \
  "s_mov_b64 exec, -1\n\ts_sub_u32 %[cnt], %[cnt], 1\n\ts_cbranch_scc0 1b\n9:\n\t"
#define LD_DUP2 "buffer_load_dwordx4 v[112:115], %[of], %[rs], 0 offen sc1\n\tbuffer_load_dwordx4 v[116:119], %[of], %[rs], %[so] offen sc1\n\t"
#define MX2(a,b) "v_max3_u32 %[t0], %[t0], v" #a ", v" #b "\n\t"
#define CHK_I0 "v_max_u32 %[t0], v112, v113\n\t" MX2(114,115)
#define CHK_IE "v_cmp_eq_u32 vcc, -1, %[t0]\n\t"
#define PCLOB "memory","vcc","scc"
#define UMASK 0x40074007u
DEV u32 upat(u32 tag,int k){ return ((((tag*0xea959c21u)>>(8*k))&255u)*0x04000801u)&UMASK; }
DEV u32 pkz(float a,float b){ return BC(u32,__builtin_amdgcn_cvt_pkrtz(a,b)); }
#define INV 0xffffffffu
DEV u32 nv(u32 v){ return umin_(v,0xfffeffffu); }
DEV u32x4 nv4(u32x4 v){ return u32x4{nv(v[0]),nv(v[1]),nv(v[2]),nv(v[3])}; }
#define OOR 0x40000000

#define CHSZ 36864
#define O_CH 512
#define O_CND 256
#define SLSZ 38080
#define O_R2 (O_CH+2*SLSZ)
#define O_X2 (O_R2+CHSZ)
#define O_TILE (O_X2+CHSZ)
#define XSTR 1216
#define TSTR2 1216
#define O_PAR (O_TILE+9728)
#define O_CAND (O_PAR+3072)
#define LDS_TOTAL (O_CAND+128)
#define C_G 0
#define C_U 8
#define C_L 4
#define C_D 5
#define C_X 12
#define C_P 13
#define C_H 14
#define C_I 17
#define C_E 18
#define C_IX 19
#define C_RS 22
#define C_T 16
#define O_IDX 96
#define O_EXP 144
#define O_RMX 192
#define O_RV 128
#define O_MD 400
#define O_RC (O_CAND+64)

struct C2 { float r,i; };
DEV C2 cmul(C2 a,C2 b){ C2 c; c.r=__builtin_fmaf(a.r,b.r,-(a.i*b.i)); c.i=__builtin_fmaf(a.r,b.i,a.i*b.r); return c; }
DEV h8 ldA(const lch* p){ u32x2 a=*(const volatile LAS u32x2*)p, b=*(const volatile LAS u32x2*)(p+8); return BC(h8,(u32x4{a[0],a[1],b[0],b[1]})); }
DEV bf16x8 trfrag(const lch* p0,const lch* p1){
  u32x2 b0=BC(u32x2,__builtin_amdgcn_ds_read_tr16_b64_v4i16((LAS v4s*)p0)), b1=BC(u32x2,__builtin_amdgcn_ds_read_tr16_b64_v4i16((LAS v4s*)p1));
  return BC(bf16x8,(u32x4{b0[0],b0[1],b1[0],b1[1]})); }

struct FP { float gg[24], bs[24]; };
template<int Z0,int Z1> DEV void final_pre(FP& f,int w4,const u32x4* xq,const u32x4* dq,const f32x2* rc){
  #pragma unroll
  for(int z=Z0;z<Z1;z++) if(z<2||w4==0){ float ea=rc[z][0]*-1.4426950408889634f, eb=rc[z][1]*-1.4426950408889634f;
    #pragma unroll
    for(int e=0;e<4;e++){ u32 xw=xq[z][e], dw=dq[z>>1][e]; int i=8*z+2*e; float a=blo(xw), b=bhi(xw);
      float pa=__builtin_fmaf(a,rc[z][0],rc[z][1]), pb=__builtin_fmaf(b,rc[z][0],rc[z][1]);
      float ga=pa*rcpf_(1.f+__builtin_amdgcn_exp2f(__builtin_fmaf(a,ea,eb))), gb=pb*rcpf_(1.f+__builtin_amdgcn_exp2f(__builtin_fmaf(b,ea,eb)));
      f.gg[i]=ga; f.gg[i+1]=gb; f.bs[i]=__builtin_fmaf(pa*blo(dw),ga,a); f.bs[i+1]=__builtin_fmaf(pb*bhi(dw),gb,b);
      asm volatile("" : "+v"(f.gg[i]),"+v"(f.gg[i+1]),"+v"(f.bs[i]),"+v"(f.bs[i+1])); } }
}
#define FTS 544
DEV u32 pkmin(u32 a,u32 b){ u32 r; asm("v_pk_min_u16 %0, %1, %2" : "=v"(r) : "v"(a),"v"(b)); return r; }
struct FA { int yo,yo3; lch *wp,*wp3; const lch* tp; u32 tt; };
DEV void final_addr(FA& a,lch* sm,int w4){ LANE(ln); int bid=BID, xg=bid&7, lb=bid>>3, cg=lb&3, T=8*xg+(lb>>2);
  a.yo=(128*cg+32*w4+(ln>>2)*2)*YST+(32*T+8*(ln&3))*2; a.yo3= ln<32? (512+32*(cg&1)+(ln&30))*YST+(32*T+16*(cg>>1)+8*(ln&1))*2 : OOR;
  lch* fb=sm+O_X2+2048+w4*4608; a.wp=fb+(ln&3)*FTS+(ln&60); a.wp3=fb+(4+(ln&1)+(ln>>5)*2)*FTS+(ln&30)*2; a.tp=fb+(ln>>5)*FTS+(ln&31)*16;
  a.tt=(u32)(32*T+(ln>>2))*0x10001u;
  asm volatile("" : "+v"(a.yo),"+v"(a.yo3),"+v"(a.wp),"+v"(a.wp3),"+v"(a.tp),"+v"(a.tt)); }
DEV void final_post(lch* sm,FP& f,const FA& fa,int w4,u16* __restrict__ out,char* ws,int xr,u32 tk,u32 td,const u32x4* xq,const u32x4* dq,const f32x2* rc){
  int lb=BID>>3, cg=lb&3, ob[3], yo=fa.yo, yo3=fa.yo3; lch *wp=fa.wp, *wp3=fa.wp3; const lch* tp=fa.tp; u32 tt=fa.tt;
  u32x4 rs=mkrs(ws+W_Y,D_*YST); rsrc_t ro=RSRC(out,L_*D_*2);
  #define RMASK { gwait<2>(sm,C_IX,1); u32x4 iq=LD(u32x4,sm+O_IDX); \
    _Pragma("unroll") for(int z=0;z<2;z++){ u32 t2=tt+z*0x100010u, m=pkmin(pkmin(iq[0]^t2,iq[1]^t2),pkmin(iq[2]^t2,iq[3]^t2)); ob[z]= umin_(m&0xffffu,m>>16)==0u? OOR : xr+z*(16*D_*2); } \
    ob[2]=((cg&2)? ob[1]:ob[0])+(512+32*(cg&1)-128*cg)*2; asm volatile("" : "+v"(ob[0]),"+v"(ob[1]),"+v"(ob[2])); }
  tgate2(tk,td);
  if(lb>=8) gwait<1>(sm,C_H,4);
  { u32 t0,t1,cnt;
    #define LD_Y "buffer_load_dwordx4 v[112:115], %[of], %[rs], 0 offen sc1\n\tbuffer_load_dwordx4 v[116:119], %[of], %[rs], %[s1] offen sc1\n\ts_cmp_lg_u32 %[w], 0\n\ts_cbranch_scc1 5f\n\t" \
      "buffer_load_dwordx4 v[120:123], %[o3], %[rs], 0 offen sc1\n\tbuffer_load_dwordx4 v[124:127], %[o3], %[rs], %[s1] offen sc1\n5:\n\t"
    #define YIN [of]"v"(yo),[rs]"s"(rs),[s1]"s"(YST),[o3]"v"(yo3),[w]"s"(w4)
    asm volatile(LD_Y :: YIN : "memory","scc");
    PRIO(DPR); if(DF) final_pre<1,2>(f,w4,xq,dq,rc);
    PRIO(3);
    RMASK
    #define YW(p,a,b,o) "v_perm_b32 %[t0], v" #b ", v" #a ", %[lo]\n\tv_perm_b32 %[t1], v" #b ", v" #a ", %[hi]\n\tds_write2_b32 %[" #p "], %[t0], %[t1] offset0:" #o " offset1:" #o "+16\n\t"
    asm volatile(POLLASM(LD_Y,CHK_I0 MX2(116,117) MX2(118,119) "s_cmp_lg_u32 %[w], 0\n\ts_cbranch_scc1 6f\n\t" MX2(120,121) MX2(122,123) MX2(124,125) MX2(126,127) "6:\n\t" CHK_IE,YSLP)
      YW(wp,112,116,0) YW(wp,113,117,32) YW(wp,114,118,64) YW(wp,115,119,96) "s_cmp_lg_u32 %[w], 0\n\ts_cbranch_scc1 7f\n\t" YW(w3,120,124,0) YW(w3,121,125,32) YW(w3,122,126,64) YW(w3,123,127,96) "7:"
      : [t0]"=&v"(t0),[t1]"=&v"(t1),[cnt]"=&s"(cnt) : YIN,[wp]"v"(wp),[w3]"v"(wp3),[lo]"s"(0x05040100u),[hi]"s"(0x07060302u) : PCLOB); }
  u32x4 r[3];
  #pragma unroll
  for(int z=0;z<3;z++) r[z]=LD(u32x4,tp+z*(2*FTS));
  #pragma unroll
  for(int z=0;z<3;z++) if(z<2||w4==0){ u32x4 ov;
    #pragma unroll
    for(int e=0;e<4;e++){ u32 rw=r[z][e]; int i=8*z+2*e; ov[e]=pk(__builtin_fmaf(blo(rw),f.gg[i],f.bs[i]),__builtin_fmaf(bhi(rw),f.gg[i+1],f.bs[i+1])); }
    BST128(ov,ro,ob[z],0,SC1); SCHED(); }
}

#define TROW 528
#define O_TB 4640
#define O_DE 34752
#define O_DO 35968
#define O_R2K 37120
#define O_R3K 37632
#define O_DUM 37952
#define O_RED O_TB
#define MH(a,b,c) __builtin_amdgcn_mfma_f32_16x16x32_f16(a,b,c,0,0,0)
#define PIN(x) asm volatile("" : "+v"(x))
DEV h8 ld4w(const lch* p){ return BC(h8,(u32x4{LD(u32,p),LD(u32,p+4),LD(u32,p+8),LD(u32,p+12)})); }
DEV u32 __umul24_(u32 a,u32 b){ u32 r; asm("v_mul_u32_u24 %0, %1, %2" : "=v"(r) : "s"(b),"v"(a)); return r; }
DEV u32x4 band(u32 mask,int g,u32 val){ u32 by=__builtin_amdgcn_ubfe(mask,8*g,8), X=by|(by<<15), k=0x10001u; u32x4 r; asm volatile("" : "+s"(k),"+s"(val));
  #pragma unroll
  for(int d=0;d<4;d++) r[d]=__umul24_((X>>(2*d))&k,val);
  return r; }
DEV h8 zpack(f32x4 e,f32x4 o){ return BC(h8,(u32x4{pkh(e[0],o[0]),pkh(e[1],o[1]),pkh(e[2],o[2]),pkh(e[3],o[3])})); }
struct C3 { float r,i,n; };
DEV C3 c3(C2 a){ C3 c; c.r=a.r; c.i=a.i; PIN(c.i); c.n=-c.i; PIN(c.n); return c; }
DEV C2 cm(C2 a,C3 b){ C2 c; c.r=__builtin_fmaf(a.r,b.r,a.i*b.n); c.i=__builtin_fmaf(a.r,b.i,a.i*b.r); return c; }
DEV u32 ph(C2 a){ return pkh(a.r,a.i); }
#define W2(p,a,b) asm volatile("ds_write2_b32 %0, %1, %2 offset1:132" :: "v"(p),"v"(a),"v"(b) : "memory")
struct CP { const lch *x0,*x1,*bd; lch *p0,*p1,*p2,*wp; u32x4 Be,Bo,AE; int yo; };
template<int ROLE> DEV void lanes(CP& c,lch* cb,int ln){ int m=ln&15, g=ln>>4;
  const lch* ad=cb+((m&1)? O_DO+2*527 : O_DE+2*536)+2*(8*g-m);
  const lch* bd=cb+2*(256+16*m+8*g); lch* dum=cb+O_DUM;
  if(ROLE==0){ int l0=128*(g>>1)+8*m+4*(g&1); c.p0=cb+O_DE+(549-l0)*2; PIN(c.p0); c.p1=c.p0+6; PIN(c.p1); c.p2=c.p0+(O_DO-O_DE-18); c.x0=ad-640; c.bd=bd-64; c.x1=cb+O_R3K+2*(126-2*m+8*g)-192; PIN(c.x1);
    c.Be=band(0x1ffu<<(m+4),g,0x3c00u); PIN(c.Be); }
  if(ROLE==1){ lch* r=cb+O_R2K+(192-64*(g-2)-2*m)*2; c.p0= g<2? dum : r-66; c.p1= ln==0? cb+O_DE+(552-256)*2 : (ln<=32? dum : r); c.p2=cb+O_DE+(544-256-8*ln)*2; c.x0=ad-128-640; c.bd=bd-128-128; PIN(c.p0); PIN(c.p1); PIN(c.p2); }
  if(ROLE==2){ int i0=16*m+4*g; c.p0= m<8? cb+O_R3K+(125-i0)*2 : dum+2; c.p1= ln==0? cb+O_R2K+128 : (ln==8? cb+O_R3K : c.p0+6); c.x0=ad-320-640; c.bd=bd-320-128; PIN(c.p0); PIN(c.p1); }
  if(ROLE==3){ c.x0=cb+O_R2K+2*(188-4*m+8*g)-256; c.bd=cb+2*(248+8*m+8*g);
    c.Be=band(0x1fu<<(m+6),g,0x3c00u); c.Bo=band(0x7u<<(m+11),g,0x3c00u); PIN(c.Be); PIN(c.Bo); }
  PIN(c.x0); PIN(c.bd); }
template<int ROLE> DEV void lanes2(CP& c,lch* cb,int ln){ int m=ln&15, g=ln>>4;
  if(ROLE==0){ c.Bo=band(0x7fu<<(m+5),g,0x3c00u); c.AE=band(3u<<(m+12),g,0xffffu); PIN(c.Bo); PIN(c.AE); }
  c.wp=cb+O_RED+ln*16; c.yo=(g&1)*512+m*32+(g>>1)*16; PIN(c.wp); PIN(c.yo); }
template<int ROLE> DEV void chan2(CP& c,lch* sm,lch* cb,const lch*& fa,const lch*& fb,int grp,int d,int one_,int emax,u64 tag,char* ws){
  const f32x4 Z4={0,0,0,0}; h8 A[6]; f32x4 acc[8];
  #define LD_U "buffer_load_dwordx4 v[112:115], %[of], %[rs], 0 offen sc1\n\tbuffer_load_dwordx4 v[116:119], %[of], %[rs], %[s1] offen sc1\n\tbuffer_load_dwordx4 v[120:123], %[of], %[rs], %[s2] offen sc1\n\tbuffer_load_dwordx4 v[124:127], %[of], %[rs], %[s3] offen sc1\n\t"
  #define UC(t,a,b,c) "v_bitop3_b32 " t ", v" #a ", v" #b ", %[" #c "] bitop3:0x7e\n\t"
  #define UD(t,a,c) "v_bitop3_b32 " t ", " t ", v" #a ", %[" #c "] bitop3:0xf6\n\t"
  #define CHK_U UC("%[t0]",112,116,c0) UC("%[t1]",113,117,c1) UD("%[t0]",120,c0) UD("%[t1]",121,c1) UD("%[t0]",124,c0) UD("%[t1]",125,c1) UD("%[t0]",114,c2) UD("%[t1]",115,c3) UD("%[t0]",118,c2) UD("%[t1]",119,c3) \
    UD("%[t0]",122,c2) UD("%[t1]",123,c3) UD("%[t0]",126,c2) UD("%[t1]",127,c3) "v_bitop3_b32 %[t0], %[t0], %[t1], %[mk] bitop3:0xa8\n\tv_cmp_ne_u32 vcc, 0, %[t0]\n\t"
  #define UX(a,b,c,d,o) "v_xor_b32 v" #a ", %[e0], v" #a "\n\tv_xor_b32 v" #b ", %[e1], v" #b "\n\tv_xor_b32 v" #c ", %[e2], v" #c "\n\tv_xor_b32 v" #d ", %[e3], v" #d "\n\tds_write_b128 %[up], v[" #a ":" #d "] offset:" #o "\n\t"
  #define UPOLL { u32 t0,t1,cnt; asm volatile(POLLASM(LD_U,CHK_U,PSLP) UX(112,113,114,115,544) UX(116,117,118,119,1056) UX(120,121,122,123,1568) UX(124,125,126,127,2080) \
      : [t0]"=&v"(t0),[t1]"=&v"(t1),[cnt]"=&s"(cnt) : [of]"v"(c.p0),[rs]"s"(rsU),[s1]"s"((32>>TL)*GST_),[s2]"s"((64>>TL)*GST_),[s3]"s"((96>>TL)*GST_),[c0]"s"(c0),[c1]"s"(c1),[c2]"s"(c2),[c3]"s"(c3_), \
        [e0]"s"(c0^0x40004u),[e1]"s"(c1^0x40004u),[e2]"s"(c2^0x40004u),[e3]"s"(c3_^0x40004u),[mk]"s"(UMASK),[up]"v"(c.p1) : PCLOB); garrive(sm,C_U+one_); }
  if(ROLE<3){
    h8 a[8], b[8];
    #pragma unroll
    for(int ks=0;ks<8;ks++){ a[ks]=LD(h8,fa+ks*64); b[ks]=LD(h8,fb+ks*64); }
    SCHED();
    LANE(ln); if(!HO) lanes<ROLE>(c,cb,ln); float fz= ln==32? 0.f : 4.76837158203125e-07f; if(ROLE==1) PIN(fz);
    SCHED();
    f32x4 k=Z4;
    #pragma unroll
    for(int ks=0;ks<8;ks++) k=MH(a[ks],b[ks],k);
    const float f=4.76837158203125e-07f;
    float k0=k[0]*f, k1=k[1]*f, k2=k[2]*f, k3=k[3]*f;
    u32 h32=pkh(k3,k2), h10=pkh(k1,k0);
    if(ROLE!=1){
      ST(u16,c.p0)=(u16)h32; ST(u32,c.p0+2)=pkh(k2,k1); ST(u16,c.p1)=(u16)(h10>>16);
      if(ROLE==0) ST(u32x2,c.p2)=u32x2{h32,h10};
    } else {
      float n0=dppf<0x130>(k[0])*fz, z1= fz==0.f? k0:k1, z2=k[2]*fz, z3=k[3]*fz; u32 h22=pkh(z2,z2);
      ST(u16,c.p0+64)=(u16)h10; ST(u16,c.p0+2)=(u16)(h32>>16); ST(u16,c.p0)=(u16)h32; ST(u16,c.p1)=(u16)(h10>>16);
      u32x4 vo={pkh(n0,z3),h22,pkh(z2,z1),pkh(k0,k0)}, ve={pkh(n0,n0),pkh(z3,z2),h22,pkh(z1,k0)};
      asm volatile("s_mov_b32 exec_hi, 1\n\tds_write_b128 %0, %1\n\tds_write_b128 %0, %2 offset:1200\n\ts_mov_b32 exec_hi, -1" :: "v"(c.p2),"v"(ve),"v"(vo) : "memory"); }
    gsync(sm,C_G+grp,7);
    u32 kv; A[0]=ld4w(c.x0+640); A[1]=ld4w(c.x0+128);
    if(ROLE==0){ A[2]=ld4w(c.x1+192); A[3]=ld4w(c.x1+128); A[4]=ld4w(c.x1+64); A[5]=ld4w(c.x1); kv=LD(u16,cb+O_R2K+128); }
    else { A[2]=ld4w(c.x0+576); A[3]=ld4w(c.x0+64); A[4]=ld4w(c.x0+512); A[5]=ld4w(c.x0); }
    SCHED(); if(!HO) lanes2<ROLE>(c,cb,ln); SCHED();
    if(ROLE==0){ kv|=kv<<16; c.AE=u32x4{c.AE[0]&kv,c.AE[1]&kv,c.AE[2]&kv,c.AE[3]&kv}; PIN(c.AE); }
    int ha=W_HT+d*8; u32 hd= one_? HD3 : (grp==0? HD0:HD1); if(ROLE==2) asm volatile("v_mov_b32 v113, %2" : "+v"(ha),"+v"(hd) : "s"((u32)tag));
    PRIO(2);
    if(BID>=112){
      gwait<4>(sm,UE? C_D:C_X,4);
    }
    gwait<1>(sm,C_U+one_,2-one_);
    if(ROLE==2) asm volatile("s_memrealtime s[98:99]\n\ts_waitcnt lgkmcnt(0)\n\tv_add_u32 v112, s98, %1\n\ts_mov_b64 exec, 1\n\tglobal_store_dwordx2 %0, v[112:113], %2 sc1\n\ts_mov_b64 exec, -1" :: "v"(ha),"v"(hd),"s"(ws) : "memory");
  } else {
    u32 tg=(u32)tag, c0=upat(tg,0), c1=upat(tg,1), c2=upat(tg,2), c3_=upat(tg,3); u32x4 rsU=mkrs(ws+W_U,256*UST);
    if(!one_) PRIO(2);
    LANE(ln); c.p0=(lch*)fa; c.p1=(lch*)fb;
    bool late= grp==0||BID>=112;
    if(!late) asm volatile(LD_U :: [of]"v"(c.p0),[rs]"s"(rsU),[s1]"s"((32>>TL)*GST_),[s2]"s"((64>>TL)*GST_),[s3]"s"((96>>TL)*GST_) : "memory");
    asm volatile("s_mov_b64 exec, 1\n\tglobal_store_dwordx2 %0, %1, %2 sc1\n\ts_mov_b64 exec, -1" :: "v"(W_FL+d*8),"v"(tag),"s"(ws) : "memory");
    if(!HO){ lanes<3>(c,cb,ln); lanes2<3>(c,cb,ln); }
    #pragma unroll
    for(int j=2;j<8;j++){ acc[j]=Z4; PIN(acc[j]); }
    if(late){ if(US&&BID<USB) __builtin_amdgcn_s_sleep(US); else if(UE==0||BID<112) gwait<2>(sm,C_G,UGATE); if(BID>=112){ gwait<1>(sm,UE? C_RS:C_D,UN); if(UE>1) __builtin_amdgcn_s_sleep(UE); } asm volatile(LD_U :: [of]"v"(c.p0),[rs]"s"(rsU),[s1]"s"((32>>TL)*GST_),[s2]"s"((64>>TL)*GST_),[s3]"s"((96>>TL)*GST_) : "memory"); }
    if(!late&&!(LO==2||(LO==1&&one_))) UPOLL
    gwait<1>(sm,C_G+grp,7);
    #pragma unroll
    for(int i=0;i<6;i++) A[i]=ldA(c.x0+320-64*i);
    if(late||LO==2||(LO==1&&one_)) UPOLL
    PRIO(2);
    gwait<1>(sm,C_U+one_,2-one_);
  }
  const lch* bd=c.bd; h8 Be=BC(h8,c.Be), Bo=BC(h8,c.Bo);
  #pragma unroll
  for(int j=0;j<(ROLE==3? 2:8);j++) acc[j]=Z4;
  #define RW(j) if((j)>=0&&((j)>>1)!=ROLE&&!(ROLE==3&&(j)<2)){ ST(f32x4,c.wp+((j)>>1)*6144+((ROLE-((j)>>1)-1)&3)*2048+((j)&1)*1024)=acc[(j)<0? 0:(j)]; SCHED(); }
  if(ROLE==1||ROLE==2){
    h8 B[8];
    #pragma unroll
    for(int q=0;q<8;q++) B[q]=LD(h8,bd+128+512*q);
    #pragma unroll
    for(int r=0;r<3;r++){
      #pragma unroll
      for(int dl=0;dl<8;dl++){ if(dl<7) acc[dl+1]=MH(A[2*r+1],B[dl],acc[dl+1]); SCHED(); acc[dl]=MH(A[2*r],B[dl],acc[dl]); SCHED();
        if(r<2){ B[dl]=LD(h8,bd+64-64*r+512*dl); SCHED(); } else RW(dl-2) }
      } }
  if(ROLE==0){
    h8 B[8], ue[4];
    #pragma unroll
    for(int kt=0;kt<4;kt++){ ue[kt]=LD(h8,bd+48+512*kt); B[kt]=LD(h8,bd+64+512*kt); }
    f32x4 ce[2],co[2]; h8 Z[2];
    ce[0]=MH(ue[0],Be,Z4); co[0]=MH(B[0],Bo,Z4); ce[1]=MH(ue[1],Be,Z4); co[1]=MH(B[1],Bo,Z4); Z[0]=zpack(ce[0],co[0]);
    #pragma unroll
    for(int kt=0;kt<4;kt++){
      SCHED();
      if(kt+2<4){ ce[kt&1]=MH(ue[kt+2],Be,Z4); co[kt&1]=MH(B[kt+2],Bo,Z4); }
      if(kt+1<4) Z[(kt+1)&1]=zpack(ce[(kt+1)&1],co[(kt+1)&1]);
      #pragma unroll
      for(int rho=0;rho+kt<4;rho++) acc[4+kt+rho]=MH(Z[kt&1],A[2+rho],acc[4+kt+rho]);
      SCHED(); B[4+kt]=LD(h8,bd+64+512*(4+kt)); if(kt==0) A[5]=BC(h8,c.AE); if(kt==1) A[4]=ld4w(c.x0-384); if(kt==2) A[3]=ld4w(c.x0+64); if(kt==3) A[2]=ld4w(c.x0+576); SCHED(); }
    #pragma unroll
    for(int dl=0;dl<8;dl++){ if(dl<4) acc[dl+4]=MH(A[5],B[dl],acc[dl+4]); SCHED(); if(dl<6) acc[dl+2]=MH(A[4],B[dl],acc[dl+2]); SCHED(); if(dl<7) acc[dl+1]=MH(A[1],B[dl],acc[dl+1]); SCHED(); acc[dl]=MH(A[0],B[dl],acc[dl]);
      SCHED(); B[dl]=LD(h8,bd+512*dl); SCHED(); }
    #pragma unroll
    for(int dl=0;dl<8;dl++){ if(dl<7) acc[dl+1]=MH(A[3],B[dl],acc[dl+1]); SCHED(); acc[dl]=MH(A[2],B[dl],acc[dl]); SCHED(); RW(dl-2) } }
  if(ROLE==3){
    h8 U[12];
    #pragma unroll
    for(int kt=0;kt<6;kt++) U[kt]=LD(h8,bd+256*kt);
    f32x4 ce[2],co[2]; h8 Z[2];
    ce[0]=MH(U[0],Be,Z4); co[0]=MH(U[0],Bo,Z4); ce[1]=MH(U[1],Be,Z4); co[1]=MH(U[1],Bo,Z4); Z[0]=zpack(ce[0],co[0]);
    #pragma unroll
    for(int kt=0;kt<12;kt++){
      SCHED();
      if(kt+2<12){ ce[kt&1]=MH(U[kt+2],Be,Z4); co[kt&1]=MH(U[kt+2],Bo,Z4); }
      if(kt+1<12) Z[(kt+1)&1]=zpack(ce[(kt+1)&1],co[(kt+1)&1]);
      #pragma unroll
      for(int rho=-1;rho<5;rho++){ int j=(kt+4+rho)>>1; if(((kt+rho)&1)==0&&j>=2&&j<8) acc[j]=MH(Z[kt&1],A[rho+1],acc[j]); }
      if(kt<6){ SCHED(); U[kt+6]=LD(h8,bd+256*(kt+6)); }
      if(kt>=3&&kt<=9&&(kt&1)){ SCHED(); RW((kt+1)/2) } }
  }
  PRIO(3);
  RW(6) RW(7)
  gsync(sm,C_G+grp,11);
  { const lch* rp=c.wp+ROLE*6144; f32x4 a[3], b[3], o0=acc[2*ROLE], o1=acc[2*ROLE+1];
    #pragma unroll
    for(int q=0;q<(ROLE==0? 2:3);q++){ a[q]=LD(f32x4,rp+q*2048); b[q]=LD(f32x4,rp+q*2048+1024); }
    SCHED();
    float sc=__builtin_amdgcn_ldexpf(1.f,emax-YSH), s0[4], s1[4];
    #pragma unroll
    for(int e=0;e<4;e++){ s0[e]=((o0[e]+a[0][e])+(ROLE==0? a[1][e] : a[1][e]+a[2][e]))*sc; s1[e]=((o1[e]+b[0][e])+(ROLE==0? b[1][e] : b[1][e]+b[2][e]))*sc; }
    auto r0=__builtin_amdgcn_permlane16_swap(nv(pk(s0[0],s0[1])),nv(pk(s1[0],s1[1])),false,false); auto r1=__builtin_amdgcn_permlane16_swap(nv(pk(s0[2],s0[3])),nv(pk(s1[2],s1[3])),false,false);
    BST128(u32x4{r0[0],r1[0],r0[1],r1[1]},RSRC(ws+W_Y+d*YST,4096),c.yo+ROLE*1024,0,SC1); }
  garrive(sm,C_E);

  asm volatile("s_endpgm" : "=v"(fa),"=v"(fb),"=v"(c.x0),"=v"(c.x1),"=v"(c.bd),"=v"(c.p0),"=v"(c.p1),"=v"(c.p2),"=v"(c.wp),"=v"(c.Be),"=v"(c.Bo),"=v"(c.AE),"=v"(c.yo) :: "memory");
}
DEV void pf2(int e,int xg,const u16* wd){ LANE(ln); u32 q=e*64+ln, r=(q*21846u)>>16, c=q-3u*r;
  asm volatile("buffer_load_dword v123, %0, %1, 0 offen" :: "v"((576*xg+288*(r&1)+287-(r>>1))*(D_*2)+384*c+256),"s"(mkrs(wd,D_*F_*2)) : "memory"); }
#define GETEP asm volatile("s_waitcnt lgkmcnt(0)\n\ts_mov_b64 %0, s[96:97]\n\ts_mov_b32 %1, s98" : "=s"(ep),"=s"(ldw));
DEV void chan(lch* sm,int grp,int w4,int d,int one_,char* ws,u32x4 q0,u32x4 q1,u32x4 q2,u32x4 q3,u32x4 q4){
  lch* cb=sm+O_CH+grp*SLSZ; int role=w4, emax;
  #define LP(v) ((lch*)(size_t)(u32)(v))
  const lch* fa=LP(q4[0]); const lch* fb=LP(q4[1]);
  u64 tag; CP c;
  { float a2,bp,br,bi,cr,ci;
    u32 fq=q3[2], fr=q3[3], one=q4[2]; float f64=BC(float,fq), f128=BC(float,fr);
    lch *t8[4]={LP(q0[0]),LP(q0[1]),LP(q0[2]),LP(q0[3])}, *tB[4]={LP(q1[0]),LP(q1[1]),LP(q1[2]),LP(q1[3])}, *t4[2]={LP(q2[0]),LP(q2[1])}, *t2[2]={LP(q2[2]),LP(q2[3])}, *t1=LP(q3[0]), *te=LP(q3[1]);
    if(HO){ LANE(ln); asm volatile("" : "=v"(c.x1),"=v"(c.p0),"=v"(c.p1),"=v"(c.p2),"=v"(c.Be),"=v"(c.Bo),"=v"(c.AE));
      if(role==0){ lanes<0>(c,cb,ln); lanes2<0>(c,cb,ln); } else { if(role==1) lanes<1>(c,cb,ln); else if(role==2) lanes<2>(c,cb,ln); else lanes<3>(c,cb,ln); lanes2<1>(c,cb,ln); }
      asm volatile("" : "+v"(c.x0),"+v"(c.x1),"+v"(c.bd),"+v"(c.p0),"+v"(c.p1),"+v"(c.p2),"+v"(c.wp),"+v"(c.Be),"+v"(c.Bo),"+v"(c.AE),"+v"(c.yo)); }
    if(IV==2) asm volatile("global_store_dwordx4 v123, v[124:127], %0 sc1" :: "s"(ws) : "memory");
    if(grp!=0) PRIO(1);
    u64 ep; u32 ldw; GETEP tag=ep+1ull;
    float dt=rb(expf_((d&1)? bhi(ldw):blo(ldw))), ca=dt*1.4426950408889634f, cb_=dt*0.15915494309189535f;
    asm volatile("s_waitcnt vmcnt(1)\n\t" PRD "v_mul_f32 %2, %8, v114\n\tv_mul_f32 %3, %8, v115\n\tv_mul_f32 %0, %6, v112\n\tv_mul_f32 %1, %7, v113\n\tv_mov_b32 %4, v116\n\tv_mov_b32 %5, v117"
      : "=&v"(a2),"=&v"(bp),"=&v"(br),"=&v"(bi),"=&v"(cr),"=&v"(ci) : "v"(ca),"v"(cb_),"v"(dt) : "memory");
    C2 W; W.r=cr*br-ci*bi; W.i=cr*bi+ci*br;
    u32 mx=BC(u32,__builtin_fmaxf(__builtin_fabsf(W.r),__builtin_fabsf(W.i)));
    #define MXS(C) mx=umax_(mx,dppu<C>(mx));
    float e8=a2*8.f, eB=__builtin_fmaf(a2,f64,14.f), p8=bp*8.f, pB=bp*f64; SCHED();
    MXS(0xB1) p8=__builtin_amdgcn_fractf(p8); pB=__builtin_amdgcn_fractf(pB); e8=__builtin_amdgcn_exp2f(e8); SCHED();
    MXS(0x4E) eB=__builtin_amdgcn_exp2f(eB); float c8=__builtin_amdgcn_cosf(p8); SCHED();
    MXS(0x141) float s8=__builtin_amdgcn_sinf(p8), cB=__builtin_amdgcn_cosf(pB); SCHED();
    float sB=__builtin_amdgcn_sinf(pB); PIN(sB); MXS(0x140) float e1=__builtin_amdgcn_exp2f(a2); PIN(e1); SCHED();
    asm volatile("s_mov_b64 exec, 0x10001\n\tds_max_u32 %0, %1\n\tds_add_u32 %0, %2 offset:4\n\ts_mov_b64 exec, -1" :: "v"(te),"v"(mx),"v"(one) : "memory");
    C3 z8; z8.r=e8*c8; z8.n=e8*s8; z8.i=-z8.n; PIN(z8.i); C2 B; B.r=eB*cB; B.i=-(eB*sB);
    float p1=__builtin_amdgcn_fractf(bp), pE=__builtin_amdgcn_fractf(bp*256.f), pV=__builtin_amdgcn_fractf(bp*f128), eE=a2*256.f, eV=a2*f128, tr[8];
    #pragma unroll
    for(int k=0;k<4;k++){ SCHED(); u32 h0=ph(B); B=cm(B,z8);
      if(k==0){ tr[0]=__builtin_amdgcn_cosf(pV); tr[1]=__builtin_amdgcn_sinf(pV); } if(k==1){ tr[2]=__builtin_amdgcn_exp2f(eV); tr[3]=__builtin_amdgcn_cosf(p1); }
      if(k==2){ tr[4]=__builtin_amdgcn_sinf(p1); tr[5]=__builtin_amdgcn_exp2f(eE); } if(k==3){ tr[6]=__builtin_amdgcn_cosf(pE); tr[7]=__builtin_amdgcn_sinf(pE); }
      PIN(tr[2*k]); PIN(tr[2*k+1]);
      u32 h1=ph(B); if(k<3) B=cm(B,z8); W2(tB[k],h0,h1); }
    asm volatile("ds_read_b64 v[118:119], %0" :: "v"(te) : "memory");
    SCHED();
    C3 z1=c3(C2{e1*tr[3],e1*tr[4]}), E=c3(C2{tr[5]*tr[6],tr[5]*tr[7]}); C2 V[8]; V[0]=cmul(W,C2{tr[2]*tr[0],tr[2]*tr[1]});
    { u32 cnt,n; PIN(V[0].r); PIN(V[0].i); PIN(z1.r); PIN(E.r);
      asm volatile("s_mov_b32 %1, 0x20000\n1:\n\ts_waitcnt lgkmcnt(0)\n\tv_readfirstlane_b32 %2, v119\n\tv_readfirstlane_b32 %0, v118\n\ts_cmp_eq_u32 %2, 8\n\ts_cbranch_scc1 2f\n\tds_read_b64 v[118:119], %3\n\ts_sub_u32 %1, %1, 1\n\ts_cbranch_scc0 1b\n2:"
        : "=&s"(emax),"=&s"(cnt),"=&s"(n) : "v"(te) : "memory","scc");
      emax=RFL(emax); emax=(emax>>23)-126; }
    V[0].r=__builtin_amdgcn_ldexpf(V[0].r,14-emax); V[0].i=__builtin_amdgcn_ldexpf(V[0].i,14-emax);
    C2 Y=cm(V[0],E), Y4; u32 y0=ph(Y);
    V[1]=cm(V[0],z1); W2(t8[0],ph(V[0]),ph(V[1])); Y=cm(Y,E); u32 y1=ph(Y);
    V[2]=cm(V[1],z1); Y=cm(Y,E); u32 y2=ph(Y);
    V[3]=cm(V[2],z1); W2(t8[1],ph(V[2]),ph(V[3])); W2(t4[0],y0,ph(cm(V[2],E)));
    V[4]=cm(V[3],z1); Y=cm(Y,E); ST(u32,t1)=ph(Y);
    V[5]=cm(V[4],z1); W2(t8[2],ph(V[4]),ph(V[5])); Y4=cm(V[4],E); u32 y4=ph(Y4);
    V[6]=cm(V[5],z1); Y=cm(Y,E); ST(u32,t1+2*TROW)=ph(Y);
    V[7]=cm(V[6],z1); W2(t8[3],ph(V[6]),ph(V[7])); Y4=cm(Y4,E); W2(t2[0],y1,ph(Y4));
    W2(t4[1],y4,ph(cm(V[6],E))); Y=cm(Y,E); ST(u32,t1+4*TROW)=ph(Y);
    Y4=cm(Y4,E); W2(t2[1],y2,ph(Y4)); Y=cm(Y,E); ST(u32,t1+6*TROW)=ph(Y);
    Y=cm(Y,E); asm volatile("s_mov_b32 exec_hi, 0\n\tds_write_b32 %0, %1 offset:4224\n\ts_mov_b32 exec_hi, -1" :: "v"(t1),"v"(ph(Y)) : "memory");
  }
  if(one_) PRIO(3);
  DRAIN();
  gsync(sm,C_G+grp,4);
  if(role==0) chan2<0>(c,sm,cb,fa,fb,grp,d,one_,emax,tag,ws);
  if(role==1) chan2<1>(c,sm,cb,fa,fb,grp,d,one_,emax,tag,ws);
  if(role==2) chan2<2>(c,sm,cb,fa,fb,grp,d,one_,emax,tag,ws);
  chan2<3>(c,sm,cb,fa,fb,grp,d,one_,emax,tag,ws);
}

struct XR { int so,x0,x2; u32x4 rx,q[4]; };
DEV void xreq(XR& X,char* ws){ asm volatile("global_load_dwordx2 v[108:109], %[so], %[ws] sc1\n\tglobal_load_dwordx2 v[110:111], %[so], %[ws] offset:128 sc1\n\tbuffer_load_dwordx4 %[a], %[x0], %[rx], 0 offen\n\tbuffer_load_dwordx4 %[b], %[x0], %[rx], %[s1] offen\n\tbuffer_load_dwordx4 %[c], %[x2], %[rx], 0 offen"
  : [a]"=&{v[96:99]}"(X.q[0]),[b]"=&{v[100:103]}"(X.q[1]),[c]"=&{v[104:107]}"(X.q[2]),"=&{v[108:111]}"(X.q[3]) : [so]"v"(X.so),[ws]"s"(ws),[x0]"v"(X.x0),[x2]"v"(X.x2),[rx]"s"(X.rx),[s1]"s"(16*D_*2) : "memory"); }
DEV u32 med3u(u32 a,u32 b,u32 c){ u32 r; asm("v_med3_u32 %0, %1, %2, %3" : "=v"(r) : "v"(a),"v"(b),"v"(c)); return r; }
DEV u32 max3u(u32 a,u32 b,u32 c){ return umax_(umax_(a,b),c); }
#define GTC(r,s) "v_cmp_gt_u32 %[" #s "], v" #r ", %[c]\n\t"
#define GTA(s) "v_addc_co_u32 %[rk], vcc, 0, %[rk], %[" #s "]\n\t"
#define GT4(a,b,c,d) GTC(a,s0) GTC(b,s1) GTC(c,s2) GTC(d,s3) GTA(s0) GTA(s1) GTA(s2) GTA(s3)
#define GTS [s0]"=&s"(q0),[s1]"=&s"(q1),[s2]"=&s"(q2),[s3]"=&s"(q3)
DEV void top8(lch* sm,int w4,int ln,u32 M,char* ws,u32& rank_,u32& key_,XR& X){
  u32 k[8], rc[8], hm=0xffff0000u; u64 em=0x0001000100010001ull,q0,q1,q2,q3;
  u32x4 rs=mkrs(ws+W_RG+(BID&(NRG-1))*8192,8192); int ro=w4*1024+ln*16;
  #pragma unroll
  for(int e=0;e<8;e++){ rc[e]=(u32)(0xfff-(w4*256+ln*4+(e&3)+(e>>2)*1024)); PIN(rc[e]); }
  lch *aW=sm+O_RMX+(w4*4+(ln>>4))*4, *aC=sm+O_RMX+(ln>>2)*4, *aO=sm+O_RMX+(ln&3)*16, *dC=sm+O_CND+(ln>>1)*4, *dO=sm+O_CND+(ln&1)*64; PIN(aW); PIN(aC); PIN(aO); PIN(dC); PIN(dO); PIN(ro); PIN(ln);
  asm volatile("" : "+s"(hm),"+s"(em),"+s"(rs));
  gwait<2>(sm,C_G,RGATE);
  { u32 cnt;
    #define KL(e,r) "v_lshl_or_b32 %[k" #e "], v" #r ", 16, %[k" #e "]\n\t"
    #define KH(e,r) "v_and_or_b32 %[k" #e "], v" #r ", %[hm], %[k" #e "]\n\t"
    #define CHK_R "v_bitop3_b32 %[t0], v113, v115, %[M] bitop3:0x7e\n\tv_bitop3_b32 %[t1], v117, v119, %[M] bitop3:0x7e\n\tv_or_b32 %[t0], %[t0], %[t1]\n\tv_cmp_ne_u32 vcc, 0, %[t0]\n\t"
    u32 t0,t1;
    asm volatile(LD_DUP2
      "s_sleep " STR(R2D) "\n\tbuffer_load_dwordx4 v[120:123], %[of], %[rs], 0 offen sc1\n\tbuffer_load_dwordx4 v[124:127], %[of], %[rs], %[so] offen sc1\n\ts_mov_b32 %[cnt], 0x20000\n\ts_waitcnt vmcnt(2)\n\t" CHK_R "s_cbranch_vccz 9f\n\t"
      "s_waitcnt vmcnt(0)\n\tv_mov_b64 v[112:113], v[120:121]\n\tv_mov_b64 v[114:115], v[122:123]\n\tv_mov_b64 v[116:117], v[124:125]\n\tv_mov_b64 v[118:119], v[126:127]\n\t"
      POLLASM(LD_DUP2,CHK_R,PSLP)
      KL(0,112) KH(1,112) KL(2,114) KH(3,114) KL(4,116) KH(5,116) KL(6,118) KH(7,118)
      : [k0]"+v"(rc[0]),[k1]"+v"(rc[1]),[k2]"+v"(rc[2]),[k3]"+v"(rc[3]),[k4]"+v"(rc[4]),[k5]"+v"(rc[5]),[k6]"+v"(rc[6]),[k7]"+v"(rc[7]),[cnt]"=&s"(cnt),[t0]"=&v"(t0),[t1]"=&v"(t1) : [of]"v"(ro),[rs]"s"(rs),[so]"s"(4096),[M]"s"(M),[hm]"s"(hm) : PCLOB); }
  if(UE) garrive(sm,C_RS);
  #pragma unroll
  for(int e=0;e<8;e++) k[e]=rc[e];
  u32 h=max3u(k[0],k[1],k[2]), m1=max3u(max3u(k[3],k[4],k[5]),max3u(k[6],k[7],h),h), mx=m1, l=med3u(k[0],k[1],k[2]), t=umin_(umin_(k[0],k[1]),k[2]);
  SCHED();
  #define INS(e) t=med3u(t,l,k[e]); l=med3u(h,l,k[e]); if(e<7) h=umax_(h,k[e]); PIN(t); PIN(l); SCHED();
  mx=umax_(mx,dppu<0xB1>(mx)); INS(3) mx=umax_(mx,dppu<0x4E>(mx)); INS(4) mx=umax_(mx,dppu<0x141>(mx)); INS(5) mx=umax_(mx,dppu<0x140>(mx));
  asm volatile("s_mov_b64 exec, %2\n\tds_write_b32 %0, %1\n\ts_mov_b64 exec, -1\n\tds_read_b32 v116, %3\n\tds_read_b128 v[112:115], %4" :: "v"(aW),"v"(mx),"s"(em),"v"(aC),"v"(aO) : "memory");
  if(XB==2) xreq(X,ws);
  INS(6) INS(7)
  u32 m2=l, m3=t, T;
  { u32 c,rk,cnt;
    asm volatile("s_mov_b32 %[cnt], 0x20000\n1:\n\ts_waitcnt lgkmcnt(0)\n\tv_cmp_eq_u32 vcc, 0, v116\n\ts_cbranch_vccz 2f\n\tds_read_b32 v116, %[aC]\n\tds_read_b128 v[112:115], %[aO]\n\ts_sub_u32 %[cnt], %[cnt], 1\n\ts_cbranch_scc0 1b\n2:\n\t"
      "v_mov_b32 %[c], v116\n\tv_mov_b32 %[rk], 0\n\t" GT4(112,113,114,115) "s_nop 1\n\tv_add_u32_dpp %[rk], %[rk], %[rk] quad_perm:[1,0,3,2] row_mask:0xf bank_mask:0xf\n\ts_nop 1\n\tv_add_u32_dpp %[rk], %[rk], %[rk] quad_perm:[2,3,0,1] row_mask:0xf bank_mask:0xf\n\t"
      "v_cmp_eq_u32 vcc, 7, %[rk]\n\ts_ff1_i32_b64 %[cnt], vcc\n\ts_nop 0\n\tv_readlane_b32 %[T], %[c], %[cnt]\n\ts_nop 1"
      : [c]"=&v"(c),[rk]"=&v"(rk),[cnt]"=&s"(cnt),[T]"=&s"(T),GTS : [aC]"v"(aC),[aO]"v"(aO) : PCLOB); T=RFL(T); }
  { u64 b1=BALLOT(m1>=T), b2=BALLOT(m2>=T), b3=BALLOT(m3>=T); int n1=__builtin_popcountll(b1), n2=n1+__builtin_popcountll(b2);
    lch* cw=sm+O_CND+w4*32;
    if(__builtin_expect(b3==0ull&&n2<=8,1)){
      int p1=(int)__builtin_amdgcn_mbcnt_hi((u32)(b1>>32),__builtin_amdgcn_mbcnt_lo((u32)b1,0u)), pf=n2+ln-p1; bool c1=m1>=T;
      if(__builtin_expect(b2!=0ull,0)){ int p2=(int)__builtin_amdgcn_mbcnt_hi((u32)(b2>>32),__builtin_amdgcn_mbcnt_lo((u32)b2,(u32)n1)); if(m2>=T) ST(u32,cw+p2*4)=m2; }
      int ps= c1? p1:pf; ps= ps<8? ps:32-8*w4; ST(u32,cw+ps*4)= c1? m1:1u;
    } else {
      u32 mine=0;
      _Pragma("unroll 1") for(int c=0;c<8;c++){ u32 lm=umax_(umax_(umax_(k[0],k[1]),umax_(k[2],k[3])),umax_(umax_(k[4],k[5]),umax_(k[6],k[7]))); u32 wm=wmax(lm); if(ln==c) mine=wm;
        #pragma unroll
        for(int e=0;e<8;e++) k[e]=(k[e]==wm)?0u:k[e]; }
      if(ln<8) ST(u32,cw+ln*4)=mine; } }
  { u32 c,rk,cnt;
    asm volatile("s_mov_b32 %[cnt], 0x20000\n\tv_mov_b32 %[rk], 0\n\ts_waitcnt vmcnt(" XBW ")\n1:\n\tds_read_b32 %[c], %[dC]\n\tds_read_b128 v[112:115], %[dO]\n\tds_read_b128 v[116:119], %[dO] offset:16\n\tds_read_b128 v[120:123], %[dO] offset:32\n\tds_read_b128 v[124:127], %[dO] offset:48\n\t"
      "s_waitcnt lgkmcnt(4)\n\tv_cmp_eq_u32 vcc, 0, %[c]\n\ts_cbranch_vccz 2f\n\ts_sub_u32 %[cnt], %[cnt], 1\n\ts_cbranch_scc0 1b\n2:\n\t"
      "s_waitcnt lgkmcnt(3)\n\t" GT4(112,113,114,115) "s_waitcnt lgkmcnt(2)\n\t" GT4(116,117,118,119) "s_waitcnt lgkmcnt(1)\n\t" GT4(120,121,122,123) "s_waitcnt lgkmcnt(0)\n\t" GT4(124,125,126,127)
      "s_nop 1\n\tv_add_u32_dpp %[rk], %[rk], %[rk] quad_perm:[1,0,3,2] row_mask:0xf bank_mask:0xf"
      : [c]"=&v"(c),[rk]"=&v"(rk),[cnt]"=&s"(cnt),GTS : [dC]"v"(dC),[dO]"v"(dO) : PCLOB);
    rank_=rk; key_=c; }
}

#define FILL_(n,WAIT) "s_mov_b32 m0, %[ld]\n\ts_mov_b32 %[c], " STR(n) "-1\n3:\n\t" WAIT "buffer_load_dwordx4 %[vo], %[rs], %[so] offen nt lds\n\ts_add_u32 m0, m0, 0x1000\n\ts_add_u32 %[so], %[so], %[st]\n\t" \
  "s_sub_u32 %[c], %[c], 1\n\ts_cbranch_scc0 3b\n\ts_mov_b32 %[ld], m0\n\t"
#define FILL(n,WAIT) do{ u32 c_; asm volatile(FILL_(n,WAIT) : [c]"=&s"(c_),[so]"+s"(wso),[ld]"+s"(wld) : [vo]"v"(wvo),[rs]"s"(wrs),[st]"s"(wst) : "memory","scc","m0"); }while(0)
#define FILLD(n,WN,WW_) do{ u32 c_,t_,s_; asm volatile(FILL_(n,"ds_read_b32 %[t], %[ad]\n\ts_waitcnt lgkmcnt(0)\n\tv_readfirstlane_b32 %[s], %[t]\n\ts_cmp_ge_u32 %[s], 4\n\ts_cbranch_scc1 4f\n\t" VW(WN) "4:\n\t" VW(WW_)) \
  : [c]"=&s"(c_),[so]"+s"(wso),[ld]"+s"(wld),[t]"=&v"(t_),[s]"=&s"(s_) : [vo]"v"(wvo),[rs]"s"(wrs),[st]"s"(wst),[ad]"v"(C_D*4) : "memory","scc","m0"); wso=RFL(wso); wld=RFL(wld); }while(0)
#define VW(n) "s_waitcnt vmcnt(" STR(n) ")\n\t"

__global__ NOPK __attribute__((amdgpu_num_vgpr(56),amdgpu_num_sgpr(102))) __launch_bounds__(1024,4) void kA(const u16* x_,char* ws_,const u16* wr_,const u16* Dsk_,const u16* wg_,const u16* wu_,const u16* wd_,u16* out_,KP kp){
  const u16 *x,*Dsk,*wg,*wu,*wd; char* ws; u16* out;
  __shared__ __attribute__((aligned(16))) char smem[LDS_TOTAL];
  lch* sm=(lch*)smem;
  u32x4 xa,xb_,wa,wb,dq[3]; u32 xc,wc;
  #define SARR "v_mov_b32 v121, 80\n\tv_mov_b32 v122, 1\n\ts_mov_b64 exec, 1\n\tds_add_u32 v121, v122\n\ts_mov_b64 exec, -1\n\t"
  asm volatile(
    KD0 "v_readfirstlane_b32 s60, %[t]\n\tv_lshlrev_b32 v119, 1, %[t]\n\tv_mov_b32 v118, 0\n\ts_cmpk_ge_u32 s60, 0x100\n\ts_cbranch_scc1 5f\n\tds_write_b16 v119, v118\n\ts_waitcnt lgkmcnt(0)\n5:\n\t"
    "s_load_dwordx16 s[64:79], %[ka], 0x0\n\ts_load_dwordx16 s[80:95], %[ka], 0x40\n\t"
    "s_and_b32 s61, %[wg], 7\n\ts_lshr_b32 s62, %[wg], 3\n\tv_and_b32 v118, 31, %[t]\n\ts_bfe_u32 s63, s60, 0x20006\n\ts_cmpk_ge_u32 s60, 0x100\n\ts_cbranch_scc1 1f\n\t"
    "s_barrier\n\ts_setprio 3\n\ts_lshl_b32 s56, s61, 5\n\ts_add_u32 s56, s56, s62\n\ts_mulk_i32 s56, 0x2400\n\tv_lshrrev_b32 v119, 5, %[t]\n\tv_mul_u32_u24 v119, 0x480, v119\n\tv_add_u32 v119, s56, v119\n\t"
    "v_lshl_add_u32 v116, v118, 4, v119\n\tv_lshl_add_u32 v117, v118, 2, v119\n\tv_lshlrev_b32 v114, 4, v118\n\tv_lshlrev_b32 v115, 2, v118\n\t"
    "s_and_b32 s57, s62, 3\n\ts_lshl2_add_u32 s57, s57, s63\n\ts_lshl_b32 s57, s57, 6\n\ts_and_b32 s58, s62, 1\n\ts_lshl_b32 s58, s58, 6\n\tv_and_b32 v112, 3, %[t]\n\tv_lshl_add_u32 v113, v112, 4, s57\n\tv_lshl_add_u32 v112, v112, 4, s58\n\t"
    "s_waitcnt lgkmcnt(0)\n\t" KD1
    "s_memrealtime s[98:99]\n\ts_load_dwordx2 s[96:97], s[66:67], 0x0\n\t"
    "global_load_dwordx4 %[wa], v114, s[68:69]\n\tglobal_load_dwordx4 %[wb], v114, s[68:69] offset:512\n\tglobal_load_dword %[wc], v115, s[68:69] offset:1024\n\t"
    "global_load_dwordx4 %[xa], v116, s[64:65]\n\tglobal_load_dwordx4 %[xb], v116, s[64:65] offset:512\n\tglobal_load_dword %[xc], v117, s[64:65] offset:1024\n\t"
    "global_load_dwordx4 %[d0], v113, s[70:71]\n\tglobal_load_dwordx4 %[d1], v112, s[70:71] offset:1024\n\t" SARR "s_setprio " STR(LP0) "\n\t"
    "s_branch 8f\n"
    "1:\n\t" PP0 "s_cmp_ge_u32 s62, " STR(CPF) "\n\ts_cbranch_scc1 6f\n\ts_getpc_b64 s[56:57]\n.Lkpc:\n\ts_and_b32 s58, s62, 3\n\ts_lshl_b32 s58, s58, 13\n\ts_sub_u32 s58, s58, 0x1000\n\tv_lshl_add_u32 v119, %[t], 4, s58\n\tv_min_u32 v119, .Lkend-.Lkpc-16, v119\n\t"
    "global_load_dword v120, v119, s[56:57]\n\t.subsection 1\n.Lkend:\n\t.subsection 0\n6:\n\ts_barrier\n\t"
    "v_mov_b32 v124, -1\n\tv_mov_b32 v125, -1\n\tv_mov_b32 v126, -1\n\tv_mov_b32 v127, -1\n\tv_lshlrev_b32 v119, 4, %[t]\n\tv_and_b32 v119, 0xff0, v119\n\t"
    "s_lshr_b32 s59, s60, 8\n\ts_cmp_lt_u32 s62, 8\n\ts_cselect_b32 s58, 2, 1\n\ts_cbranch_scc1 2f\n\ts_cmp_eq_u32 s59, 3\n\ts_cbranch_scc1 4f\n"
    "2:\n\ts_sub_u32 s57, s59, 2\n\ts_cmp_eq_u32 s59, 1\n\ts_cselect_b32 s58, s58, s57\n\ts_cmp_lt_u32 s62, 8\n\ts_cselect_b32 s56, 0, 8\n\ts_lshl1_add_u32 s56, s62, s56\n\ts_mulk_i32 s61, 72\n\ts_add_u32 s51, s56, s61\n\ts_sub_u32 s56, 2, s59\n\ts_sub_u32 s57, 16, s62\n\ts_cmp_eq_u32 s59, 3\n\ts_cselect_b32 s50, s57, s56\n\ts_add_u32 s56, s50, s51\n\t"
    "v_mov_b32 v112, 0\n\tv_mov_b32 v113, 0\n\tv_mov_b32 v114, 0\n\tv_mov_b32 v115, 0\n\tv_mov_b32 v116, 0\n\tv_mov_b32 v117, 0\n\ts_setprio 2\n\t"
    "s_lshl_b32 s57, s56, 2\n\ts_add_u32 s57, s57, s63\n\ts_lshl_b32 s57, s57, 6\n\tv_lshl_add_u32 v22, v118, 1, s57\n\ts_mul_i32 s57, s56, %[yst]\n\ts_add_u32 s57, s57, %[wy]\n\tv_add_u32 v23, s57, v119\n\ts_lshl_b32 s56, s56, 1\n\ts_and_b32 s56, s56, -4\n\t"
    "s_waitcnt lgkmcnt(0)\n\t" KD1
    "global_load_short_d16_hi v112, v22, s[80:81] nt\n\tglobal_load_short_d16_hi v113, v22, s[82:83] nt\n\tglobal_load_short_d16_hi v114, v22, s[84:85] nt\n\t"
    "global_load_short_d16_hi v115, v22, s[86:87] nt\n\tglobal_load_short_d16_hi v116, v22, s[88:89] nt\n\tglobal_load_short_d16_hi v117, v22, s[90:91] nt\n\t"
    "s_load_dwordx2 s[96:97], s[66:67], 0x0\n\ts_load_dword s98, s[92:93], s56\n\t"
    SARR INV0
    "s_mul_i32 s52, s58, %[sl]\n\ts_add_u32 s52, s52, %[tb]\n\ts_lshl_b32 s53, s63, 7\n\ts_add_u32 s53, s53, s52\n\tv_lshl_add_u32 v30, v118, 2, s53\n\tv_bfe_u32 v31, %[t], 5, 1\n\ts_movk_i32 s54, 528\n\tv_mul_u32_u24 v29, s54, v31\n\t"
    "v_lshl_add_u32 v2, v29, 3, v30\n\ts_movk_i32 s53, 1056\n\tv_add_u32 v3, s53, v2\n\tv_add_u32 v4, s53, v3\n\tv_add_u32 v5, s53, v4\n\tv_add_u32 v6, 41*528, v2\n\tv_add_u32 v7, s53, v6\n\tv_add_u32 v8, s53, v7\n\tv_add_u32 v9, s53, v8\n\t"
    "v_lshl_add_u32 v10, v29, 2, v30\n\tv_add_u32 v10, 16*528, v10\n\tv_add_u32 v11, s53, v10\n\tv_lshl_add_u32 v12, v29, 1, v30\n\tv_add_u32 v12, 24*528, v12\n\tv_add_u32 v13, 2112, v12\n\tv_add_u32 v14, v29, v30\n\tv_add_u32 v14, 32*528, v14\n\t"
    "s_lshl_b32 s53, s58, 4\n\ts_add_u32 s53, s53, %[ex]\n\tv_mov_b32 v15, s53\n\tv_cvt_f32_ubyte0 v31, v31\n\tv_mul_f32 v16, 0x42800000, v31\n\tv_add_f32 v17, v16, v16\n\tv_mov_b32 v20, 1\n\t"
    "v_and_b32 v29, 15, %[t]\n\tv_and_b32 v30, 48, %[t]\n\tv_mad_u32_u24 v30, v29, s54, v30\n\ts_lshl_b32 s55, s59, 1\n\ts_lshr_b32 s55, 0x48, s55\n\ts_add_u32 s55, s55, s63\n\ts_and_b32 s55, s55, 3\n\ts_mul_i32 s53, s55, 16*528\n\ts_movk_i32 s57, 41*528\n\ts_cmp_eq_u32 s55, 2\n\ts_cselect_b32 s53, s57, s53\n\ts_cselect_b32 s57, 32*528, s57\n\t"
    "s_add_u32 s53, s53, s52\n\ts_add_u32 s57, s57, s52\n\tv_add_u32 v18, s53, v30\n\tv_add_u32 v19, s57, v30\n\t"
    "v_min_u32 v29, 237*16, v119\n\tv_subrev_u32 v30, 204*16, v29\n\tv_add_u32 v29, %[de], v29\n\tv_min_u32 v29, v29, v30\n\ts_sub_u32 s53, s52, 4640\n\tv_add_u32 v29, s53, v29\n\tv_mov_b32 v24, 0\n\tv_mov_b32 v25, 0\n\tv_mov_b32 v26, 0\n\tv_mov_b32 v27, 0\n\tds_write_b128 v29, v[24:27]\n\t"
    "s_cmp_lg_u32 s55, 3\n\ts_cbranch_scc1 10f\n\t"
    "v_bfe_u32 v18, %[t], 1, 5\n\tv_and_b32 v19, 1, %[t]\n\ts_cmp_lt_u32 s62, 8\n\ts_cselect_b32 s53, 2, 1\n\ts_mul_i32 s53, s53, %[sl]\n\tv_mul_u32_u24 v21, s53, v19\n\ts_sub_u32 s55, 2, s59\n\ts_lshl_b32 s55, s55, 7\n\tv_add_u32 v18, s55, v18\n\ts_cmp_lg_u32 s59, 3\n\ts_cbranch_scc1 7f\n\t"
    "v_bfe_u32 v18, %[t], 5, 1\n\tv_lshl_add_u32 v18, v18, 7, v118\n\tv_mov_b32 v19, s50\n\tv_mov_b32 v21, %[sl]\n"
    "7:\n\tv_add_u32 v19, s51, v19\n\tv_lshl_add_u32 v21, v18, 4, v21\n\tv_lshrrev_b32 v24, 3-" STR(TL) ", v19\n\tv_and_b32 v19, 7>>" STR(TL) ", v19\n\tv_lshlrev_b32 v19, 4, v19\n\tv_lshl_add_u32 v19, v24, 7, v19\n\t"
    "v_and_b32 v24, (1<<" STR(TL) ")-1, v18\n\tv_lshl_add_u32 v19, v24, 7-" STR(TL) ", v19\n\tv_lshrrev_b32 v18, " STR(TL) ", v18\n\tv_mul_u32_u24 v18, %[ust], v18\n\tv_add_u32 v18, v18, v19\n\tv_add_u32 v19, 512, v21\n10:\n\t"
    INV1 "s_branch 9f\n"
    "4:\n\t"
    "v_bfe_u32 v118, %[t], 3, 5\n\tv_lshrrev_b32 v117, 1, v118\n\tv_and_b32 v116, 6, v117\n\tv_and_b32 v115, 7, %[t]\n\tv_xor_b32 v115, v115, v116\n\tv_lshlrev_b32 v115, 4, v115\n\t"
    "s_mul_i32 s54, s61, 9\n\ts_sub_u32 s55, s62, 14\n\ts_lshr_b32 s55, s55, 1\n\ts_add_u32 s54, s54, s55\n\ts_lshl_b32 s54, s54, 7\n\t"
    "s_mul_i32 s52, s61, 0xa2000\n\ts_mul_i32 s53, s62, 0x180\n\ts_add_u32 s52, s52, s53\n\ts_cmp_ge_u32 s62, 11\n\ts_cselect_b32 s53, 0x51000-0x480, 0\n\ts_add_u32 s52, s52, s53\n\ts_sub_u32 s52, s52, 0xc00\n\t"
    "s_movk_i32 s55, 0x480\n\ts_cmp_ge_u32 s62, 14\n\ts_cselect_b32 s54, s54, s52\n\ts_cselect_b32 s55, 0x2400, s55\n\tv_mad_u32_u24 v118, v118, s55, v115\n\ts_lshl_b32 s55, s55, 5\n\t"
    "s_lshl_b32 s52, s63, 10\n\ts_add_u32 m0, s52, %[r2]\n\ts_mov_b32 s58, 0x510000\n\ts_mov_b32 s59, 0x20000\n\ts_cmp_ge_u32 s62, 14\n\ts_cselect_b32 s53, " STR(PRE1) "-1, " STR(PRE2) "-1\n\ts_setprio 3\n\t"
    "s_waitcnt lgkmcnt(0)\n\t" KD1 "s_memrealtime s[98:99]\n\t"
    "s_bitcmp1_b32 %[wg], 3\n\ts_cselect_b64 s[56:57], s[74:75], s[72:73]\n\ts_cmp_ge_u32 s62, 14\n\ts_cselect_b64 s[56:57], s[56:57], s[76:77]\n\ts_cselect_b32 s52, 0x180, -1\n\ts_and_b32 s57, s57, 0xffff\n\ts_add_u32 s60, s56, s54\n\ts_and_b32 s60, s60, 0x380\n\ts_cmp_eq_u32 s60, s52\n\ts_cselect_b32 s53, " STR(PRE3) "-1, s53\n\t"
    "s_waitcnt vmcnt(0)\n\tv_mov_b32 v121, 80\n\ts_movk_i32 s52, 0x400\n12:\n\tds_read_b32 v122, v121\n\ts_waitcnt lgkmcnt(0)\n\tv_readfirstlane_b32 s60, v122\n\ts_cmp_ge_u32 s60, " STR(SN) "\n\ts_cbranch_scc1 13f\n\ts_sleep 1\n\ts_sub_u32 s52, s52, 1\n\ts_cbranch_scc0 12b\n13:\n\t"
    "s_load_dwordx2 s[96:97], s[66:67], 0x0\n\ts_sleep " STR(PD) "\n\t"
    "\n3:\n\t" "buffer_load_dwordx4 v118, s[56:59], s54 offen nt lds\n\ts_add_u32 m0, m0, 0x1000\n\ts_add_u32 s54, s54, s55\n\ts_sub_u32 s53, s53, 1\n\ts_cbranch_scc0 3b\n"
    "9:\n8:"
    : [xa]"=&{v[2:5]}"(xa),[xb]"=&{v[6:9]}"(xb_),[xc]"=&{v30}"(xc),[wa]"=&{v[10:13]}"(wa),[wb]"=&{v[14:17]}"(wb),[wc]"=&{v31}"(wc),[d0]"=&{v[18:21]}"(dq[0]),[d1]"=&{v[22:25]}"(dq[1]),[d2]"=&{v[26:29]}"(dq[2]),
      "=&{s[64:65]}"(x),"=&{s[66:67]}"(ws),"=&{s[70:71]}"(Dsk),"=&{s[72:73]}"(wg),"=&{s[74:75]}"(wu),"=&{s[76:77]}"(wd),"=&{s[78:79]}"(out)
    : [ka]"s"(__builtin_amdgcn_kernarg_segment_ptr()),[wg]"s"(BID),[t]"v"(TID),[yst]"i"(YST),[wy]"i"(W_Y),[r2]"i"(O_R2),[sl]"i"(SLSZ),[tb]"i"(O_CH+O_TB),[ex]"i"(O_EXP),[de]"i"(O_DE),[ust]"i"(GST_),[pa]"s"(O_PAR-1536)
    : "memory","scc","m0","v127",KDC "s50","s51","s52","s53","s54","s55","s56","s57","s58","s59","s60","s61","s62","s63","s68","s69",
      "s80","s81","s82","s83","s84","s85","s86","s87","s88","s89","s90","s91","s92","s93","s94","s95","s99");
  int wv=RFL(TID>>6), bid=BID, q4=wv>>2, w4=wv&3;
  int xg=bid&7, lb=bid>>3, tb=32*xg+lb, nch=2+(lb<8);
  u64 ep; u32 ldw;
  {
  }
  #define KILLIN "=v"(xa),"=v"(xb_),"=v"(xc),"=v"(wa),"=v"(wb),"=v"(wc),"=v"(dq[0]),"=v"(dq[1]),"=v"(dq[2])
  if(__builtin_expect(q4==3&&lb>=8,0)){
    int fu=18*xg+lb-14, kq=2*xg+(lb>=11), cgp=lb-8-3*(lb>=11);
  u32 wst= lb>=14? F_*64:D_*64;
  u32 wso=(lb>=14? (fu>>1)*128 : kq*(288*D_*2)+384*cgp)+(lb>=14? PRE1:PRE2)*wst, wld=(u32)(size_t)sm+O_R2+w4*1024+(lb>=14? PRE1:PRE2)*4096; u32x4 wrs=mkrs(lb>=14? ((fu&1)? wu:wg):wd,D_*F_*2);
  GETEP
  #define FLANE(RS) LANE(ln); int g=ln>>4, li=ln&15, qq=li>>2, pq=li&3; u32 wvo=(u32)(8*w4+(ln>>3))*(RS)+(u32)((ln&7)^(((ln>>4)&2)|((w4&1)<<2)))*16u; \
    const lch* tp=sm+O_R2+(16*(g>>1)+8*(qq>>1)+4*(g&1)+(qq&1))*128+((w4^((g&1)|(qq&2)))*32)+pq*8;
  #define WFR(img,ks) BC(u32x4,trfrag(tp+(img)*CHSZ+(ks)*4096,tp+(img)*CHSZ+(ks)*4096+256))
  #define WF2(img,s) __builtin_shufflevector(WFR(img,2*(s)),WFR(img,2*(s)+1),0,1,2,3,4,5,6,7)
  f32x4 ac[3]={{0,0,0,0},{0,0,0,0},{0,0,0,0}};
  #define AC0 "+{v[100:103]}"(ac[0])
  #define AC1 "+{v[104:107]}"(ac[1])
  #define AC2 "+{v[108:111]}"(ac[2])
  #define SMF(i,a,b) asm volatile("v_smfmac_f32_16x16x64_bf16 %0, %1, %2, %3" : AC##i : "v"(a),"v"(b),"v"(ix) : "memory"); SCHED();
  #define SMR(i,r,b) asm volatile("v_smfmac_f32_16x16x64_bf16 %0, v[" #r ":" #r "+3], %1, %2" : AC##i : "v"(b),"v"(ix) : "memory"); SCHED();
  #define GW4(c,p) { u32 tv,cnt,sv; asm volatile("s_mov_b32 %1, 0x20000\n1:\n\tds_read_b32 %0, %3\n\tds_read_b128 v[112:115], %4\n\tds_read_b128 v[116:119], %4 offset:128\n\tds_read_b128 v[120:123], %4 offset:256\n\tds_read_b128 v[124:127], %4 offset:384\n\ts_waitcnt lgkmcnt(4)\n\t" \
    "v_readfirstlane_b32 %2, %0\n\ts_cmp_ge_u32 %2, 4\n\ts_cbranch_scc1 2f\n\ts_sleep 1\n\ts_sub_u32 %1, %1, 1\n\ts_cbranch_scc0 1b\n2:" : "=&v"(tv),"=&s"(cnt),"=&s"(sv) : "v"((c)*4),"v"(p) : "memory","scc"); }
  #define LGW(n) asm volatile("s_waitcnt lgkmcnt(" #n ")" ::: "memory");
  u64 tag=ep+1ull; const u32x4 I4={INV,INV,INV,INV};
  if(lb>=14){
    FLANE(F_*2)
    int mt=fu&1;
    const lch* xp=sm+O_TILE+(li&7)*XSTR+g*32+(li>>3)*16; int of=(mt*288+(fu>>1)*4+w4)*256+ln*4; u32 ix= (li&8)? 0xeeeeu:0x4444u;
    const lch* mp=sm+O_RC+(g&1)*16+(g>>1)*8;
    asm volatile("" : "+v"(xp),"+v"(of),"+v"(mp),"+v"(ix));
    rsrc_t rsG=RSRC(ws+W_GU,2*8*F_*2);
    if(w4==0) BST128(I4,rsG,(mt*288+(fu>>1)*4)*256+ln*16,0,SC1);
    bool slow=(((u32)wrs[0]+wso)&0x380u)==0x180u;
    if(slow){ wso-=(PRE1-PRE3)*wst; wld-=(PRE1-PRE3)*4096; FILL(S3-PRE3,VW(W3)); } else FILLD(5,W1,W1B);
    if(w4==0){ if(slow) VMW(S3-PRE3<5? S3-PRE3:5); else VMW(5); if(ln==0) GST(ws+W_FL+(576+fu)*8,tag); }
    if(slow){ wso=RFL(wso); wld=RFL(wld); FILL(18-S3,VW(W3B)); } else FILLD(13-PRE1,W1,W1B);
    u32x8 fB[9]; u32x4 one4={0x3f803f80u,0x3f803f80u,0x3f803f80u,0x3f803f80u}; const lch* tq=tp+65536; asm volatile("" : AC0,AC1,"+v"(one4),"+v"(tq));
    VMW(4); gsync(sm,C_P,4);
    #pragma unroll
    for(int s=0;s<7;s++) fB[s]=WF2(0,s);
    #pragma unroll
    for(int s=0;s<7;s++){ SMF(1,one4,fB[s]) }
    DRAIN(); gsync(sm,C_P,8);
    fB[7]=WF2(0,7); { const lch* tp=tq; fB[8]=WF2(0,0); } SMF(1,one4,fB[7]) SMF(1,one4,fB[8])
    { u32x4 xb[5]; u32x2 md;
      GW4(C_X,xp)
      md=LD(u32x2,mp);
      #pragma unroll
      for(int s=0;s<5;s++) xb[s]=LD(u32x4,xp+512+s*128);
      SCHED(); LGW(6)
      SMR(0,112,fB[0])
      float S; asm volatile("v_permlane32_swap_b32 v104, v105\n\tv_add_f32 %0, v104, v105" : "=v"(S),AC1); SCHED();
      SMR(0,116,fB[1]) SMR(0,120,fB[2])
      float ra=rcpf_(bhi(md[0])), rb_=rcpf_(bhi(md[1])); PIN(ra); PIN(rb_); SCHED();
      SMR(0,124,fB[3])
      float na=-blo(md[0])*S, nb=-blo(md[1])*S; PIN(na); PIN(nb); SCHED();
      SMF(0,xb[0],fB[4])
      float ea=ra*-1.4426950408889634f, eb=rb_*-1.4426950408889634f; PIN(ea); PIN(eb); SCHED();
      SMF(0,xb[1],fB[5]) SMF(0,xb[2],fB[6]) SMF(0,xb[3],fB[7]) SMF(0,xb[4],fB[8])
      float v0,v1,s0,s1;
      asm volatile("s_nop 7\n\tv_permlane32_swap_b32 v100, v102\n\tv_permlane32_swap_b32 v101, v103\n\tv_add_f32 v100, %5, v100\n\tv_add_f32 v101, %6, v101\n\tv_add_f32 v100, v100, v102\n\tv_add_f32 v101, v101, v103\n\t"
        "v_mul_f32 %2, %9, v100\n\tv_mul_f32 %3, %10, v101\n\tv_mul_f32 %0, %7, v100\n\tv_mul_f32 %1, %8, v101" : "=&v"(v0),"=&v"(v1),"=&v"(s0),"=&v"(s1),AC0 : "v"(na),"v"(nb),"v"(ra),"v"(rb_),"v"(ea),"v"(eb));
      if(mt==0){ v0*=rcpf_(1.f+__builtin_amdgcn_exp2f(s0)); v1*=rcpf_(1.f+__builtin_amdgcn_exp2f(s1)); }
      __builtin_amdgcn_raw_buffer_store_b32(nv(pk(v0,v1)),rsG,of,0,SC1); garrive(sm,C_H); }
    if(bid==112&&w4==1){
      _Pragma("unroll 1") for(int sp=0;sp<SPINCAP;sp++){ u64 bad=0, f[12];
        #pragma unroll
        for(int i=0;i<12;i++) f[i]=GLD(ws+W_FL+(i*64+ln)*8);
        #pragma unroll
        for(int i=0;i<12;i++) bad|=f[i]^tag;
        if(BALLOT(bad!=0ull)==0ull) break; __builtin_amdgcn_s_sleep(8); }
      if(ln==0) GST(ws+W_EPOCH,tag); }
    if(mt==0&&w4==0){
    LANE(l2); ln=l2; li=ln&15; g=ln>>4; gwait<1>(sm,C_IX,1);
    int c9=(lb-14)>>1, t=RFL((int)LD(u16,sm+O_IDX+xg*2)), c=64*c9+4*li+g; f32x4 pr;
    { u32 u=LD(u16,sm+O_IDX+16+xg*2)^0x8000u, xb=LD(u16,sm+O_TILE+xg*XSTR+c*2), sv=LD(u32,sm+O_RC+xg*4); float ds=b2f(((const GAS u16*)Dsk)[c]); u^=(u32)(((int)(u<<16))>>31)&0x7fffu;
      poll1<0>(ws+W_FL+(64*c9+ln)*8,tag); poll1<0>(ws+W_FL+(720+(ln<48? ln:47))*8,tag);
      float xn=(b2f(xb)-blo(sv))*rcpf_(bhi(sv)); pr=f32x4{b2f(xb),xn,xn*ds,sigm_f(b2f(u))}; }
    { gwait<2>(sm,C_E,4); u32 ti=now_(), mx=0;
      #pragma unroll
      for(int k=0;k<3;k++){ int i=ln+64*k; i= i<144? i:143; int q=(i*3641)>>16; u64 h=GLD(ws+W_HT+(72*q+36+2*(i-18*q))*8); mx=umax_(mx,(u32)(h>>32)==(u32)tag? (u32)h-ldw : ti-ldw+HM); }
      u32 tr=umin_(wmax(mx),ti-ldw+160u); u32 hk=ti-ldw+HKR; if(tr>hk) tr-=(tr-hk)>>1; tgate2(ldw,tr+HR); }
    { u32x4 rs=mkrs(ws+W_PART+(xg*9+c9)*4096,4096), ry=mkrs(ws+W_Y,D_*YST);
      int yo=c*YST+(t&~1)*2, po=ln*16; u32 a[4], t0,cnt,yb; u16* op=out+t*D_+c;
      #define LD_P "buffer_load_dwordx4 v[112:115], %[of], %[rs], 0 offen sc1\n\tbuffer_load_dwordx4 v[116:119], %[of], %[rs], 0 offen offset:1024 sc1\n\tbuffer_load_dwordx4 v[120:123], %[of], %[rs], 0 offen offset:2048 sc1\n\tbuffer_load_dwordx4 v[124:127], %[of], %[rs], 0 offen offset:3072 sc1\n\t"
      #define LD_YB "buffer_load_dword %[yb], %[yo], %[ry], 0 offen sc1\n\t"
      #define AD4(o,a,b,c,d) "v_add_f32 %[t0], v" #a ", v" #b "\n\tv_add_f32 %[" #o "], v" #c ", v" #d "\n\tv_add_f32 %[" #o "], %[t0], %[" #o "]\n\t"
      u64 ym;
      asm volatile(LD_YB LD_P "s_mov_b32 %[cnt], 0x20000\n1:\n\ts_waitcnt vmcnt(0)\n\tv_cmp_eq_u32 %[ym], -1, %[yb]\n\t" CHK_I0 MX2(116,117) MX2(118,119) MX2(120,121) MX2(122,123) MX2(124,125) MX2(126,127) CHK_IE
        "s_or_b64 exec, vcc, %[ym]\n\ts_cbranch_scc0 9f\n\ts_sleep " STR(QSLP) "\n\ts_mov_b64 exec, %[ym]\n\ts_cbranch_execz 2f\n\t" LD_YB "2:\n\ts_mov_b64 exec, vcc\n\ts_cbranch_execz 3f\n\t" LD_P
        "3:\n\ts_mov_b64 exec, -1\n\ts_sub_u32 %[cnt], %[cnt], 1\n\ts_cbranch_scc0 1b\n9:\n\ts_mov_b64 exec, -1\n\t"
        AD4(a0,112,116,120,124) AD4(a1,113,117,121,125) AD4(a2,114,118,122,126) AD4(a3,115,119,123,127) "s_nop 1"
        : [t0]"=&v"(t0),[cnt]"=&s"(cnt),[a0]"=&v"(a[0]),[a1]"=&v"(a[1]),[a2]"=&v"(a[2]),[a3]"=&v"(a[3]),[yb]"=&v"(yb),[ym]"=&s"(ym) : [of]"v"(po),[rs]"s"(rs),[yo]"v"(yo),[ry]"s"(ry),"v"(op),"v"(pr) : PCLOB);
      auto s0=__builtin_amdgcn_permlane32_swap(a[0],a[2],false,false); auto s1=__builtin_amdgcn_permlane32_swap(a[1],a[3],false,false);
      u32 x0=s0[0],x1=s0[1],y0=s1[0],y1=s1[1]; u32 p0=BC(u32,BC(float,x0)+BC(float,x1)), p1=BC(u32,BC(float,y0)+BC(float,y1));
      auto s2=__builtin_amdgcn_permlane16_swap(p0,p1,false,false); u32 z0=s2[0], z1=s2[1]; float s=BC(float,z0)+BC(float,z1);
      float gq=silu_f(__builtin_fmaf(s,pr[3],pr[1]));
      float sy=((t&1)? bhi(yb):blo(yb))+pr[2];
      float o=__builtin_fmaf(sy,gq,pr[0]);
      asm volatile("global_store_short %0, %1, off sc1" :: "v"(op),"v"(pk(o,o)) : "memory");
      }
    }
  } else
  {
    FLANE(D_*2)
    u32x8 fr[12]; u32x4 fl[3];
    rsrc_t rsP=RSRC(ws+W_PART+cgp*12288+kq*256+w4*64,8*9*4096-2*12288);
    #pragma unroll
    for(int i=0;i<3;i++) BST128(I4,rsP,li<8? li*(9*4096)+g*16+i*4096 : OOR,0,SC1);
    if(PRE2<9) FILLD(9-PRE2,W2_,W2B);
    wso+=128u-9u*wst;
    FILLD(9,W2_,W2B);
    VMW(9); gsync(sm,C_P,4);
    #pragma unroll
    for(int s=0;s<4;s++) fr[s]=WF2(0,s);
    fl[0]=WFR(0,8);
    gsync(sm,C_P,8); if(w4==0&&ln==0) GST(ws+W_FL+(720+6*xg+lb-8)*8,tag);
    wso+=128u-9u*wst; wld-=2*CHSZ;
    int fo=W_FL+(576+((9*kq)&~1)+(ln<9? ln:9))*8;
    FILL(9,VW(W2C));
    if(w4==0) asm volatile("global_load_dwordx2 v[112:113], %0, %1 sc1\n\ts_waitcnt vmcnt(10)" :: "v"(fo),"s"(ws) : "memory"); else VMW(9);
    gsync(sm,C_P,12);
    #pragma unroll
    for(int s=0;s<4;s++) fr[4+s]=WF2(1,s);
    fl[1]=WFR(1,8);
    { int t2=TID; asm volatile("" : "+v"(t2)); ln=t2&63; li=ln&15; g=ln>>4; }
    const lch* hp=sm+O_TILE+(li&7)*576+g*32+(li>>3)*16; int po=(4*(g&1)+2*(g>>1))*(9*4096)+li*4; u32 ix= (li&8)? 0xeeeeu:0x4444u; PIN(hp); PIN(po); PIN(ix);
    const lch* hq=hp+512-(g>>1)*32-(g&1)*24; PIN(hq);
    u32x4 rs=mkrs(ws+W_GU,2*8*F_*2);
    int of=((ln>>5)*288+18*kq+2*w4)*256+(ln&31)*16; lch* hd=sm+O_TILE+((ln&4)+((ln>>2)&2))*576+w4*64+(ln&16)*2+(ln&3)*8+(ln>>5)*4; PIN(of); PIN(hd); asm volatile("" : "+s"(rs));
    u32x4 rp=mkrs(ws+W_PART+cgp*12288+kq*256+w4*64,8*9*4096-2*12288); int po1=po+9*4096; u32 nvm=0xfffeffffu; asm volatile("" : AC0,AC1,AC2,"+v"(po1),"+s"(rp),"+s"(nvm));
    DRAIN();
    if(w4==0){ u64 bad; asm volatile("v_cmp_ne_u64 %0, %1, v[112:113]" : "=s"(bad) : "s"(tag) : "memory");
      if(bad!=0ull) poll1<0>(ws+fo,tag); }
    gsync(sm,C_P,16);
    { u32 t0,cnt;
      #define LD_G "buffer_load_dwordx4 v[112:115], %[of], %[rs], 0 offen sc1\n\tbuffer_load_dwordx4 v[116:119], %[of], %[rs], 0 offen offset:2048 sc1\n\tbuffer_load_dwordx4 v[120:123], %[of], %[rs], %[so] offen sc1\n\t"
      #define HM1(t,a,b) "v_lshlrev_b32 " t ", 16, v" #a "\n\tv_lshlrev_b32 %[t1], 16, v" #b "\n\tv_and_b32 v" #a ", %[hm], v" #a "\n\tv_and_b32 v" #b ", %[hm], v" #b "\n\tv_mul_f32 " t ", " t ", %[t1]\n\tv_mul_f32 v" #a ", v" #a ", v" #b "\n\t"
      #define HMUL(a,b,c,d,o) "v_permlane32_swap_b32 v" #a ", v" #c "\n\tv_permlane32_swap_b32 v" #b ", v" #d "\n\t" HM1("%[t0]",a,c) HM1("v" #c,b,d) "v_cvt_pk_bf16_f32 %[t0], %[t0], v" #c "\n\tv_cvt_pk_bf16_f32 v" #a ", v" #a ", v" #b "\n\tds_write2_b32 %[hd], %[t0], v" #a " offset0:" #o " offset1:" #o "+144\n\t"
      u32 t1;
      asm volatile(LD_G :: [of]"v"(of),[rs]"s"(rs),[so]"s"(w4==0? 4096:0) : "memory");
      fr[8]=WF2(0,0); PIN(fr[8]); fl[2]=WFR(0,8); PIN(fl[2]);
      asm volatile(POLLASM(LD_G,CHK_I0 MX2(116,117) MX2(118,119) MX2(120,121) MX2(122,123) CHK_IE,GSLP)
        HMUL(112,113,114,115,0) HMUL(116,117,118,119,64) "s_cmp_eq_u32 %[so], 0\n\ts_cbranch_scc1 8f\n\tv_add_u32 %[hd], 512, %[hd]\n\t" HMUL(120,121,122,123,0) "8:\n\ts_mov_b64 exec, 1\n\tds_add_u32 %[ch], %[one]\n\ts_mov_b64 exec, -1"
        : [t0]"=&v"(t0),[t1]"=&v"(t1),[cnt]"=&s"(cnt),[hd]"+v"(hd) : [of]"v"(of),[rs]"s"(rs),[so]"s"(w4==0? 4096:0),[hm]"s"(0xffff0000u),[ch]"v"(C_H*4),[one]"v"(1),"v"(hp),"v"(po) : PCLOB);
      }
    {
      GW4(C_H,hp)
      u32x2 h8=LD(u32x2,hq); SCHED(); LGW(1)
      #define SM3(i,b) asm volatile("v_smfmac_f32_16x16x32_bf16 %0, %1, %2, %3" : AC##i : "v"(h8),"v"(b),"v"(ix) : "memory"); SCHED();
      #define PST(a,b,c,d,o) "v_permlane32_swap_b32 v" #a ", v" #c "\n\tv_permlane32_swap_b32 v" #b ", v" #d "\n\tv_add_f32 v" #a ", v" #a ", v" #c "\n\tv_add_f32 v" #b ", v" #b ", v" #d "\n\tv_min_u32 v" #a ", %[nv], v" #a "\n\tv_min_u32 v" #b ", %[nv], v" #b "\n\t" \
        "buffer_store_dword v" #a ", %[p0], %[rp], " o " offen sc1\n\tbuffer_store_dword v" #b ", %[p1], %[rp], " o " offen sc1\n\t"
      SMR(0,112,fr[0]) fr[9]=WF2(0,1); SCHED(); SMR(1,112,fr[4]) fr[10]=WF2(0,2); SCHED(); SMR(2,112,fr[8]) fr[11]=WF2(0,3); SCHED();
      SMR(0,116,fr[1]) SMR(1,116,fr[5]) SMR(0,120,fr[2]) SM3(2,fl[2]) SMR(0,124,fr[3]) SMR(1,120,fr[6]) SM3(0,fl[0]) SMR(1,124,fr[7]) SMR(2,116,fr[9]) SM3(1,fl[1]) SMR(2,120,fr[10]) SMR(2,124,fr[11])
      asm volatile("s_nop 2\n\t" PST(100,101,102,103,"0") PST(104,105,106,107,"%[o1]") PST(108,109,110,111,"%[o2]") : AC0,AC1,AC2 : [p0]"v"(po),[p1]"v"(po1),[rp]"s"(rp),[nv]"s"(nvm),[o1]"s"(4096),[o2]"s"(8192) : "memory"); }

  }
  asm volatile("s_endpgm" : KILLIN :: "memory"); }
  {
  if(__builtin_expect(q4!=0,0)){ int grp= q4==1? nch-1 : q4-2;
    chan(sm,grp,(w4+(0x48>>(2*q4)))&3,72*xg+(q4==3? 16+lb : 2*lb+(lb<8? 0:8)+2-q4),(q4+1)>>2,ws,xa,xb_,wa,wb,dq[0]);
    asm volatile("s_endpgm" : KILLIN :: "memory"); }
  u32x4 xq[3]; f32x2 rc[3]; int xr_,xr2_;
  {
    LANE(ln); int g=ln>>4, li=ln&15, qq=li>>2, pq=li&3, j=ln&31, rl=2*w4+(ln>>5);
    lch* tile=sm+O_TILE; u32 Tr=(ln>>1)&16;
    int uo=W_U+(tb>>TL)*GST_+(tb&((1<<TL)-1))*(128>>TL)+(w4*1024<<TL)+(ln>>(3-TL))*128+(ln&(7>>TL))*16, uo1=uo+(4096<<TL), uo2=uo+(8192<<TL); PIN(uo); PIN(uo1); PIN(uo2);
    lch* tw=tile+rl*TSTR2+j*16; lch* tw2=tw+1024-j*12; u32 one=0x3f803f80u; const lch* tp=tile+qq*TSTR2+(w4*64+g*16+pq*4)*2;
    int go= ln<32? W_RG+(ln>>2)*8192+(tb*4+(ln&3))*8 : W_ST+(tb*8+(ln&7))*8; lch* kw=sm+O_RV+rl*2; const lch* kr=sm+(ln<32? O_RV+(ln&3)*4 : O_MD+(ln&7)*4); PIN(kw); PIN(kr);
    int xr=(32*(tb>>2)+(ln>>2))*(D_*2)+256*(lb&3)+64*w4+pq*16, xr2= w4==0? xr+(lb&2)*(8*D_*2)+1024+64*(lb&1)-256*(lb&3):OOR;
    rsrc_t rx=RSRC(x,L_*D_*2); u64 gm=((1ull<<(4*NRG))-1ull)|0xff00000000ull; u32 um=~UMASK, km=0x80008000u; asm volatile("" : "+s"(gm),"+s"(um),"+s"(km),"+s"(ws));
    #define D2(a,b,c) __builtin_amdgcn_fdot2_f32_bf16(BC(bf2,a),BC(bf2,b),c,false)
    PRIO(3);
    asm volatile("s_waitcnt vmcnt(5)" : "+v"(wa),"+v"(wb),"+v"(wc),"+v"(Tr),"+v"(tw),"+v"(tw2),"+v"(tp),"+v"(one),"+v"(go),"+v"(xr),"+v"(xr2) :: "memory");
    u32 wq[9]={wa[0],wa[1],wa[2],wa[3],wb[0],wb[1],wb[2],wb[3],wc}; float sw;
    { float a[3];
      #pragma unroll
      for(int i=0;i<9;i++) a[i%3]=D2(wq[i],one,i<3? 0.f:a[i%3]);
      sw=add16x(sum16((a[0]+a[1])+a[2])); }
    GETEP u32 tg=(u32)ep+1u; Tr=((upat(tg,w4)>>Tr)&0xffffu)*0x10001u; u32 tgv=tg;
    float z_[9]={0,0,0,0,0,0,0,0,0};
    asm volatile("s_waitcnt vmcnt(2)" : "+v"(xa),"+v"(xb_),"+v"(xc),"+v"(Tr),"+v"(sw),"+v"(tgv),"+v"(z_[0]),"+v"(z_[1]),"+v"(z_[2]),"+v"(z_[3]),"+v"(z_[4]),"+v"(z_[5]) :: "memory");
    u32 v[9]={xa[0],xa[1],xa[2],xa[3],xb_[0],xb_[1],xb_[2],xb_[3],xc};
    { float xf[18], s1, s2, s3;
      #pragma unroll
      for(int i=0;i<9;i++){ z_[i&1]=D2(v[i],one,z_[i&1]); z_[2+(i&1)]=D2(v[i],v[i],z_[2+(i&1)]); z_[4+(i&1)]=D2(v[i],wq[i],z_[4+(i&1)]); }
      s1=z_[0]+z_[1]; s2=z_[2]+z_[3]; s3=z_[4]+z_[5];
      SCHED();
      #define RSTEP(C) s1+=dppf<C>(s1); s2+=dppf<C>(s2); s3+=dppf<C>(s3); SCHED();
      RSTEP(0xB1) RSTEP(0x4E) RSTEP(0x141) RSTEP(0x140)
      s1=add16x(s1)*(1.f/576.f); s2=add16x(s2)*(1.f/576.f); s3=add16x(s3);
      float mm=s1*s1, var=s2-mm;
      if(__builtin_expect(BALLOT(!(mm<=64.f*var))!=0ull,0)){
        float q=0.f;
        #pragma unroll
        for(int i=0;i<9;i++){ u32 t=v[i]; PIN(t); float d0=blo(t)-s1, d1=bhi(t)-s1; q=__builtin_fmaf(d0,d0,q); q=__builtin_fmaf(d1,d1,q); }
        var=add16x(sum16(q))*(1.f/576.f); }
      #define UNP(i) PIN(v[i]); xf[2*(i)]=blo(v[i]); xf[2*(i)+1]=bhi(v[i]); PIN(xf[2*(i)]); PIN(xf[2*(i)+1]); SCHED();
      u32 vb=pk(var,s1); UNP(0)
      float den=blo(vb)+1e-5f, m=bhi(vb); UNP(1)
      vb=pk(den,den); s3=__builtin_fmaf(-m,sw,s3); UNP(2)
      den=__builtin_amdgcn_sqrtf(bhi(vb)); UNP(3)
      u32 md=pk(m,den); UNP(4)
      float ri=rcpf_(bhi(md)); UNP(5)
      s3*=ri; float rs_=ri*0.0625f; UNP(6)
      u32 u=pk(s3,s3); float cs_=-m*rs_; UNP(7)
      { u32 sg=(u32)((int)u>>31); PIN(sg); u^=sg|km; } UNP(8)
      asm volatile("s_mov_b64 exec, 0x1\n\ts_mov_b32 exec_hi, 1\n\tds_write_b16 %0, %1\n\tv_add_u32 %0, %0, %0\n\tds_write_b32 %0, %2 offset:%3\n\ts_mov_b32 exec_hi, 0\n\tds_add_u32 %4, %5\n\ts_mov_b64 exec, -1" : "+v"(kw) : "v"(u),"v"(md),"i"(O_MD-2*O_RV),"v"(C_I*4),"v"(1) : "memory");
      if(w4==3){ gwait<0>(sm,C_I,4); asm volatile("ds_read_b32 v118, %4\n\tv_mov_b32 v119, %1\n\ts_mov_b64 exec, %3\n\ts_waitcnt lgkmcnt(0)\n\tglobal_store_dwordx2 %0, v[118:119], %2 sc1\n\ts_mov_b64 exec, -1" :: "v"(go),"v"(tgv),"s"(ws),"s"(gm),"v"(kr) : "memory"); }
      SCHED();
      u32 h[9];
      #define UH(i) h[i]=(pkz(__builtin_fmaf(xf[2*(i)],rs_,cs_),__builtin_fmaf(xf[2*(i)+1],rs_,cs_))&um)|Tr;
      UH(0) UH(1) UH(2) UH(3) ST(u32x4,tw)=u32x4{h[0],h[1],h[2],h[3]}; UH(4) UH(5) UH(6) UH(7) ST(u32x4,tw+512)=u32x4{h[4],h[5],h[6],h[7]};
      UH(8) ST(u32,tw2)=h[8]; }
    gsync(sm,C_L,4);
    { u32x4 uq[3];
      #pragma unroll
      for(int z=0;z<3;z++) uq[z]=BC(u32x4,trfrag(tp+z*512,tp+z*512+4*TSTR2));
      asm volatile("global_store_dwordx4 %0, %1, %2 sc1" :: "v"(uo),"v"(uq[0]),"s"(ws) : "memory");
      asm volatile("global_store_dwordx4 %0, %1, %2 sc1" :: "v"(uo1),"v"(uq[1]),"s"(ws) : "memory");
      asm volatile("s_cmp_lg_u32 %3, 0\n\ts_cbranch_scc1 1f\n\tglobal_store_dwordx4 %0, %1, %2 sc1\n1:\n\ts_nop 1" :: "v"(uo2),"v"(uq[2]),"s"(ws),"s"(w4),"v"(uq[0]),"v"(uq[1]) : "memory","scc");
      asm volatile("" : "+v"(dq[0]),"+v"(dq[1]));
      xr_=xr; xr2_=xr2;
    }
    if(lb>=2&&lb<8){
      const u16* wm=(lb&1)? wu:wg; int co=(3-(int)((u64)wm>>7)-xg)&7, r_=576-12*PFN+(((lb>>1)-1)*4+w4)+12*j;
      asm volatile("buffer_load_dword v120, %0, %1, 0 offen" :: "v"(j<PFN&&(ln<32||co==0)? r_*(F_*2)+(9*xg+co)*128+(ln>>5)*1024 : OOR),"s"(mkrs(wm,D_*F_*2)) : "memory"); }
    if(lb>=8&&lb<14) garrive(sm,C_D);
  }
  FP fp; FA fa; u32 ti,td;
  { LANE(ln); u64 tag=ep+1ull;
    int so_=W_ST+(32*(tb>>2)+(ln>>2))*8, fo_=W_FL+((ln>=32&&w4==0? 512+32*(lb&1) : 128*(lb&3)+32*w4)+(ln&31))*8; u32x4 rx=mkrs(x,L_*D_*2); PIN(so_); PIN(fo_);
    XR X; X.so=so_; X.x0=xr_; X.x2=xr2_; X.rx=rx;
    if(lb==CTLB||lb>=14){
      u32 M=(u32)tag, rank,key; int j=ln&31, j16=j*16, j12=j*12, st0=W_ST; lch* xw=sm+O_TILE+(2*w4+(ln>>5))*XSTR+j*16; lch* rcw=sm+O_RC+(2*w4+(ln>>5))*4; PIN(rcw); PIN(j12); PIN(xw); PIN(j16); asm volatile("" : "+s"(rx),"+s"(st0));
      top8(sm,w4,ln,M,ws,rank,key,X);
      {
        int t0=__builtin_amdgcn_readlane((int)key,__builtin_ctzll(BALLOT(rank==(u32)(2*w4)))), t1=__builtin_amdgcn_readlane((int)key,__builtin_ctzll(BALLOT(rank==(u32)(2*w4+1))));
        int tsel=~((ln>>5)? t1:t0)&2047, ro=tsel*1152+j16, sr=tsel*8+st0;
        u32x4 a,b; u32 c; u64 f1;
        asm volatile("buffer_load_dwordx4 %[a], %[ro], %[rx], 0 offen\n\tbuffer_load_dwordx4 %[b], %[ro], %[rx], 0 offen offset:512\n\tbuffer_load_dword %[c], %[r2], %[rx], 0 offen offset:1024\n\tglobal_load_dwordx2 %[f], %[sr], %[ws] sc1\n\t"
          "s_mov_b64 exec, 1\n\tds_add_u32 %[cd], %[one]\n\ts_mov_b64 exec, -1\n\ts_waitcnt vmcnt(0)"
          : [a]"=&v"(a),[b]"=&v"(b),[c]"=&v"(c),[f]"=&v"(f1) : [ro]"v"(ro),[r2]"v"(ro-j12),[sr]"v"(sr),[cd]"v"(C_D*4),[one]"v"(1),[rx]"s"(rx),[ws]"s"(ws) : "memory");
        if(__builtin_expect(BALLOT((u32)(f1>>32)!=(u32)tag)!=0ull,0)){ f1=poll1<32>(ws+sr,tag<<32); __builtin_amdgcn_s_waitcnt(0x0f70); }
        ST(u32x4,xw)=a; ST(u32x4,xw+512)=b; ST(u32,xw+1024-j12)=c; ST(u32,rcw)=(u32)f1;
        garrive(sm,C_X);
        if(w4==0){ if(rank<8) ST(u16,sm+O_IDX+(ln&1)*16+rank*2)=(u16)((ln&1)? key>>16 : ~key&2047u); garrive(sm,C_IX); }
        if(lb==CTLB&&rank<8&&!(ln&1)) GST(ws+W_IDX+xg*1024+w4*128+rank*8,(tag<<16)|(~key&2047u));
      }
      PRIO(0); if(XB!=2) xreq(X,ws);
    } else { PRIO(0); if(XC&&lb>=8){ gwait<4>(sm,C_G,XG); __builtin_amdgcn_s_sleep(XC); xreq(X,ws); gwait<8>(sm,C_U,XU); } else { gwait<8>(sm,C_U,2); xreq(X,ws); if(AW&&lb<8&&w4>=4-AW) pf2(lb-1+7*(3-w4),xg,wd); } }
    { u32 e=now_()-ldw, nm= lb<8&&lb!=CTLB? ANA : (lb>=8&&lb<14? ANC:ANB); if(e>nm) ldw+=e-nm; }
    final_addr(fa,sm,w4);
    bool ix= w4==3&&lb<14&&lb!=CTLB; int po=W_IDX+(xg*8+(lb&3))*128+(ln&7)*8, ho_=fo_+(W_HT-W_FL), pq_= ix? po:fo_; PIN(ho_); PIN(pq_);
    { u64 fs,fs1;
      asm volatile("s_waitcnt vmcnt(0)" : "+{v[96:99]}"(X.q[0]),"+{v[100:103]}"(X.q[1]),"+{v[104:107]}"(X.q[2]),"+{v[108:111]}"(X.q[3]) :: "memory");
      fs=(u64)X.q[3][0]|((u64)X.q[3][1]<<32); fs1=(u64)X.q[3][2]|((u64)X.q[3][3]<<32);
      if(__builtin_expect(BALLOT((u32)(fs>>32)!=(u32)tag||(u32)(fs1>>32)!=(u32)tag)!=0ull,0)){ fs=poll1<32>(ws+so_,tag<<32); fs1=poll1<32>(ws+so_+128,tag<<32); __builtin_amdgcn_s_waitcnt(0x0f70); }
      float ri=rcpf_(bhi((u32)fs)), rj=rcpf_(bhi((u32)fs1)); rc[0]=f32x2{ri,-blo((u32)fs)*ri}; rc[1]=f32x2{rj,-blo((u32)fs1)*rj}; rc[2]= (lb&2)? rc[1]:rc[0];
      #pragma unroll
      for(int z=0;z<3;z++) xq[z]=X.q[z]; }
    if(DF) final_pre<2,3>(fp,w4,xq,dq,rc); else final_pre<0,1>(fp,w4,xq,dq,rc);
    ti=now_()-ldw;
    asm volatile("global_load_dwordx2 v[112:113], %0, %2 sc1\n\tglobal_load_dwordx2 v[114:115], %1, %2 sc1\n\tv_mov_b32 v117, %5\n\ts_cmp_lt_u32 %4, " STR(HT) "\n\ts_cbranch_scc1 1f\n\tglobal_load_dwordx2 v[116:117], %3, %2 sc1\n1:"
      :: "v"(fo_),"v"(pq_),"s"(ws),"v"(ho_),"s"(ti),"s"(~(u32)tag) : "memory","scc");
    if(DF) final_pre<0,1>(fp,w4,xq,dq,rc); else final_pre<1,3>(fp,w4,xq,dq,rc);
    PRIO(EPR);
    { u64 ff,f; u32x2 h;
      asm volatile("s_waitcnt vmcnt(0)\n\tv_mov_b64 %0, v[112:113]\n\tv_mov_b64 %1, v[114:115]\n\tv_mov_b64 %2, v[116:117]" : "=v"(ff),"=v"(f),"=v"(h) :: "memory");
      _Pragma("unroll 1") for(int k=0;k<2;k++){ if(BALLOT(h[1]!=(u32)tag)==0ull||ti>=H2) break;
        tgate2(ldw,ti<HT? HT:H2); ti=now_()-ldw; if(ti>=HL){ h=u32x2{ldw,(u32)tag}; break; }
        asm volatile("global_load_dwordx2 %0, %1, %2 sc1\n\ts_waitcnt vmcnt(0)" : "=&v"(h) : "v"(ho_),"s"(ws) : "memory"); }
      u32 rel= h[1]==(u32)tag? h[0]-ldw : ti+HM; td=umin_(wmax(rel),ti+160u);
      if(td>HK) td-=(td-HK)>>1;
      if(__builtin_expect(BALLOT(ff!=tag)!=0ull,0)) poll1<0>(ws+fo_,tag);
      if(ix){ if(BALLOT(((f^(tag<<16))>>16)!=0ull)!=0ull) f=poll1<16>(ws+pq_,tag<<16);
        if(ln<8) ST(u16,sm+O_IDX+ln*2)=(u16)f; garrive(sm,C_IX); }
      __builtin_amdgcn_s_waitcnt(0x0f70); }
  }
  final_post(sm,fp,fa,w4,out,ws,xr_,ldw,td,xq,dq,rc);
  }
}

extern "C" void kernel_launch(void* const* d_in, const int* in_sizes, int n_in, void* d_out, int out_size, void* d_ws, size_t ws_size, hipStream_t stream) {
  const u16** I=(const u16**)d_in;
  KP kp; kp.Are=I[1]; kp.Aim=I[2]; kp.Bre=I[3]; kp.Bim=I[4]; kp.Cre=I[5]; kp.Cim=I[6]; kp.ldt=I[7]; kp.pad=I[7];
  kA<<<256,1024,0,stream>>>(I[0],(char*)d_ws,I[10],I[8],I[11],I[12],I[13],(u16*)d_out,kp);
}
```

```cpp
#define STAMPS
#define STSEL 0x2080
#define PIDX 7
#define PGM 0x911
#define PAMP 8u
#define PBLK -1
#define LS 0
#define LO 0
#include <hip/hip_runtime.h>
#include <hip/hip_bf16.h>
#include <stdint.h>
#if defined(__HIP_DEVICE_COMPILE__)
#define NOPK __attribute__((target("no-packed-fp32-ops")))
#else
#define NOPK
#endif
#define DEV __device__ NOPK __forceinline__
typedef unsigned short u16;
typedef unsigned int u32;
typedef unsigned long long u64;
typedef short bf16x8 __attribute__((ext_vector_type(8)));
typedef _Float16 h8 __attribute__((ext_vector_type(8)));
typedef _Float16 h2 __attribute__((ext_vector_type(2)));
typedef float f32x4 __attribute__((ext_vector_type(4)));
typedef float f32x2 __attribute__((ext_vector_type(2)));
typedef u32 u32x4 __attribute__((ext_vector_type(4)));
typedef u32 u32x2 __attribute__((ext_vector_type(2)));
typedef u32 u32x8 __attribute__((ext_vector_type(8)));
typedef __bf16 bf2 __attribute__((ext_vector_type(2)));
typedef short v4s __attribute__((ext_vector_type(4)));
typedef __amdgpu_buffer_rsrc_t rsrc_t;
#define GAS __attribute__((address_space(1)))
#define LAS __attribute__((address_space(3)))
typedef LAS char lch;
#define L_ 2048
#define D_ 576
#define F_ 4608
#define BC(T,v) __builtin_bit_cast(T,v)
#define TID ((int)__builtin_amdgcn_workitem_id_x())
#define BID ((int)__builtin_amdgcn_workgroup_id_x())
#define RFL(x) __builtin_amdgcn_readfirstlane(x)
#define RLX_AGENT __ATOMIC_RELAXED, __HIP_MEMORY_SCOPE_AGENT
#define LD(T,p) (*(const LAS T*)(p))
#define ST(T,p) (*(LAS T*)(p))
#define SPINCAP (1<<17)
#define DRAIN() asm volatile("s_waitcnt vmcnt(0)" ::: "memory")
#define GLD(p) __hip_atomic_load((const GAS u64*)(p),RLX_AGENT)
#define GST(p,v) __hip_atomic_store((GAS u64*)(p),(u64)(v),RLX_AGENT)
#define BALLOT(c) __builtin_amdgcn_ballot_w64(c)
#define SC1 16
#define LANE(l) int l; { int t2_=TID; asm volatile("" : "+v"(t2_)); l=t2_&63; }
#define XB 1
#define XBW "0"
#define UE 6
#define UN 4
#define US 0
#define USB 64
#define PD 0
#define HO 1
#define DF 1
#define DPR 3
#define EPR 0
#define XC 0
#define XG 7
#define XU 2
#define W2C 7
#define AW 0
#define S3 9
#define W3B 6
#define UP 1
#define RL 0
#define RLA "s_nop 1\n\t"
#define RLB "s_branch 4f\n"
#define LKS 0
#define TPR 1
#define OPR 3
#define GPR 1
#define SCHED() __builtin_amdgcn_sched_barrier(0)
#define RSRC(p,n) __builtin_amdgcn_make_buffer_rsrc((void*)(p),0,(n),0x00020000)
#define BLD128 __builtin_amdgcn_raw_buffer_load_b128
#define BLD32 __builtin_amdgcn_raw_buffer_load_b32
#define BST128 __builtin_amdgcn_raw_buffer_store_b128
#define PRIO(n) __builtin_amdgcn_s_setprio(n)

DEV float b2f(u32 v){ return BC(float,v<<16); }
DEV float blo(u32 v){ return BC(float,v<<16); }
DEV float bhi(u32 v){ return BC(float,v&0xffff0000u); }
DEV u32 pk(float a,float b){ f32x2 v={a,b}; bf2 r=__builtin_convertvector(v,bf2); return BC(u32,r); }
DEV u32 pkh(float a,float b){ f32x2 v={a,b}; h2 r=__builtin_convertvector(v,h2); return BC(u32,r); }
DEV float rb(float f){ return BC(float,pk(f,f)<<16); }
DEV float rcpf_(float x){ return __builtin_amdgcn_rcpf(x); }
DEV float expf_(float x){
  const float L2E=1.4426950216293335f, L2El=1.9259629911266175e-8f; float h=x*L2E; float l=__builtin_fmaf(x,L2E,-h)+x*L2El;
  float e=__builtin_amdgcn_exp2f(h); return __builtin_fmaf(e*l,0.6931471805599453f,e); }
DEV float sigm_f(float x){ return rcpf_(1.f+__builtin_amdgcn_exp2f(-x*1.4426950408889634f)); }
DEV float silu_f(float x){ return x*sigm_f(x); }
DEV u32 umax_(u32 a,u32 b){ return a>b?a:b; }
DEV u32 umin_(u32 a,u32 b){ return a<b?a:b; }

struct SB { unsigned t[14]; };
#define RT32() ((unsigned)__builtin_amdgcn_s_memrealtime()|0x80000000u)
#define SBDECL SB sb; _Pragma("unroll") for(int i_=0;i_<14;i_++) sb.t[i_]=0; sb.t[12]=tearly;
#define RCNT(i,c) sb.t[i]=sb.t[12]+(0x20000u-(c))*100u
#define SBARG ,SB& sb
#define SBPASS ,sb
#define STAMP(i) do{ if((STSEL>>(i))&1){ __builtin_amdgcn_sched_barrier(0); sb.t[i]=RT32(); __builtin_amdgcn_sched_barrier(0); } }while(0)
#define SFLUSH() do{ int t3_=RFL(TID>>6), lb_=BID>>3, gr_=(lb_<8? 0:(lb_<14? 1:2))*4+(t3_>>2); sb.t[7]=sb.t[7]>sb.t[13]? sb.t[7]:sb.t[13]; if(((PGM>>gr_)&1)&&sb.t[PIDX]&&(PBLK<0||BID==PBLK)){ u32 t0_; asm volatile("ds_read_b32 %0, %1\n\ts_waitcnt lgkmcnt(0)" : "=v"(t0_) : "v"(92) : "memory"); t0_=~(u32)RFL(t0_); \
  u32 d_=1200u+PAMP*(sb.t[PIDX]-t0_); if(d_>20000u) d_=20000u; for(int n_=0;n_<100000;n_++){ if(RT32()-t0_>=d_) break; __builtin_amdgcn_s_sleep(2); } } }while(0)

#define YSH 3
#define PRE1 6
#define PRE3 4
#define PRE2 5
#define W1 5
#define W3 3
#define W2_ 4
#define W1B 20
#define W2B 7
#define LAST1 4
#define RGATE 4
#define UGATE 7
#define R2D 12
#define PFN 22
#define CPF 8
#define HD0 174
#define HD1 148
#define HD3 165
#define HM 100
#define HT 555
#define H2 600
#define HL 650
#define HK 655
#define HKR 65
#define HR 12
#define ANA 410
#define ANB 470
#define ANC 540
#define NRG 8
#define W_EPOCH 0
#define W_RG 16384
#define W_ST (W_RG+32*8192)
#define W_U (W_ST+32768)
#define UST (D_*16+128)
#define YST (L_*2+128)
#define TL 1
#define GST_ ((D_*16<<TL)+128)
#define W_Y (W_U+256*UST)
#define W_PART (W_Y+D_*YST)
#define W_GU (W_PART+8*9*16*256)
#define W_FL 9216
#define W_HT (W_RG+16*8192)
#define W_IDX 1024
#define CTLB 14
struct KP { const u16 *Are,*Aim,*Bre,*Bim,*Cre,*Cim,*ldt,*pad; };

template<int CTRL> DEV float dppf(float v){ return BC(float,__builtin_amdgcn_update_dpp(0u,BC(u32,v),CTRL,0xf,0xf,false)); }
template<int CTRL> DEV u32 dppu(u32 v){ return (u32)__builtin_amdgcn_update_dpp(0u,v,CTRL,0xf,0xf,false); }
DEV float sum16(float v){ v+=dppf<0xB1>(v); v+=dppf<0x4E>(v); v+=dppf<0x141>(v); v+=dppf<0x140>(v); return v; }
DEV float add16x(float v){ auto r=__builtin_amdgcn_permlane16_swap(BC(u32,v),BC(u32,v),false,false); u32 a=r[0], b=r[1]; return BC(float,a)+BC(float,b); }
DEV u32 wmax(u32 v){
  v=umax_(v,(u32)__builtin_amdgcn_update_dpp(0u,v,0x111,0xf,0xf,false));
  v=umax_(v,(u32)__builtin_amdgcn_update_dpp(0u,v,0x112,0xf,0xf,false));
  v=umax_(v,(u32)__builtin_amdgcn_update_dpp(0u,v,0x114,0xf,0xf,false));
  v=umax_(v,(u32)__builtin_amdgcn_update_dpp(0u,v,0x118,0xf,0xf,false));
  v=umax_(v,(u32)__builtin_amdgcn_update_dpp(0u,v,0x142,0xa,0xf,false));
  v=umax_(v,(u32)__builtin_amdgcn_update_dpp(0u,v,0x143,0xc,0xf,false));
  return (u32)__builtin_amdgcn_readlane((int)v,63); }

DEV void garrive(lch* sm,int c){ u32 ad=(u32)(size_t)sm+c*4, one=1;
  asm volatile("s_mov_b64 exec, 1\n\tds_add_u32 %0, %1\n\ts_mov_b64 exec, -1" :: "v"(ad),"v"(one) : "memory"); }
template<int SLP> DEV void gwait(lch* sm,int c,u32 target){ u32 ad=(u32)(size_t)sm+c*4, tv, cnt, sv; target=RFL(target);
  asm volatile("s_mov_b32 %1, 0x20000\n"
    "1:\n\tds_read_b32 %0, %3\n\ts_waitcnt lgkmcnt(0)\n\tv_readfirstlane_b32 %2, %0\n\ts_cmp_ge_u32 %2, %4\n\ts_cbranch_scc1 2f\n\ts_sleep %5\n\ts_sub_u32 %1, %1, 1\n\ts_cbranch_scc0 1b\n"
    "2:" : "=&v"(tv),"=&s"(cnt),"=&s"(sv) : "v"(ad),"s"(target),"i"(SLP) : "memory","scc"); }
DEV u32 now_(){ u32 t; asm volatile("s_memrealtime s[98:99]\n\ts_waitcnt lgkmcnt(0)\n\ts_mov_b32 %0, s98" : "=s"(t) :: "memory"); return t; }
DEV void tgate2(u32 t0,u32 d){ u32 cnt; asm volatile("s_mov_b32 %0, 100\n1:\n\ts_memrealtime s[98:99]\n\ts_waitcnt lgkmcnt(0)\n\ts_sub_u32 s98, s98, %1\n\ts_cmp_ge_u32 s98, %2\n\ts_cbranch_scc1 2f\n\ts_sleep 1\n\ts_sub_u32 %0, %0, 1\n\ts_cbranch_scc0 1b\n2:" : "=&s"(cnt) : "s"(t0),"s"(d) : "memory","scc"); }
DEV void gsync(lch* sm,int c,u32 target){ garrive(sm,c); gwait<0>(sm,c,target); }
template<int SH> DEV u64 poll1(const char* p,u64 want){ u64 f=GLD(p);
  _Pragma("unroll 1") for(int sp=0;sp<SPINCAP;sp++){ bool bad=((f^want)>>SH)!=0ull; if(BALLOT(bad)==0ull) break; __builtin_amdgcn_s_sleep(1); if(bad) f=GLD(p); }
  __builtin_amdgcn_fence(__ATOMIC_ACQUIRE,"wavefront"); return f; }
#define STR_(x) #x
#define STR(x) STR_(x)
DEV u32x4 mkrs(const void* p,u32 n){ u64 a=(u64)p; return u32x4{(u32)RFL((int)(u32)a),(u32)RFL((int)((u32)(a>>32)&0xffffu)),n,0x00020000u}; }
#define VMW(n) asm volatile("s_waitcnt vmcnt(%0)" :: "i"(n) : "memory")
#define YSLP 1
#define GSLP 1
#define QSLP 1
#define PSLP 3
#define POLLASM(LOADS,CHECK,K) "s_mov_b32 %[cnt], 0x20000\n1:\n\ts_waitcnt vmcnt(0)\n\t" CHECK "s_cbranch_vccz 9f\n\ts_sleep " STR(K) "\n\ts_mov_b64 exec, vcc\n\t" LOADS \
  "s_mov_b64 exec, -1\n\ts_sub_u32 %[cnt], %[cnt], 1\n\ts_cbranch_scc0 1b\n9:\n\t"
#define LD_DUP2 "buffer_load_dwordx4 v[112:115], %[of], %[rs], 0 offen sc1\n\tbuffer_load_dwordx4 v[116:119], %[of], %[rs], %[so] offen sc1\n\t"
#define MX2(a,b) "v_max3_u32 %[t0], %[t0], v" #a ", v" #b "\n\t"
#define CHK_I0 "v_max_u32 %[t0], v112, v113\n\t" MX2(114,115)
#define CHK_IE "v_cmp_eq_u32 vcc, -1, %[t0]\n\t"
#define PCLOB "memory","vcc","scc"
#define UMASK 0x40074007u
DEV u32 upat(u32 tag,int k){ return ((((tag*0xea959c21u)>>(8*k))&255u)*0x04000801u)&UMASK; }
DEV u32 pkz(float a,float b){ return BC(u32,__builtin_amdgcn_cvt_pkrtz(a,b)); }
#define INV 0xffffffffu
DEV u32 nv(u32 v){ return umin_(v,0xfffeffffu); }
DEV u32x4 nv4(u32x4 v){ return u32x4{nv(v[0]),nv(v[1]),nv(v[2]),nv(v[3])}; }
#define OOR 0x40000000

#define CHSZ 36864
#define O_CH 512
#define O_CND 256
#define SLSZ 38080
#define O_R2 (O_CH+2*SLSZ)
#define O_X2 (O_R2+CHSZ)
#define O_TILE (O_X2+CHSZ)
#define XSTR 1216
#define TSTR2 1216
#define O_CAND (O_TILE+9728)
#define LDS_TOTAL (O_CAND+128)
#define C_G 0
#define C_U 8
#define C_L 4
#define C_D 5
#define C_X 12
#define C_P 13
#define C_H 14
#define C_I 17
#define C_E 18
#define C_IX 19
#define C_RS 22
#define C_T 16
#define O_IDX 96
#define O_EXP 144
#define O_RMX 192
#define O_RV 128
#define O_MD 400
#define O_RC (O_CAND+64)

struct C2 { float r,i; };
DEV C2 cmul(C2 a,C2 b){ C2 c; c.r=__builtin_fmaf(a.r,b.r,-(a.i*b.i)); c.i=__builtin_fmaf(a.r,b.i,a.i*b.r); return c; }
DEV h8 ldA(const lch* p){ u32x2 a=*(const volatile LAS u32x2*)p, b=*(const volatile LAS u32x2*)(p+8); return BC(h8,(u32x4{a[0],a[1],b[0],b[1]})); }
DEV bf16x8 trfrag(const lch* p0,const lch* p1){
  u32x2 b0=BC(u32x2,__builtin_amdgcn_ds_read_tr16_b64_v4i16((LAS v4s*)p0)), b1=BC(u32x2,__builtin_amdgcn_ds_read_tr16_b64_v4i16((LAS v4s*)p1));
  return BC(bf16x8,(u32x4{b0[0],b0[1],b1[0],b1[1]})); }

struct FP { float gg[24], bs[24]; };
template<int Z0,int Z1> DEV void final_pre(FP& f,int w4,const u32x4* xq,const u32x4* dq,const f32x2* rc){
  #pragma unroll
  for(int z=Z0;z<Z1;z++) if(z<2||w4==0){ float ea=rc[z][0]*-1.4426950408889634f, eb=rc[z][1]*-1.4426950408889634f;
    #pragma unroll
    for(int e=0;e<4;e++){ u32 xw=xq[z][e], dw=dq[z>>1][e]; int i=8*z+2*e; float a=blo(xw), b=bhi(xw);
      float pa=__builtin_fmaf(a,rc[z][0],rc[z][1]), pb=__builtin_fmaf(b,rc[z][0],rc[z][1]);
      float ga=pa*rcpf_(1.f+__builtin_amdgcn_exp2f(__builtin_fmaf(a,ea,eb))), gb=pb*rcpf_(1.f+__builtin_amdgcn_exp2f(__builtin_fmaf(b,ea,eb)));
      f.gg[i]=ga; f.gg[i+1]=gb; f.bs[i]=__builtin_fmaf(pa*blo(dw),ga,a); f.bs[i+1]=__builtin_fmaf(pb*bhi(dw),gb,b);
      asm volatile("" : "+v"(f.gg[i]),"+v"(f.gg[i+1]),"+v"(f.bs[i]),"+v"(f.bs[i+1])); } }
}
#define FTS 544
DEV u32 pkmin(u32 a,u32 b){ u32 r; asm("v_pk_min_u16 %0, %1, %2" : "=v"(r) : "v"(a),"v"(b)); return r; }
struct FA { int yo,yo3; lch *wp,*wp3; const lch* tp; u32 tt; };
DEV void final_addr(FA& a,lch* sm,int w4){ LANE(ln); int bid=BID, xg=bid&7, lb=bid>>3, cg=lb&3, T=8*xg+(lb>>2);
  a.yo=(128*cg+32*w4+(ln>>2)*2)*YST+(32*T+8*(ln&3))*2; a.yo3= ln<32? (512+32*(cg&1)+(ln&30))*YST+(32*T+16*(cg>>1)+8*(ln&1))*2 : OOR;
  lch* fb=sm+O_X2+2048+w4*4608; a.wp=fb+(ln&3)*FTS+(ln&60); a.wp3=fb+(4+(ln&1)+(ln>>5)*2)*FTS+(ln&30)*2; a.tp=fb+(ln>>5)*FTS+(ln&31)*16;
  a.tt=(u32)(32*T+(ln>>2))*0x10001u;
  asm volatile("" : "+v"(a.yo),"+v"(a.yo3),"+v"(a.wp),"+v"(a.wp3),"+v"(a.tp),"+v"(a.tt)); }
DEV void final_post(lch* sm,FP& f,const FA& fa,int w4,u16* __restrict__ out,char* ws,int xr,u32 tk,u32 td,const u32x4* xq,const u32x4* dq,const f32x2* rc SBARG){
  int lb=BID>>3, cg=lb&3, ob[3], yo=fa.yo, yo3=fa.yo3; lch *wp=fa.wp, *wp3=fa.wp3; const lch* tp=fa.tp; u32 tt=fa.tt;
  u32x4 rs=mkrs(ws+W_Y,D_*YST); rsrc_t ro=RSRC(out,L_*D_*2);
  #define RMASK { gwait<2>(sm,C_IX,1); u32x4 iq=LD(u32x4,sm+O_IDX); \
    _Pragma("unroll") for(int z=0;z<2;z++){ u32 t2=tt+z*0x100010u, m=pkmin(pkmin(iq[0]^t2,iq[1]^t2),pkmin(iq[2]^t2,iq[3]^t2)); ob[z]= umin_(m&0xffffu,m>>16)==0u? OOR : xr+z*(16*D_*2); } \
    ob[2]=((cg&2)? ob[1]:ob[0])+(512+32*(cg&1)-128*cg)*2; asm volatile("" : "+v"(ob[0]),"+v"(ob[1]),"+v"(ob[2])); }
  tgate2(tk,td);
  STAMP(10);
  if(lb>=8) gwait<1>(sm,C_H,4);
  { u32 t0,t1,cnt;
    #define LD_Y "buffer_load_dwordx4 v[112:115], %[of], %[rs], 0 offen sc1\n\tbuffer_load_dwordx4 v[116:119], %[of], %[rs], %[s1] offen sc1\n\ts_cmp_lg_u32 %[w], 0\n\ts_cbranch_scc1 5f\n\t" \
      "buffer_load_dwordx4 v[120:123], %[o3], %[rs], 0 offen sc1\n\tbuffer_load_dwordx4 v[124:127], %[o3], %[rs], %[s1] offen sc1\n5:\n\t"
    #define YIN [of]"v"(yo),[rs]"s"(rs),[s1]"s"(YST),[o3]"v"(yo3),[w]"s"(w4)
    asm volatile(LD_Y :: YIN : "memory","scc");
    PRIO(DPR); if(DF) final_pre<1,2>(f,w4,xq,dq,rc);
    PRIO(3);
    RMASK
    #define YW(p,a,b,o) "v_perm_b32 %[t0], v" #b ", v" #a ", %[lo]\n\tv_perm_b32 %[t1], v" #b ", v" #a ", %[hi]\n\tds_write2_b32 %[" #p "], %[t0], %[t1] offset0:" #o " offset1:" #o "+16\n\t"
    asm volatile(POLLASM(LD_Y,CHK_I0 MX2(116,117) MX2(118,119) "s_cmp_lg_u32 %[w], 0\n\ts_cbranch_scc1 6f\n\t" MX2(120,121) MX2(122,123) MX2(124,125) MX2(126,127) "6:\n\t" CHK_IE,YSLP)
      YW(wp,112,116,0) YW(wp,113,117,32) YW(wp,114,118,64) YW(wp,115,119,96) "s_cmp_lg_u32 %[w], 0\n\ts_cbranch_scc1 7f\n\t" YW(w3,120,124,0) YW(w3,121,125,32) YW(w3,122,126,64) YW(w3,123,127,96) "7:"
      : [t0]"=&v"(t0),[t1]"=&v"(t1),[cnt]"=&s"(cnt) : YIN,[wp]"v"(wp),[w3]"v"(wp3),[lo]"s"(0x05040100u),[hi]"s"(0x07060302u) : PCLOB); RCNT(8,cnt); }
  STAMP(11);
  u32x4 r[3];
  #pragma unroll
  for(int z=0;z<3;z++) r[z]=LD(u32x4,tp+z*(2*FTS));
  #pragma unroll
  for(int z=0;z<3;z++) if(z<2||w4==0){ u32x4 ov;
    #pragma unroll
    for(int e=0;e<4;e++){ u32 rw=r[z][e]; int i=8*z+2*e; ov[e]=pk(__builtin_fmaf(blo(rw),f.gg[i],f.bs[i]),__builtin_fmaf(bhi(rw),f.gg[i+1],f.bs[i+1])); }
    BST128(ov,ro,ob[z],0,SC1); SCHED(); }
  DRAIN();
  STAMP(7);
}

#define TROW 528
#define O_TB 4640
#define O_DE 34752
#define O_DO 35968
#define O_R2K 37120
#define O_R3K 37632
#define O_DUM 37952
#define O_RED O_TB
#define MH(a,b,c) __builtin_amdgcn_mfma_f32_16x16x32_f16(a,b,c,0,0,0)
#define PIN(x) asm volatile("" : "+v"(x))
DEV h8 ld4w(const lch* p){ return BC(h8,(u32x4{LD(u32,p),LD(u32,p+4),LD(u32,p+8),LD(u32,p+12)})); }
DEV u32 __umul24_(u32 a,u32 b){ u32 r; asm("v_mul_u32_u24 %0, %1, %2" : "=v"(r) : "s"(b),"v"(a)); return r; }
DEV u32x4 band(u32 mask,int g,u32 val){ u32 by=__builtin_amdgcn_ubfe(mask,8*g,8), X=by|(by<<15), k=0x10001u; u32x4 r; asm volatile("" : "+s"(k),"+s"(val));
  #pragma unroll
  for(int d=0;d<4;d++) r[d]=__umul24_((X>>(2*d))&k,val);
  return r; }
DEV h8 zpack(f32x4 e,f32x4 o){ return BC(h8,(u32x4{pkh(e[0],o[0]),pkh(e[1],o[1]),pkh(e[2],o[2]),pkh(e[3],o[3])})); }
struct C3 { float r,i,n; };
DEV C3 c3(C2 a){ C3 c; c.r=a.r; c.i=a.i; PIN(c.i); c.n=-c.i; PIN(c.n); return c; }
DEV C2 cm(C2 a,C3 b){ C2 c; c.r=__builtin_fmaf(a.r,b.r,a.i*b.n); c.i=__builtin_fmaf(a.r,b.i,a.i*b.r); return c; }
DEV u32 ph(C2 a){ return pkh(a.r,a.i); }
#define W2(p,a,b) asm volatile("ds_write2_b32 %0, %1, %2 offset1:132" :: "v"(p),"v"(a),"v"(b) : "memory")
struct CP { const lch *x0,*x1,*bd; lch *p0,*p1,*p2,*wp; u32x4 Be,Bo,AE; int yo; };
template<int ROLE> DEV void lanes(CP& c,lch* cb,int ln){ int m=ln&15, g=ln>>4;
  const lch* ad=cb+((m&1)? O_DO+2*527 : O_DE+2*536)+2*(8*g-m);
  const lch* bd=cb+2*(256+16*m+8*g); lch* dum=cb+O_DUM;
  if(ROLE==0){ int l0=128*(g>>1)+8*m+4*(g&1); c.p0=cb+O_DE+(549-l0)*2; PIN(c.p0); c.p1=c.p0+6; PIN(c.p1); c.p2=c.p0+(O_DO-O_DE-18); c.x0=ad-640; c.bd=bd-64; c.x1=cb+O_R3K+2*(126-2*m+8*g)-192; PIN(c.x1);
    c.Be=band(0x1ffu<<(m+4),g,0x3c00u); PIN(c.Be); }
  if(ROLE==1){ lch* r=cb+O_R2K+(192-64*(g-2)-2*m)*2; c.p0= g<2? dum : r-66; c.p1= ln==0? cb+O_DE+(552-256)*2 : (ln<=32? dum : r); c.p2=cb+O_DE+(544-256-8*ln)*2; c.x0=ad-128-640; c.bd=bd-128-128; PIN(c.p0); PIN(c.p1); PIN(c.p2); }
  if(ROLE==2){ int i0=16*m+4*g; c.p0= m<8? cb+O_R3K+(125-i0)*2 : dum+2; c.p1= ln==0? cb+O_R2K+128 : (ln==8? cb+O_R3K : c.p0+6); c.x0=ad-320-640; c.bd=bd-320-128; PIN(c.p0); PIN(c.p1); }
  if(ROLE==3){ c.x0=cb+O_R2K+2*(188-4*m+8*g)-256; c.bd=cb+2*(248+8*m+8*g);
    c.Be=band(0x1fu<<(m+6),g,0x3c00u); c.Bo=band(0x7u<<(m+11),g,0x3c00u); PIN(c.Be); PIN(c.Bo); }
  PIN(c.x0); PIN(c.bd); }
template<int ROLE> DEV void lanes2(CP& c,lch* cb,int ln){ int m=ln&15, g=ln>>4;
  if(ROLE==0){ c.Bo=band(0x7fu<<(m+5),g,0x3c00u); c.AE=band(3u<<(m+12),g,0xffffu); PIN(c.Bo); PIN(c.AE); }
  c.wp=cb+O_RED+ln*16; c.yo=(g&1)*512+m*32+(g>>1)*16; PIN(c.wp); PIN(c.yo); }
template<int ROLE> DEV void chan2(CP& c,lch* sm,lch* cb,const lch*& fa,const lch*& fb,int grp,int d,int one_,int emax,u64 tag,char* ws SBARG){
  const f32x4 Z4={0,0,0,0}; h8 A[6]; f32x4 acc[8]; u32x4 rsY; float sc; u32 nvm; int ca;
  #define YPRE rsY=mkrs(ws+W_Y+d*YST,4096); sc=__builtin_amdgcn_ldexpf(1.f,emax-YSH); nvm=0xfffeffffu; asm volatile("" : "+s"(rsY),"+s"(nvm)); PIN(sc); ca=(C_G+grp)*4; PIN(ca);
  #define LD_U "buffer_load_dwordx4 v[112:115], %[of], %[rs], 0 offen sc1\n\tbuffer_load_dwordx4 v[116:119], %[of], %[rs], %[s1] offen sc1\n\tbuffer_load_dwordx4 v[120:123], %[of], %[rs], %[s2] offen sc1\n\tbuffer_load_dwordx4 v[124:127], %[of], %[rs], %[s3] offen sc1\n\t"
  #define UC(t,a,b,c) "v_bitop3_b32 " t ", v" #a ", v" #b ", %[" #c "] bitop3:0x7e\n\t"
  #define UD(t,a,c) "v_bitop3_b32 " t ", " t ", v" #a ", %[" #c "] bitop3:0xf6\n\t"
  #define CHK_U UC("%[t0]",112,116,c0) UC("%[t1]",113,117,c1) UD("%[t0]",120,c0) UD("%[t1]",121,c1) UD("%[t0]",124,c0) UD("%[t1]",125,c1) UD("%[t0]",114,c2) UD("%[t1]",115,c3) UD("%[t0]",118,c2) UD("%[t1]",119,c3) \
    UD("%[t0]",122,c2) UD("%[t1]",123,c3) UD("%[t0]",126,c2) UD("%[t1]",127,c3) "v_bitop3_b32 %[t0], %[t0], %[t1], %[mk] bitop3:0xa8\n\tv_cmp_ne_u32 vcc, 0, %[t0]\n\t"
  #define UX(a,b,c,d,o) "v_xor_b32 v" #a ", %[e0], v" #a "\n\tv_xor_b32 v" #b ", %[e1], v" #b "\n\tv_xor_b32 v" #c ", %[e2], v" #c "\n\tv_xor_b32 v" #d ", %[e3], v" #d "\n\tds_write_b128 %[up], v[" #a ":" #d "] offset:" #o "\n\t"
  #define UQ(n,a,b,c,d,o) "s_waitcnt vmcnt(" #n ")\n\t" UD("%[t0]",a,c0) UD("%[t1]",b,c1) UD("%[t0]",c,c2) UD("%[t1]",d,c3) UX(a,b,c,d,o)
  #define UPOLL { u32 t0,t1,cnt; asm volatile("s_mov_b32 %[cnt], 0x20000\n1:\n\tv_mov_b32 %[t0], 0\n\tv_mov_b32 %[t1], 0\n\t" UQ(3,112,113,114,115,544) UQ(2,116,117,118,119,1056) UQ(1,120,121,122,123,1568) UQ(0,124,125,126,127,2080) \
      "v_bitop3_b32 %[t0], %[t0], %[t1], %[mk] bitop3:0xa8\n\tv_cmp_ne_u32 vcc, 0, %[t0]\n\ts_cbranch_vccz 9f\n\ts_sleep " STR(PSLP) "\n\ts_mov_b64 exec, vcc\n\t" LD_U "s_sub_u32 %[cnt], %[cnt], 1\n\ts_cbranch_scc0 1b\n9:\n\t" \
      "s_mov_b64 exec, 1\n\tds_add_u32 %[cu], %[on]\n\ts_mov_b64 exec, -1" \
      : [t0]"=&v"(t0),[t1]"=&v"(t1),[cnt]"=&s"(cnt) : [of]"v"(c.p0),[rs]"s"(rsU),[s1]"s"((32>>TL)*GST_),[s2]"s"((64>>TL)*GST_),[s3]"s"((96>>TL)*GST_),[c0]"s"(c0),[c1]"s"(c1),[c2]"s"(c2),[c3]"s"(c3_), \
        [e0]"s"(c0^0x40004u),[e1]"s"(c1^0x40004u),[e2]"s"(c2^0x40004u),[e3]"s"(c3_^0x40004u),[mk]"s"(UMASK),[up]"v"(c.p1),[cu]"v"((C_U+one_)*4),[on]"v"(1) : PCLOB); STAMP(4); }
  if(ROLE<3){
    h8 a[8], b[8];
    #pragma unroll
    for(int ks=0;ks<8;ks++){ a[ks]=LD(h8,fa+ks*64); b[ks]=LD(h8,fb+ks*64); }
    SCHED();
    LANE(ln); if(!HO) lanes<ROLE>(c,cb,ln); float fz= ln==32? 0.f : 4.76837158203125e-07f; if(ROLE==1) PIN(fz);
    SCHED();
    f32x4 k=Z4;
    #pragma unroll
    for(int ks=0;ks<8;ks++) k=MH(a[ks],b[ks],k);
    const float f=4.76837158203125e-07f;
    float k0=k[0]*f, k1=k[1]*f, k2=k[2]*f, k3=k[3]*f;
    u32 h32=pkh(k3,k2), h10=pkh(k1,k0);
    if(ROLE!=1){
      ST(u16,c.p0)=(u16)h32; ST(u32,c.p0+2)=pkh(k2,k1); ST(u16,c.p1)=(u16)(h10>>16);
      if(ROLE==0) ST(u32x2,c.p2)=u32x2{h32,h10};
    } else {
      float n0=dppf<0x130>(k[0])*fz, z1= fz==0.f? k0:k1, z2=k[2]*fz, z3=k[3]*fz; u32 h22=pkh(z2,z2);
      ST(u16,c.p0+64)=(u16)h10; ST(u16,c.p0+2)=(u16)(h32>>16); ST(u16,c.p0)=(u16)h32; ST(u16,c.p1)=(u16)(h10>>16);
      u32x4 vo={pkh(n0,z3),h22,pkh(z2,z1),pkh(k0,k0)}, ve={pkh(n0,n0),pkh(z3,z2),h22,pkh(z1,k0)};
      asm volatile("s_mov_b32 exec_hi, 1\n\tds_write_b128 %0, %1\n\tds_write_b128 %0, %2 offset:1200\n\ts_mov_b32 exec_hi, -1" :: "v"(c.p2),"v"(ve),"v"(vo) : "memory"); }
    gsync(sm,C_G+grp,7); STAMP(3);
    u32 kv; A[0]=ld4w(c.x0+640); A[1]=ld4w(c.x0+128);
    if(ROLE==0){ A[2]=ld4w(c.x1+192); A[3]=ld4w(c.x1+128); A[4]=ld4w(c.x1+64); A[5]=ld4w(c.x1); kv=LD(u16,cb+O_R2K+128); }
    else { A[2]=ld4w(c.x0+576); A[3]=ld4w(c.x0+64); A[4]=ld4w(c.x0+512); A[5]=ld4w(c.x0); }
    SCHED(); if(!HO) lanes2<ROLE>(c,cb,ln); SCHED();
    if(ROLE==0){ kv|=kv<<16; c.AE=u32x4{c.AE[0]&kv,c.AE[1]&kv,c.AE[2]&kv,c.AE[3]&kv}; PIN(c.AE); }
    int ha=W_HT+d*8; u32 hd= one_? HD3 : (grp==0? HD0:HD1); if(ROLE==2) asm volatile("v_mov_b32 v113, %2" : "+v"(ha),"+v"(hd) : "s"((u32)tag));
    YPRE
    PRIO(2);
    if(BID>=112){
      gwait<4>(sm,UE? C_D:C_X,4);
    }
    gwait<1>(sm,C_U+one_,2-one_);
    if(ROLE==2) asm volatile("s_memrealtime s[98:99]\n\ts_waitcnt lgkmcnt(0)\n\tv_add_u32 v112, s98, %1\n\ts_mov_b64 exec, 1\n\tglobal_store_dwordx2 %0, v[112:113], %2 sc1\n\ts_mov_b64 exec, -1" :: "v"(ha),"v"(hd),"s"(ws) : "memory");
  } else {
    u32 tg=(u32)tag, c0=upat(tg,0), c1=upat(tg,1), c2=upat(tg,2), c3_=upat(tg,3); u32x4 rsU=mkrs(ws+W_U,256*UST);
    if(!one_) PRIO(2);
    LANE(ln); c.p0=(lch*)fa; c.p1=(lch*)fb;
    bool late= grp==0||BID>=112;
    asm volatile("s_mov_b64 exec, 1\n\tglobal_store_dwordx2 %0, %1, %2 sc1\n\ts_mov_b64 exec, -1" :: "v"(W_FL+d*8),"v"(tag),"s"(ws) : "memory");
    if(!late) asm volatile(LD_U :: [of]"v"(c.p0),[rs]"s"(rsU),[s1]"s"((32>>TL)*GST_),[s2]"s"((64>>TL)*GST_),[s3]"s"((96>>TL)*GST_) : "memory");
    if(!HO){ lanes<3>(c,cb,ln); lanes2<3>(c,cb,ln); }
    #pragma unroll
    for(int j=2;j<8;j++){ acc[j]=Z4; PIN(acc[j]); }
    YPRE
    if(late){ if(US&&BID<USB) __builtin_amdgcn_s_sleep(US); else if(UE==0||BID<112) gwait<2>(sm,C_G,UGATE); if(BID>=112){ gwait<1>(sm,UE? C_RS:C_D,UN); if(UE>1) __builtin_amdgcn_s_sleep(UE); } asm volatile(LD_U :: [of]"v"(c.p0),[rs]"s"(rsU),[s1]"s"((32>>TL)*GST_),[s2]"s"((64>>TL)*GST_),[s3]"s"((96>>TL)*GST_) : "memory"); }
    if(!late&&!(LO==2||(LO==1&&one_))) UPOLL
    gwait<1>(sm,C_G+grp,7); STAMP(3);
    #pragma unroll
    for(int i=0;i<6;i++) A[i]=ldA(c.x0+320-64*i);
    if(late||LO==2||(LO==1&&one_)) UPOLL
    PRIO(2);
    gwait<1>(sm,C_U+one_,2-one_);
  }
  STAMP(5);
  const lch* bd=c.bd; h8 Be=BC(h8,c.Be), Bo=BC(h8,c.Bo);
  #pragma unroll
  for(int j=0;j<(ROLE==3? 2:8);j++) acc[j]=Z4;
  #define RW(j) if((j)>=0&&((j)>>1)!=ROLE&&!(ROLE==3&&(j)<2)){ ST(f32x4,c.wp+((j)>>1)*6144+((ROLE-((j)>>1)-1)&3)*2048+((j)&1)*1024)=acc[(j)<0? 0:(j)]; SCHED(); }
  if(ROLE==1||ROLE==2){
    h8 B[8];
    #pragma unroll
    for(int q=0;q<8;q++) B[q]=LD(h8,bd+128+512*q);
    #pragma unroll
    for(int r=0;r<3;r++){
      #pragma unroll
      for(int dl=0;dl<8;dl++){ if(dl<7) acc[dl+1]=MH(A[2*r+1],B[dl],acc[dl+1]); SCHED(); acc[dl]=MH(A[2*r],B[dl],acc[dl]); SCHED();
        if(r<2){ B[dl]=LD(h8,bd+64-64*r+512*dl); SCHED(); } else RW(dl-2) }
      } }
  if(ROLE==0){
    h8 B[8], ue[4];
    #pragma unroll
    for(int kt=0;kt<4;kt++){ ue[kt]=LD(h8,bd+48+512*kt); B[kt]=LD(h8,bd+64+512*kt); }
    f32x4 ce[2],co[2]; h8 Z[2];
    ce[0]=MH(ue[0],Be,Z4); co[0]=MH(B[0],Bo,Z4); ce[1]=MH(ue[1],Be,Z4); co[1]=MH(B[1],Bo,Z4); Z[0]=zpack(ce[0],co[0]);
    #pragma unroll
    for(int kt=0;kt<4;kt++){
      SCHED();
      if(kt+2<4){ ce[kt&1]=MH(ue[kt+2],Be,Z4); co[kt&1]=MH(B[kt+2],Bo,Z4); }
      if(kt+1<4) Z[(kt+1)&1]=zpack(ce[(kt+1)&1],co[(kt+1)&1]);
      #pragma unroll
      for(int rho=0;rho+kt<4;rho++) acc[4+kt+rho]=MH(Z[kt&1],A[2+rho],acc[4+kt+rho]);
      SCHED(); B[4+kt]=LD(h8,bd+64+512*(4+kt)); if(kt==0) A[5]=BC(h8,c.AE); if(kt==1) A[4]=ld4w(c.x0-384); if(kt==2) A[3]=ld4w(c.x0+64); if(kt==3) A[2]=ld4w(c.x0+576); SCHED(); }
    #pragma unroll
    for(int dl=0;dl<8;dl++){ if(dl<4) acc[dl+4]=MH(A[5],B[dl],acc[dl+4]); SCHED(); if(dl<6) acc[dl+2]=MH(A[4],B[dl],acc[dl+2]); SCHED(); if(dl<7) acc[dl+1]=MH(A[1],B[dl],acc[dl+1]); SCHED(); acc[dl]=MH(A[0],B[dl],acc[dl]);
      SCHED(); B[dl]=LD(h8,bd+512*dl); SCHED(); }
    #pragma unroll
    for(int dl=0;dl<8;dl++){ if(dl<7) acc[dl+1]=MH(A[3],B[dl],acc[dl+1]); SCHED(); acc[dl]=MH(A[2],B[dl],acc[dl]); SCHED(); RW(dl-2) } }
  if(ROLE==3){
    h8 U[12];
    #pragma unroll
    for(int kt=0;kt<6;kt++) U[kt]=LD(h8,bd+256*kt);
    f32x4 ce[2],co[2]; h8 Z[2];
    ce[0]=MH(U[0],Be,Z4); co[0]=MH(U[0],Bo,Z4); ce[1]=MH(U[1],Be,Z4); co[1]=MH(U[1],Bo,Z4); Z[0]=zpack(ce[0],co[0]);
    #pragma unroll
    for(int kt=0;kt<12;kt++){
      SCHED();
      if(kt+2<12){ ce[kt&1]=MH(U[kt+2],Be,Z4); co[kt&1]=MH(U[kt+2],Bo,Z4); }
      if(kt+1<12) Z[(kt+1)&1]=zpack(ce[(kt+1)&1],co[(kt+1)&1]);
      #pragma unroll
      for(int rho=-1;rho<5;rho++){ int j=(kt+4+rho)>>1; if(((kt+rho)&1)==0&&j>=2&&j<8) acc[j]=MH(Z[kt&1],A[rho+1],acc[j]); }
      if(kt<6){ SCHED(); U[kt+6]=LD(h8,bd+256*(kt+6)); }
      if(kt>=3&&kt<=9&&(kt&1)){ SCHED(); RW((kt+1)/2) } }
  }
  STAMP(6);
  PRIO(TPR);
  RW(6) RW(7)
  asm volatile("s_mov_b64 exec, 1\n\tds_add_u32 %0, %1\n\ts_mov_b64 exec, -1" :: "v"(ca),"v"(1) : "memory");
  { float s0[4], s1[4];
    f32x4 o0=acc[2*ROLE], o1=acc[2*ROLE+1];
    #define RD6 "ds_read_b128 v[112:115], %[rp] offset:%[o]\n\tds_read_b128 v[116:119], %[rp] offset:%[o]+1024\n\tds_read_b128 v[120:123], %[rp] offset:%[o]+2048\n\tds_read_b128 v[124:127], %[rp] offset:%[o]+3072\n\t.if %[o]\n\tds_read_b128 v[104:107], %[rp] offset:%[o]+4096\n\tds_read_b128 v[108:111], %[rp] offset:%[o]+5120\n\t.endif\n\t"
    #define MU4(p) "v_mul_f32 %[" #p "0], %[sc], %[i" #p "0]\n\tv_mul_f32 %[" #p "1], %[sc], %[i" #p "1]\n\tv_mul_f32 %[" #p "2], %[sc], %[i" #p "2]\n\tv_mul_f32 %[" #p "3], %[sc], %[i" #p "3]\n\t"
    #define FM4(p,r,k) "s_waitcnt lgkmcnt(%[n]-" #k ")\n\tv_fmac_f32 %[" #p "0], %[sc], v[" #r "]\n\tv_fmac_f32 %[" #p "1], %[sc], v[" #r "+1]\n\tv_fmac_f32 %[" #p "2], %[sc], v[" #r "+2]\n\tv_fmac_f32 %[" #p "3], %[sc], v[" #r "+3]\n\t"
    { u32 cnt,sv; asm volatile("ds_read_b32 v103, %[ca]\n\t" RLA MU4(a) MU4(b) "s_mov_b32 %[cnt], 0x20000\n\t" RLB
        "1:\n\tds_read_b32 v103, %[ca]\n4:\n\ts_waitcnt lgkmcnt(0)\n\tv_readfirstlane_b32 %[sv], v103\n\ts_cmp_ge_u32 %[sv], 11\n\ts_cbranch_scc1 2f\n\ts_sleep " STR(LKS) "\n\ts_sub_u32 %[cnt], %[cnt], 1\n\ts_cbranch_scc0 1b\n2:\n\t" RD6 "3:\n\t"
        FM4(a,112,1) FM4(b,116,2) FM4(a,120,3) FM4(b,124,4) ".if %[o]\n\t" FM4(a,104,5) FM4(b,108,6) ".endif"
        : [cnt]"=&s"(cnt),[sv]"=&s"(sv),[a0]"=&v"(s0[0]),[a1]"=&v"(s0[1]),[a2]"=&v"(s0[2]),[a3]"=&v"(s0[3]),[b0]"=&v"(s1[0]),[b1]"=&v"(s1[1]),[b2]"=&v"(s1[2]),[b3]"=&v"(s1[3])
        : [ia0]"v"(o0[0]),[ia1]"v"(o0[1]),[ia2]"v"(o0[2]),[ia3]"v"(o0[3]),[ib0]"v"(o1[0]),[ib1]"v"(o1[1]),[ib2]"v"(o1[2]),[ib3]"v"(o1[3]),[ca]"v"(ca),[rp]"v"(c.wp),[sc]"v"(sc),[o]"i"(ROLE*6144),[n]"i"(ROLE==0? 4:6) : "memory","scc","v103","v104","v105","v106","v107","v108","v109","v110","v111"); }
    #define NV(v) umin_(v,nvm)
    auto r0=__builtin_amdgcn_permlane16_swap(NV(pk(s0[0],s0[1])),NV(pk(s1[0],s1[1])),false,false); auto r1=__builtin_amdgcn_permlane16_swap(NV(pk(s0[2],s0[3])),NV(pk(s1[2],s1[3])),false,false);
    asm volatile("buffer_store_dwordx4 %0, %1, %2, 0 offen offset:%3 sc1\n\ts_nop 1" :: "v"(u32x4{r0[0],r1[0],r0[1],r1[1]}),"v"(c.yo),"s"(rsY),"i"(ROLE*1024) : "memory"); }
  garrive(sm,C_E);
  STAMP(9);
  SFLUSH();
  asm volatile("s_endpgm" : "=v"(fa),"=v"(fb),"=v"(c.x0),"=v"(c.x1),"=v"(c.bd),"=v"(c.p0),"=v"(c.p1),"=v"(c.p2),"=v"(c.wp),"=v"(c.Be),"=v"(c.Bo),"=v"(c.AE),"=v"(c.yo) :: "memory");
}
DEV void pf2(int e,int xg,const u16* wd){ LANE(ln); u32 q=e*64+ln, r=(q*21846u)>>16, c=q-3u*r;
  asm volatile("buffer_load_dword v123, %0, %1, 0 offen" :: "v"((576*xg+288*(r&1)+287-(r>>1))*(D_*2)+384*c+256),"s"(mkrs(wd,D_*F_*2)) : "memory"); }
#define GETEP asm volatile("s_waitcnt lgkmcnt(0)\n\ts_mov_b64 %0, s[96:97]\n\ts_mov_b32 %1, s98" : "=s"(ep),"=s"(ldw));
DEV void chan(lch* sm,int grp,int w4,int d,int one_,char* ws,u32x4 q0,u32x4 q1,u32x4 q2,u32x4 q3,u32x4 q4 SBARG){
  lch* cb=sm+O_CH+grp*SLSZ; int role=w4, emax;
  #define LP(v) ((lch*)(size_t)(u32)(v))
  const lch* fa=LP(q4[0]); const lch* fb=LP(q4[1]);
  u64 tag; CP c;
  { float a2,bp,br,bi,cr,ci;
    u32 fq=q3[2], fr=q3[3], one=q4[2]; float f64=BC(float,fq), f128=BC(float,fr);
    lch *t8[4]={LP(q0[0]),LP(q0[1]),LP(q0[2]),LP(q0[3])}, *tB[4]={LP(q1[0]),LP(q1[1]),LP(q1[2]),LP(q1[3])}, *t4[2]={LP(q2[0]),LP(q2[1])}, *t2[2]={LP(q2[2]),LP(q2[3])}, *t1=LP(q3[0]), *te=LP(q3[1]);
    if(HO){ LANE(ln); asm volatile("" : "=v"(c.x1),"=v"(c.p0),"=v"(c.p1),"=v"(c.p2),"=v"(c.Be),"=v"(c.Bo),"=v"(c.AE));
      if(role==0){ lanes<0>(c,cb,ln); lanes2<0>(c,cb,ln); } else { if(role==1) lanes<1>(c,cb,ln); else if(role==2) lanes<2>(c,cb,ln); else lanes<3>(c,cb,ln); lanes2<1>(c,cb,ln); }
      asm volatile("" : "+v"(c.x0),"+v"(c.x1),"+v"(c.bd),"+v"(c.p0),"+v"(c.p1),"+v"(c.p2),"+v"(c.wp),"+v"(c.Be),"+v"(c.Bo),"+v"(c.AE),"+v"(c.yo)); }
    if(grp!=0) PRIO(GPR);
    STAMP(7);
    u64 ep; u32 ldw; GETEP tag=ep+1ull;
    float dt=rb(expf_((d&1)? bhi(ldw):blo(ldw))), ca=dt*1.4426950408889634f, cb_=dt*0.15915494309189535f;
    asm volatile("s_waitcnt vmcnt(1)\n\t" "v_mul_f32 %2, %8, v114\n\tv_mul_f32 %3, %8, v115\n\tv_mul_f32 %0, %6, v112\n\tv_mul_f32 %1, %7, v113\n\tv_mov_b32 %4, v116\n\tv_mov_b32 %5, v117"
      : "=&v"(a2),"=&v"(bp),"=&v"(br),"=&v"(bi),"=&v"(cr),"=&v"(ci) : "v"(ca),"v"(cb_),"v"(dt) : "memory");
    STAMP(1);
    C2 W; W.r=cr*br-ci*bi; W.i=cr*bi+ci*br;
    u32 mx=BC(u32,__builtin_fmaxf(__builtin_fabsf(W.r),__builtin_fabsf(W.i)));
    #define MXS(C) mx=umax_(mx,dppu<C>(mx));
    float e8=a2*8.f, eB=__builtin_fmaf(a2,f64,14.f), p8=bp*8.f, pB=bp*f64; SCHED();
    MXS(0xB1) p8=__builtin_amdgcn_fractf(p8); pB=__builtin_amdgcn_fractf(pB); e8=__builtin_amdgcn_exp2f(e8); SCHED();
    MXS(0x4E) eB=__builtin_amdgcn_exp2f(eB); float c8=__builtin_amdgcn_cosf(p8); SCHED();
    MXS(0x141) float s8=__builtin_amdgcn_sinf(p8), cB=__builtin_amdgcn_cosf(pB); SCHED();
    float sB=__builtin_amdgcn_sinf(pB); PIN(sB); MXS(0x140) float e1=__builtin_amdgcn_exp2f(a2); PIN(e1); SCHED();
    asm volatile("s_mov_b64 exec, 0x10001\n\tds_max_u32 %0, %1\n\tds_add_u32 %0, %2 offset:4\n\ts_mov_b64 exec, -1" :: "v"(te),"v"(mx),"v"(one) : "memory");
    C3 z8; z8.r=e8*c8; z8.n=e8*s8; z8.i=-z8.n; PIN(z8.i); C2 B; B.r=eB*cB; B.i=-(eB*sB);
    float p1=__builtin_amdgcn_fractf(bp), pE=__builtin_amdgcn_fractf(bp*256.f), pV=__builtin_amdgcn_fractf(bp*f128), eE=a2*256.f, eV=a2*f128, tr[8];
    #pragma unroll
    for(int k=0;k<4;k++){ SCHED(); u32 h0=ph(B); B=cm(B,z8);
      if(k==0){ tr[0]=__builtin_amdgcn_cosf(pV); tr[1]=__builtin_amdgcn_sinf(pV); } if(k==1){ tr[2]=__builtin_amdgcn_exp2f(eV); tr[3]=__builtin_amdgcn_cosf(p1); }
      if(k==2){ tr[4]=__builtin_amdgcn_sinf(p1); tr[5]=__builtin_amdgcn_exp2f(eE); } if(k==3){ tr[6]=__builtin_amdgcn_cosf(pE); tr[7]=__builtin_amdgcn_sinf(pE); }
      PIN(tr[2*k]); PIN(tr[2*k+1]);
      u32 h1=ph(B); if(k<3) B=cm(B,z8); W2(tB[k],h0,h1); }
    asm volatile("ds_read_b64 v[118:119], %0" :: "v"(te) : "memory");
    SCHED();
    C3 z1=c3(C2{e1*tr[3],e1*tr[4]}), E=c3(C2{tr[5]*tr[6],tr[5]*tr[7]}); C2 V[8]; V[0]=cmul(W,C2{tr[2]*tr[0],tr[2]*tr[1]});
    { u32 cnt,n; PIN(V[0].r); PIN(V[0].i); PIN(z1.r); PIN(E.r);
      asm volatile("s_mov_b32 %1, 0x20000\n1:\n\ts_waitcnt lgkmcnt(0)\n\tv_readfirstlane_b32 %2, v119\n\tv_readfirstlane_b32 %0, v118\n\ts_cmp_eq_u32 %2, 8\n\ts_cbranch_scc1 2f\n\tds_read_b64 v[118:119], %3\n\ts_sub_u32 %1, %1, 1\n\ts_cbranch_scc0 1b\n2:"
        : "=&s"(emax),"=&s"(cnt),"=&s"(n) : "v"(te) : "memory","scc");
      emax=RFL(emax); emax=(emax>>23)-126; }
    V[0].r=__builtin_amdgcn_ldexpf(V[0].r,14-emax); V[0].i=__builtin_amdgcn_ldexpf(V[0].i,14-emax);
    C2 Y=cm(V[0],E), Y4; u32 y0=ph(Y);
    V[1]=cm(V[0],z1); W2(t8[0],ph(V[0]),ph(V[1])); Y=cm(Y,E); u32 y1=ph(Y);
    V[2]=cm(V[1],z1); Y=cm(Y,E); u32 y2=ph(Y);
    V[3]=cm(V[2],z1); W2(t8[1],ph(V[2]),ph(V[3])); W2(t4[0],y0,ph(cm(V[2],E)));
    V[4]=cm(V[3],z1); Y=cm(Y,E); ST(u32,t1)=ph(Y);
    V[5]=cm(V[4],z1); W2(t8[2],ph(V[4]),ph(V[5])); Y4=cm(V[4],E); u32 y4=ph(Y4);
    V[6]=cm(V[5],z1); Y=cm(Y,E); ST(u32,t1+2*TROW)=ph(Y);
    V[7]=cm(V[6],z1); W2(t8[3],ph(V[6]),ph(V[7])); Y4=cm(Y4,E); W2(t2[0],y1,ph(Y4));
    W2(t4[1],y4,ph(cm(V[6],E))); Y=cm(Y,E); ST(u32,t1+4*TROW)=ph(Y);
    Y4=cm(Y4,E); W2(t2[1],y2,ph(Y4)); Y=cm(Y,E); ST(u32,t1+6*TROW)=ph(Y);
    Y=cm(Y,E); asm volatile("s_mov_b32 exec_hi, 0\n\tds_write_b32 %0, %1 offset:4224\n\ts_mov_b32 exec_hi, -1" :: "v"(t1),"v"(ph(Y)) : "memory");
  }
  STAMP(8);
  if(one_) PRIO(OPR);
  DRAIN();
  gsync(sm,C_G+grp,4); STAMP(2);
  if(role==0) chan2<0>(c,sm,cb,fa,fb,grp,d,one_,emax,tag,ws SBPASS);
  if(role==1) chan2<1>(c,sm,cb,fa,fb,grp,d,one_,emax,tag,ws SBPASS);
  if(role==2) chan2<2>(c,sm,cb,fa,fb,grp,d,one_,emax,tag,ws SBPASS);
  chan2<3>(c,sm,cb,fa,fb,grp,d,one_,emax,tag,ws SBPASS);
}

struct XR { int so,x0,x2; u32x4 rx,q[4]; };
DEV void xreq(XR& X,char* ws){ asm volatile("global_load_dwordx2 v[108:109], %[so], %[ws] sc1\n\tglobal_load_dwordx2 v[110:111], %[so], %[ws] offset:128 sc1\n\tbuffer_load_dwordx4 %[a], %[x0], %[rx], 0 offen\n\tbuffer_load_dwordx4 %[b], %[x0], %[rx], %[s1] offen\n\tbuffer_load_dwordx4 %[c], %[x2], %[rx], 0 offen"
  : [a]"=&{v[96:99]}"(X.q[0]),[b]"=&{v[100:103]}"(X.q[1]),[c]"=&{v[104:107]}"(X.q[2]),"=&{v[108:111]}"(X.q[3]) : [so]"v"(X.so),[ws]"s"(ws),[x0]"v"(X.x0),[x2]"v"(X.x2),[rx]"s"(X.rx),[s1]"s"(16*D_*2) : "memory"); }
DEV u32 med3u(u32 a,u32 b,u32 c){ u32 r; asm("v_med3_u32 %0, %1, %2, %3" : "=v"(r) : "v"(a),"v"(b),"v"(c)); return r; }
DEV u32 max3u(u32 a,u32 b,u32 c){ return umax_(umax_(a,b),c); }
#define GTC(r,s) "v_cmp_gt_u32 %[" #s "], v" #r ", %[c]\n\t"
#define GTA(s) "v_addc_co_u32 %[rk], vcc, 0, %[rk], %[" #s "]\n\t"
#define GT4(a,b,c,d) GTC(a,s0) GTC(b,s1) GTC(c,s2) GTC(d,s3) GTA(s0) GTA(s1) GTA(s2) GTA(s3)
#define GTS [s0]"=&s"(q0),[s1]"=&s"(q1),[s2]"=&s"(q2),[s3]"=&s"(q3)
DEV void top8(lch* sm,int w4,int ln,u32 M,char* ws,u32& rank_,u32& key_,XR& X SBARG){
  u32 k[8], rc[8], hm=0xffff0000u; u64 em=0x0001000100010001ull,q0,q1,q2,q3;
  u32x4 rs=mkrs(ws+W_RG+(BID&(NRG-1))*8192,8192); int ro=w4*1024+ln*16;
  #pragma unroll
  for(int e=0;e<8;e++){ rc[e]=(u32)(0xfff-(w4*256+ln*4+(e&3)+(e>>2)*1024)); PIN(rc[e]); }
  lch *aW=sm+O_RMX+(w4*4+(ln>>4))*4, *aC=sm+O_RMX+(ln>>2)*4, *aO=sm+O_RMX+(ln&3)*16, *dC=sm+O_CND+(ln>>1)*4, *dO=sm+O_CND+(ln&1)*64; PIN(aW); PIN(aC); PIN(aO); PIN(dC); PIN(dO); PIN(ro); PIN(ln);
  asm volatile("" : "+s"(hm),"+s"(em),"+s"(rs));
  gwait<2>(sm,C_G,RGATE);
  { u32 cnt;
    #define KL(e,r) "v_lshl_or_b32 %[k" #e "], v" #r ", 16, %[k" #e "]\n\t"
    #define KH(e,r) "v_and_or_b32 %[k" #e "], v" #r ", %[hm], %[k" #e "]\n\t"
    #define CHK_R "v_bitop3_b32 %[t0], v113, v115, %[M] bitop3:0x7e\n\tv_bitop3_b32 %[t1], v117, v119, %[M] bitop3:0x7e\n\tv_or_b32 %[t0], %[t0], %[t1]\n\tv_cmp_ne_u32 vcc, 0, %[t0]\n\t"
    u32 t0,t1;
    asm volatile(LD_DUP2
      "s_sleep " STR(R2D) "\n\tbuffer_load_dwordx4 v[120:123], %[of], %[rs], 0 offen sc1\n\tbuffer_load_dwordx4 v[124:127], %[of], %[rs], %[so] offen sc1\n\ts_mov_b32 %[cnt], 0x20000\n\ts_waitcnt vmcnt(2)\n\t" CHK_R "s_cbranch_vccz 9f\n\t"
      "s_waitcnt vmcnt(0)\n\tv_mov_b64 v[112:113], v[120:121]\n\tv_mov_b64 v[114:115], v[122:123]\n\tv_mov_b64 v[116:117], v[124:125]\n\tv_mov_b64 v[118:119], v[126:127]\n\t"
      POLLASM(LD_DUP2,CHK_R,PSLP)
      KL(0,112) KH(1,112) KL(2,114) KH(3,114) KL(4,116) KH(5,116) KL(6,118) KH(7,118)
      : [k0]"+v"(rc[0]),[k1]"+v"(rc[1]),[k2]"+v"(rc[2]),[k3]"+v"(rc[3]),[k4]"+v"(rc[4]),[k5]"+v"(rc[5]),[k6]"+v"(rc[6]),[k7]"+v"(rc[7]),[cnt]"=&s"(cnt),[t0]"=&v"(t0),[t1]"=&v"(t1) : [of]"v"(ro),[rs]"s"(rs),[so]"s"(4096),[M]"s"(M),[hm]"s"(hm) : PCLOB); RCNT(9,cnt); }
  STAMP(3); if(UE) garrive(sm,C_RS);
  #pragma unroll
  for(int e=0;e<8;e++) k[e]=rc[e];
  u32 h=max3u(k[0],k[1],k[2]), m1=max3u(max3u(k[3],k[4],k[5]),max3u(k[6],k[7],h),h), mx=m1, l=med3u(k[0],k[1],k[2]), t=umin_(umin_(k[0],k[1]),k[2]);
  SCHED();
  #define INS(e) t=med3u(t,l,k[e]); l=med3u(h,l,k[e]); if(e<7) h=umax_(h,k[e]); PIN(t); PIN(l); SCHED();
  mx=umax_(mx,dppu<0xB1>(mx)); INS(3) mx=umax_(mx,dppu<0x4E>(mx)); INS(4) mx=umax_(mx,dppu<0x141>(mx)); INS(5) mx=umax_(mx,dppu<0x140>(mx));
  asm volatile("s_mov_b64 exec, %2\n\tds_write_b32 %0, %1\n\ts_mov_b64 exec, -1\n\tds_read_b32 v116, %3\n\tds_read_b128 v[112:115], %4" :: "v"(aW),"v"(mx),"s"(em),"v"(aC),"v"(aO) : "memory");
  if(XB==2) xreq(X,ws);
  INS(6) INS(7)
  u32 m2=l, m3=t, T;
  { u32 c,rk,cnt;
    asm volatile("s_mov_b32 %[cnt], 0x20000\n1:\n\ts_waitcnt lgkmcnt(0)\n\tv_cmp_eq_u32 vcc, 0, v116\n\ts_cbranch_vccz 2f\n\tds_read_b32 v116, %[aC]\n\tds_read_b128 v[112:115], %[aO]\n\ts_sub_u32 %[cnt], %[cnt], 1\n\ts_cbranch_scc0 1b\n2:\n\t"
      "v_mov_b32 %[c], v116\n\tv_mov_b32 %[rk], 0\n\t" GT4(112,113,114,115) "s_nop 1\n\tv_add_u32_dpp %[rk], %[rk], %[rk] quad_perm:[1,0,3,2] row_mask:0xf bank_mask:0xf\n\ts_nop 1\n\tv_add_u32_dpp %[rk], %[rk], %[rk] quad_perm:[2,3,0,1] row_mask:0xf bank_mask:0xf\n\t"
      "v_cmp_eq_u32 vcc, 7, %[rk]\n\ts_ff1_i32_b64 %[cnt], vcc\n\ts_nop 0\n\tv_readlane_b32 %[T], %[c], %[cnt]\n\ts_nop 1"
      : [c]"=&v"(c),[rk]"=&v"(rk),[cnt]"=&s"(cnt),[T]"=&s"(T),GTS : [aC]"v"(aC),[aO]"v"(aO) : PCLOB); T=RFL(T); }
  { u64 b1=BALLOT(m1>=T), b2=BALLOT(m2>=T), b3=BALLOT(m3>=T); int n1=__builtin_popcountll(b1), n2=n1+__builtin_popcountll(b2);
    lch* cw=sm+O_CND+w4*32;
    if(__builtin_expect(b3==0ull&&n2<=8,1)){
      int p1=(int)__builtin_amdgcn_mbcnt_hi((u32)(b1>>32),__builtin_amdgcn_mbcnt_lo((u32)b1,0u)), pf=n2+ln-p1; bool c1=m1>=T;
      if(__builtin_expect(b2!=0ull,0)){ int p2=(int)__builtin_amdgcn_mbcnt_hi((u32)(b2>>32),__builtin_amdgcn_mbcnt_lo((u32)b2,(u32)n1)); if(m2>=T) ST(u32,cw+p2*4)=m2; }
      int ps= c1? p1:pf; ps= ps<8? ps:32-8*w4; ST(u32,cw+ps*4)= c1? m1:1u;
    } else {
      u32 mine=0;
      _Pragma("unroll 1") for(int c=0;c<8;c++){ u32 lm=umax_(umax_(umax_(k[0],k[1]),umax_(k[2],k[3])),umax_(umax_(k[4],k[5]),umax_(k[6],k[7]))); u32 wm=wmax(lm); if(ln==c) mine=wm;
        #pragma unroll
        for(int e=0;e<8;e++) k[e]=(k[e]==wm)?0u:k[e]; }
      if(ln<8) ST(u32,cw+ln*4)=mine; } }
  { u32 c,rk,cnt;
    asm volatile("s_mov_b32 %[cnt], 0x20000\n\tv_mov_b32 %[rk], 0\n\ts_waitcnt vmcnt(" XBW ")\n1:\n\tds_read_b32 %[c], %[dC]\n\tds_read_b128 v[112:115], %[dO]\n\tds_read_b128 v[116:119], %[dO] offset:16\n\tds_read_b128 v[120:123], %[dO] offset:32\n\tds_read_b128 v[124:127], %[dO] offset:48\n\t"
      "s_waitcnt lgkmcnt(4)\n\tv_cmp_eq_u32 vcc, 0, %[c]\n\ts_cbranch_vccz 2f\n\ts_sub_u32 %[cnt], %[cnt], 1\n\ts_cbranch_scc0 1b\n2:\n\t"
      "s_waitcnt lgkmcnt(3)\n\t" GT4(112,113,114,115) "s_waitcnt lgkmcnt(2)\n\t" GT4(116,117,118,119) "s_waitcnt lgkmcnt(1)\n\t" GT4(120,121,122,123) "s_waitcnt lgkmcnt(0)\n\t" GT4(124,125,126,127)
      "s_nop 1\n\tv_add_u32_dpp %[rk], %[rk], %[rk] quad_perm:[1,0,3,2] row_mask:0xf bank_mask:0xf"
      : [c]"=&v"(c),[rk]"=&v"(rk),[cnt]"=&s"(cnt),GTS : [dC]"v"(dC),[dO]"v"(dO) : PCLOB);
    rank_=rk; key_=c; }
}

#define FILL_(n,WAIT) "s_mov_b32 m0, %[ld]\n\ts_mov_b32 %[c], " STR(n) "-1\n3:\n\t" WAIT "buffer_load_dwordx4 %[vo], %[rs], %[so] offen nt lds\n\ts_add_u32 m0, m0, 0x1000\n\ts_add_u32 %[so], %[so], %[st]\n\t" \
  "s_sub_u32 %[c], %[c], 1\n\ts_cbranch_scc0 3b\n\ts_mov_b32 %[ld], m0\n\t"
#define FILL(n,WAIT) do{ u32 c_; asm volatile(FILL_(n,WAIT) : [c]"=&s"(c_),[so]"+s"(wso),[ld]"+s"(wld) : [vo]"v"(wvo),[rs]"s"(wrs),[st]"s"(wst) : "memory","scc","m0"); }while(0)
#define FILLD(n,WN,WW_) do{ u32 c_,t_,s_; asm volatile(FILL_(n,"ds_read_b32 %[t], %[ad]\n\ts_waitcnt lgkmcnt(0)\n\tv_readfirstlane_b32 %[s], %[t]\n\ts_cmp_ge_u32 %[s], 4\n\ts_cbranch_scc1 4f\n\t" VW(WN) "4:\n\t" VW(WW_)) \
  : [c]"=&s"(c_),[so]"+s"(wso),[ld]"+s"(wld),[t]"=&v"(t_),[s]"=&s"(s_) : [vo]"v"(wvo),[rs]"s"(wrs),[st]"s"(wst),[ad]"v"(C_D*4) : "memory","scc","m0"); wso=RFL(wso); wld=RFL(wld); }while(0)
#define VW(n) "s_waitcnt vmcnt(" STR(n) ")\n\t"

__global__ NOPK __attribute__((amdgpu_num_vgpr(56),amdgpu_num_sgpr(102))) __launch_bounds__(1024,4) void kA(const u16* x_,char* ws_,const u16* wr_,const u16* Dsk_,const u16* wg_,const u16* wu_,const u16* wd_,u16* out_,KP kp){
  const u16 *x,*Dsk,*wg,*wu,*wd; char* ws; u16* out;
  __shared__ __attribute__((aligned(16))) char smem[LDS_TOTAL];
  lch* sm=(lch*)smem;
  unsigned long long tearly_; asm volatile("s_memrealtime %0" : "={s[48:49]}"(tearly_)); __builtin_amdgcn_sched_barrier(0);
  u32x4 xa,xb_,wa,wb,dq[3],la,lb_; u32x2 lc; u32 xc,wc;
  #define SARR "v_mov_b32 v121, 80\n\tv_mov_b32 v122, 1\n\ts_mov_b64 exec, 1\n\tds_add_u32 v121, v122\n\ts_mov_b64 exec, -1\n\t"
  asm volatile(
    "v_readfirstlane_b32 s60, %[t]\n\tv_lshlrev_b32 v119, 1, %[t]\n\tv_mov_b32 v118, 0\n\ts_cmpk_ge_u32 s60, 0x100\n\ts_cbranch_scc1 5f\n\tds_write_b16 v119, v118\n\ts_waitcnt lgkmcnt(0)\n5:\n\t"
    "s_load_dwordx16 s[64:79], %[ka], 0x0\n\ts_load_dwordx16 s[80:95], %[ka], 0x40\n\t"
    "s_and_b32 s61, %[wg], 7\n\ts_lshr_b32 s62, %[wg], 3\n\tv_and_b32 v118, 31, %[t]\n\ts_bfe_u32 s63, s60, 0x20006\n\ts_cmpk_ge_u32 s60, 0x100\n\ts_cbranch_scc1 1f\n\t"
    "s_barrier\n\ts_setprio 3\n\ts_lshl_b32 s56, s61, 5\n\ts_add_u32 s56, s56, s62\n\ts_mulk_i32 s56, 0x2400\n\tv_lshrrev_b32 v119, 5, %[t]\n\tv_mul_u32_u24 v119, 0x480, v119\n\tv_add_u32 v119, s56, v119\n\t"
    "v_lshl_add_u32 v116, v118, 4, v119\n\tv_lshl_add_u32 v117, v118, 2, v119\n\tv_lshlrev_b32 v114, 4, v118\n\tv_lshlrev_b32 v115, 2, v118\n\t"
    "s_and_b32 s57, s62, 3\n\ts_lshl2_add_u32 s57, s57, s63\n\ts_lshl_b32 s57, s57, 6\n\ts_and_b32 s58, s62, 1\n\ts_lshl_b32 s58, s58, 6\n\tv_and_b32 v112, 3, %[t]\n\tv_lshl_add_u32 v113, v112, 4, s57\n\tv_lshl_add_u32 v112, v112, 4, s58\n\t"
    "s_waitcnt lgkmcnt(0)\n\t"
    "s_memrealtime s[98:99]\n\ts_load_dwordx2 s[96:97], s[66:67], 0x0\n\t"
    "global_load_dwordx4 %[wa], v114, s[68:69]\n\tglobal_load_dwordx4 %[wb], v114, s[68:69] offset:512\n\tglobal_load_dword %[wc], v115, s[68:69] offset:1024\n\t"
    "global_load_dwordx4 %[xa], v116, s[64:65]\n\tglobal_load_dwordx4 %[xb], v116, s[64:65] offset:512\n\tglobal_load_dword %[xc], v117, s[64:65] offset:1024\n\t"
    "global_load_dwordx4 %[d0], v113, s[70:71]\n\tglobal_load_dwordx4 %[d1], v112, s[70:71] offset:1024\n\t" SARR
    "s_branch 8f\n"
    "1:\n\t" "s_cmp_ge_u32 s62, " STR(CPF) "\n\ts_cbranch_scc1 6f\n\ts_getpc_b64 s[56:57]\n.Lkpc:\n\ts_and_b32 s58, s62, 3\n\ts_lshl_b32 s58, s58, 13\n\ts_sub_u32 s58, s58, 0x1000\n\tv_lshl_add_u32 v119, %[t], 4, s58\n\tv_min_u32 v119, .Lkend-.Lkpc-16, v119\n\t"
    "global_load_dword v120, v119, s[56:57]\n\t.subsection 1\n.Lkend:\n\t.subsection 0\n6:\n\ts_barrier\n\t"
    "v_mov_b32 v124, -1\n\tv_mov_b32 v125, -1\n\tv_mov_b32 v126, -1\n\tv_mov_b32 v127, -1\n\tv_lshlrev_b32 v119, 4, %[t]\n\tv_and_b32 v119, 0xff0, v119\n\t"
    "s_lshr_b32 s59, s60, 8\n\ts_cmp_lt_u32 s62, 8\n\ts_cselect_b32 s58, 2, 1\n\ts_cbranch_scc1 2f\n\ts_cmp_eq_u32 s59, 3\n\ts_cbranch_scc1 4f\n"
    "2:\n\ts_sub_u32 s57, s59, 2\n\ts_cmp_eq_u32 s59, 1\n\ts_cselect_b32 s58, s58, s57\n\ts_cmp_lt_u32 s62, 8\n\ts_cselect_b32 s56, 0, 8\n\ts_lshl1_add_u32 s56, s62, s56\n\ts_mulk_i32 s61, 72\n\ts_add_u32 s51, s56, s61\n\ts_sub_u32 s56, 2, s59\n\ts_sub_u32 s57, 16, s62\n\ts_cmp_eq_u32 s59, 3\n\ts_cselect_b32 s50, s57, s56\n\ts_add_u32 s56, s50, s51\n\t"
    "v_mov_b32 v112, 0\n\tv_mov_b32 v113, 0\n\tv_mov_b32 v114, 0\n\tv_mov_b32 v115, 0\n\tv_mov_b32 v116, 0\n\tv_mov_b32 v117, 0\n\ts_setprio 2\n\t"
    "s_lshl_b32 s57, s56, 2\n\ts_add_u32 s57, s57, s63\n\ts_lshl_b32 s57, s57, 6\n\tv_lshl_add_u32 v22, v118, 1, s57\n\ts_mul_i32 s57, s56, %[yst]\n\ts_add_u32 s57, s57, %[wy]\n\tv_add_u32 v23, s57, v119\n\ts_lshl_b32 s56, s56, 1\n\ts_and_b32 s56, s56, -4\n\t"
    "s_waitcnt lgkmcnt(0)\n\t"
    "global_load_short_d16_hi v112, v22, s[80:81] nt\n\tglobal_load_short_d16_hi v113, v22, s[82:83] nt\n\tglobal_load_short_d16_hi v114, v22, s[84:85] nt\n\t"
    "global_load_short_d16_hi v115, v22, s[86:87] nt\n\tglobal_load_short_d16_hi v116, v22, s[88:89] nt\n\tglobal_load_short_d16_hi v117, v22, s[90:91] nt\n\t"
    "s_load_dwordx2 s[96:97], s[66:67], 0x0\n\ts_load_dword s98, s[92:93], s56\n\t"
    SARR
    "s_mul_i32 s52, s58, %[sl]\n\ts_add_u32 s52, s52, %[tb]\n\ts_lshl_b32 s53, s63, 7\n\ts_add_u32 s53, s53, s52\n\tv_lshl_add_u32 v30, v118, 2, s53\n\tv_bfe_u32 v31, %[t], 5, 1\n\ts_movk_i32 s54, 528\n\tv_mul_u32_u24 v29, s54, v31\n\t"
    "v_lshl_add_u32 v2, v29, 3, v30\n\ts_movk_i32 s53, 1056\n\tv_add_u32 v3, s53, v2\n\tv_add_u32 v4, s53, v3\n\tv_add_u32 v5, s53, v4\n\tv_add_u32 v6, 41*528, v2\n\tv_add_u32 v7, s53, v6\n\tv_add_u32 v8, s53, v7\n\tv_add_u32 v9, s53, v8\n\t"
    "v_lshl_add_u32 v10, v29, 2, v30\n\tv_add_u32 v10, 16*528, v10\n\tv_add_u32 v11, s53, v10\n\tv_lshl_add_u32 v12, v29, 1, v30\n\tv_add_u32 v12, 24*528, v12\n\tv_add_u32 v13, 2112, v12\n\tv_add_u32 v14, v29, v30\n\tv_add_u32 v14, 32*528, v14\n\t"
    "s_lshl_b32 s53, s58, 4\n\ts_add_u32 s53, s53, %[ex]\n\tv_mov_b32 v15, s53\n\tv_cvt_f32_ubyte0 v31, v31\n\tv_mul_f32 v16, 0x42800000, v31\n\tv_add_f32 v17, v16, v16\n\tv_mov_b32 v20, 1\n\t"
    "v_and_b32 v29, 15, %[t]\n\tv_and_b32 v30, 48, %[t]\n\tv_mad_u32_u24 v30, v29, s54, v30\n\ts_lshl_b32 s55, s59, 1\n\ts_lshr_b32 s55, 0x48, s55\n\ts_add_u32 s55, s55, s63\n\ts_and_b32 s55, s55, 3\n\ts_mul_i32 s53, s55, 16*528\n\ts_movk_i32 s57, 41*528\n\ts_cmp_eq_u32 s55, 2\n\ts_cselect_b32 s53, s57, s53\n\ts_cselect_b32 s57, 32*528, s57\n\t"
    "s_add_u32 s53, s53, s52\n\ts_add_u32 s57, s57, s52\n\tv_add_u32 v18, s53, v30\n\tv_add_u32 v19, s57, v30\n\t"
    "v_min_u32 v29, 237*16, v119\n\tv_subrev_u32 v30, 204*16, v29\n\tv_add_u32 v29, %[de], v29\n\tv_min_u32 v29, v29, v30\n\ts_sub_u32 s53, s52, 4640\n\tv_add_u32 v29, s53, v29\n\tv_mov_b32 v24, 0\n\tv_mov_b32 v25, 0\n\tv_mov_b32 v26, 0\n\tv_mov_b32 v27, 0\n\tds_write_b128 v29, v[24:27]\n\t"
    "s_cmp_lg_u32 s55, 3\n\ts_cbranch_scc1 10f\n\t"
    "v_bfe_u32 v18, %[t], 1, 5\n\tv_and_b32 v19, 1, %[t]\n\ts_cmp_lt_u32 s62, 8\n\ts_cselect_b32 s53, 2, 1\n\ts_mul_i32 s53, s53, %[sl]\n\tv_mul_u32_u24 v21, s53, v19\n\ts_sub_u32 s55, 2, s59\n\ts_lshl_b32 s55, s55, 7\n\tv_add_u32 v18, s55, v18\n\ts_cmp_lg_u32 s59, 3\n\ts_cbranch_scc1 7f\n\t"
    "v_bfe_u32 v18, %[t], 5, 1\n\tv_lshl_add_u32 v18, v18, 7, v118\n\tv_mov_b32 v19, s50\n\tv_mov_b32 v21, %[sl]\n"
    "7:\n\tv_add_u32 v19, s51, v19\n\tv_lshl_add_u32 v21, v18, 4, v21\n\tv_lshrrev_b32 v24, 3-" STR(TL) ", v19\n\tv_and_b32 v19, 7>>" STR(TL) ", v19\n\tv_lshlrev_b32 v19, 4, v19\n\tv_lshl_add_u32 v19, v24, 7, v19\n\t"
    "v_and_b32 v24, (1<<" STR(TL) ")-1, v18\n\tv_lshl_add_u32 v19, v24, 7-" STR(TL) ", v19\n\tv_lshrrev_b32 v18, " STR(TL) ", v18\n\tv_mul_u32_u24 v18, %[ust], v18\n\tv_add_u32 v18, v18, v19\n\tv_add_u32 v19, 512, v21\n10:\n\t"
    "global_store_dwordx4 v23, v[124:127], s[66:67] sc1\n\t" "s_branch 9f\n"
    "4:\n\t"
    "v_bfe_u32 v118, %[t], 3, 5\n\tv_lshrrev_b32 v117, 1, v118\n\tv_and_b32 v116, 6, v117\n\tv_and_b32 v115, 7, %[t]\n\tv_xor_b32 v115, v115, v116\n\tv_lshlrev_b32 v115, 4, v115\n\t"
    "s_mul_i32 s54, s61, 9\n\ts_sub_u32 s55, s62, 14\n\ts_lshr_b32 s55, s55, 1\n\ts_add_u32 s54, s54, s55\n\ts_lshl_b32 s54, s54, 7\n\t"
    "s_mul_i32 s52, s61, 0xa2000\n\ts_mul_i32 s53, s62, 0x180\n\ts_add_u32 s52, s52, s53\n\ts_cmp_ge_u32 s62, 11\n\ts_cselect_b32 s53, 0x51000-0x480, 0\n\ts_add_u32 s52, s52, s53\n\ts_sub_u32 s52, s52, 0xc00\n\t"
    "s_movk_i32 s55, 0x480\n\ts_cmp_ge_u32 s62, 14\n\ts_cselect_b32 s54, s54, s52\n\ts_cselect_b32 s55, 0x2400, s55\n\tv_mad_u32_u24 v118, v118, s55, v115\n\ts_lshl_b32 s55, s55, 5\n\t"
    "s_lshl_b32 s52, s63, 10\n\ts_add_u32 m0, s52, %[r2]\n\ts_mov_b32 s58, 0x510000\n\ts_mov_b32 s59, 0x20000\n\ts_cmp_ge_u32 s62, 14\n\ts_cselect_b32 s53, " STR(PRE1) "-1, " STR(PRE2) "-1\n\ts_setprio 3\n\t"
    "s_waitcnt lgkmcnt(0)\n\t" "s_memrealtime s[98:99]\n\t"
    "s_bitcmp1_b32 %[wg], 3\n\ts_cselect_b64 s[56:57], s[74:75], s[72:73]\n\ts_cmp_ge_u32 s62, 14\n\ts_cselect_b64 s[56:57], s[56:57], s[76:77]\n\ts_cselect_b32 s52, 0x180, -1\n\ts_and_b32 s57, s57, 0xffff\n\ts_add_u32 s60, s56, s54\n\ts_and_b32 s60, s60, 0x380\n\ts_cmp_eq_u32 s60, s52\n\ts_cselect_b32 s53, " STR(PRE3) "-1, s53\n\t"
    "v_mov_b32 v121, 80\n\ts_movk_i32 s52, 0x400\n12:\n\tds_read_b32 v122, v121\n\ts_waitcnt lgkmcnt(0)\n\tv_readfirstlane_b32 s60, v122\n\ts_cmp_ge_u32 s60, 12\n\ts_cbranch_scc1 13f\n\ts_sleep 1\n\ts_sub_u32 s52, s52, 1\n\ts_cbranch_scc0 12b\n13:\n\t"
    "s_load_dwordx2 s[96:97], s[66:67], 0x0\n\ts_sleep " STR(PD) "\n\t"
    "\n3:\n\t" "buffer_load_dwordx4 v118, s[56:59], s54 offen nt lds\n\ts_add_u32 m0, m0, 0x1000\n\ts_add_u32 s54, s54, s55\n\ts_sub_u32 s53, s53, 1\n\ts_cbranch_scc0 3b\n"
    "9:\n8:"
    : [xa]"=&{v[2:5]}"(xa),[xb]"=&{v[6:9]}"(xb_),[xc]"=&{v30}"(xc),[wa]"=&{v[10:13]}"(wa),[wb]"=&{v[14:17]}"(wb),[wc]"=&{v31}"(wc),[d0]"=&{v[18:21]}"(dq[0]),[d1]"=&{v[22:25]}"(dq[1]),[d2]"=&{v[26:29]}"(dq[2]),"=&{v[32:35]}"(la),"=&{v[36:39]}"(lb_),"=&{v[40:41]}"(lc),
      "=&{s[64:65]}"(x),"=&{s[66:67]}"(ws),"=&{s[70:71]}"(Dsk),"=&{s[72:73]}"(wg),"=&{s[74:75]}"(wu),"=&{s[76:77]}"(wd),"=&{s[78:79]}"(out)
    : [ka]"s"(__builtin_amdgcn_kernarg_segment_ptr()),[wg]"s"(BID),[t]"v"(TID),[yst]"i"(YST),[wy]"i"(W_Y),[r2]"i"(O_R2),[sl]"i"(SLSZ),[tb]"i"(O_CH+O_TB),[ex]"i"(O_EXP),[de]"i"(O_DE),[ust]"i"(GST_),[wu]"i"(W_U),[ti]"i"(O_TILE),[rg]"i"(W_RG),[st]"i"(W_ST)
    : "memory","scc","m0","v127","s50","s51","s52","s53","s54","s55","s56","s57","s58","s59","s60","s61","s62","s63","s68","s69",
      "s80","s81","s82","s83","s84","s85","s86","s87","s88","s89","s90","s91","s92","s93","s94","s95","s99");
  asm volatile("" : "+{s[48:49]}"(tearly_));
  unsigned tearly=(unsigned)tearly_|0x80000000u; asm volatile("ds_max_u32 %0, %1" :: "v"(92),"v"(~tearly) : "memory");
  int wv=RFL(TID>>6), bid=BID, q4=wv>>2, w4=wv&3;
  int xg=bid&7, lb=bid>>3, tb=32*xg+lb, nch=2+(lb<8);
  u64 ep; u32 ldw;
  {
  }
  #define KILLIN "=v"(xa),"=v"(xb_),"=v"(xc),"=v"(wa),"=v"(wb),"=v"(wc),"=v"(dq[0]),"=v"(dq[1]),"=v"(dq[2]),"=v"(la),"=v"(lb_),"=v"(lc)
  if(__builtin_expect(q4==3&&lb>=8,0)){
  SBDECL STAMP(0);
    int fu=18*xg+lb-14, kq=2*xg+(lb>=11), cgp=lb-8-3*(lb>=11);
  u32 wst= lb>=14? F_*64:D_*64;
  u32 wso=(lb>=14? (fu>>1)*128 : kq*(288*D_*2)+384*cgp)+(lb>=14? PRE1:PRE2)*wst, wld=(u32)(size_t)sm+O_R2+w4*1024+(lb>=14? PRE1:PRE2)*4096; u32x4 wrs=mkrs(lb>=14? ((fu&1)? wu:wg):wd,D_*F_*2);
  GETEP
  #define FLANE(RS) LANE(ln); int g=ln>>4, li=ln&15, qq=li>>2, pq=li&3; u32 wvo=(u32)(8*w4+(ln>>3))*(RS)+(u32)((ln&7)^(((ln>>4)&2)|((w4&1)<<2)))*16u; \
    const lch* tp=sm+O_R2+(16*(g>>1)+8*(qq>>1)+4*(g&1)+(qq&1))*128+((w4^((g&1)|(qq&2)))*32)+pq*8;
  #define WFR(img,ks) BC(u32x4,trfrag(tp+(img)*CHSZ+(ks)*4096,tp+(img)*CHSZ+(ks)*4096+256))
  #define WF2(img,s) __builtin_shufflevector(WFR(img,2*(s)),WFR(img,2*(s)+1),0,1,2,3,4,5,6,7)
  f32x4 ac[3]={{0,0,0,0},{0,0,0,0},{0,0,0,0}};
  #define AC0 "+{v[100:103]}"(ac[0])
  #define AC1 "+{v[104:107]}"(ac[1])
  #define AC2 "+{v[108:111]}"(ac[2])
  #define SMF(i,a,b) asm volatile("v_smfmac_f32_16x16x64_bf16 %0, %1, %2, %3" : AC##i : "v"(a),"v"(b),"v"(ix) : "memory"); SCHED();
  #define SMR(i,r,b) asm volatile("v_smfmac_f32_16x16x64_bf16 %0, v[" #r ":" #r "+3], %1, %2" : AC##i : "v"(b),"v"(ix) : "memory"); SCHED();
  #define GW4(c,p) { u32 tv,cnt,sv; asm volatile("s_mov_b32 %1, 0x20000\n1:\n\tds_read_b32 %0, %3\n\tds_read_b128 v[112:115], %4\n\tds_read_b128 v[116:119], %4 offset:128\n\tds_read_b128 v[120:123], %4 offset:256\n\tds_read_b128 v[124:127], %4 offset:384\n\ts_waitcnt lgkmcnt(4)\n\t" \
    "v_readfirstlane_b32 %2, %0\n\ts_cmp_ge_u32 %2, 4\n\ts_cbranch_scc1 2f\n\ts_sleep 1\n\ts_sub_u32 %1, %1, 1\n\ts_cbranch_scc0 1b\n2:" : "=&v"(tv),"=&s"(cnt),"=&s"(sv) : "v"((c)*4),"v"(p) : "memory","scc"); }
  #define LGW(n) asm volatile("s_waitcnt lgkmcnt(" #n ")" ::: "memory");
  u64 tag=ep+1ull; const u32x4 I4={INV,INV,INV,INV};
  if(lb>=14){
    FLANE(F_*2)
    int mt=fu&1;
    const lch* xp=sm+O_TILE+(li&7)*XSTR+g*32+(li>>3)*16; int of=(mt*288+(fu>>1)*4+w4)*256+ln*4; u32 ix= (li&8)? 0xeeeeu:0x4444u;
    const lch* mp=sm+O_RC+(g&1)*16+(g>>1)*8;
    asm volatile("" : "+v"(xp),"+v"(of),"+v"(mp),"+v"(ix));
    rsrc_t rsG=RSRC(ws+W_GU,2*8*F_*2);
    if(w4==0) BST128(I4,rsG,(mt*288+(fu>>1)*4)*256+ln*16,0,SC1);
    bool slow=(((u32)wrs[0]+wso)&0x380u)==0x180u;
    if(slow){ wso-=(PRE1-PRE3)*wst; wld-=(PRE1-PRE3)*4096; FILL(S3-PRE3,VW(W3)); } else FILLD(5,W1,W1B);
    if(w4==0){ if(slow) VMW(S3-PRE3<5? S3-PRE3:5); else VMW(5); if(ln==0) GST(ws+W_FL+(576+fu)*8,tag); }
    STAMP(5);
    if(slow){ wso=RFL(wso); wld=RFL(wld); FILL(18-S3,VW(W3B)); } else FILLD(13-PRE1,W1,W1B);
    STAMP(1);
    u32x8 fB[9]; u32x4 one4={0x3f803f80u,0x3f803f80u,0x3f803f80u,0x3f803f80u}; const lch* tq=tp+65536; asm volatile("" : AC0,AC1,"+v"(one4),"+v"(tq));
    VMW(4); STAMP(2); gsync(sm,C_P,4);
    #pragma unroll
    for(int s=0;s<7;s++) fB[s]=WF2(0,s);
    #pragma unroll
    for(int s=0;s<7;s++){ SMF(1,one4,fB[s]) }
    DRAIN(); STAMP(6); gsync(sm,C_P,8);
    fB[7]=WF2(0,7); { const lch* tp=tq; fB[8]=WF2(0,0); } SMF(1,one4,fB[7]) SMF(1,one4,fB[8])
    STAMP(3);
    { u32x4 xb[5]; u32x2 md;
      GW4(C_X,xp) STAMP(4);
      md=LD(u32x2,mp);
      #pragma unroll
      for(int s=0;s<5;s++) xb[s]=LD(u32x4,xp+512+s*128);
      SCHED(); LGW(6)
      SMR(0,112,fB[0])
      float S; asm volatile("v_permlane32_swap_b32 v104, v105\n\tv_add_f32 %0, v104, v105" : "=v"(S),AC1); SCHED();
      SMR(0,116,fB[1]) SMR(0,120,fB[2])
      float ra=rcpf_(bhi(md[0])), rb_=rcpf_(bhi(md[1])); PIN(ra); PIN(rb_); SCHED();
      SMR(0,124,fB[3])
      float na=-blo(md[0])*S, nb=-blo(md[1])*S; PIN(na); PIN(nb); SCHED();
      SMF(0,xb[0],fB[4])
      float ea=ra*-1.4426950408889634f, eb=rb_*-1.4426950408889634f; PIN(ea); PIN(eb); SCHED();
      SMF(0,xb[1],fB[5]) SMF(0,xb[2],fB[6]) SMF(0,xb[3],fB[7]) SMF(0,xb[4],fB[8])
      float v0,v1,s0,s1;
      asm volatile("s_nop 7\n\tv_permlane32_swap_b32 v100, v102\n\tv_permlane32_swap_b32 v101, v103\n\tv_add_f32 v100, %5, v100\n\tv_add_f32 v101, %6, v101\n\tv_add_f32 v100, v100, v102\n\tv_add_f32 v101, v101, v103\n\t"
        "v_mul_f32 %2, %9, v100\n\tv_mul_f32 %3, %10, v101\n\tv_mul_f32 %0, %7, v100\n\tv_mul_f32 %1, %8, v101" : "=&v"(v0),"=&v"(v1),"=&v"(s0),"=&v"(s1),AC0 : "v"(na),"v"(nb),"v"(ra),"v"(rb_),"v"(ea),"v"(eb));
      if(mt==0){ v0*=rcpf_(1.f+__builtin_amdgcn_exp2f(s0)); v1*=rcpf_(1.f+__builtin_amdgcn_exp2f(s1)); }
      __builtin_amdgcn_raw_buffer_store_b32(nv(pk(v0,v1)),rsG,of,0,SC1); garrive(sm,C_H); }
    STAMP(7);
    if((STSEL>>8)&1) DRAIN(); STAMP(8);
    if(bid==112&&w4==1){
      _Pragma("unroll 1") for(int sp=0;sp<SPINCAP;sp++){ u64 bad=0, f[12];
        #pragma unroll
        for(int i=0;i<12;i++) f[i]=GLD(ws+W_FL+(i*64+ln)*8);
        #pragma unroll
        for(int i=0;i<12;i++) bad|=f[i]^tag;
        if(BALLOT(bad!=0ull)==0ull) break; __builtin_amdgcn_s_sleep(8); }
      if(ln==0) GST(ws+W_EPOCH,tag); }
    if(mt==0&&w4==0){
    LANE(l2); ln=l2; li=ln&15; g=ln>>4; gwait<1>(sm,C_IX,1);
    int c9=(lb-14)>>1, t=RFL((int)LD(u16,sm+O_IDX+xg*2)), c=64*c9+4*li+g; f32x4 pr;
    { u32 u=LD(u16,sm+O_IDX+16+xg*2)^0x8000u, xb=LD(u16,sm+O_TILE+xg*XSTR+c*2), sv=LD(u32,sm+O_RC+xg*4); float ds=b2f(((const GAS u16*)Dsk)[c]); u^=(u32)(((int)(u<<16))>>31)&0x7fffu;
      poll1<0>(ws+W_FL+(64*c9+ln)*8,tag); poll1<0>(ws+W_FL+(720+(ln<48? ln:47))*8,tag);
      float xn=(b2f(xb)-blo(sv))*rcpf_(bhi(sv)); pr=f32x4{b2f(xb),xn,xn*ds,sigm_f(b2f(u))}; }
    { gwait<2>(sm,C_E,4); u32 ti=now_(), mx=0;
      #pragma unroll
      for(int k=0;k<3;k++){ int i=ln+64*k; i= i<144? i:143; int q=(i*3641)>>16; u64 h=GLD(ws+W_HT+(72*q+36+2*(i-18*q))*8); mx=umax_(mx,(u32)(h>>32)==(u32)tag? (u32)h-ldw : ti-ldw+HM); }
      u32 tr=umin_(wmax(mx),ti-ldw+160u); u32 hk=ti-ldw+HKR; if(tr>hk) tr-=(tr-hk)>>1; tgate2(ldw,tr+HR); }
    STAMP(10);
    { u32x4 rs=mkrs(ws+W_PART+(xg*9+c9)*4096,4096), ry=mkrs(ws+W_Y,D_*YST);
      int yo=c*YST+(t&~1)*2, po=ln*16; u32 a[4], t0,cnt,yb; u16* op=out+t*D_+c;
      #define LD_P "buffer_load_dwordx4 v[112:115], %[of], %[rs], 0 offen sc1\n\tbuffer_load_dwordx4 v[116:119], %[of], %[rs], 0 offen offset:1024 sc1\n\tbuffer_load_dwordx4 v[120:123], %[of], %[rs], 0 offen offset:2048 sc1\n\tbuffer_load_dwordx4 v[124:127], %[of], %[rs], 0 offen offset:3072 sc1\n\t"
      #define LD_YB "buffer_load_dword %[yb], %[yo], %[ry], 0 offen sc1\n\t"
      #define AD4(o,a,b,c,d) "v_add_f32 %[t0], v" #a ", v" #b "\n\tv_add_f32 %[" #o "], v" #c ", v" #d "\n\tv_add_f32 %[" #o "], %[t0], %[" #o "]\n\t"
      u64 ym;
      asm volatile(LD_YB LD_P "s_mov_b32 %[cnt], 0x20000\n1:\n\ts_waitcnt vmcnt(0)\n\tv_cmp_eq_u32 %[ym], -1, %[yb]\n\t" CHK_I0 MX2(116,117) MX2(118,119) MX2(120,121) MX2(122,123) MX2(124,125) MX2(126,127) CHK_IE
        "s_or_b64 exec, vcc, %[ym]\n\ts_cbranch_scc0 9f\n\ts_sleep " STR(QSLP) "\n\ts_mov_b64 exec, %[ym]\n\ts_cbranch_execz 2f\n\t" LD_YB "2:\n\ts_mov_b64 exec, vcc\n\ts_cbranch_execz 3f\n\t" LD_P
        "3:\n\ts_mov_b64 exec, -1\n\ts_sub_u32 %[cnt], %[cnt], 1\n\ts_cbranch_scc0 1b\n9:\n\ts_mov_b64 exec, -1\n\t"
        AD4(a0,112,116,120,124) AD4(a1,113,117,121,125) AD4(a2,114,118,122,126) AD4(a3,115,119,123,127) "s_nop 1"
        : [t0]"=&v"(t0),[cnt]"=&s"(cnt),[a0]"=&v"(a[0]),[a1]"=&v"(a[1]),[a2]"=&v"(a[2]),[a3]"=&v"(a[3]),[yb]"=&v"(yb),[ym]"=&s"(ym) : [of]"v"(po),[rs]"s"(rs),[yo]"v"(yo),[ry]"s"(ry),"v"(op),"v"(pr) : PCLOB);
      STAMP(11);
      auto s0=__builtin_amdgcn_permlane32_swap(a[0],a[2],false,false); auto s1=__builtin_amdgcn_permlane32_swap(a[1],a[3],false,false);
      u32 x0=s0[0],x1=s0[1],y0=s1[0],y1=s1[1]; u32 p0=BC(u32,BC(float,x0)+BC(float,x1)), p1=BC(u32,BC(float,y0)+BC(float,y1));
      auto s2=__builtin_amdgcn_permlane16_swap(p0,p1,false,false); u32 z0=s2[0], z1=s2[1]; float s=BC(float,z0)+BC(float,z1);
      float gq=silu_f(__builtin_fmaf(s,pr[3],pr[1]));
      float sy=((t&1)? bhi(yb):blo(yb))+pr[2];
      float o=__builtin_fmaf(sy,gq,pr[0]);
      asm volatile("global_store_short %0, %1, off sc1" :: "v"(op),"v"(pk(o,o)) : "memory");
      }
    DRAIN();
    STAMP(13); }
    SFLUSH();
  } else
  {
    FLANE(D_*2)
    u32x8 fr[12]; u32x4 fl[3];
    rsrc_t rsP=RSRC(ws+W_PART+cgp*12288+kq*256+w4*64,8*9*4096-2*12288);
    #pragma unroll
    for(int i=0;i<3;i++) BST128(I4,rsP,li<8? li*(9*4096)+g*16+i*4096 : OOR,0,SC1);
    if(PRE2<9) FILLD(9-PRE2,W2_,W2B);
    wso+=128u-9u*wst;
    FILLD(9,W2_,W2B);
    VMW(9); gsync(sm,C_P,4);
    #pragma unroll
    for(int s=0;s<4;s++) fr[s]=WF2(0,s);
    fl[0]=WFR(0,8);
    gsync(sm,C_P,8); if(w4==0&&ln==0) GST(ws+W_FL+(720+6*xg+lb-8)*8,tag);
    wso+=128u-9u*wst; wld-=2*CHSZ;
    int fo=W_FL+(576+((9*kq)&~1)+(ln<9? ln:9))*8;
    FILL(9,VW(W2C));
    if(w4==0) asm volatile("global_load_dwordx2 v[112:113], %0, %1 sc1\n\ts_waitcnt vmcnt(10)" :: "v"(fo),"s"(ws) : "memory"); else VMW(9);
    gsync(sm,C_P,12);
    #pragma unroll
    for(int s=0;s<4;s++) fr[4+s]=WF2(1,s);
    fl[1]=WFR(1,8);
    { int t2=TID; asm volatile("" : "+v"(t2)); ln=t2&63; li=ln&15; g=ln>>4; }
    const lch* hp=sm+O_TILE+(li&7)*576+g*32+(li>>3)*16; int po=(4*(g&1)+2*(g>>1))*(9*4096)+li*4; u32 ix= (li&8)? 0xeeeeu:0x4444u; PIN(hp); PIN(po); PIN(ix);
    const lch* hq=hp+512-(g>>1)*32-(g&1)*24; PIN(hq);
    u32x4 rs=mkrs(ws+W_GU,2*8*F_*2);
    int of=((ln>>5)*288+18*kq+2*w4)*256+(ln&31)*16; lch* hd=sm+O_TILE+((ln&4)+((ln>>2)&2))*576+w4*64+(ln&16)*2+(ln&3)*8+(ln>>5)*4; PIN(of); PIN(hd); asm volatile("" : "+s"(rs));
    u32x4 rp=mkrs(ws+W_PART+cgp*12288+kq*256+w4*64,8*9*4096-2*12288); int po1=po+9*4096; u32 nvm=0xfffeffffu; asm volatile("" : AC0,AC1,AC2,"+v"(po1),"+s"(rp),"+s"(nvm));
    DRAIN();
    if(w4==0){ u64 bad; asm volatile("v_cmp_ne_u64 %0, %1, v[112:113]" : "=s"(bad) : "s"(tag) : "memory");
      if(bad!=0ull) poll1<0>(ws+fo,tag); }
    gsync(sm,C_P,16);
    STAMP(3);
    { u32 t0,cnt;
      #define LD_G "buffer_load_dwordx4 v[112:115], %[of], %[rs], 0 offen sc1\n\tbuffer_load_dwordx4 v[116:119], %[of], %[rs], 0 offen offset:2048 sc1\n\tbuffer_load_dwordx4 v[120:123], %[of], %[rs], %[so] offen sc1\n\t"
      #define HM1(t,a,b) "v_lshlrev_b32 " t ", 16, v" #a "\n\tv_lshlrev_b32 %[t1], 16, v" #b "\n\tv_and_b32 v" #a ", %[hm], v" #a "\n\tv_and_b32 v" #b ", %[hm], v" #b "\n\tv_mul_f32 " t ", " t ", %[t1]\n\tv_mul_f32 v" #a ", v" #a ", v" #b "\n\t"
      #define HMUL(a,b,c,d,o) "v_permlane32_swap_b32 v" #a ", v" #c "\n\tv_permlane32_swap_b32 v" #b ", v" #d "\n\t" HM1("%[t0]",a,c) HM1("v" #c,b,d) "v_cvt_pk_bf16_f32 %[t0], %[t0], v" #c "\n\tv_cvt_pk_bf16_f32 v" #a ", v" #a ", v" #b "\n\tds_write2_b32 %[hd], %[t0], v" #a " offset0:" #o " offset1:" #o "+144\n\t"
      u32 t1;
      asm volatile(LD_G :: [of]"v"(of),[rs]"s"(rs),[so]"s"(w4==0? 4096:0) : "memory");
      fr[8]=WF2(0,0); PIN(fr[8]); fl[2]=WFR(0,8); PIN(fl[2]);
      asm volatile(POLLASM(LD_G,CHK_I0 MX2(116,117) MX2(118,119) MX2(120,121) MX2(122,123) CHK_IE,GSLP)
        HMUL(112,113,114,115,0) HMUL(116,117,118,119,64) "s_cmp_eq_u32 %[so], 0\n\ts_cbranch_scc1 8f\n\tv_add_u32 %[hd], 512, %[hd]\n\t" HMUL(120,121,122,123,0) "8:\n\ts_mov_b64 exec, 1\n\tds_add_u32 %[ch], %[one]\n\ts_mov_b64 exec, -1"
        : [t0]"=&v"(t0),[t1]"=&v"(t1),[cnt]"=&s"(cnt),[hd]"+v"(hd) : [of]"v"(of),[rs]"s"(rs),[so]"s"(w4==0? 4096:0),[hm]"s"(0xffff0000u),[ch]"v"(C_H*4),[one]"v"(1),"v"(hp),"v"(po) : PCLOB);
      STAMP(10); RCNT(9,cnt); }
    {
      GW4(C_H,hp) STAMP(5);
      u32x2 h8=LD(u32x2,hq); SCHED(); LGW(1)
      #define SM3(i,b) asm volatile("v_smfmac_f32_16x16x32_bf16 %0, %1, %2, %3" : AC##i : "v"(h8),"v"(b),"v"(ix) : "memory"); SCHED();
      #define PST(a,b,c,d,o) "v_permlane32_swap_b32 v" #a ", v" #c "\n\tv_permlane32_swap_b32 v" #b ", v" #d "\n\tv_add_f32 v" #a ", v" #a ", v" #c "\n\tv_add_f32 v" #b ", v" #b ", v" #d "\n\tv_min_u32 v" #a ", %[nv], v" #a "\n\tv_min_u32 v" #b ", %[nv], v" #b "\n\t" \
        "buffer_store_dword v" #a ", %[p0], %[rp], " o " offen sc1\n\tbuffer_store_dword v" #b ", %[p1], %[rp], " o " offen sc1\n\t"
      SMR(0,112,fr[0]) fr[9]=WF2(0,1); SCHED(); SMR(1,112,fr[4]) fr[10]=WF2(0,2); SCHED(); SMR(2,112,fr[8]) fr[11]=WF2(0,3); SCHED();
      SMR(0,116,fr[1]) SMR(1,116,fr[5]) SMR(0,120,fr[2]) SM3(2,fl[2]) SMR(0,124,fr[3]) SMR(1,120,fr[6]) SM3(0,fl[0]) SMR(1,124,fr[7]) SMR(2,116,fr[9]) SM3(1,fl[1]) SMR(2,120,fr[10]) SMR(2,124,fr[11])
      asm volatile("s_nop 2\n\t" PST(100,101,102,103,"0") PST(104,105,106,107,"%[o1]") PST(108,109,110,111,"%[o2]") : AC0,AC1,AC2 : [p0]"v"(po),[p1]"v"(po1),[rp]"s"(rp),[nv]"s"(nvm),[o1]"s"(4096),[o2]"s"(8192) : "memory"); }
    STAMP(6);
    SFLUSH();
  }
  asm volatile("s_endpgm" : KILLIN :: "memory"); }
  {
  SBDECL
  if(__builtin_expect(q4!=0,0)){ int grp= q4==1? nch-1 : q4-2;
    chan(sm,grp,(w4+(0x48>>(2*q4)))&3,72*xg+(q4==3? 16+lb : 2*lb+(lb<8? 0:8)+2-q4),(q4+1)>>2,ws,xa,xb_,wa,wb,dq[0] SBPASS);
    asm volatile("s_endpgm" : KILLIN :: "memory"); }
  u32x4 xq[3]; f32x2 rc[3]; int xr_,xr2_;
  {
    LANE(ln); int g=ln>>4, li=ln&15, qq=li>>2, pq=li&3, j=ln&31, rl=2*w4+(ln>>5);
    lch* tile=sm+O_TILE; u32 Tr=(ln>>1)&16;
    int uo=W_U+(tb>>TL)*GST_+(tb&((1<<TL)-1))*(128>>TL)+(w4*1024<<TL)+(ln>>(3-TL))*128+(ln&(7>>TL))*16, uo1=uo+(4096<<TL), uo2=uo+(8192<<TL); PIN(uo); PIN(uo1); PIN(uo2);
    lch* tw=tile+rl*TSTR2+j*16; lch* tw2=tw+1024-j*12; u32 one=0x3f803f80u; const lch* tp=tile+qq*TSTR2+(w4*64+g*16+pq*4)*2;
    int go= ln<32? W_RG+(ln>>2)*8192+(tb*4+(ln&3))*8 : W_ST+(tb*8+(ln&7))*8; lch* kw=sm+O_RV+rl*2; const lch* kr=sm+(ln<32? O_RV+(ln&3)*4 : O_MD+(ln&7)*4); PIN(kw); PIN(kr);
    int xr=(32*(tb>>2)+(ln>>2))*(D_*2)+256*(lb&3)+64*w4+pq*16, xr2= w4==0? xr+(lb&2)*(8*D_*2)+1024+64*(lb&1)-256*(lb&3):OOR;
    rsrc_t rx=RSRC(x,L_*D_*2); u64 gm=((1ull<<(4*NRG))-1ull)|0xff00000000ull; u32 um=~UMASK, km=0x80008000u; asm volatile("" : "+s"(gm),"+s"(um),"+s"(km),"+s"(ws));
    #define D2(a,b,c) __builtin_amdgcn_fdot2_f32_bf16(BC(bf2,a),BC(bf2,b),c,false)
    asm volatile("s_waitcnt vmcnt(5)" : "+v"(wa),"+v"(wb),"+v"(wc),"+v"(one)
      ,"+v"(Tr),"+v"(tw),"+v"(tw2),"+v"(tp),"+v"(go),"+v"(xr),"+v"(xr2)
      :: "memory");
    u32 wq[9]={wa[0],wa[1],wa[2],wa[3],wb[0],wb[1],wb[2],wb[3],wc}; float sw;
    { float a[3];
      #pragma unroll
      for(int i=0;i<9;i++) a[i%3]=D2(wq[i],one,i<3? 0.f:a[i%3]);
      sw=add16x(sum16((a[0]+a[1])+a[2])); }
    GETEP u32 tg=(u32)ep+1u; Tr=((upat(tg,w4)>>Tr)&0xffffu)*0x10001u; u32 tgv=tg;
    float z_[9]={0,0,0,0,0,0,0,0,0};
    asm volatile("s_waitcnt vmcnt(2)" : "+v"(xa),"+v"(xb_),"+v"(xc),"+v"(Tr),"+v"(sw),"+v"(tgv),"+v"(z_[0]),"+v"(z_[1]),"+v"(z_[2]),"+v"(z_[3]),"+v"(z_[4]),"+v"(z_[5]) :: "memory"); STAMP(0);
    u32 v[9]={xa[0],xa[1],xa[2],xa[3],xb_[0],xb_[1],xb_[2],xb_[3],xc};
    { float xf[18], s1, s2, s3;
      #pragma unroll
      for(int i=0;i<9;i++){ z_[i&1]=D2(v[i],one,z_[i&1]); z_[2+(i&1)]=D2(v[i],v[i],z_[2+(i&1)]); z_[4+(i&1)]=D2(v[i],wq[i],z_[4+(i&1)]); }
      s1=z_[0]+z_[1]; s2=z_[2]+z_[3]; s3=z_[4]+z_[5];
      SCHED();
      #define RSTEP(C) s1+=dppf<C>(s1); s2+=dppf<C>(s2); s3+=dppf<C>(s3); SCHED();
      RSTEP(0xB1) RSTEP(0x4E) RSTEP(0x141) RSTEP(0x140)
      s1=add16x(s1)*(1.f/576.f); s2=add16x(s2)*(1.f/576.f); s3=add16x(s3);
      float mm=s1*s1, var=s2-mm;
      if(__builtin_expect(BALLOT(!(mm<=64.f*var))!=0ull,0)){
        float q=0.f;
        #pragma unroll
        for(int i=0;i<9;i++){ u32 t=v[i]; PIN(t); float d0=blo(t)-s1, d1=bhi(t)-s1; q=__builtin_fmaf(d0,d0,q); q=__builtin_fmaf(d1,d1,q); }
        var=add16x(sum16(q))*(1.f/576.f); }
      #define UNP(i) PIN(v[i]); xf[2*(i)]=blo(v[i]); xf[2*(i)+1]=bhi(v[i]); PIN(xf[2*(i)]); PIN(xf[2*(i)+1]); SCHED();
      u32 vb=pk(var,s1); UNP(0)
      float den=blo(vb)+1e-5f, m=bhi(vb); UNP(1)
      vb=pk(den,den); s3=__builtin_fmaf(-m,sw,s3); UNP(2)
      den=__builtin_amdgcn_sqrtf(bhi(vb)); UNP(3)
      u32 md=pk(m,den); UNP(4)
      float ri=rcpf_(bhi(md)); UNP(5)
      s3*=ri; float rs_=ri*0.0625f; UNP(6)
      u32 u=pk(s3,s3); float cs_=-m*rs_; UNP(7)
      { u32 sg=(u32)((int)u>>31); PIN(sg); u^=sg|km; } UNP(8)
      asm volatile("s_mov_b64 exec, 0x1\n\ts_mov_b32 exec_hi, 1\n\tds_write_b16 %0, %1\n\tv_add_u32 %0, %0, %0\n\tds_write_b32 %0, %2 offset:%3\n\ts_mov_b32 exec_hi, 0\n\tds_add_u32 %4, %5\n\ts_mov_b64 exec, -1" : "+v"(kw) : "v"(u),"v"(md),"i"(O_MD-2*O_RV),"v"(C_I*4),"v"(1) : "memory");
      if(w4==3){ gwait<0>(sm,C_I,4); asm volatile("ds_read_b32 v118, %4\n\tv_mov_b32 v119, %1\n\ts_mov_b64 exec, %3\n\ts_waitcnt lgkmcnt(0)\n\tglobal_store_dwordx2 %0, v[118:119], %2 sc1\n\ts_mov_b64 exec, -1" :: "v"(go),"v"(tgv),"s"(ws),"s"(gm),"v"(kr) : "memory"); }
      SCHED();
      u32 h[9];
      #define UH(i) h[i]=(pkz(__builtin_fmaf(xf[2*(i)],rs_,cs_),__builtin_fmaf(xf[2*(i)+1],rs_,cs_))&um)|Tr;
      UH(0) UH(1) UH(2) UH(3) ST(u32x4,tw)=u32x4{h[0],h[1],h[2],h[3]}; UH(4) UH(5) UH(6) UH(7) ST(u32x4,tw+512)=u32x4{h[4],h[5],h[6],h[7]};
      UH(8) ST(u32,tw2)=h[8]; }
    gsync(sm,C_L,4); STAMP(1);
    { u32x4 uq[3];
      #pragma unroll
      for(int z=0;z<3;z++) uq[z]=BC(u32x4,trfrag(tp+z*512,tp+z*512+4*TSTR2));
      asm volatile("global_store_dwordx4 %0, %1, %2 sc1" :: "v"(uo),"v"(uq[0]),"s"(ws) : "memory");
      asm volatile("global_store_dwordx4 %0, %1, %2 sc1" :: "v"(uo1),"v"(uq[1]),"s"(ws) : "memory");
      asm volatile("s_cmp_lg_u32 %3, 0\n\ts_cbranch_scc1 1f\n\tglobal_store_dwordx4 %0, %1, %2 sc1\n1:\n\ts_nop 1" :: "v"(uo2),"v"(uq[2]),"s"(ws),"s"(w4),"v"(uq[0]),"v"(uq[1]) : "memory","scc");
      asm volatile("" : "+v"(dq[0]),"+v"(dq[1]));
      xr_=xr; xr2_=xr2;
    }
    if(lb<8){
      const u16* wm=(lb&1)? wu:wg; int co=(3-(int)((u64)wm>>7)-xg)&7, r_=576-16*PFN+((lb>>1)*4+w4)+16*j;
      asm volatile("buffer_load_dword v120, %0, %1, 0 offen" :: "v"(j<PFN&&(ln<32||co==0)? r_*(F_*2)+(9*xg+co)*128+(ln>>5)*1024 : OOR),"s"(mkrs(wm,D_*F_*2)) : "memory"); }
    STAMP(2);
    if(lb>=8&&lb<14) garrive(sm,C_D);
  }
  FP fp; FA fa; u32 ti,td;
  { LANE(ln); u64 tag=ep+1ull;
    int so_=W_ST+(32*(tb>>2)+(ln>>2))*8, fo_=W_FL+((ln>=32&&w4==0? 512+32*(lb&1) : 128*(lb&3)+32*w4)+(ln&31))*8; u32x4 rx=mkrs(x,L_*D_*2); PIN(so_); PIN(fo_);
    XR X; X.so=so_; X.x0=xr_; X.x2=xr2_; X.rx=rx;
    if(lb==CTLB||lb>=14){
      u32 M=(u32)tag, rank,key; int j=ln&31, j16=j*16, j12=j*12, st0=W_ST; lch* xw=sm+O_TILE+(2*w4+(ln>>5))*XSTR+j*16; lch* rcw=sm+O_RC+(2*w4+(ln>>5))*4; PIN(rcw); PIN(j12); PIN(xw); PIN(j16); asm volatile("" : "+s"(rx),"+s"(st0));
      top8(sm,w4,ln,M,ws,rank,key,X SBPASS);
      {
        int t0=__builtin_amdgcn_readlane((int)key,__builtin_ctzll(BALLOT(rank==(u32)(2*w4)))), t1=__builtin_amdgcn_readlane((int)key,__builtin_ctzll(BALLOT(rank==(u32)(2*w4+1))));
        int tsel=~((ln>>5)? t1:t0)&2047, ro=tsel*1152+j16, sr=tsel*8+st0;
        STAMP(4);
        u32x4 a,b; u32 c; u64 f1;
        asm volatile("buffer_load_dwordx4 %[a], %[ro], %[rx], 0 offen\n\tbuffer_load_dwordx4 %[b], %[ro], %[rx], 0 offen offset:512\n\tbuffer_load_dword %[c], %[r2], %[rx], 0 offen offset:1024\n\tglobal_load_dwordx2 %[f], %[sr], %[ws] sc1\n\t"
          "s_mov_b64 exec, 1\n\tds_add_u32 %[cd], %[one]\n\ts_mov_b64 exec, -1\n\ts_waitcnt vmcnt(0)"
          : [a]"=&v"(a),[b]"=&v"(b),[c]"=&v"(c),[f]"=&v"(f1) : [ro]"v"(ro),[r2]"v"(ro-j12),[sr]"v"(sr),[cd]"v"(C_D*4),[one]"v"(1),[rx]"s"(rx),[ws]"s"(ws) : "memory");
        if(__builtin_expect(BALLOT((u32)(f1>>32)!=(u32)tag)!=0ull,0)){ f1=poll1<32>(ws+sr,tag<<32); __builtin_amdgcn_s_waitcnt(0x0f70); }
        ST(u32x4,xw)=a; ST(u32x4,xw+512)=b; ST(u32,xw+1024-j12)=c; ST(u32,rcw)=(u32)f1;
        garrive(sm,C_X); STAMP(5);
        if(w4==0){ if(rank<8) ST(u16,sm+O_IDX+(ln&1)*16+rank*2)=(u16)((ln&1)? key>>16 : ~key&2047u); garrive(sm,C_IX); }
        if(lb==CTLB&&rank<8&&!(ln&1)) GST(ws+W_IDX+xg*1024+w4*128+rank*8,(tag<<16)|(~key&2047u));
      }
      PRIO(0); if(XB!=2) xreq(X,ws);
    } else { PRIO(0); if(XC&&lb>=8){ gwait<4>(sm,C_G,XG); __builtin_amdgcn_s_sleep(XC); xreq(X,ws); gwait<8>(sm,C_U,XU); } else { gwait<8>(sm,C_U,2); xreq(X,ws); if(AW&&lb<8&&w4>=4-AW) pf2(lb-1+7*(3-w4),xg,wd); } }
    { u32 e=now_()-ldw, nm= lb<8&&lb!=CTLB? ANA : (lb>=8&&lb<14? ANC:ANB); if(e>nm) ldw+=e-nm; }
    final_addr(fa,sm,w4);
    bool ix= w4==3&&lb<14&&lb!=CTLB; int po=W_IDX+(xg*8+(lb&3))*128+(ln&7)*8, ho_=fo_+(W_HT-W_FL), pq_= ix? po:fo_; PIN(ho_); PIN(pq_);
    { u64 fs,fs1;
      asm volatile("s_waitcnt vmcnt(0)" : "+{v[96:99]}"(X.q[0]),"+{v[100:103]}"(X.q[1]),"+{v[104:107]}"(X.q[2]),"+{v[108:111]}"(X.q[3]) :: "memory");
      fs=(u64)X.q[3][0]|((u64)X.q[3][1]<<32); fs1=(u64)X.q[3][2]|((u64)X.q[3][3]<<32);
      if(__builtin_expect(BALLOT((u32)(fs>>32)!=(u32)tag||(u32)(fs1>>32)!=(u32)tag)!=0ull,0)){ fs=poll1<32>(ws+so_,tag<<32); fs1=poll1<32>(ws+so_+128,tag<<32); __builtin_amdgcn_s_waitcnt(0x0f70); }
      float ri=rcpf_(bhi((u32)fs)), rj=rcpf_(bhi((u32)fs1)); rc[0]=f32x2{ri,-blo((u32)fs)*ri}; rc[1]=f32x2{rj,-blo((u32)fs1)*rj}; rc[2]= (lb&2)? rc[1]:rc[0];
      #pragma unroll
      for(int z=0;z<3;z++) xq[z]=X.q[z]; }
    if(DF) final_pre<2,3>(fp,w4,xq,dq,rc); else final_pre<0,1>(fp,w4,xq,dq,rc);
    ti=now_()-ldw;
    asm volatile("global_load_dwordx2 v[112:113], %0, %2 sc1\n\tglobal_load_dwordx2 v[114:115], %1, %2 sc1\n\tv_mov_b32 v117, %5\n\ts_cmp_lt_u32 %4, " STR(HT) "\n\ts_cbranch_scc1 1f\n\tglobal_load_dwordx2 v[116:117], %3, %2 sc1\n1:"
      :: "v"(fo_),"v"(pq_),"s"(ws),"v"(ho_),"s"(ti),"s"(~(u32)tag) : "memory","scc");
    if(DF) final_pre<0,1>(fp,w4,xq,dq,rc); else final_pre<1,3>(fp,w4,xq,dq,rc);
    STAMP(6); PRIO(EPR);
    { u64 ff,f; u32x2 h;
      asm volatile("s_waitcnt vmcnt(0)\n\tv_mov_b64 %0, v[112:113]\n\tv_mov_b64 %1, v[114:115]\n\tv_mov_b64 %2, v[116:117]" : "=v"(ff),"=v"(f),"=v"(h) :: "memory");
      _Pragma("unroll 1") for(int k=0;k<2;k++){ if(BALLOT(h[1]!=(u32)tag)==0ull||ti>=H2) break;
        tgate2(ldw,ti<HT? HT:H2); ti=now_()-ldw; if(ti>=HL){ h=u32x2{ldw,(u32)tag}; break; }
        asm volatile("global_load_dwordx2 %0, %1, %2 sc1\n\ts_waitcnt vmcnt(0)" : "=&v"(h) : "v"(ho_),"s"(ws) : "memory"); }
      u32 rel= h[1]==(u32)tag? h[0]-ldw : ti+HM; td=umin_(wmax(rel),ti+160u);
      if(td>HK) td-=(td-HK)>>1;
      if(__builtin_expect(BALLOT(ff!=tag)!=0ull,0)) poll1<0>(ws+fo_,tag);
      if(ix){ if(BALLOT(((f^(tag<<16))>>16)!=0ull)!=0ull) f=poll1<16>(ws+pq_,tag<<16);
        if(ln<8) ST(u16,sm+O_IDX+ln*2)=(u16)f; garrive(sm,C_IX); }
      __builtin_amdgcn_s_waitcnt(0x0f70); }
  }
  final_post(sm,fp,fa,w4,out,ws,xr_,ldw,td,xq,dq,rc SBPASS);
  SFLUSH(); }
}

extern "C" void kernel_launch(void* const* d_in, const int* in_sizes, int n_in, void* d_out, int out_size, void* d_ws, size_t ws_size, hipStream_t stream) {
  const u16** I=(const u16**)d_in;
  KP kp; kp.Are=I[1]; kp.Aim=I[2]; kp.Bre=I[3]; kp.Bim=I[4]; kp.Cre=I[5]; kp.Cim=I[6]; kp.ldt=I[7]; kp.pad=I[7];
  kA<<<256,1024,0,stream>>>(I[0],(char*)d_ws,I[10],I[8],I[11],I[12],I[13],(u16*)d_out,kp);
}
```

```cpp
#include <hip/hip_runtime.h>
#include <hip/hip_bf16.h>
#include <stdint.h>
#if defined(__HIP_DEVICE_COMPILE__)
#define NOPK __attribute__((target("no-packed-fp32-ops")))
#else
#define NOPK
#endif
#define DEV __device__ NOPK __forceinline__
typedef unsigned short u16;
typedef unsigned int u32;
typedef unsigned long long u64;
typedef short bf16x8 __attribute__((ext_vector_type(8)));
typedef _Float16 h8 __attribute__((ext_vector_type(8)));
typedef _Float16 h2 __attribute__((ext_vector_type(2)));
typedef float f32x4 __attribute__((ext_vector_type(4)));
typedef float f32x2 __attribute__((ext_vector_type(2)));
typedef u32 u32x4 __attribute__((ext_vector_type(4)));
typedef u32 u32x2 __attribute__((ext_vector_type(2)));
typedef u32 u32x8 __attribute__((ext_vector_type(8)));
typedef __bf16 bf2 __attribute__((ext_vector_type(2)));
typedef short v4s __attribute__((ext_vector_type(4)));
typedef __amdgpu_buffer_rsrc_t rsrc_t;
#define GAS __attribute__((address_space(1)))
#define LAS __attribute__((address_space(3)))
typedef LAS char lch;
#define L_ 2048
#define D_ 576
#define F_ 4608
#define BC(T,v) __builtin_bit_cast(T,v)
#define TID ((int)__builtin_amdgcn_workitem_id_x())
#define BID ((int)__builtin_amdgcn_workgroup_id_x())
#define RFL(x) __builtin_amdgcn_readfirstlane(x)
#define RLX_AGENT __ATOMIC_RELAXED, __HIP_MEMORY_SCOPE_AGENT
#define LD(T,p) (*(const LAS T*)(p))
#define ST(T,p) (*(LAS T*)(p))
#define SPINCAP (1<<17)
#define DRAIN() asm volatile("s_waitcnt vmcnt(0)" ::: "memory")
#define GLD(p) __hip_atomic_load((const GAS u64*)(p),RLX_AGENT)
#define GST(p,v) __hip_atomic_store((GAS u64*)(p),(u64)(v),RLX_AGENT)
#define BALLOT(c) __builtin_amdgcn_ballot_w64(c)
#define SC1 16
#define LANE(l) int l; { int t2_=TID; asm volatile("" : "+v"(t2_)); l=t2_&63; }
#define UE 6
#define W2C 7
#define S3 9
#define W3B 6
#define SCHED() __builtin_amdgcn_sched_barrier(0)
#define RSRC(p,n) __builtin_amdgcn_make_buffer_rsrc((void*)(p),0,(n),0x00020000)
#define BLD128 __builtin_amdgcn_raw_buffer_load_b128
#define BLD32 __builtin_amdgcn_raw_buffer_load_b32
#define BST128 __builtin_amdgcn_raw_buffer_store_b128
#define PRIO(n) __builtin_amdgcn_s_setprio(n)

DEV float b2f(u32 v){ return BC(float,v<<16); }
DEV float blo(u32 v){ return BC(float,v<<16); }
DEV float bhi(u32 v){ return BC(float,v&0xffff0000u); }
DEV u32 pk(float a,float b){ f32x2 v={a,b}; bf2 r=__builtin_convertvector(v,bf2); return BC(u32,r); }
DEV u32 pkh(float a,float b){ f32x2 v={a,b}; h2 r=__builtin_convertvector(v,h2); return BC(u32,r); }
DEV float rb(float f){ return BC(float,pk(f,f)<<16); }
DEV float rcpf_(float x){ return __builtin_amdgcn_rcpf(x); }
DEV float expf_(float x){
  const float L2E=1.4426950216293335f, L2El=1.9259629911266175e-8f; float h=x*L2E; float l=__builtin_fmaf(x,L2E,-h)+x*L2El;
  float e=__builtin_amdgcn_exp2f(h); return __builtin_fmaf(e*l,0.6931471805599453f,e); }
DEV float sigm_f(float x){ return rcpf_(1.f+__builtin_amdgcn_exp2f(-x*1.4426950408889634f)); }
DEV float silu_f(float x){ return x*sigm_f(x); }
DEV u32 umax_(u32 a,u32 b){ return a>b?a:b; }
DEV u32 umin_(u32 a,u32 b){ return a<b?a:b; }

#define YSH 3
#define PRE1 6
#define PRE3 4
#define PRE2 5
#define W1 5
#define W3 3
#define W2_ 4
#define W1B 20
#define W2B 7
#define RGATE 4
#define UGATE 7
#define R2D 12
#define PFN 22
#define CPF 8
#define HD0 174
#define HD1 148
#define HD3 165
#define HM 100
#define HT 555
#define H2 600
#define HL 650
#define HK 655
#define HKR 65
#define HR 12
#define ANA 410
#define ANB 470
#define ANC 540
#define NRG 8
#define W_EPOCH 0
#define W_RG 16384
#define W_ST (W_RG+32*8192)
#define W_U (W_ST+32768)
#define UST (D_*16+128)
#define YST (L_*2+128)
#define TL 1
#define GST_ ((D_*16<<TL)+128)
#define W_Y (W_U+256*UST)
#define W_PART (W_Y+D_*YST)
#define W_GU (W_PART+8*9*16*256)
#define W_FL 9216
#define W_HT (W_RG+16*8192)
#define W_IDX 1024
struct KP { const u16 *Are,*Aim,*Bre,*Bim,*Cre,*Cim,*ldt,*pad; };

template<int CTRL> DEV float dppf(float v){ return BC(float,__builtin_amdgcn_update_dpp(0u,BC(u32,v),CTRL,0xf,0xf,false)); }
template<int CTRL> DEV u32 dppu(u32 v){ return (u32)__builtin_amdgcn_update_dpp(0u,v,CTRL,0xf,0xf,false); }
DEV float sum16(float v){ v+=dppf<0xB1>(v); v+=dppf<0x4E>(v); v+=dppf<0x141>(v); v+=dppf<0x140>(v); return v; }
DEV float add16x(float v){ auto r=__builtin_amdgcn_permlane16_swap(BC(u32,v),BC(u32,v),false,false); u32 a=r[0], b=r[1]; return BC(float,a)+BC(float,b); }
DEV u32 wmax(u32 v){
  v=umax_(v,(u32)__builtin_amdgcn_update_dpp(0u,v,0x111,0xf,0xf,false));
  v=umax_(v,(u32)__builtin_amdgcn_update_dpp(0u,v,0x112,0xf,0xf,false));
  v=umax_(v,(u32)__builtin_amdgcn_update_dpp(0u,v,0x114,0xf,0xf,false));
  v=umax_(v,(u32)__builtin_amdgcn_update_dpp(0u,v,0x118,0xf,0xf,false));
  v=umax_(v,(u32)__builtin_amdgcn_update_dpp(0u,v,0x142,0xa,0xf,false));
  v=umax_(v,(u32)__builtin_amdgcn_update_dpp(0u,v,0x143,0xc,0xf,false));
  return (u32)__builtin_amdgcn_readlane((int)v,63); }

DEV void garrive(lch* sm,int c){ u32 ad=(u32)(size_t)sm+c*4, one=1;
  asm volatile("s_mov_b64 exec, 1\n\tds_add_u32 %0, %1\n\ts_mov_b64 exec, -1" :: "v"(ad),"v"(one) : "memory"); }
template<int SLP> DEV void gwait(lch* sm,int c,u32 target){ u32 ad=(u32)(size_t)sm+c*4, tv, cnt, sv; target=RFL(target);
  asm volatile("s_mov_b32 %1, 0x20000\n"
    "1:\n\tds_read_b32 %0, %3\n\ts_waitcnt lgkmcnt(0)\n\tv_readfirstlane_b32 %2, %0\n\ts_cmp_ge_u32 %2, %4\n\ts_cbranch_scc1 2f\n\ts_sleep %5\n\ts_sub_u32 %1, %1, 1\n\ts_cbranch_scc0 1b\n"
    "2:" : "=&v"(tv),"=&s"(cnt),"=&s"(sv) : "v"(ad),"s"(target),"i"(SLP) : "memory","scc"); }
DEV u32 now_(){ u32 t; asm volatile("s_memrealtime s[98:99]\n\ts_waitcnt lgkmcnt(0)\n\ts_mov_b32 %0, s98" : "=s"(t) :: "memory"); return t; }
DEV void tgate2(u32 t0,u32 d){ u32 cnt; asm volatile("s_mov_b32 %0, 100\n1:\n\ts_memrealtime s[98:99]\n\ts_waitcnt lgkmcnt(0)\n\ts_sub_u32 s98, s98, %1\n\ts_cmp_ge_u32 s98, %2\n\ts_cbranch_scc1 2f\n\ts_sleep 1\n\ts_sub_u32 %0, %0, 1\n\ts_cbranch_scc0 1b\n2:" : "=&s"(cnt) : "s"(t0),"s"(d) : "memory","scc"); }
DEV void gsync(lch* sm,int c,u32 target){ garrive(sm,c); gwait<0>(sm,c,target); }
template<int SH> DEV u64 poll1(const char* p,u64 want){ u64 f=GLD(p);
  _Pragma("unroll 1") for(int sp=0;sp<SPINCAP;sp++){ bool bad=((f^want)>>SH)!=0ull; if(BALLOT(bad)==0ull) break; __builtin_amdgcn_s_sleep(1); if(bad) f=GLD(p); }
  __builtin_amdgcn_fence(__ATOMIC_ACQUIRE,"wavefront"); return f; }
#define STR_(x) #x
#define STR(x) STR_(x)
DEV u32x4 mkrs(const void* p,u32 n){ u64 a=(u64)p; return u32x4{(u32)RFL((int)(u32)a),(u32)RFL((int)((u32)(a>>32)&0xffffu)),n,0x00020000u}; }
#define VMW(n) asm volatile("s_waitcnt vmcnt(%0)" :: "i"(n) : "memory")
#define YSLP 1
#define GSLP 1
#define QSLP 1
#define PSLP 3
#define POLLASM(LOADS,CHECK,K) "s_mov_b32 %[cnt], 0x20000\n1:\n\ts_waitcnt vmcnt(0)\n\t" CHECK "s_cbranch_vccz 9f\n\ts_sleep " STR(K) "\n\ts_mov_b64 exec, vcc\n\t" LOADS \
  "s_mov_b64 exec, -1\n\ts_sub_u32 %[cnt], %[cnt], 1\n\ts_cbranch_scc0 1b\n9:\n\t"
#define LD_DUP2 "buffer_load_dwordx4 v[112:115], %[of], %[rs], 0 offen sc1\n\tbuffer_load_dwordx4 v[116:119], %[of], %[rs], %[so] offen sc1\n\t"
#define MX2(a,b) "v_max3_u32 %[t0], %[t0], v" #a ", v" #b "\n\t"
#define CHK_I0 "v_max_u32 %[t0], v112, v113\n\t" MX2(114,115)
#define CHK_IE "v_cmp_eq_u32 vcc, -1, %[t0]\n\t"
#define PCLOB "memory","vcc","scc"
#define UMASK 0x40074007u
DEV u32 upat(u32 tag,int k){ return ((((tag*0xea959c21u)>>(8*k))&255u)*0x04000801u)&UMASK; }
DEV u32 pkz(float a,float b){ return BC(u32,__builtin_amdgcn_cvt_pkrtz(a,b)); }
#define INV 0xffffffffu
DEV u32 nv(u32 v){ return umin_(v,0xfffeffffu); }
DEV u32x4 nv4(u32x4 v){ return u32x4{nv(v[0]),nv(v[1]),nv(v[2]),nv(v[3])}; }
#define OOR 0x40000000

#define CHSZ 36864
#define O_CH 512
#define O_CND 256
#define SLSZ 38080
#define O_R2 (O_CH+2*SLSZ)
#define O_X2 (O_R2+CHSZ)
#define O_TILE (O_X2+CHSZ)
#define XSTR 1216
#define TSTR2 1216
#define O_CAND (O_TILE+9728)
#define LDS_TOTAL (O_CAND+128)
#define C_G 0
#define C_U 8
#define C_L 4
#define C_D 5
#define C_X 12
#define C_P 13
#define C_H 14
#define C_I 17
#define C_E 18
#define C_IX 19
#define C_RS 22
#define O_IDX 96
#define O_EXP 144
#define O_RMX 192
#define O_RV 128
#define O_MD 400
#define O_RC (O_CAND+64)

struct C2 { float r,i; };
DEV C2 cmul(C2 a,C2 b){ C2 c; c.r=__builtin_fmaf(a.r,b.r,-(a.i*b.i)); c.i=__builtin_fmaf(a.r,b.i,a.i*b.r); return c; }
DEV h8 ldA(const lch* p){ u32x2 a=*(const volatile LAS u32x2*)p, b=*(const volatile LAS u32x2*)(p+8); return BC(h8,(u32x4{a[0],a[1],b[0],b[1]})); }
DEV bf16x8 trfrag(const lch* p0,const lch* p1){
  u32x2 b0=BC(u32x2,__builtin_amdgcn_ds_read_tr16_b64_v4i16((LAS v4s*)p0)), b1=BC(u32x2,__builtin_amdgcn_ds_read_tr16_b64_v4i16((LAS v4s*)p1));
  return BC(bf16x8,(u32x4{b0[0],b0[1],b1[0],b1[1]})); }

struct FP { float gg[24], bs[24]; };
template<int Z0,int Z1> DEV void final_pre(FP& f,int w4,const u32x4* xq,const u32x4* dq,const f32x2* rc){
  #pragma unroll
  for(int z=Z0;z<Z1;z++) if(z<2||w4==0){ float ea=rc[z][0]*-1.4426950408889634f, eb=rc[z][1]*-1.4426950408889634f;
    #pragma unroll
    for(int e=0;e<4;e++){ u32 xw=xq[z][e], dw=dq[z>>1][e]; int i=8*z+2*e; float a=blo(xw), b=bhi(xw);
      float pa=__builtin_fmaf(a,rc[z][0],rc[z][1]), pb=__builtin_fmaf(b,rc[z][0],rc[z][1]);
      float ga=pa*rcpf_(1.f+__builtin_amdgcn_exp2f(__builtin_fmaf(a,ea,eb))), gb=pb*rcpf_(1.f+__builtin_amdgcn_exp2f(__builtin_fmaf(b,ea,eb)));
      f.gg[i]=ga; f.gg[i+1]=gb; f.bs[i]=__builtin_fmaf(pa*blo(dw),ga,a); f.bs[i+1]=__builtin_fmaf(pb*bhi(dw),gb,b);
      asm volatile("" : "+v"(f.gg[i]),"+v"(f.gg[i+1]),"+v"(f.bs[i]),"+v"(f.bs[i+1])); } }
}
#define FTS 544
DEV u32 pkmin(u32 a,u32 b){ u32 r; asm("v_pk_min_u16 %0, %1, %2" : "=v"(r) : "v"(a),"v"(b)); return r; }
struct FA { int yo,yo3; lch *wp,*wp3; const lch* tp; u32 tt; };
DEV void final_addr(FA& a,lch* sm,int w4){ LANE(ln); int bid=BID, xg=bid&7, lb=bid>>3, cg=lb&3, T=8*xg+(lb>>2);
  a.yo=(128*cg+32*w4+(ln>>2)*2)*YST+(32*T+8*(ln&3))*2; a.yo3= ln<32? (512+32*(cg&1)+(ln&30))*YST+(32*T+16*(cg>>1)+8*(ln&1))*2 : OOR;
  lch* fb=sm+O_X2+2048+w4*4608; a.wp=fb+(ln&3)*FTS+(ln&60); a.wp3=fb+(4+(ln&1)+(ln>>5)*2)*FTS+(ln&30)*2; a.tp=fb+(ln>>5)*FTS+(ln&31)*16;
  a.tt=(u32)(32*T+(ln>>2))*0x10001u;
  asm volatile("" : "+v"(a.yo),"+v"(a.yo3),"+v"(a.wp),"+v"(a.wp3),"+v"(a.tp),"+v"(a.tt)); }
DEV void final_post(lch* sm,FP& f,const FA& fa,int w4,u16* __restrict__ out,char* ws,int xr,u32 tk,u32 td,const u32x4* xq,const u32x4* dq,const f32x2* rc){
  int lb=BID>>3, cg=lb&3, ob[3], yo=fa.yo, yo3=fa.yo3; lch *wp=fa.wp, *wp3=fa.wp3; const lch* tp=fa.tp; u32 tt=fa.tt;
  u32x4 rs=mkrs(ws+W_Y,D_*YST); rsrc_t ro=RSRC(out,L_*D_*2);
  #define RMASK { gwait<2>(sm,C_IX,1); u32x4 iq=LD(u32x4,sm+O_IDX); \
    _Pragma("unroll") for(int z=0;z<2;z++){ u32 t2=tt+z*0x100010u, m=pkmin(pkmin(iq[0]^t2,iq[1]^t2),pkmin(iq[2]^t2,iq[3]^t2)); ob[z]= umin_(m&0xffffu,m>>16)==0u? OOR : xr+z*(16*D_*2); } \
    ob[2]=((cg&2)? ob[1]:ob[0])+(512+32*(cg&1)-128*cg)*2; asm volatile("" : "+v"(ob[0]),"+v"(ob[1]),"+v"(ob[2])); }
  tgate2(tk,td);
  if(lb>=8) gwait<1>(sm,C_H,4);
  { u32 t0,t1,cnt;
    #define LD_Y "buffer_load_dwordx4 v[112:115], %[of], %[rs], 0 offen sc1\n\tbuffer_load_dwordx4 v[116:119], %[of], %[rs], %[s1] offen sc1\n\ts_cmp_lg_u32 %[w], 0\n\ts_cbranch_scc1 5f\n\t" \
      "buffer_load_dwordx4 v[120:123], %[o3], %[rs], 0 offen sc1\n\tbuffer_load_dwordx4 v[124:127], %[o3], %[rs], %[s1] offen sc1\n5:\n\t"
    #define YIN [of]"v"(yo),[rs]"s"(rs),[s1]"s"(YST),[o3]"v"(yo3),[w]"s"(w4)
    asm volatile(LD_Y :: YIN : "memory","scc");
    PRIO(3); final_pre<1,2>(f,w4,xq,dq,rc);
    RMASK
    #define YW(p,a,b,o) "v_perm_b32 %[t0], v" #b ", v" #a ", %[lo]\n\tv_perm_b32 %[t1], v" #b ", v" #a ", %[hi]\n\tds_write2_b32 %[" #p "], %[t0], %[t1] offset0:" #o " offset1:" #o "+16\n\t"
    asm volatile(POLLASM(LD_Y,CHK_I0 MX2(116,117) MX2(118,119) "s_cmp_lg_u32 %[w], 0\n\ts_cbranch_scc1 6f\n\t" MX2(120,121) MX2(122,123) MX2(124,125) MX2(126,127) "6:\n\t" CHK_IE,YSLP)
      YW(wp,112,116,0) YW(wp,113,117,32) YW(wp,114,118,64) YW(wp,115,119,96) "s_cmp_lg_u32 %[w], 0\n\ts_cbranch_scc1 7f\n\t" YW(w3,120,124,0) YW(w3,121,125,32) YW(w3,122,126,64) YW(w3,123,127,96) "7:"
      : [t0]"=&v"(t0),[t1]"=&v"(t1),[cnt]"=&s"(cnt) : YIN,[wp]"v"(wp),[w3]"v"(wp3),[lo]"s"(0x05040100u),[hi]"s"(0x07060302u) : PCLOB); }
  u32x4 r[3];
  #pragma unroll
  for(int z=0;z<3;z++) r[z]=LD(u32x4,tp+z*(2*FTS));
  #pragma unroll
  for(int z=0;z<3;z++) if(z<2||w4==0){ u32x4 ov;
    #pragma unroll
    for(int e=0;e<4;e++){ u32 rw=r[z][e]; int i=8*z+2*e; ov[e]=pk(__builtin_fmaf(blo(rw),f.gg[i],f.bs[i]),__builtin_fmaf(bhi(rw),f.gg[i+1],f.bs[i+1])); }
    BST128(ov,ro,ob[z],0,SC1); SCHED(); }
}

#define TROW 528
#define O_TB 4640
#define O_DE 34752
#define O_DO 35968
#define O_R2K 37120
#define O_R3K 37632
#define O_DUM 37952
#define O_RED O_TB
#define MH(a,b,c) __builtin_amdgcn_mfma_f32_16x16x32_f16(a,b,c,0,0,0)
#define PIN(x) asm volatile("" : "+v"(x))
DEV h8 ld4w(const lch* p){ return BC(h8,(u32x4{LD(u32,p),LD(u32,p+4),LD(u32,p+8),LD(u32,p+12)})); }
DEV u32 __umul24_(u32 a,u32 b){ u32 r; asm("v_mul_u32_u24 %0, %1, %2" : "=v"(r) : "s"(b),"v"(a)); return r; }
DEV u32x4 band(u32 mask,int g,u32 val){ u32 by=__builtin_amdgcn_ubfe(mask,8*g,8), X=by|(by<<15), k=0x10001u; u32x4 r; asm volatile("" : "+s"(k),"+s"(val));
  #pragma unroll
  for(int d=0;d<4;d++) r[d]=__umul24_((X>>(2*d))&k,val);
  return r; }
DEV h8 zpack(f32x4 e,f32x4 o){ return BC(h8,(u32x4{pkh(e[0],o[0]),pkh(e[1],o[1]),pkh(e[2],o[2]),pkh(e[3],o[3])})); }
struct C3 { float r,i,n; };
DEV C3 c3(C2 a){ C3 c; c.r=a.r; c.i=a.i; PIN(c.i); c.n=-c.i; PIN(c.n); return c; }
DEV C2 cm(C2 a,C3 b){ C2 c; c.r=__builtin_fmaf(a.r,b.r,a.i*b.n); c.i=__builtin_fmaf(a.r,b.i,a.i*b.r); return c; }
DEV u32 ph(C2 a){ return pkh(a.r,a.i); }
#define W2(p,a,b) asm volatile("ds_write2_b32 %0, %1, %2 offset1:132" :: "v"(p),"v"(a),"v"(b) : "memory")
struct CP { const lch *x0,*x1,*bd; lch *p0,*p1,*p2,*wp; u32x4 Be,Bo,AE; int yo; };
template<int ROLE> DEV void lanes(CP& c,lch* cb,int ln){ int m=ln&15, g=ln>>4;
  const lch* ad=cb+((m&1)? O_DO+2*527 : O_DE+2*536)+2*(8*g-m);
  const lch* bd=cb+2*(256+16*m+8*g); lch* dum=cb+O_DUM;
  if(ROLE==0){ int l0=128*(g>>1)+8*m+4*(g&1); c.p0=cb+O_DE+(549-l0)*2; PIN(c.p0); c.p1=c.p0+6; PIN(c.p1); c.p2=c.p0+(O_DO-O_DE-18); c.x0=ad-640; c.bd=bd-64; c.x1=cb+O_R3K+2*(126-2*m+8*g)-192; PIN(c.x1);
    c.Be=band(0x1ffu<<(m+4),g,0x3c00u); PIN(c.Be); }
  if(ROLE==1){ lch* r=cb+O_R2K+(192-64*(g-2)-2*m)*2; c.p0= g<2? dum : r-66; c.p1= ln==0? cb+O_DE+(552-256)*2 : (ln<=32? dum : r); c.p2=cb+O_DE+(544-256-8*ln)*2; c.x0=ad-128-640; c.bd=bd-128-128; PIN(c.p0); PIN(c.p1); PIN(c.p2); }
  if(ROLE==2){ int i0=16*m+4*g; c.p0= m<8? cb+O_R3K+(125-i0)*2 : dum+2; c.p1= ln==0? cb+O_R2K+128 : (ln==8? cb+O_R3K : c.p0+6); c.x0=ad-320-640; c.bd=bd-320-128; PIN(c.p0); PIN(c.p1); }
  if(ROLE==3){ c.x0=cb+O_R2K+2*(188-4*m+8*g)-256; c.bd=cb+2*(248+8*m+8*g);
    c.Be=band(0x1fu<<(m+6),g,0x3c00u); c.Bo=band(0x7u<<(m+11),g,0x3c00u); PIN(c.Be); PIN(c.Bo); }
  PIN(c.x0); PIN(c.bd); }
template<int ROLE> DEV void lanes2(CP& c,lch* cb,int ln){ int m=ln&15, g=ln>>4;
  if(ROLE==0){ c.Bo=band(0x7fu<<(m+5),g,0x3c00u); c.AE=band(3u<<(m+12),g,0xffffu); PIN(c.Bo); PIN(c.AE); }
  c.wp=cb+O_RED+ln*16; c.yo=(g&1)*512+m*32+(g>>1)*16; PIN(c.wp); PIN(c.yo); }
template<int ROLE> DEV void chan2(CP& c,lch* sm,lch* cb,const lch*& fa,const lch*& fb,int grp,int d,int one_,int emax,u64 tag,char* ws){
  const f32x4 Z4={0,0,0,0}; h8 A[6]; f32x4 acc[8]; u32x4 rsY; float sc; u32 nvm; int ca;
  #define YPRE rsY=mkrs(ws+W_Y+d*YST,4096); sc=__builtin_amdgcn_ldexpf(1.f,emax-YSH); nvm=0xfffeffffu; asm volatile("" : "+s"(rsY),"+s"(nvm)); PIN(sc); ca=(C_G+grp)*4; PIN(ca);
  #define LD_U "buffer_load_dwordx4 v[112:115], %[of], %[rs], 0 offen sc1\n\tbuffer_load_dwordx4 v[116:119], %[of], %[rs], %[s1] offen sc1\n\tbuffer_load_dwordx4 v[120:123], %[of], %[rs], %[s2] offen sc1\n\tbuffer_load_dwordx4 v[124:127], %[of], %[rs], %[s3] offen sc1\n\t"
  #define UD(t,a,c) "v_bitop3_b32 " t ", " t ", v" #a ", %[" #c "] bitop3:0xf6\n\t"
  #define UX(a,b,c,d,o) "v_xor_b32 v" #a ", %[e0], v" #a "\n\tv_xor_b32 v" #b ", %[e1], v" #b "\n\tv_xor_b32 v" #c ", %[e2], v" #c "\n\tv_xor_b32 v" #d ", %[e3], v" #d "\n\tds_write_b128 %[up], v[" #a ":" #d "] offset:" #o "\n\t"
  #define UQ(n,a,b,c,d,o) "s_waitcnt vmcnt(" #n ")\n\t" UD("%[t0]",a,c0) UD("%[t1]",b,c1) UD("%[t0]",c,c2) UD("%[t1]",d,c3) UX(a,b,c,d,o)
  #define UPOLL { u32 t0,t1,cnt; asm volatile("s_mov_b32 %[cnt], 0x20000\n1:\n\tv_mov_b32 %[t0], 0\n\tv_mov_b32 %[t1], 0\n\t" UQ(3,112,113,114,115,544) UQ(2,116,117,118,119,1056) UQ(1,120,121,122,123,1568) UQ(0,124,125,126,127,2080) \
      "v_bitop3_b32 %[t0], %[t0], %[t1], %[mk] bitop3:0xa8\n\tv_cmp_ne_u32 vcc, 0, %[t0]\n\ts_cbranch_vccz 9f\n\ts_sleep " STR(PSLP) "\n\ts_mov_b64 exec, vcc\n\t" LD_U "s_sub_u32 %[cnt], %[cnt], 1\n\ts_cbranch_scc0 1b\n9:\n\t" \
      "s_mov_b64 exec, 1\n\tds_add_u32 %[cu], %[on]\n\ts_mov_b64 exec, -1" \
      : [t0]"=&v"(t0),[t1]"=&v"(t1),[cnt]"=&s"(cnt) : [of]"v"(c.p0),[rs]"s"(rsU),[s1]"s"((32>>TL)*GST_),[s2]"s"((64>>TL)*GST_),[s3]"s"((96>>TL)*GST_),[c0]"s"(c0),[c1]"s"(c1),[c2]"s"(c2),[c3]"s"(c3_), \
        [e0]"s"(c0^0x40004u),[e1]"s"(c1^0x40004u),[e2]"s"(c2^0x40004u),[e3]"s"(c3_^0x40004u),[mk]"s"(UMASK),[up]"v"(c.p1),[cu]"v"((C_U+one_)*4),[on]"v"(1) : PCLOB); }
  if(ROLE<3){
    h8 a[8], b[8];
    #pragma unroll
    for(int ks=0;ks<8;ks++){ a[ks]=LD(h8,fa+ks*64); b[ks]=LD(h8,fb+ks*64); }
    SCHED();
    LANE(ln); float fz= ln==32? 0.f : 4.76837158203125e-07f; if(ROLE==1) PIN(fz);
    SCHED();
    f32x4 k=Z4;
    #pragma unroll
    for(int ks=0;ks<8;ks++) k=MH(a[ks],b[ks],k);
    const float f=4.76837158203125e-07f;
    float k0=k[0]*f, k1=k[1]*f, k2=k[2]*f, k3=k[3]*f;
    u32 h32=pkh(k3,k2), h10=pkh(k1,k0);
    if(ROLE!=1){
      ST(u16,c.p0)=(u16)h32; ST(u32,c.p0+2)=pkh(k2,k1); ST(u16,c.p1)=(u16)(h10>>16);
      if(ROLE==0) ST(u32x2,c.p2)=u32x2{h32,h10};
    } else {
      float n0=dppf<0x130>(k[0])*fz, z1= fz==0.f? k0:k1, z2=k[2]*fz, z3=k[3]*fz; u32 h22=pkh(z2,z2);
      ST(u16,c.p0+64)=(u16)h10; ST(u16,c.p0+2)=(u16)(h32>>16); ST(u16,c.p0)=(u16)h32; ST(u16,c.p1)=(u16)(h10>>16);
      u32x4 vo={pkh(n0,z3),h22,pkh(z2,z1),pkh(k0,k0)}, ve={pkh(n0,n0),pkh(z3,z2),h22,pkh(z1,k0)};
      asm volatile("s_mov_b32 exec_hi, 1\n\tds_write_b128 %0, %1\n\tds_write_b128 %0, %2 offset:1200\n\ts_mov_b32 exec_hi, -1" :: "v"(c.p2),"v"(ve),"v"(vo) : "memory"); }
    gsync(sm,C_G+grp,7);
    u32 kv; A[0]=ld4w(c.x0+640); A[1]=ld4w(c.x0+128);
    if(ROLE==0){ A[2]=ld4w(c.x1+192); A[3]=ld4w(c.x1+128); A[4]=ld4w(c.x1+64); A[5]=ld4w(c.x1); kv=LD(u16,cb+O_R2K+128); }
    else { A[2]=ld4w(c.x0+576); A[3]=ld4w(c.x0+64); A[4]=ld4w(c.x0+512); A[5]=ld4w(c.x0); }
    SCHED();
    if(ROLE==0){ kv|=kv<<16; c.AE=u32x4{c.AE[0]&kv,c.AE[1]&kv,c.AE[2]&kv,c.AE[3]&kv}; PIN(c.AE); }
    int ha=W_HT+d*8; u32 hd= one_? HD3 : (grp==0? HD0:HD1); if(ROLE==2) asm volatile("v_mov_b32 v113, %2" : "+v"(ha),"+v"(hd) : "s"((u32)tag));
    YPRE
    PRIO(2);
    if(BID>=112) gwait<4>(sm,C_D,4);
    gwait<1>(sm,C_U+one_,2-one_);
    if(ROLE==2) asm volatile("s_memrealtime s[98:99]\n\ts_waitcnt lgkmcnt(0)\n\tv_add_u32 v112, s98, %1\n\ts_mov_b64 exec, 1\n\tglobal_store_dwordx2 %0, v[112:113], %2 sc1\n\ts_mov_b64 exec, -1" :: "v"(ha),"v"(hd),"s"(ws) : "memory");
  } else {
    u32 tg=(u32)tag, c0=upat(tg,0), c1=upat(tg,1), c2=upat(tg,2), c3_=upat(tg,3); u32x4 rsU=mkrs(ws+W_U,256*UST);
    if(!one_) PRIO(2);
    c.p0=(lch*)fa; c.p1=(lch*)fb;
    bool late= grp==0||BID>=112;
    asm volatile("s_mov_b64 exec, 1\n\tglobal_store_dwordx2 %0, %1, %2 sc1\n\ts_mov_b64 exec, -1" :: "v"(W_FL+d*8),"v"(tag),"s"(ws) : "memory");
    if(!late) asm volatile(LD_U :: [of]"v"(c.p0),[rs]"s"(rsU),[s1]"s"((32>>TL)*GST_),[s2]"s"((64>>TL)*GST_),[s3]"s"((96>>TL)*GST_) : "memory");
    #pragma unroll
    for(int j=2;j<8;j++){ acc[j]=Z4; PIN(acc[j]); }
    YPRE
    if(late){ if(BID<112) gwait<2>(sm,C_G,UGATE); else { gwait<1>(sm,C_RS,4); __builtin_amdgcn_s_sleep(UE); } asm volatile(LD_U :: [of]"v"(c.p0),[rs]"s"(rsU),[s1]"s"((32>>TL)*GST_),[s2]"s"((64>>TL)*GST_),[s3]"s"((96>>TL)*GST_) : "memory"); }
    if(!late) UPOLL
    gwait<1>(sm,C_G+grp,7);
    #pragma unroll
    for(int i=0;i<6;i++) A[i]=ldA(c.x0+320-64*i);
    if(late) UPOLL
    PRIO(2);
    gwait<1>(sm,C_U+one_,2-one_);
  }
  const lch* bd=c.bd; h8 Be=BC(h8,c.Be), Bo=BC(h8,c.Bo);
  #pragma unroll
  for(int j=0;j<(ROLE==3? 2:8);j++) acc[j]=Z4;
  #define RW(j) if((j)>=0&&((j)>>1)!=ROLE&&!(ROLE==3&&(j)<2)){ ST(f32x4,c.wp+((j)>>1)*6144+((ROLE-((j)>>1)-1)&3)*2048+((j)&1)*1024)=acc[(j)<0? 0:(j)]; SCHED(); }
  if(ROLE==1||ROLE==2){
    h8 B[8];
    #pragma unroll
    for(int q=0;q<8;q++) B[q]=LD(h8,bd+128+512*q);
    #pragma unroll
    for(int r=0;r<3;r++){
      #pragma unroll
      for(int dl=0;dl<8;dl++){ if(dl<7) acc[dl+1]=MH(A[2*r+1],B[dl],acc[dl+1]); SCHED(); acc[dl]=MH(A[2*r],B[dl],acc[dl]); SCHED();
        if(r<2){ B[dl]=LD(h8,bd+64-64*r+512*dl); SCHED(); } else RW(dl-2) }
      } }
  if(ROLE==0){
    h8 B[8], ue[4];
    #pragma unroll
    for(int kt=0;kt<4;kt++){ ue[kt]=LD(h8,bd+48+512*kt); B[kt]=LD(h8,bd+64+512*kt); }
    f32x4 ce[2],co[2]; h8 Z[2];
    ce[0]=MH(ue[0],Be,Z4); co[0]=MH(B[0],Bo,Z4); ce[1]=MH(ue[1],Be,Z4); co[1]=MH(B[1],Bo,Z4); Z[0]=zpack(ce[0],co[0]);
    #pragma unroll
    for(int kt=0;kt<4;kt++){
      SCHED();
      if(kt+2<4){ ce[kt&1]=MH(ue[kt+2],Be,Z4); co[kt&1]=MH(B[kt+2],Bo,Z4); }
      if(kt+1<4) Z[(kt+1)&1]=zpack(ce[(kt+1)&1],co[(kt+1)&1]);
      #pragma unroll
      for(int rho=0;rho+kt<4;rho++) acc[4+kt+rho]=MH(Z[kt&1],A[2+rho],acc[4+kt+rho]);
      SCHED(); B[4+kt]=LD(h8,bd+64+512*(4+kt)); if(kt==0) A[5]=BC(h8,c.AE); if(kt==1) A[4]=ld4w(c.x0-384); if(kt==2) A[3]=ld4w(c.x0+64); if(kt==3) A[2]=ld4w(c.x0+576); SCHED(); }
    #pragma unroll
    for(int dl=0;dl<8;dl++){ if(dl<4) acc[dl+4]=MH(A[5],B[dl],acc[dl+4]); SCHED(); if(dl<6) acc[dl+2]=MH(A[4],B[dl],acc[dl+2]); SCHED(); if(dl<7) acc[dl+1]=MH(A[1],B[dl],acc[dl+1]); SCHED(); acc[dl]=MH(A[0],B[dl],acc[dl]);
      SCHED(); B[dl]=LD(h8,bd+512*dl); SCHED(); }
    #pragma unroll
    for(int dl=0;dl<8;dl++){ if(dl<7) acc[dl+1]=MH(A[3],B[dl],acc[dl+1]); SCHED(); acc[dl]=MH(A[2],B[dl],acc[dl]); SCHED(); RW(dl-2) } }
  if(ROLE==3){
    h8 U[12];
    #pragma unroll
    for(int kt=0;kt<6;kt++) U[kt]=LD(h8,bd+256*kt);
    f32x4 ce[2],co[2]; h8 Z[2];
    ce[0]=MH(U[0],Be,Z4); co[0]=MH(U[0],Bo,Z4); ce[1]=MH(U[1],Be,Z4); co[1]=MH(U[1],Bo,Z4); Z[0]=zpack(ce[0],co[0]);
    #pragma unroll
    for(int kt=0;kt<12;kt++){
      SCHED();
      if(kt+2<12){ ce[kt&1]=MH(U[kt+2],Be,Z4); co[kt&1]=MH(U[kt+2],Bo,Z4); }
      if(kt+1<12) Z[(kt+1)&1]=zpack(ce[(kt+1)&1],co[(kt+1)&1]);
      #pragma unroll
      for(int rho=-1;rho<5;rho++){ int j=(kt+4+rho)>>1; if(((kt+rho)&1)==0&&j>=2&&j<8) acc[j]=MH(Z[kt&1],A[rho+1],acc[j]); }
      if(kt<6){ SCHED(); U[kt+6]=LD(h8,bd+256*(kt+6)); }
      if(kt>=3&&kt<=9&&(kt&1)){ SCHED(); RW((kt+1)/2) } }
  }
  PRIO(1);
  RW(6) RW(7)
  asm volatile("s_mov_b64 exec, 1\n\tds_add_u32 %0, %1\n\ts_mov_b64 exec, -1" :: "v"(ca),"v"(1) : "memory");
  { float s0[4], s1[4];
    f32x4 o0=acc[2*ROLE], o1=acc[2*ROLE+1];
    #define RD6 "ds_read_b128 v[112:115], %[rp] offset:%[o]\n\tds_read_b128 v[116:119], %[rp] offset:%[o]+1024\n\tds_read_b128 v[120:123], %[rp] offset:%[o]+2048\n\tds_read_b128 v[124:127], %[rp] offset:%[o]+3072\n\t.if %[o]\n\tds_read_b128 v[104:107], %[rp] offset:%[o]+4096\n\tds_read_b128 v[108:111], %[rp] offset:%[o]+5120\n\t.endif\n\t"
    #define MU4(p) "v_mul_f32 %[" #p "0], %[sc], %[i" #p "0]\n\tv_mul_f32 %[" #p "1], %[sc], %[i" #p "1]\n\tv_mul_f32 %[" #p "2], %[sc], %[i" #p "2]\n\tv_mul_f32 %[" #p "3], %[sc], %[i" #p "3]\n\t"
    #define FM4(p,r,k) "s_waitcnt lgkmcnt(%[n]-" #k ")\n\tv_fmac_f32 %[" #p "0], %[sc], v[" #r "]\n\tv_fmac_f32 %[" #p "1], %[sc], v[" #r "+1]\n\tv_fmac_f32 %[" #p "2], %[sc], v[" #r "+2]\n\tv_fmac_f32 %[" #p "3], %[sc], v[" #r "+3]\n\t"
    { u32 cnt,sv; asm volatile("ds_read_b32 v103, %[ca]\n\ts_nop 1\n\t" MU4(a) MU4(b) "s_mov_b32 %[cnt], 0x20000\n\ts_branch 4f\n"
        "1:\n\tds_read_b32 v103, %[ca]\n4:\n\ts_waitcnt lgkmcnt(0)\n\tv_readfirstlane_b32 %[sv], v103\n\ts_cmp_ge_u32 %[sv], 11\n\ts_cbranch_scc1 2f\n\ts_sleep 0\n\ts_sub_u32 %[cnt], %[cnt], 1\n\ts_cbranch_scc0 1b\n2:\n\t" RD6
        FM4(a,112,1) FM4(b,116,2) FM4(a,120,3) FM4(b,124,4) ".if %[o]\n\t" FM4(a,104,5) FM4(b,108,6) ".endif"
        : [cnt]"=&s"(cnt),[sv]"=&s"(sv),[a0]"=&v"(s0[0]),[a1]"=&v"(s0[1]),[a2]"=&v"(s0[2]),[a3]"=&v"(s0[3]),[b0]"=&v"(s1[0]),[b1]"=&v"(s1[1]),[b2]"=&v"(s1[2]),[b3]"=&v"(s1[3])
        : [ia0]"v"(o0[0]),[ia1]"v"(o0[1]),[ia2]"v"(o0[2]),[ia3]"v"(o0[3]),[ib0]"v"(o1[0]),[ib1]"v"(o1[1]),[ib2]"v"(o1[2]),[ib3]"v"(o1[3]),[ca]"v"(ca),[rp]"v"(c.wp),[sc]"v"(sc),[o]"i"(ROLE*6144),[n]"i"(ROLE==0? 4:6) : "memory","scc","v103","v104","v105","v106","v107","v108","v109","v110","v111"); }
    #define NV(v) umin_(v,nvm)
    auto r0=__builtin_amdgcn_permlane16_swap(NV(pk(s0[0],s0[1])),NV(pk(s1[0],s1[1])),false,false); auto r1=__builtin_amdgcn_permlane16_swap(NV(pk(s0[2],s0[3])),NV(pk(s1[2],s1[3])),false,false);
    asm volatile("buffer_store_dwordx4 %0, %1, %2, 0 offen offset:%3 sc1\n\ts_nop 1" :: "v"(u32x4{r0[0],r1[0],r0[1],r1[1]}),"v"(c.yo),"s"(rsY),"i"(ROLE*1024) : "memory"); }
  garrive(sm,C_E);

  asm volatile("s_endpgm" : "=v"(fa),"=v"(fb),"=v"(c.x0),"=v"(c.x1),"=v"(c.bd),"=v"(c.p0),"=v"(c.p1),"=v"(c.p2),"=v"(c.wp),"=v"(c.Be),"=v"(c.Bo),"=v"(c.AE),"=v"(c.yo) :: "memory");
}
#define GETEP asm volatile("s_waitcnt lgkmcnt(0)\n\ts_mov_b64 %0, s[96:97]\n\ts_mov_b32 %1, s98" : "=s"(ep),"=s"(ldw));
DEV void chan(lch* sm,int grp,int w4,int d,int one_,char* ws,u32x4 q0,u32x4 q1,u32x4 q2,u32x4 q3,u32x4 q4){
  lch* cb=sm+O_CH+grp*SLSZ; int role=w4, emax;
  #define LP(v) ((lch*)(size_t)(u32)(v))
  const lch* fa=LP(q4[0]); const lch* fb=LP(q4[1]);
  u64 tag; CP c;
  { float a2,bp,br,bi,cr,ci;
    u32 fq=q3[2], fr=q3[3], one=q4[2]; float f64=BC(float,fq), f128=BC(float,fr);
    lch *t8[4]={LP(q0[0]),LP(q0[1]),LP(q0[2]),LP(q0[3])}, *tB[4]={LP(q1[0]),LP(q1[1]),LP(q1[2]),LP(q1[3])}, *t4[2]={LP(q2[0]),LP(q2[1])}, *t2[2]={LP(q2[2]),LP(q2[3])}, *t1=LP(q3[0]), *te=LP(q3[1]);
    { LANE(ln); asm volatile("" : "=v"(c.x1),"=v"(c.p0),"=v"(c.p1),"=v"(c.p2),"=v"(c.Be),"=v"(c.Bo),"=v"(c.AE));
      if(role==0){ lanes<0>(c,cb,ln); lanes2<0>(c,cb,ln); } else { if(role==1) lanes<1>(c,cb,ln); else if(role==2) lanes<2>(c,cb,ln); else lanes<3>(c,cb,ln); lanes2<1>(c,cb,ln); }
      asm volatile("" : "+v"(c.x0),"+v"(c.x1),"+v"(c.bd),"+v"(c.p0),"+v"(c.p1),"+v"(c.p2),"+v"(c.wp),"+v"(c.Be),"+v"(c.Bo),"+v"(c.AE),"+v"(c.yo)); }
    if(grp!=0) PRIO(1);
    u64 ep; u32 ldw; GETEP tag=ep+1ull;
    float dt=rb(expf_((d&1)? bhi(ldw):blo(ldw))), ca=dt*1.4426950408889634f, cb_=dt*0.15915494309189535f;
    asm volatile("s_waitcnt vmcnt(1)\n\t" "v_mul_f32 %2, %8, v114\n\tv_mul_f32 %3, %8, v115\n\tv_mul_f32 %0, %6, v112\n\tv_mul_f32 %1, %7, v113\n\tv_mov_b32 %4, v116\n\tv_mov_b32 %5, v117"
      : "=&v"(a2),"=&v"(bp),"=&v"(br),"=&v"(bi),"=&v"(cr),"=&v"(ci) : "v"(ca),"v"(cb_),"v"(dt) : "memory");
    C2 W; W.r=cr*br-ci*bi; W.i=cr*bi+ci*br;
    u32 mx=BC(u32,__builtin_fmaxf(__builtin_fabsf(W.r),__builtin_fabsf(W.i)));
    #define MXS(C) mx=umax_(mx,dppu<C>(mx));
    float e8=a2*8.f, eB=__builtin_fmaf(a2,f64,14.f), p8=bp*8.f, pB=bp*f64; SCHED();
    MXS(0xB1) p8=__builtin_amdgcn_fractf(p8); pB=__builtin_amdgcn_fractf(pB); e8=__builtin_amdgcn_exp2f(e8); SCHED();
    MXS(0x4E) eB=__builtin_amdgcn_exp2f(eB); float c8=__builtin_amdgcn_cosf(p8); SCHED();
    MXS(0x141) float s8=__builtin_amdgcn_sinf(p8), cB=__builtin_amdgcn_cosf(pB); SCHED();
    float sB=__builtin_amdgcn_sinf(pB); PIN(sB); MXS(0x140) float e1=__builtin_amdgcn_exp2f(a2); PIN(e1); SCHED();
    asm volatile("s_mov_b64 exec, 0x10001\n\tds_max_u32 %0, %1\n\tds_add_u32 %0, %2 offset:4\n\ts_mov_b64 exec, -1" :: "v"(te),"v"(mx),"v"(one) : "memory");
    C3 z8; z8.r=e8*c8; z8.n=e8*s8; z8.i=-z8.n; PIN(z8.i); C2 B; B.r=eB*cB; B.i=-(eB*sB);
    float p1=__builtin_amdgcn_fractf(bp), pE=__builtin_amdgcn_fractf(bp*256.f), pV=__builtin_amdgcn_fractf(bp*f128), eE=a2*256.f, eV=a2*f128, tr[8];
    #pragma unroll
    for(int k=0;k<4;k++){ SCHED(); u32 h0=ph(B); B=cm(B,z8);
      if(k==0){ tr[0]=__builtin_amdgcn_cosf(pV); tr[1]=__builtin_amdgcn_sinf(pV); } if(k==1){ tr[2]=__builtin_amdgcn_exp2f(eV); tr[3]=__builtin_amdgcn_cosf(p1); }
      if(k==2){ tr[4]=__builtin_amdgcn_sinf(p1); tr[5]=__builtin_amdgcn_exp2f(eE); } if(k==3){ tr[6]=__builtin_amdgcn_cosf(pE); tr[7]=__builtin_amdgcn_sinf(pE); }
      PIN(tr[2*k]); PIN(tr[2*k+1]);
      u32 h1=ph(B); if(k<3) B=cm(B,z8); W2(tB[k],h0,h1); }
    asm volatile("ds_read_b64 v[118:119], %0" :: "v"(te) : "memory");
    SCHED();
    C3 z1=c3(C2{e1*tr[3],e1*tr[4]}), E=c3(C2{tr[5]*tr[6],tr[5]*tr[7]}); C2 V[8]; V[0]=cmul(W,C2{tr[2]*tr[0],tr[2]*tr[1]});
    { u32 cnt,n; PIN(V[0].r); PIN(V[0].i); PIN(z1.r); PIN(E.r);
      asm volatile("s_mov_b32 %1, 0x20000\n1:\n\ts_waitcnt lgkmcnt(0)\n\tv_readfirstlane_b32 %2, v119\n\tv_readfirstlane_b32 %0, v118\n\ts_cmp_eq_u32 %2, 8\n\ts_cbranch_scc1 2f\n\tds_read_b64 v[118:119], %3\n\ts_sub_u32 %1, %1, 1\n\ts_cbranch_scc0 1b\n2:"
        : "=&s"(emax),"=&s"(cnt),"=&s"(n) : "v"(te) : "memory","scc");
      emax=RFL(emax); emax=(emax>>23)-126; }
    V[0].r=__builtin_amdgcn_ldexpf(V[0].r,14-emax); V[0].i=__builtin_amdgcn_ldexpf(V[0].i,14-emax);
    C2 Y=cm(V[0],E), Y4; u32 y0=ph(Y);
    V[1]=cm(V[0],z1); W2(t8[0],ph(V[0]),ph(V[1])); Y=cm(Y,E); u32 y1=ph(Y);
    V[2]=cm(V[1],z1); Y=cm(Y,E); u32 y2=ph(Y);
    V[3]=cm(V[2],z1); W2(t8[1],ph(V[2]),ph(V[3])); W2(t4[0],y0,ph(cm(V[2],E)));
    V[4]=cm(V[3],z1); Y=cm(Y,E); ST(u32,t1)=ph(Y);
    V[5]=cm(V[4],z1); W2(t8[2],ph(V[4]),ph(V[5])); Y4=cm(V[4],E); u32 y4=ph(Y4);
    V[6]=cm(V[5],z1); Y=cm(Y,E); ST(u32,t1+2*TROW)=ph(Y);
    V[7]=cm(V[6],z1); W2(t8[3],ph(V[6]),ph(V[7])); Y4=cm(Y4,E); W2(t2[0],y1,ph(Y4));
    W2(t4[1],y4,ph(cm(V[6],E))); Y=cm(Y,E); ST(u32,t1+4*TROW)=ph(Y);
    Y4=cm(Y4,E); W2(t2[1],y2,ph(Y4)); Y=cm(Y,E); ST(u32,t1+6*TROW)=ph(Y);
    Y=cm(Y,E); asm volatile("s_mov_b32 exec_hi, 0\n\tds_write_b32 %0, %1 offset:4224\n\ts_mov_b32 exec_hi, -1" :: "v"(t1),"v"(ph(Y)) : "memory");
  }
  if(one_) PRIO(3);
  DRAIN();
  gsync(sm,C_G+grp,4);
  if(role==0) chan2<0>(c,sm,cb,fa,fb,grp,d,one_,emax,tag,ws);
  if(role==1) chan2<1>(c,sm,cb,fa,fb,grp,d,one_,emax,tag,ws);
  if(role==2) chan2<2>(c,sm,cb,fa,fb,grp,d,one_,emax,tag,ws);
  chan2<3>(c,sm,cb,fa,fb,grp,d,one_,emax,tag,ws);
}

struct XR { int so,x0,x2; u32x4 rx,q[4]; };
DEV void xreq(XR& X,char* ws){ asm volatile("global_load_dwordx2 v[108:109], %[so], %[ws] sc1\n\tglobal_load_dwordx2 v[110:111], %[so], %[ws] offset:128 sc1\n\tbuffer_load_dwordx4 %[a], %[x0], %[rx], 0 offen\n\tbuffer_load_dwordx4 %[b], %[x0], %[rx], %[s1] offen\n\tbuffer_load_dwordx4 %[c], %[x2], %[rx], 0 offen"
  : [a]"=&{v[96:99]}"(X.q[0]),[b]"=&{v[100:103]}"(X.q[1]),[c]"=&{v[104:107]}"(X.q[2]),"=&{v[108:111]}"(X.q[3]) : [so]"v"(X.so),[ws]"s"(ws),[x0]"v"(X.x0),[x2]"v"(X.x2),[rx]"s"(X.rx),[s1]"s"(16*D_*2) : "memory"); }
DEV u32 med3u(u32 a,u32 b,u32 c){ u32 r; asm("v_med3_u32 %0, %1, %2, %3" : "=v"(r) : "v"(a),"v"(b),"v"(c)); return r; }
DEV u32 max3u(u32 a,u32 b,u32 c){ return umax_(umax_(a,b),c); }
#define GTC(r,s) "v_cmp_gt_u32 %[" #s "], v" #r ", %[c]\n\t"
#define GTA(s) "v_addc_co_u32 %[rk], vcc, 0, %[rk], %[" #s "]\n\t"
#define GT4(a,b,c,d) GTC(a,s0) GTC(b,s1) GTC(c,s2) GTC(d,s3) GTA(s0) GTA(s1) GTA(s2) GTA(s3)
#define GTS [s0]"=&s"(q0),[s1]"=&s"(q1),[s2]"=&s"(q2),[s3]"=&s"(q3)
DEV void top8(lch* sm,int w4,int ln,u32 M,char* ws,u32& rank_,u32& key_){
  u32 k[8], rc[8], hm=0xffff0000u; u64 em=0x0001000100010001ull,q0,q1,q2,q3;
  u32x4 rs=mkrs(ws+W_RG+(BID&(NRG-1))*8192,8192); int ro=w4*1024+ln*16;
  #pragma unroll
  for(int e=0;e<8;e++){ rc[e]=(u32)(0xfff-(w4*256+ln*4+(e&3)+(e>>2)*1024)); PIN(rc[e]); }
  lch *aW=sm+O_RMX+(w4*4+(ln>>4))*4, *aC=sm+O_RMX+(ln>>2)*4, *aO=sm+O_RMX+(ln&3)*16, *dC=sm+O_CND+(ln>>1)*4, *dO=sm+O_CND+(ln&1)*64; PIN(aW); PIN(aC); PIN(aO); PIN(dC); PIN(dO); PIN(ro); PIN(ln);
  asm volatile("" : "+s"(hm),"+s"(em),"+s"(rs));
  gwait<2>(sm,C_G,RGATE);
  { u32 cnt;
    #define KL(e,r) "v_lshl_or_b32 %[k" #e "], v" #r ", 16, %[k" #e "]\n\t"
    #define KH(e,r) "v_and_or_b32 %[k" #e "], v" #r ", %[hm], %[k" #e "]\n\t"
    #define CHK_R "v_bitop3_b32 %[t0], v113, v115, %[M] bitop3:0x7e\n\tv_bitop3_b32 %[t1], v117, v119, %[M] bitop3:0x7e\n\tv_or_b32 %[t0], %[t0], %[t1]\n\tv_cmp_ne_u32 vcc, 0, %[t0]\n\t"
    u32 t0,t1;
    asm volatile(LD_DUP2
      "s_sleep " STR(R2D) "\n\tbuffer_load_dwordx4 v[120:123], %[of], %[rs], 0 offen sc1\n\tbuffer_load_dwordx4 v[124:127], %[of], %[rs], %[so] offen sc1\n\ts_mov_b32 %[cnt], 0x20000\n\ts_waitcnt vmcnt(2)\n\t" CHK_R "s_cbranch_vccz 9f\n\t"
      "s_waitcnt vmcnt(0)\n\tv_mov_b64 v[112:113], v[120:121]\n\tv_mov_b64 v[114:115], v[122:123]\n\tv_mov_b64 v[116:117], v[124:125]\n\tv_mov_b64 v[118:119], v[126:127]\n\t"
      POLLASM(LD_DUP2,CHK_R,PSLP)
      KL(0,112) KH(1,112) KL(2,114) KH(3,114) KL(4,116) KH(5,116) KL(6,118) KH(7,118)
      : [k0]"+v"(rc[0]),[k1]"+v"(rc[1]),[k2]"+v"(rc[2]),[k3]"+v"(rc[3]),[k4]"+v"(rc[4]),[k5]"+v"(rc[5]),[k6]"+v"(rc[6]),[k7]"+v"(rc[7]),[cnt]"=&s"(cnt),[t0]"=&v"(t0),[t1]"=&v"(t1) : [of]"v"(ro),[rs]"s"(rs),[so]"s"(4096),[M]"s"(M),[hm]"s"(hm) : PCLOB); }
  garrive(sm,C_RS);
  #pragma unroll
  for(int e=0;e<8;e++) k[e]=rc[e];
  u32 h=max3u(k[0],k[1],k[2]), m1=max3u(max3u(k[3],k[4],k[5]),max3u(k[6],k[7],h),h), mx=m1, l=med3u(k[0],k[1],k[2]), t=umin_(umin_(k[0],k[1]),k[2]);
  SCHED();
  #define INS(e) t=med3u(t,l,k[e]); l=med3u(h,l,k[e]); if(e<7) h=umax_(h,k[e]); PIN(t); PIN(l); SCHED();
  mx=umax_(mx,dppu<0xB1>(mx)); INS(3) mx=umax_(mx,dppu<0x4E>(mx)); INS(4) mx=umax_(mx,dppu<0x141>(mx)); INS(5) mx=umax_(mx,dppu<0x140>(mx));
  asm volatile("s_mov_b64 exec, %2\n\tds_write_b32 %0, %1\n\ts_mov_b64 exec, -1\n\tds_read_b32 v116, %3\n\tds_read_b128 v[112:115], %4" :: "v"(aW),"v"(mx),"s"(em),"v"(aC),"v"(aO) : "memory");
  INS(6) INS(7)
  u32 m2=l, m3=t, T;
  { u32 c,rk,cnt;
    asm volatile("s_mov_b32 %[cnt], 0x20000\n1:\n\ts_waitcnt lgkmcnt(0)\n\tv_cmp_eq_u32 vcc, 0, v116\n\ts_cbranch_vccz 2f\n\tds_read_b32 v116, %[aC]\n\tds_read_b128 v[112:115], %[aO]\n\ts_sub_u32 %[cnt], %[cnt], 1\n\ts_cbranch_scc0 1b\n2:\n\t"
      "v_mov_b32 %[c], v116\n\tv_mov_b32 %[rk], 0\n\t" GT4(112,113,114,115) "s_nop 1\n\tv_add_u32_dpp %[rk], %[rk], %[rk] quad_perm:[1,0,3,2] row_mask:0xf bank_mask:0xf\n\ts_nop 1\n\tv_add_u32_dpp %[rk], %[rk], %[rk] quad_perm:[2,3,0,1] row_mask:0xf bank_mask:0xf\n\t"
      "v_cmp_eq_u32 vcc, 7, %[rk]\n\ts_ff1_i32_b64 %[cnt], vcc\n\ts_nop 0\n\tv_readlane_b32 %[T], %[c], %[cnt]\n\ts_nop 1"
      : [c]"=&v"(c),[rk]"=&v"(rk),[cnt]"=&s"(cnt),[T]"=&s"(T),GTS : [aC]"v"(aC),[aO]"v"(aO) : PCLOB); T=RFL(T); }
  { u64 b1=BALLOT(m1>=T), b2=BALLOT(m2>=T), b3=BALLOT(m3>=T); int n1=__builtin_popcountll(b1), n2=n1+__builtin_popcountll(b2);
    lch* cw=sm+O_CND+w4*32;
    if(__builtin_expect(b3==0ull&&n2<=8,1)){
      int p1=(int)__builtin_amdgcn_mbcnt_hi((u32)(b1>>32),__builtin_amdgcn_mbcnt_lo((u32)b1,0u)), pf=n2+ln-p1; bool c1=m1>=T;
      if(__builtin_expect(b2!=0ull,0)){ int p2=(int)__builtin_amdgcn_mbcnt_hi((u32)(b2>>32),__builtin_amdgcn_mbcnt_lo((u32)b2,(u32)n1)); if(m2>=T) ST(u32,cw+p2*4)=m2; }
      int ps= c1? p1:pf; ps= ps<8? ps:32-8*w4; ST(u32,cw+ps*4)= c1? m1:1u;
    } else {
      u32 mine=0;
      _Pragma("unroll 1") for(int c=0;c<8;c++){ u32 lm=umax_(umax_(umax_(k[0],k[1]),umax_(k[2],k[3])),umax_(umax_(k[4],k[5]),umax_(k[6],k[7]))); u32 wm=wmax(lm); if(ln==c) mine=wm;
        #pragma unroll
        for(int e=0;e<8;e++) k[e]=(k[e]==wm)?0u:k[e]; }
      if(ln<8) ST(u32,cw+ln*4)=mine; } }
  { u32 c,rk,cnt;
    asm volatile("s_mov_b32 %[cnt], 0x20000\n\tv_mov_b32 %[rk], 0\n\ts_waitcnt vmcnt(0)\n1:\n\tds_read_b32 %[c], %[dC]\n\tds_read_b128 v[112:115], %[dO]\n\tds_read_b128 v[116:119], %[dO] offset:16\n\tds_read_b128 v[120:123], %[dO] offset:32\n\tds_read_b128 v[124:127], %[dO] offset:48\n\t"
      "s_waitcnt lgkmcnt(4)\n\tv_cmp_eq_u32 vcc, 0, %[c]\n\ts_cbranch_vccz 2f\n\ts_sub_u32 %[cnt], %[cnt], 1\n\ts_cbranch_scc0 1b\n2:\n\t"
      "s_waitcnt lgkmcnt(3)\n\t" GT4(112,113,114,115) "s_waitcnt lgkmcnt(2)\n\t" GT4(116,117,118,119) "s_waitcnt lgkmcnt(1)\n\t" GT4(120,121,122,123) "s_waitcnt lgkmcnt(0)\n\t" GT4(124,125,126,127)
      "s_nop 1\n\tv_add_u32_dpp %[rk], %[rk], %[rk] quad_perm:[1,0,3,2] row_mask:0xf bank_mask:0xf"
      : [c]"=&v"(c),[rk]"=&v"(rk),[cnt]"=&s"(cnt),GTS : [dC]"v"(dC),[dO]"v"(dO) : PCLOB);
    rank_=rk; key_=c; }
}

#define FILL_(n,WAIT) "s_mov_b32 m0, %[ld]\n\ts_mov_b32 %[c], " STR(n) "-1\n3:\n\t" WAIT "buffer_load_dwordx4 %[vo], %[rs], %[so] offen nt lds\n\ts_add_u32 m0, m0, 0x1000\n\ts_add_u32 %[so], %[so], %[st]\n\t" \
  "s_sub_u32 %[c], %[c], 1\n\ts_cbranch_scc0 3b\n\ts_mov_b32 %[ld], m0\n\t"
#define FILL(n,WAIT) do{ u32 c_; asm volatile(FILL_(n,WAIT) : [c]"=&s"(c_),[so]"+s"(wso),[ld]"+s"(wld) : [vo]"v"(wvo),[rs]"s"(wrs),[st]"s"(wst) : "memory","scc","m0"); }while(0)
#define FILLD(n,WN,WW_) do{ u32 c_,t_,s_; asm volatile(FILL_(n,"ds_read_b32 %[t], %[ad]\n\ts_waitcnt lgkmcnt(0)\n\tv_readfirstlane_b32 %[s], %[t]\n\ts_cmp_ge_u32 %[s], 4\n\ts_cbranch_scc1 4f\n\t" VW(WN) "4:\n\t" VW(WW_)) \
  : [c]"=&s"(c_),[so]"+s"(wso),[ld]"+s"(wld),[t]"=&v"(t_),[s]"=&s"(s_) : [vo]"v"(wvo),[rs]"s"(wrs),[st]"s"(wst),[ad]"v"(C_D*4) : "memory","scc","m0"); wso=RFL(wso); wld=RFL(wld); }while(0)
#define VW(n) "s_waitcnt vmcnt(" STR(n) ")\n\t"

__global__ NOPK __attribute__((amdgpu_num_vgpr(56),amdgpu_num_sgpr(102))) __launch_bounds__(1024,4) void kA(const u16* x_,char* ws_,const u16* wr_,const u16* Dsk_,const u16* wg_,const u16* wu_,const u16* wd_,u16* out_,KP kp){
  const u16 *x,*Dsk,*wg,*wu,*wd; char* ws; u16* out;
  __shared__ __attribute__((aligned(16))) char smem[LDS_TOTAL];
  lch* sm=(lch*)smem;
  u32x4 xa,xb_,wa,wb,dq[3],la,lb_; u32x2 lc; u32 xc,wc;
  #define SARR "v_mov_b32 v121, 80\n\tv_mov_b32 v122, 1\n\ts_mov_b64 exec, 1\n\tds_add_u32 v121, v122\n\ts_mov_b64 exec, -1\n\t"
  asm volatile(
    "v_readfirstlane_b32 s60, %[t]\n\tv_lshlrev_b32 v119, 1, %[t]\n\tv_mov_b32 v118, 0\n\ts_cmpk_ge_u32 s60, 0x100\n\ts_cbranch_scc1 5f\n\tds_write_b16 v119, v118\n\ts_waitcnt lgkmcnt(0)\n5:\n\t"
    "s_load_dwordx16 s[64:79], %[ka], 0x0\n\ts_load_dwordx16 s[80:95], %[ka], 0x40\n\t"
    "s_and_b32 s61, %[wg], 7\n\ts_lshr_b32 s62, %[wg], 3\n\tv_and_b32 v118, 31, %[t]\n\ts_bfe_u32 s63, s60, 0x20006\n\ts_cmpk_ge_u32 s60, 0x100\n\ts_cbranch_scc1 1f\n\t"
    "s_barrier\n\ts_setprio 3\n\ts_lshl_b32 s56, s61, 5\n\ts_add_u32 s56, s56, s62\n\ts_mulk_i32 s56, 0x2400\n\tv_lshrrev_b32 v119, 5, %[t]\n\tv_mul_u32_u24 v119, 0x480, v119\n\tv_add_u32 v119, s56, v119\n\t"
    "v_lshl_add_u32 v116, v118, 4, v119\n\tv_lshl_add_u32 v117, v118, 2, v119\n\tv_lshlrev_b32 v114, 4, v118\n\tv_lshlrev_b32 v115, 2, v118\n\t"
    "s_and_b32 s57, s62, 3\n\ts_lshl2_add_u32 s57, s57, s63\n\ts_lshl_b32 s57, s57, 6\n\ts_and_b32 s58, s62, 1\n\ts_lshl_b32 s58, s58, 6\n\tv_and_b32 v112, 3, %[t]\n\tv_lshl_add_u32 v113, v112, 4, s57\n\tv_lshl_add_u32 v112, v112, 4, s58\n\t"
    "s_lshl_b32 s52, s61, 5\n\ts_add_u32 s52, s52, s62\n\ts_lshr_b32 s53, s52, 1\n\ts_mulk_i32 s53, %[ust]\n\ts_add_u32 s53, s53, s58\n\ts_add_u32 s53, s53, %[wu]\n\tv_and_b32 v41, 3, %[t]\n\tv_lshrrev_b32 v40, 2, %[t]\n\tv_lshl_add_u32 v32, v41, 4, s53\n\tv_lshl_add_u32 v32, v40, 7, v32\n\t"
    "v_add_u32 v33, 0x2000, v32\n\tv_add_u32 v34, 0x4000, v32\n\tv_lshrrev_b32 v35, 1, %[t]\n\tv_and_b32 v35, 16, v35\n\t"
    "v_lshrrev_b32 v39, 5, %[t]\n\ts_movk_i32 s54, 1216\n\tv_mov_b32 v38, %[ti]\n\tv_mad_u32_u24 v37, v39, s54, v38\n\tv_add_u32 v36, v37, v114\n\tv_add_u32 v37, v37, v115\n\tv_lshlrev_b32 v39, 1, v39\n\tv_add_u32 v39, 128, v39\n\t"
    "v_and_b32 v119, 3, v40\n\tv_mad_u32_u24 v38, v119, s54, v38\n\tv_and_b32 v119, 0xf0, %[t]\n\tv_lshl_add_u32 v38, v119, 1, v38\n\tv_lshl_add_u32 v38, v41, 3, v38\n\t"
    "s_lshl_b32 s53, s52, 5\n\ts_add_u32 s53, s53, %[rg]\n\tv_and_b32 v40, 7, v40\n\tv_lshl_add_u32 v40, v40, 13, s53\n\tv_lshl_add_u32 v40, v41, 3, v40\n\tv_lshlrev_b32 v41, 2, v41\n\tv_add_u32 v41, 128, v41\n\t"
    "s_lshl_b32 s53, s52, 6\n\ts_add_u32 s53, s53, %[st]\n\ts_mov_b32 exec_lo, 0\n\tv_and_b32 v41, 7, %[t]\n\tv_lshl_add_u32 v40, v41, 3, s53\n\tv_lshlrev_b32 v41, 2, v41\n\tv_add_u32 v41, 400, v41\n\ts_mov_b32 exec_lo, -1\n\t"
    "s_waitcnt lgkmcnt(0)\n\t"
    "s_memrealtime s[98:99]\n\ts_load_dwordx2 s[96:97], s[66:67], 0x0\n\t"
    "global_load_dwordx4 %[wa], v114, s[68:69]\n\tglobal_load_dwordx4 %[wb], v114, s[68:69] offset:512\n\tglobal_load_dword %[wc], v115, s[68:69] offset:1024\n\t"
    "global_load_dwordx4 %[xa], v116, s[64:65]\n\tglobal_load_dwordx4 %[xb], v116, s[64:65] offset:512\n\tglobal_load_dword %[xc], v117, s[64:65] offset:1024\n\t"
    "global_load_dwordx4 %[d0], v113, s[70:71]\n\tglobal_load_dwordx4 %[d1], v112, s[70:71] offset:1024\n\t" SARR
    "s_branch 8f\n"
    "1:\n\t" "s_cmp_ge_u32 s62, " STR(CPF) "\n\ts_cbranch_scc1 6f\n\ts_getpc_b64 s[56:57]\n.Lkpc:\n\ts_and_b32 s58, s62, 3\n\ts_lshl_b32 s58, s58, 13\n\ts_sub_u32 s58, s58, 0x1000\n\tv_lshl_add_u32 v119, %[t], 4, s58\n\tv_min_u32 v119, .Lkend-.Lkpc-16, v119\n\t"
    "global_load_dword v120, v119, s[56:57]\n\t.subsection 1\n.Lkend:\n\t.subsection 0\n6:\n\ts_barrier\n\t"
    "v_mov_b32 v124, -1\n\tv_mov_b32 v125, -1\n\tv_mov_b32 v126, -1\n\tv_mov_b32 v127, -1\n\tv_lshlrev_b32 v119, 4, %[t]\n\tv_and_b32 v119, 0xff0, v119\n\t"
    "s_lshr_b32 s59, s60, 8\n\ts_cmp_lt_u32 s62, 8\n\ts_cselect_b32 s58, 2, 1\n\ts_cbranch_scc1 2f\n\ts_cmp_eq_u32 s59, 3\n\ts_cbranch_scc1 4f\n"
    "2:\n\ts_sub_u32 s57, s59, 2\n\ts_cmp_eq_u32 s59, 1\n\ts_cselect_b32 s58, s58, s57\n\ts_cmp_lt_u32 s62, 8\n\ts_cselect_b32 s56, 0, 8\n\ts_lshl1_add_u32 s56, s62, s56\n\ts_mulk_i32 s61, 72\n\ts_add_u32 s51, s56, s61\n\ts_sub_u32 s56, 2, s59\n\ts_sub_u32 s57, 16, s62\n\ts_cmp_eq_u32 s59, 3\n\ts_cselect_b32 s50, s57, s56\n\ts_add_u32 s56, s50, s51\n\t"
    "v_mov_b32 v112, 0\n\tv_mov_b32 v113, 0\n\tv_mov_b32 v114, 0\n\tv_mov_b32 v115, 0\n\tv_mov_b32 v116, 0\n\tv_mov_b32 v117, 0\n\ts_setprio 2\n\t"
    "s_lshl_b32 s57, s56, 2\n\ts_add_u32 s57, s57, s63\n\ts_lshl_b32 s57, s57, 6\n\tv_lshl_add_u32 v22, v118, 1, s57\n\ts_mul_i32 s57, s56, %[yst]\n\ts_add_u32 s57, s57, %[wy]\n\tv_add_u32 v23, s57, v119\n\ts_lshl_b32 s56, s56, 1\n\ts_and_b32 s56, s56, -4\n\t"
    "s_waitcnt lgkmcnt(0)\n\t"
    "global_load_short_d16_hi v112, v22, s[80:81] nt\n\tglobal_load_short_d16_hi v113, v22, s[82:83] nt\n\tglobal_load_short_d16_hi v114, v22, s[84:85] nt\n\t"
    "global_load_short_d16_hi v115, v22, s[86:87] nt\n\tglobal_load_short_d16_hi v116, v22, s[88:89] nt\n\tglobal_load_short_d16_hi v117, v22, s[90:91] nt\n\t"
    "s_load_dwordx2 s[96:97], s[66:67], 0x0\n\ts_load_dword s98, s[92:93], s56\n\t"
    SARR
    "s_mul_i32 s52, s58, %[sl]\n\ts_add_u32 s52, s52, %[tb]\n\ts_lshl_b32 s53, s63, 7\n\ts_add_u32 s53, s53, s52\n\tv_lshl_add_u32 v30, v118, 2, s53\n\tv_bfe_u32 v31, %[t], 5, 1\n\ts_movk_i32 s54, 528\n\tv_mul_u32_u24 v29, s54, v31\n\t"
    "v_lshl_add_u32 v2, v29, 3, v30\n\ts_movk_i32 s53, 1056\n\tv_add_u32 v3, s53, v2\n\tv_add_u32 v4, s53, v3\n\tv_add_u32 v5, s53, v4\n\tv_add_u32 v6, 41*528, v2\n\tv_add_u32 v7, s53, v6\n\tv_add_u32 v8, s53, v7\n\tv_add_u32 v9, s53, v8\n\t"
    "v_lshl_add_u32 v10, v29, 2, v30\n\tv_add_u32 v10, 16*528, v10\n\tv_add_u32 v11, s53, v10\n\tv_lshl_add_u32 v12, v29, 1, v30\n\tv_add_u32 v12, 24*528, v12\n\tv_add_u32 v13, 2112, v12\n\tv_add_u32 v14, v29, v30\n\tv_add_u32 v14, 32*528, v14\n\t"
    "s_lshl_b32 s53, s58, 4\n\ts_add_u32 s53, s53, %[ex]\n\tv_mov_b32 v15, s53\n\tv_cvt_f32_ubyte0 v31, v31\n\tv_mul_f32 v16, 0x42800000, v31\n\tv_add_f32 v17, v16, v16\n\tv_mov_b32 v20, 1\n\t"
    "v_and_b32 v29, 15, %[t]\n\tv_and_b32 v30, 48, %[t]\n\tv_mad_u32_u24 v30, v29, s54, v30\n\ts_lshl_b32 s55, s59, 1\n\ts_lshr_b32 s55, 0x48, s55\n\ts_add_u32 s55, s55, s63\n\ts_and_b32 s55, s55, 3\n\ts_mul_i32 s53, s55, 16*528\n\ts_movk_i32 s57, 41*528\n\ts_cmp_eq_u32 s55, 2\n\ts_cselect_b32 s53, s57, s53\n\ts_cselect_b32 s57, 32*528, s57\n\t"
    "s_add_u32 s53, s53, s52\n\ts_add_u32 s57, s57, s52\n\tv_add_u32 v18, s53, v30\n\tv_add_u32 v19, s57, v30\n\t"
    "v_min_u32 v29, 237*16, v119\n\tv_subrev_u32 v30, 204*16, v29\n\tv_add_u32 v29, %[de], v29\n\tv_min_u32 v29, v29, v30\n\ts_sub_u32 s53, s52, 4640\n\tv_add_u32 v29, s53, v29\n\tv_mov_b32 v24, 0\n\tv_mov_b32 v25, 0\n\tv_mov_b32 v26, 0\n\tv_mov_b32 v27, 0\n\tds_write_b128 v29, v[24:27]\n\t"
    "s_cmp_lg_u32 s55, 3\n\ts_cbranch_scc1 10f\n\t"
    "v_bfe_u32 v18, %[t], 1, 5\n\tv_and_b32 v19, 1, %[t]\n\ts_cmp_lt_u32 s62, 8\n\ts_cselect_b32 s53, 2, 1\n\ts_mul_i32 s53, s53, %[sl]\n\tv_mul_u32_u24 v21, s53, v19\n\ts_sub_u32 s55, 2, s59\n\ts_lshl_b32 s55, s55, 7\n\tv_add_u32 v18, s55, v18\n\ts_cmp_lg_u32 s59, 3\n\ts_cbranch_scc1 7f\n\t"
    "v_bfe_u32 v18, %[t], 5, 1\n\tv_lshl_add_u32 v18, v18, 7, v118\n\tv_mov_b32 v19, s50\n\tv_mov_b32 v21, %[sl]\n"
    "7:\n\tv_add_u32 v19, s51, v19\n\tv_lshl_add_u32 v21, v18, 4, v21\n\tv_lshrrev_b32 v24, 3-" STR(TL) ", v19\n\tv_and_b32 v19, 7>>" STR(TL) ", v19\n\tv_lshlrev_b32 v19, 4, v19\n\tv_lshl_add_u32 v19, v24, 7, v19\n\t"
    "v_and_b32 v24, (1<<" STR(TL) ")-1, v18\n\tv_lshl_add_u32 v19, v24, 7-" STR(TL) ", v19\n\tv_lshrrev_b32 v18, " STR(TL) ", v18\n\tv_mul_u32_u24 v18, %[ust], v18\n\tv_add_u32 v18, v18, v19\n\tv_add_u32 v19, 512, v21\n10:\n\t"
    "global_store_dwordx4 v23, v[124:127], s[66:67] sc1\n\t" "s_branch 9f\n"
    "4:\n\t"
    "v_bfe_u32 v118, %[t], 3, 5\n\tv_lshrrev_b32 v117, 1, v118\n\tv_and_b32 v116, 6, v117\n\tv_and_b32 v115, 7, %[t]\n\tv_xor_b32 v115, v115, v116\n\tv_lshlrev_b32 v115, 4, v115\n\t"
    "s_mul_i32 s54, s61, 9\n\ts_sub_u32 s55, s62, 14\n\ts_lshr_b32 s55, s55, 1\n\ts_add_u32 s54, s54, s55\n\ts_lshl_b32 s54, s54, 7\n\t"
    "s_mul_i32 s52, s61, 0xa2000\n\ts_mul_i32 s53, s62, 0x180\n\ts_add_u32 s52, s52, s53\n\ts_cmp_ge_u32 s62, 11\n\ts_cselect_b32 s53, 0x51000-0x480, 0\n\ts_add_u32 s52, s52, s53\n\ts_sub_u32 s52, s52, 0xc00\n\t"
    "s_movk_i32 s55, 0x480\n\ts_cmp_ge_u32 s62, 14\n\ts_cselect_b32 s54, s54, s52\n\ts_cselect_b32 s55, 0x2400, s55\n\tv_mad_u32_u24 v118, v118, s55, v115\n\ts_lshl_b32 s55, s55, 5\n\t"
    "s_lshl_b32 s52, s63, 10\n\ts_add_u32 m0, s52, %[r2]\n\ts_mov_b32 s58, 0x510000\n\ts_mov_b32 s59, 0x20000\n\ts_cmp_ge_u32 s62, 14\n\ts_cselect_b32 s53, " STR(PRE1) "-1, " STR(PRE2) "-1\n\ts_setprio 3\n\t"
    "s_waitcnt lgkmcnt(0)\n\t" "s_memrealtime s[98:99]\n\t"
    "s_bitcmp1_b32 %[wg], 3\n\ts_cselect_b64 s[56:57], s[74:75], s[72:73]\n\ts_cmp_ge_u32 s62, 14\n\ts_cselect_b64 s[56:57], s[56:57], s[76:77]\n\ts_cselect_b32 s52, 0x180, -1\n\ts_and_b32 s57, s57, 0xffff\n\ts_add_u32 s60, s56, s54\n\ts_and_b32 s60, s60, 0x380\n\ts_cmp_eq_u32 s60, s52\n\ts_cselect_b32 s53, " STR(PRE3) "-1, s53\n\t"
    "v_mov_b32 v121, 80\n\ts_movk_i32 s52, 0x400\n12:\n\tds_read_b32 v122, v121\n\ts_waitcnt lgkmcnt(0)\n\tv_readfirstlane_b32 s60, v122\n\ts_cmp_ge_u32 s60, 12\n\ts_cbranch_scc1 13f\n\ts_sleep 1\n\ts_sub_u32 s52, s52, 1\n\ts_cbranch_scc0 12b\n13:\n\t"
    "s_load_dwordx2 s[96:97], s[66:67], 0x0\n\t"
    "\n3:\n\t" "buffer_load_dwordx4 v118, s[56:59], s54 offen nt lds\n\ts_add_u32 m0, m0, 0x1000\n\ts_add_u32 s54, s54, s55\n\ts_sub_u32 s53, s53, 1\n\ts_cbranch_scc0 3b\n"
    "9:\n8:"
    : [xa]"=&{v[2:5]}"(xa),[xb]"=&{v[6:9]}"(xb_),[xc]"=&{v30}"(xc),[wa]"=&{v[10:13]}"(wa),[wb]"=&{v[14:17]}"(wb),[wc]"=&{v31}"(wc),[d0]"=&{v[18:21]}"(dq[0]),[d1]"=&{v[22:25]}"(dq[1]),[d2]"=&{v[26:29]}"(dq[2]),"=&{v[32:35]}"(la),"=&{v[36:39]}"(lb_),"=&{v[40:41]}"(lc),
      "=&{s[64:65]}"(x),"=&{s[66:67]}"(ws),"=&{s[70:71]}"(Dsk),"=&{s[72:73]}"(wg),"=&{s[74:75]}"(wu),"=&{s[76:77]}"(wd),"=&{s[78:79]}"(out)
    : [ka]"s"(__builtin_amdgcn_kernarg_segment_ptr()),[wg]"s"(BID),[t]"v"(TID),[yst]"i"(YST),[wy]"i"(W_Y),[r2]"i"(O_R2),[sl]"i"(SLSZ),[tb]"i"(O_CH+O_TB),[ex]"i"(O_EXP),[de]"i"(O_DE),[ust]"i"(GST_),[wu]"i"(W_U),[ti]"i"(O_TILE),[rg]"i"(W_RG),[st]"i"(W_ST)
    : "memory","scc","m0","v127","s50","s51","s52","s53","s54","s55","s56","s57","s58","s59","s60","s61","s62","s63","s68","s69",
      "s80","s81","s82","s83","s84","s85","s86","s87","s88","s89","s90","s91","s92","s93","s94","s95","s99");
  int wv=RFL(TID>>6), bid=BID, q4=wv>>2, w4=wv&3;
  int xg=bid&7, lb=bid>>3, tb=32*xg+lb, nch=2+(lb<8);
  u64 ep; u32 ldw;
  {
  }
  #define KILLIN "=v"(xa),"=v"(xb_),"=v"(xc),"=v"(wa),"=v"(wb),"=v"(wc),"=v"(dq[0]),"=v"(dq[1]),"=v"(dq[2]),"=v"(la),"=v"(lb_),"=v"(lc)
  if(__builtin_expect(q4==3&&lb>=8,0)){
    int fu=18*xg+lb-14, kq=2*xg+(lb>=11), cgp=lb-8-3*(lb>=11);
  u32 wst= lb>=14? F_*64:D_*64;
  u32 wso=(lb>=14? (fu>>1)*128 : kq*(288*D_*2)+384*cgp)+(lb>=14? PRE1:PRE2)*wst, wld=(u32)(size_t)sm+O_R2+w4*1024+(lb>=14? PRE1:PRE2)*4096; u32x4 wrs=mkrs(lb>=14? ((fu&1)? wu:wg):wd,D_*F_*2);
  GETEP
  #define FLANE(RS) LANE(ln); int g=ln>>4, li=ln&15, qq=li>>2, pq=li&3; u32 wvo=(u32)(8*w4+(ln>>3))*(RS)+(u32)((ln&7)^(((ln>>4)&2)|((w4&1)<<2)))*16u; \
    const lch* tp=sm+O_R2+(16*(g>>1)+8*(qq>>1)+4*(g&1)+(qq&1))*128+((w4^((g&1)|(qq&2)))*32)+pq*8;
  #define WFR(img,ks) BC(u32x4,trfrag(tp+(img)*CHSZ+(ks)*4096,tp+(img)*CHSZ+(ks)*4096+256))
  #define WF2(img,s) __builtin_shufflevector(WFR(img,2*(s)),WFR(img,2*(s)+1),0,1,2,3,4,5,6,7)
  f32x4 ac[3]={{0,0,0,0},{0,0,0,0},{0,0,0,0}};
  #define AC0 "+{v[100:103]}"(ac[0])
  #define AC1 "+{v[104:107]}"(ac[1])
  #define AC2 "+{v[108:111]}"(ac[2])
  #define SMF(i,a,b) asm volatile("v_smfmac_f32_16x16x64_bf16 %0, %1, %2, %3" : AC##i : "v"(a),"v"(b),"v"(ix) : "memory"); SCHED();
  #define SMR(i,r,b) asm volatile("v_smfmac_f32_16x16x64_bf16 %0, v[" #r ":" #r "+3], %1, %2" : AC##i : "v"(b),"v"(ix) : "memory"); SCHED();
  #define GW4(c,p) { u32 tv,cnt,sv; asm volatile("s_mov_b32 %1, 0x20000\n1:\n\tds_read_b32 %0, %3\n\tds_read_b128 v[112:115], %4\n\tds_read_b128 v[116:119], %4 offset:128\n\tds_read_b128 v[120:123], %4 offset:256\n\tds_read_b128 v[124:127], %4 offset:384\n\ts_waitcnt lgkmcnt(4)\n\t" \
    "v_readfirstlane_b32 %2, %0\n\ts_cmp_ge_u32 %2, 4\n\ts_cbranch_scc1 2f\n\ts_sleep 1\n\ts_sub_u32 %1, %1, 1\n\ts_cbranch_scc0 1b\n2:" : "=&v"(tv),"=&s"(cnt),"=&s"(sv) : "v"((c)*4),"v"(p) : "memory","scc"); }
  #define LGW(n) asm volatile("s_waitcnt lgkmcnt(" #n ")" ::: "memory");
  u64 tag=ep+1ull; const u32x4 I4={INV,INV,INV,INV};
  if(lb>=14){
    FLANE(F_*2)
    int mt=fu&1;
    const lch* xp=sm+O_TILE+(li&7)*XSTR+g*32+(li>>3)*16; int of=(mt*288+(fu>>1)*4+w4)*256+ln*4; u32 ix= (li&8)? 0xeeeeu:0x4444u;
    const lch* mp=sm+O_RC+(g&1)*16+(g>>1)*8;
    asm volatile("" : "+v"(xp),"+v"(of),"+v"(mp),"+v"(ix));
    rsrc_t rsG=RSRC(ws+W_GU,2*8*F_*2);
    if(w4==0) BST128(I4,rsG,(mt*288+(fu>>1)*4)*256+ln*16,0,SC1);
    bool slow=(((u32)wrs[0]+wso)&0x380u)==0x180u;
    if(slow){ wso-=(PRE1-PRE3)*wst; wld-=(PRE1-PRE3)*4096; FILL(S3-PRE3,VW(W3)); } else FILLD(5,W1,W1B);
    if(w4==0){ if(slow) VMW(S3-PRE3<5? S3-PRE3:5); else VMW(5); if(ln==0) GST(ws+W_FL+(576+fu)*8,tag); }
    if(slow){ wso=RFL(wso); wld=RFL(wld); FILL(18-S3,VW(W3B)); } else FILLD(13-PRE1,W1,W1B);
    u32x8 fB[9]; u32x4 one4={0x3f803f80u,0x3f803f80u,0x3f803f80u,0x3f803f80u}; const lch* tq=tp+65536; asm volatile("" : AC0,AC1,"+v"(one4),"+v"(tq));
    VMW(4); gsync(sm,C_P,4);
    #pragma unroll
    for(int s=0;s<7;s++) fB[s]=WF2(0,s);
    #pragma unroll
    for(int s=0;s<7;s++){ SMF(1,one4,fB[s]) }
    DRAIN(); gsync(sm,C_P,8);
    fB[7]=WF2(0,7); { const lch* tp=tq; fB[8]=WF2(0,0); } SMF(1,one4,fB[7]) SMF(1,one4,fB[8])
    { u32x4 xb[5]; u32x2 md;
      GW4(C_X,xp)
      md=LD(u32x2,mp);
      #pragma unroll
      for(int s=0;s<5;s++) xb[s]=LD(u32x4,xp+512+s*128);
      SCHED(); LGW(6)
      SMR(0,112,fB[0])
      float S; asm volatile("v_permlane32_swap_b32 v104, v105\n\tv_add_f32 %0, v104, v105" : "=v"(S),AC1); SCHED();
      SMR(0,116,fB[1]) SMR(0,120,fB[2])
      float ra=rcpf_(bhi(md[0])), rb_=rcpf_(bhi(md[1])); PIN(ra); PIN(rb_); SCHED();
      SMR(0,124,fB[3])
      float na=-blo(md[0])*S, nb=-blo(md[1])*S; PIN(na); PIN(nb); SCHED();
      SMF(0,xb[0],fB[4])
      float ea=ra*-1.4426950408889634f, eb=rb_*-1.4426950408889634f; PIN(ea); PIN(eb); SCHED();
      SMF(0,xb[1],fB[5]) SMF(0,xb[2],fB[6]) SMF(0,xb[3],fB[7]) SMF(0,xb[4],fB[8])
      float v0,v1,s0,s1;
      asm volatile("s_nop 7\n\tv_permlane32_swap_b32 v100, v102\n\tv_permlane32_swap_b32 v101, v103\n\tv_add_f32 v100, %5, v100\n\tv_add_f32 v101, %6, v101\n\tv_add_f32 v100, v100, v102\n\tv_add_f32 v101, v101, v103\n\t"
        "v_mul_f32 %2, %9, v100\n\tv_mul_f32 %3, %10, v101\n\tv_mul_f32 %0, %7, v100\n\tv_mul_f32 %1, %8, v101" : "=&v"(v0),"=&v"(v1),"=&v"(s0),"=&v"(s1),AC0 : "v"(na),"v"(nb),"v"(ra),"v"(rb_),"v"(ea),"v"(eb));
      if(mt==0){ v0*=rcpf_(1.f+__builtin_amdgcn_exp2f(s0)); v1*=rcpf_(1.f+__builtin_amdgcn_exp2f(s1)); }
      __builtin_amdgcn_raw_buffer_store_b32(nv(pk(v0,v1)),rsG,of,0,SC1); garrive(sm,C_H); }
    if(bid==112&&w4==1){
      _Pragma("unroll 1") for(int sp=0;sp<SPINCAP;sp++){ u64 bad=0, f[12];
        #pragma unroll
        for(int i=0;i<12;i++) f[i]=GLD(ws+W_FL+(i*64+ln)*8);
        #pragma unroll
        for(int i=0;i<12;i++) bad|=f[i]^tag;
        if(BALLOT(bad!=0ull)==0ull) break; __builtin_amdgcn_s_sleep(8); }
      if(ln==0) GST(ws+W_EPOCH,tag); }
    if(mt==0&&w4==0){
    LANE(l2); ln=l2; li=ln&15; g=ln>>4; gwait<1>(sm,C_IX,1);
    int c9=(lb-14)>>1, t=RFL((int)LD(u16,sm+O_IDX+xg*2)), c=64*c9+4*li+g; f32x4 pr;
    { u32 u=LD(u16,sm+O_IDX+16+xg*2)^0x8000u, xb=LD(u16,sm+O_TILE+xg*XSTR+c*2), sv=LD(u32,sm+O_RC+xg*4); float ds=b2f(((const GAS u16*)Dsk)[c]); u^=(u32)(((int)(u<<16))>>31)&0x7fffu;
      poll1<0>(ws+W_FL+(64*c9+ln)*8,tag); poll1<0>(ws+W_FL+(720+(ln<48? ln:47))*8,tag);
      float xn=(b2f(xb)-blo(sv))*rcpf_(bhi(sv)); pr=f32x4{b2f(xb),xn,xn*ds,sigm_f(b2f(u))}; }
    { gwait<2>(sm,C_E,4); u32 ti=now_(), mx=0;
      #pragma unroll
      for(int k=0;k<3;k++){ int i=ln+64*k; i= i<144? i:143; int q=(i*3641)>>16; u64 h=GLD(ws+W_HT+(72*q+36+2*(i-18*q))*8); mx=umax_(mx,(u32)(h>>32)==(u32)tag? (u32)h-ldw : ti-ldw+HM); }
      u32 tr=umin_(wmax(mx),ti-ldw+160u); u32 hk=ti-ldw+HKR; if(tr>hk) tr-=(tr-hk)>>1; tgate2(ldw,tr+HR); }
    { u32x4 rs=mkrs(ws+W_PART+(xg*9+c9)*4096,4096), ry=mkrs(ws+W_Y,D_*YST);
      int yo=c*YST+(t&~1)*2, po=ln*16; u32 a[4], t0,cnt,yb; u16* op=out+t*D_+c;
      #define LD_P "buffer_load_dwordx4 v[112:115], %[of], %[rs], 0 offen sc1\n\tbuffer_load_dwordx4 v[116:119], %[of], %[rs], 0 offen offset:1024 sc1\n\tbuffer_load_dwordx4 v[120:123], %[of], %[rs], 0 offen offset:2048 sc1\n\tbuffer_load_dwordx4 v[124:127], %[of], %[rs], 0 offen offset:3072 sc1\n\t"
      #define LD_YB "buffer_load_dword %[yb], %[yo], %[ry], 0 offen sc1\n\t"
      #define AD4(o,a,b,c,d) "v_add_f32 %[t0], v" #a ", v" #b "\n\tv_add_f32 %[" #o "], v" #c ", v" #d "\n\tv_add_f32 %[" #o "], %[t0], %[" #o "]\n\t"
      u64 ym;
      asm volatile(LD_YB LD_P "s_mov_b32 %[cnt], 0x20000\n1:\n\ts_waitcnt vmcnt(0)\n\tv_cmp_eq_u32 %[ym], -1, %[yb]\n\t" CHK_I0 MX2(116,117) MX2(118,119) MX2(120,121) MX2(122,123) MX2(124,125) MX2(126,127) CHK_IE
        "s_or_b64 exec, vcc, %[ym]\n\ts_cbranch_scc0 9f\n\ts_sleep " STR(QSLP) "\n\ts_mov_b64 exec, %[ym]\n\ts_cbranch_execz 2f\n\t" LD_YB "2:\n\ts_mov_b64 exec, vcc\n\ts_cbranch_execz 3f\n\t" LD_P
        "3:\n\ts_mov_b64 exec, -1\n\ts_sub_u32 %[cnt], %[cnt], 1\n\ts_cbranch_scc0 1b\n9:\n\ts_mov_b64 exec, -1\n\t"
        AD4(a0,112,116,120,124) AD4(a1,113,117,121,125) AD4(a2,114,118,122,126) AD4(a3,115,119,123,127) "s_nop 1"
        : [t0]"=&v"(t0),[cnt]"=&s"(cnt),[a0]"=&v"(a[0]),[a1]"=&v"(a[1]),[a2]"=&v"(a[2]),[a3]"=&v"(a[3]),[yb]"=&v"(yb),[ym]"=&s"(ym) : [of]"v"(po),[rs]"s"(rs),[yo]"v"(yo),[ry]"s"(ry),"v"(op),"v"(pr) : PCLOB);
      auto s0=__builtin_amdgcn_permlane32_swap(a[0],a[2],false,false); auto s1=__builtin_amdgcn_permlane32_swap(a[1],a[3],false,false);
      u32 x0=s0[0],x1=s0[1],y0=s1[0],y1=s1[1]; u32 p0=BC(u32,BC(float,x0)+BC(float,x1)), p1=BC(u32,BC(float,y0)+BC(float,y1));
      auto s2=__builtin_amdgcn_permlane16_swap(p0,p1,false,false); u32 z0=s2[0], z1=s2[1]; float s=BC(float,z0)+BC(float,z1);
      float gq=silu_f(__builtin_fmaf(s,pr[3],pr[1]));
      float sy=((t&1)? bhi(yb):blo(yb))+pr[2];
      float o=__builtin_fmaf(sy,gq,pr[0]);
      asm volatile("global_store_short %0, %1, off sc1" :: "v"(op),"v"(pk(o,o)) : "memory");
      }
    }
  } else
  {
    FLANE(D_*2)
    u32x8 fr[12]; u32x4 fl[3];
    rsrc_t rsP=RSRC(ws+W_PART+cgp*12288+kq*256+w4*64,8*9*4096-2*12288);
    #pragma unroll
    for(int i=0;i<3;i++) BST128(I4,rsP,li<8? li*(9*4096)+g*16+i*4096 : OOR,0,SC1);
    if(PRE2<9) FILLD(9-PRE2,W2_,W2B);
    wso+=128u-9u*wst;
    FILLD(9,W2_,W2B);
    VMW(9); gsync(sm,C_P,4);
    #pragma unroll
    for(int s=0;s<4;s++) fr[s]=WF2(0,s);
    fl[0]=WFR(0,8);
    gsync(sm,C_P,8); if(w4==0&&ln==0) GST(ws+W_FL+(720+6*xg+lb-8)*8,tag);
    wso+=128u-9u*wst; wld-=2*CHSZ;
    int fo=W_FL+(576+((9*kq)&~1)+(ln<9? ln:9))*8;
    FILL(9,VW(W2C));
    if(w4==0) asm volatile("global_load_dwordx2 v[112:113], %0, %1 sc1\n\ts_waitcnt vmcnt(10)" :: "v"(fo),"s"(ws) : "memory"); else VMW(9);
    gsync(sm,C_P,12);
    #pragma unroll
    for(int s=0;s<4;s++) fr[4+s]=WF2(1,s);
    fl[1]=WFR(1,8);
    { int t2=TID; asm volatile("" : "+v"(t2)); ln=t2&63; li=ln&15; g=ln>>4; }
    const lch* hp=sm+O_TILE+(li&7)*576+g*32+(li>>3)*16; int po=(4*(g&1)+2*(g>>1))*(9*4096)+li*4; u32 ix= (li&8)? 0xeeeeu:0x4444u; PIN(hp); PIN(po); PIN(ix);
    const lch* hq=hp+512-(g>>1)*32-(g&1)*24; PIN(hq);
    u32x4 rs=mkrs(ws+W_GU,2*8*F_*2);
    int of=((ln>>5)*288+18*kq+2*w4)*256+(ln&31)*16; lch* hd=sm+O_TILE+((ln&4)+((ln>>2)&2))*576+w4*64+(ln&16)*2+(ln&3)*8+(ln>>5)*4; PIN(of); PIN(hd); asm volatile("" : "+s"(rs));
    u32x4 rp=mkrs(ws+W_PART+cgp*12288+kq*256+w4*64,8*9*4096-2*12288); int po1=po+9*4096; u32 nvm=0xfffeffffu; asm volatile("" : AC0,AC1,AC2,"+v"(po1),"+s"(rp),"+s"(nvm));
    DRAIN();
    if(w4==0){ u64 bad; asm volatile("v_cmp_ne_u64 %0, %1, v[112:113]" : "=s"(bad) : "s"(tag) : "memory");
      if(bad!=0ull) poll1<0>(ws+fo,tag); }
    gsync(sm,C_P,16);
    { u32 t0,cnt;
      #define LD_G "buffer_load_dwordx4 v[112:115], %[of], %[rs], 0 offen sc1\n\tbuffer_load_dwordx4 v[116:119], %[of], %[rs], 0 offen offset:2048 sc1\n\tbuffer_load_dwordx4 v[120:123], %[of], %[rs], %[so] offen sc1\n\t"
      #define HM1(t,a,b) "v_lshlrev_b32 " t ", 16, v" #a "\n\tv_lshlrev_b32 %[t1], 16, v" #b "\n\tv_and_b32 v" #a ", %[hm], v" #a "\n\tv_and_b32 v" #b ", %[hm], v" #b "\n\tv_mul_f32 " t ", " t ", %[t1]\n\tv_mul_f32 v" #a ", v" #a ", v" #b "\n\t"
      #define HMUL(a,b,c,d,o) "v_permlane32_swap_b32 v" #a ", v" #c "\n\tv_permlane32_swap_b32 v" #b ", v" #d "\n\t" HM1("%[t0]",a,c) HM1("v" #c,b,d) "v_cvt_pk_bf16_f32 %[t0], %[t0], v" #c "\n\tv_cvt_pk_bf16_f32 v" #a ", v" #a ", v" #b "\n\tds_write2_b32 %[hd], %[t0], v" #a " offset0:" #o " offset1:" #o "+144\n\t"
      u32 t1;
      asm volatile(LD_G :: [of]"v"(of),[rs]"s"(rs),[so]"s"(w4==0? 4096:0) : "memory");
      fr[8]=WF2(0,0); PIN(fr[8]); fl[2]=WFR(0,8); PIN(fl[2]);
      asm volatile(POLLASM(LD_G,CHK_I0 MX2(116,117) MX2(118,119) MX2(120,121) MX2(122,123) CHK_IE,GSLP)
        HMUL(112,113,114,115,0) HMUL(116,117,118,119,64) "s_cmp_eq_u32 %[so], 0\n\ts_cbranch_scc1 8f\n\tv_add_u32 %[hd], 512, %[hd]\n\t" HMUL(120,121,122,123,0) "8:\n\ts_mov_b64 exec, 1\n\tds_add_u32 %[ch], %[one]\n\ts_mov_b64 exec, -1"
        : [t0]"=&v"(t0),[t1]"=&v"(t1),[cnt]"=&s"(cnt),[hd]"+v"(hd) : [of]"v"(of),[rs]"s"(rs),[so]"s"(w4==0? 4096:0),[hm]"s"(0xffff0000u),[ch]"v"(C_H*4),[one]"v"(1),"v"(hp),"v"(po) : PCLOB);
      }
    {
      GW4(C_H,hp)
      u32x2 h8=LD(u32x2,hq); SCHED(); LGW(1)
      #define SM3(i,b) asm volatile("v_smfmac_f32_16x16x32_bf16 %0, %1, %2, %3" : AC##i : "v"(h8),"v"(b),"v"(ix) : "memory"); SCHED();
      #define PST(a,b,c,d,o) "v_permlane32_swap_b32 v" #a ", v" #c "\n\tv_permlane32_swap_b32 v" #b ", v" #d "\n\tv_add_f32 v" #a ", v" #a ", v" #c "\n\tv_add_f32 v" #b ", v" #b ", v" #d "\n\tv_min_u32 v" #a ", %[nv], v" #a "\n\tv_min_u32 v" #b ", %[nv], v" #b "\n\t" \
        "buffer_store_dword v" #a ", %[p0], %[rp], " o " offen sc1\n\tbuffer_store_dword v" #b ", %[p1], %[rp], " o " offen sc1\n\t"
      SMR(0,112,fr[0]) fr[9]=WF2(0,1); SCHED(); SMR(1,112,fr[4]) fr[10]=WF2(0,2); SCHED(); SMR(2,112,fr[8]) fr[11]=WF2(0,3); SCHED();
      SMR(0,116,fr[1]) SMR(1,116,fr[5]) SMR(0,120,fr[2]) SM3(2,fl[2]) SMR(0,124,fr[3]) SMR(1,120,fr[6]) SM3(0,fl[0]) SMR(1,124,fr[7]) SMR(2,116,fr[9]) SM3(1,fl[1]) SMR(2,120,fr[10]) SMR(2,124,fr[11])
      asm volatile("s_nop 2\n\t" PST(100,101,102,103,"0") PST(104,105,106,107,"%[o1]") PST(108,109,110,111,"%[o2]") : AC0,AC1,AC2 : [p0]"v"(po),[p1]"v"(po1),[rp]"s"(rp),[nv]"s"(nvm),[o1]"s"(4096),[o2]"s"(8192) : "memory"); }

  }
  asm volatile("s_endpgm" : KILLIN :: "memory"); }
  {
  if(__builtin_expect(q4!=0,0)){ int grp= q4==1? nch-1 : q4-2;
    chan(sm,grp,(w4+(0x48>>(2*q4)))&3,72*xg+(q4==3? 16+lb : 2*lb+(lb<8? 0:8)+2-q4),(q4+1)>>2,ws,xa,xb_,wa,wb,dq[0]);
    asm volatile("s_endpgm" : KILLIN :: "memory"); }
  u32x4 xq[3]; f32x2 rc[3]; int xr_,xr2_;
  {
    LANE(ln); int g=ln>>4, li=ln&15, qq=li>>2, pq=li&3, j=ln&31, rl=2*w4+(ln>>5);
    static_assert(TL==1&&O_RV==128&&O_MD==400&&TSTR2==1216,"prologue");
    u32 Tr=la[3], one=0x3f803f80u; int uo=la[0], uo1=la[1], uo2=la[2], go=lc[0], xr=0, xr2=0;
    lch *tw=LP(lb_[0]), *tw2=LP(lb_[1])+1024, *kw=LP(lb_[3]); const lch *tp=LP(lb_[2]), *kr=LP(lc[1]);
    rsrc_t rx=RSRC(x,L_*D_*2); u64 gm=((1ull<<(4*NRG))-1ull)|0xff00000000ull; u32 um=~UMASK, km=0x80008000u; asm volatile("" : "+s"(gm),"+s"(um),"+s"(km),"+s"(ws));
    #define D2(a,b,c) __builtin_amdgcn_fdot2_f32_bf16(BC(bf2,a),BC(bf2,b),c,false)
    asm volatile("s_waitcnt vmcnt(5)" : "+v"(wa),"+v"(wb),"+v"(wc),"+v"(one)
      :: "memory");
    u32 wq[9]={wa[0],wa[1],wa[2],wa[3],wb[0],wb[1],wb[2],wb[3],wc}; float sw;
    { float a[3];
      #pragma unroll
      for(int i=0;i<9;i++) a[i%3]=D2(wq[i],one,i<3? 0.f:a[i%3]);
      sw=add16x(sum16((a[0]+a[1])+a[2])); }
    GETEP u32 tg=(u32)ep+1u; Tr=((upat(tg,w4)>>Tr)&0xffffu)*0x10001u; u32 tgv=tg;
    float z_[9]={0,0,0,0,0,0,0,0,0};
    asm volatile("s_waitcnt vmcnt(2)" : "+v"(xa),"+v"(xb_),"+v"(xc),"+v"(Tr),"+v"(sw),"+v"(tgv),"+v"(z_[0]),"+v"(z_[1]),"+v"(z_[2]),"+v"(z_[3]),"+v"(z_[4]),"+v"(z_[5]) :: "memory");
    u32 v[9]={xa[0],xa[1],xa[2],xa[3],xb_[0],xb_[1],xb_[2],xb_[3],xc};
    { float xf[18], s1, s2, s3;
      #pragma unroll
      for(int i=0;i<9;i++){ z_[i&1]=D2(v[i],one,z_[i&1]); z_[2+(i&1)]=D2(v[i],v[i],z_[2+(i&1)]); z_[4+(i&1)]=D2(v[i],wq[i],z_[4+(i&1)]); }
      s1=z_[0]+z_[1]; s2=z_[2]+z_[3]; s3=z_[4]+z_[5];
      SCHED();
      #define RSTEP(C) s1+=dppf<C>(s1); s2+=dppf<C>(s2); s3+=dppf<C>(s3); SCHED();
      RSTEP(0xB1) RSTEP(0x4E) RSTEP(0x141) RSTEP(0x140)
      s1=add16x(s1)*(1.f/576.f); s2=add16x(s2)*(1.f/576.f); s3=add16x(s3);
      float mm=s1*s1, var=s2-mm;
      if(__builtin_expect(BALLOT(!(mm<=64.f*var))!=0ull,0)){
        float q=0.f;
        #pragma unroll
        for(int i=0;i<9;i++){ u32 t=v[i]; PIN(t); float d0=blo(t)-s1, d1=bhi(t)-s1; q=__builtin_fmaf(d0,d0,q); q=__builtin_fmaf(d1,d1,q); }
        var=add16x(sum16(q))*(1.f/576.f); }
      #define UNP(i) PIN(v[i]); xf[2*(i)]=blo(v[i]); xf[2*(i)+1]=bhi(v[i]); PIN(xf[2*(i)]); PIN(xf[2*(i)+1]); SCHED();
      u32 vb=pk(var,s1); UNP(0)
      float den=blo(vb)+1e-5f, m=bhi(vb); UNP(1)
      vb=pk(den,den); s3=__builtin_fmaf(-m,sw,s3); UNP(2)
      den=__builtin_amdgcn_sqrtf(bhi(vb)); UNP(3)
      u32 md=pk(m,den); UNP(4)
      float ri=rcpf_(bhi(md)); UNP(5)
      s3*=ri; float rs_=ri*0.0625f; UNP(6)
      u32 u=pk(s3,s3); float cs_=-m*rs_; UNP(7)
      { u32 sg=(u32)((int)u>>31); PIN(sg); u^=sg|km; } UNP(8)
      asm volatile("s_mov_b64 exec, 0x1\n\ts_mov_b32 exec_hi, 1\n\tds_write_b16 %0, %1\n\tv_add_u32 %0, %0, %0\n\tds_write_b32 %0, %2 offset:%3\n\ts_mov_b32 exec_hi, 0\n\tds_add_u32 %4, %5\n\ts_mov_b64 exec, -1" : "+v"(kw) : "v"(u),"v"(md),"i"(O_MD-2*O_RV),"v"(C_I*4),"v"(1) : "memory");
      if(w4==3){ gwait<0>(sm,C_I,4); asm volatile("ds_read_b32 v118, %4\n\tv_mov_b32 v119, %1\n\ts_mov_b64 exec, %3\n\ts_waitcnt lgkmcnt(0)\n\tglobal_store_dwordx2 %0, v[118:119], %2 sc1\n\ts_mov_b64 exec, -1" :: "v"(go),"v"(tgv),"s"(ws),"s"(gm),"v"(kr) : "memory"); }
      SCHED();
      u32 h[9];
      #define UH(i) h[i]=(pkz(__builtin_fmaf(xf[2*(i)],rs_,cs_),__builtin_fmaf(xf[2*(i)+1],rs_,cs_))&um)|Tr;
      UH(0) UH(1) UH(2) UH(3) ST(u32x4,tw)=u32x4{h[0],h[1],h[2],h[3]}; UH(4) UH(5) UH(6) UH(7) ST(u32x4,tw+512)=u32x4{h[4],h[5],h[6],h[7]};
      UH(8) ST(u32,tw2)=h[8]; }
    gsync(sm,C_L,4);
    { u32x4 uq[3];
      #pragma unroll
      for(int z=0;z<3;z++) uq[z]=BC(u32x4,trfrag(tp+z*512,tp+z*512+4*TSTR2));
      asm volatile("global_store_dwordx4 %0, %1, %2 sc1" :: "v"(uo),"v"(uq[0]),"s"(ws) : "memory");
      asm volatile("global_store_dwordx4 %0, %1, %2 sc1" :: "v"(uo1),"v"(uq[1]),"s"(ws) : "memory");
      asm volatile("s_cmp_lg_u32 %3, 0\n\ts_cbranch_scc1 1f\n\tglobal_store_dwordx4 %0, %1, %2 sc1\n1:\n\ts_nop 1" :: "v"(uo2),"v"(uq[2]),"s"(ws),"s"(w4),"v"(uq[0]),"v"(uq[1]) : "memory","scc");
      asm volatile("" : "+v"(dq[0]),"+v"(dq[1]));
      { LANE(l2); xr=(32*(tb>>2)+(l2>>2))*(D_*2)+256*(lb&3)+64*w4+(l2&3)*16; xr2= w4==0? xr+(lb&2)*(8*D_*2)+1024+64*(lb&1)-256*(lb&3):OOR; }
      xr_=xr; xr2_=xr2;
    }
    if(lb<8){
      const u16* wm=(lb&1)? wu:wg; int co=(3-(int)((u64)wm>>7)-xg)&7, r_=576-16*PFN+((lb>>1)*4+w4)+16*j;
      asm volatile("buffer_load_dword v120, %0, %1, 0 offen" :: "v"(j<PFN&&(ln<32||co==0)? r_*(F_*2)+(9*xg+co)*128+(ln>>5)*1024 : OOR),"s"(mkrs(wm,D_*F_*2)) : "memory"); }
    if(lb>=8&&lb<14) garrive(sm,C_D);
  }
  FP fp; FA fa; u32 ti,td;
  { LANE(ln); u64 tag=ep+1ull;
    int so_=W_ST+(32*(tb>>2)+(ln>>2))*8, fo_=W_FL+((ln>=32&&w4==0? 512+32*(lb&1) : 128*(lb&3)+32*w4)+(ln&31))*8; u32x4 rx=mkrs(x,L_*D_*2); PIN(so_); PIN(fo_);
    XR X; X.so=so_; X.x0=xr_; X.x2=xr2_; X.rx=rx;
    if(lb>=14){
      u32 M=(u32)tag, rank,key; int j=ln&31, j16=j*16, j12=j*12, st0=W_ST; lch* xw=sm+O_TILE+(2*w4+(ln>>5))*XSTR+j*16; lch* rcw=sm+O_RC+(2*w4+(ln>>5))*4; PIN(rcw); PIN(j12); PIN(xw); PIN(j16); asm volatile("" : "+s"(rx),"+s"(st0));
      top8(sm,w4,ln,M,ws,rank,key);
      {
        int t0=__builtin_amdgcn_readlane((int)key,__builtin_ctzll(BALLOT(rank==(u32)(2*w4)))), t1=__builtin_amdgcn_readlane((int)key,__builtin_ctzll(BALLOT(rank==(u32)(2*w4+1))));
        int tsel=~((ln>>5)? t1:t0)&2047, ro=tsel*1152+j16, sr=tsel*8+st0;
        u32x4 a,b; u32 c; u64 f1;
        asm volatile("buffer_load_dwordx4 %[a], %[ro], %[rx], 0 offen\n\tbuffer_load_dwordx4 %[b], %[ro], %[rx], 0 offen offset:512\n\tbuffer_load_dword %[c], %[r2], %[rx], 0 offen offset:1024\n\tglobal_load_dwordx2 %[f], %[sr], %[ws] sc1\n\t"
          "s_mov_b64 exec, 1\n\tds_add_u32 %[cd], %[one]\n\ts_mov_b64 exec, -1\n\ts_waitcnt vmcnt(0)"
          : [a]"=&v"(a),[b]"=&v"(b),[c]"=&v"(c),[f]"=&v"(f1) : [ro]"v"(ro),[r2]"v"(ro-j12),[sr]"v"(sr),[cd]"v"(C_D*4),[one]"v"(1),[rx]"s"(rx),[ws]"s"(ws) : "memory");
        if(__builtin_expect(BALLOT((u32)(f1>>32)!=(u32)tag)!=0ull,0)){ f1=poll1<32>(ws+sr,tag<<32); __builtin_amdgcn_s_waitcnt(0x0f70); }
        ST(u32x4,xw)=a; ST(u32x4,xw+512)=b; ST(u32,xw+1024-j12)=c; ST(u32,rcw)=(u32)f1;
        garrive(sm,C_X);
        if(w4==0){ if(rank<8) ST(u16,sm+O_IDX+(ln&1)*16+rank*2)=(u16)((ln&1)? key>>16 : ~key&2047u); garrive(sm,C_IX); }
        if(lb==14&&rank<8&&!(ln&1)) GST(ws+W_IDX+xg*1024+w4*128+rank*8,(tag<<16)|(~key&2047u));
      }
      PRIO(0); xreq(X,ws);
    } else { PRIO(0); gwait<8>(sm,C_U,2); xreq(X,ws); }
    { u32 e=now_()-ldw, nm= lb<8? ANA : (lb<14? ANC:ANB); if(e>nm) ldw+=e-nm; }
    final_addr(fa,sm,w4);
    bool ix= w4==3&&lb<14; int po=W_IDX+(xg*8+(lb&3))*128+(ln&7)*8, ho_=fo_+(W_HT-W_FL), pq_= ix? po:fo_; PIN(ho_); PIN(pq_);
    { u64 fs,fs1;
      asm volatile("s_waitcnt vmcnt(0)" : "+{v[96:99]}"(X.q[0]),"+{v[100:103]}"(X.q[1]),"+{v[104:107]}"(X.q[2]),"+{v[108:111]}"(X.q[3]) :: "memory");
      fs=(u64)X.q[3][0]|((u64)X.q[3][1]<<32); fs1=(u64)X.q[3][2]|((u64)X.q[3][3]<<32);
      if(__builtin_expect(BALLOT((u32)(fs>>32)!=(u32)tag||(u32)(fs1>>32)!=(u32)tag)!=0ull,0)){ fs=poll1<32>(ws+so_,tag<<32); fs1=poll1<32>(ws+so_+128,tag<<32); __builtin_amdgcn_s_waitcnt(0x0f70); }
      float ri=rcpf_(bhi((u32)fs)), rj=rcpf_(bhi((u32)fs1)); rc[0]=f32x2{ri,-blo((u32)fs)*ri}; rc[1]=f32x2{rj,-blo((u32)fs1)*rj}; rc[2]= (lb&2)? rc[1]:rc[0];
      #pragma unroll
      for(int z=0;z<3;z++) xq[z]=X.q[z]; }
    final_pre<2,3>(fp,w4,xq,dq,rc);
    ti=now_()-ldw;
    asm volatile("global_load_dwordx2 v[112:113], %0, %2 sc1\n\tglobal_load_dwordx2 v[114:115], %1, %2 sc1\n\tv_mov_b32 v117, %5\n\ts_cmp_lt_u32 %4, " STR(HT) "\n\ts_cbranch_scc1 1f\n\tglobal_load_dwordx2 v[116:117], %3, %2 sc1\n1:"
      :: "v"(fo_),"v"(pq_),"s"(ws),"v"(ho_),"s"(ti),"s"(~(u32)tag) : "memory","scc");
    final_pre<0,1>(fp,w4,xq,dq,rc);
    { u64 ff,f; u32x2 h;
      asm volatile("s_waitcnt vmcnt(0)\n\tv_mov_b64 %0, v[112:113]\n\tv_mov_b64 %1, v[114:115]\n\tv_mov_b64 %2, v[116:117]" : "=v"(ff),"=v"(f),"=v"(h) :: "memory");
      _Pragma("unroll 1") for(int k=0;k<2;k++){ if(BALLOT(h[1]!=(u32)tag)==0ull||ti>=H2) break;
        tgate2(ldw,ti<HT? HT:H2); ti=now_()-ldw; if(ti>=HL){ h=u32x2{ldw,(u32)tag}; break; }
        asm volatile("global_load_dwordx2 %0, %1, %2 sc1\n\ts_waitcnt vmcnt(0)" : "=&v"(h) : "v"(ho_),"s"(ws) : "memory"); }
      u32 rel= h[1]==(u32)tag? h[0]-ldw : ti+HM; td=umin_(wmax(rel),ti+160u);
      if(td>HK) td-=(td-HK)>>1;
      if(__builtin_expect(BALLOT(ff!=tag)!=0ull,0)) poll1<0>(ws+fo_,tag);
      if(ix){ if(BALLOT(((f^(tag<<16))>>16)!=0ull)!=0ull) f=poll1<16>(ws+pq_,tag<<16);
        if(ln<8) ST(u16,sm+O_IDX+ln*2)=(u16)f; garrive(sm,C_IX); }
      __builtin_amdgcn_s_waitcnt(0x0f70); }
  }
  final_post(sm,fp,fa,w4,out,ws,xr_,ldw,td,xq,dq,rc);
  }
}

extern "C" void kernel_launch(void* const* d_in, const int* in_sizes, int n_in, void* d_out, int out_size, void* d_ws, size_t ws_size, hipStream_t stream) {
  const u16** I=(const u16**)d_in;
  KP kp; kp.Are=I[1]; kp.Aim=I[2]; kp.Bre=I[3]; kp.Bim=I[4]; kp.Cre=I[5]; kp.Cim=I[6]; kp.ldt=I[7]; kp.pad=I[7];
  kA<<<256,1024,0,stream>>>(I[0],(char*)d_ws,I[10],I[8],I[11],I[12],I[13],(u16*)d_out,kp);
}
```

```cpp
#include <hip/hip_runtime.h>
#include <hip/hip_bf16.h>
#include <stdint.h>
#if defined(__HIP_DEVICE_COMPILE__)
#define NOPK __attribute__((target("no-packed-fp32-ops")))
#else
#define NOPK
#endif
#define DEV __device__ NOPK __forceinline__
typedef unsigned short u16;
typedef unsigned int u32;
typedef unsigned long long u64;
typedef short bf16x8 __attribute__((ext_vector_type(8)));
typedef _Float16 h8 __attribute__((ext_vector_type(8)));
typedef _Float16 h2 __attribute__((ext_vector_type(2)));
typedef float f32x4 __attribute__((ext_vector_type(4)));
typedef float f32x2 __attribute__((ext_vector_type(2)));
typedef u32 u32x4 __attribute__((ext_vector_type(4)));
typedef u32 u32x2 __attribute__((ext_vector_type(2)));
typedef u32 u32x8 __attribute__((ext_vector_type(8)));
typedef __bf16 bf2 __attribute__((ext_vector_type(2)));
typedef short v4s __attribute__((ext_vector_type(4)));
typedef __amdgpu_buffer_rsrc_t rsrc_t;
#define GAS __attribute__((address_space(1)))
#define LAS __attribute__((address_space(3)))
typedef LAS char lch;
#define L_ 2048
#define D_ 576
#define F_ 4608
#define BC(T,v) __builtin_bit_cast(T,v)
#define TID ((int)__builtin_amdgcn_workitem_id_x())
#define BID ((int)__builtin_amdgcn_workgroup_id_x())
#define RFL(x) __builtin_amdgcn_readfirstlane(x)
#define RLX_AGENT __ATOMIC_RELAXED, __HIP_MEMORY_SCOPE_AGENT
#define LD(T,p) (*(const LAS T*)(p))
#define ST(T,p) (*(LAS T*)(p))
#define SPINCAP (1<<17)
#define DRAIN() asm volatile("s_waitcnt vmcnt(0)" ::: "memory")
#define GLD(p) __hip_atomic_load((const GAS u64*)(p),RLX_AGENT)
#define GST(p,v) __hip_atomic_store((GAS u64*)(p),(u64)(v),RLX_AGENT)
#define BALLOT(c) __builtin_amdgcn_ballot_w64(c)
#define SC1 16
#define LANE(l) int l; { int t2_=TID; asm volatile("" : "+v"(t2_)); l=t2_&63; }
#define UE 6
#define W2C 7
#define S3 9
#define W3B 6
#define PIN(x) asm volatile("" : "+v"(x))
#define PPR 0
#define RDL 0
#define SCHED() __builtin_amdgcn_sched_barrier(0)
#define RSRC(p,n) __builtin_amdgcn_make_buffer_rsrc((void*)(p),0,(n),0x00020000)
#define BLD128 __builtin_amdgcn_raw_buffer_load_b128
#define BLD32 __builtin_amdgcn_raw_buffer_load_b32
#define BST128 __builtin_amdgcn_raw_buffer_store_b128
#define PRIO(n) __builtin_amdgcn_s_setprio(n)

DEV float b2f(u32 v){ return BC(float,v<<16); }
DEV float blo(u32 v){ return BC(float,v<<16); }
DEV float bhi(u32 v){ return BC(float,v&0xffff0000u); }
DEV u32 pk(float a,float b){ f32x2 v={a,b}; bf2 r=__builtin_convertvector(v,bf2); return BC(u32,r); }
DEV u32 pkh(float a,float b){ f32x2 v={a,b}; h2 r=__builtin_convertvector(v,h2); return BC(u32,r); }
DEV float rb(float f){ return BC(float,pk(f,f)<<16); }
DEV float rcpf_(float x){ return __builtin_amdgcn_rcpf(x); }
DEV float expf_(float x){
  const float L2E=1.4426950216293335f, L2El=1.9259629911266175e-8f; float h=x*L2E; float l=__builtin_fmaf(x,L2E,-h)+x*L2El;
  float e=__builtin_amdgcn_exp2f(h); return __builtin_fmaf(e*l,0.6931471805599453f,e); }
DEV float sigm_f(float x){ return rcpf_(1.f+__builtin_amdgcn_exp2f(-x*1.4426950408889634f)); }
DEV float silu_f(float x){ return x*sigm_f(x); }
DEV u32 umax_(u32 a,u32 b){ return a>b?a:b; }
DEV u32 umin_(u32 a,u32 b){ return a<b?a:b; }

#define YSH 3
#define PRE1 6
#define PRE3 4
#define PRE2 5
#define W1 4
#define W3 3
#define W2_ 4
#define W1B 20
#define W2B 7
#define RGATE 4
#define UGATE 7
#define R2D 0
#define PFN 22
#define CPF 8
#define HD0 174
#define HD1 148
#define HD3 165
#define HM 100
#define HT 545
#define H2 590
#define HL 640
#define HK 648
#define HKR 65
#define HR 8
#define ANA 410
#define ANB 470
#define ANC 540
#define NRG 8
#define W_EPOCH 0
#define W_RG 16384
#define W_ST (W_RG+32*8192)
#define W_U (W_ST+32768)
#define UST (D_*16+128)
#define YST (L_*2+128)
#define TL 1
#define GST_ ((D_*16<<TL)+128)
#define W_Y (W_U+256*UST)
#define W_PART (W_Y+D_*YST)
#define W_GU (W_PART+8*9*16*256)
#define W_FL 9216
#define W_HT (W_RG+16*8192)
#define W_IDX 1024
struct KP { const u16 *Are,*Aim,*Bre,*Bim,*Cre,*Cim,*ldt,*pad; };

template<int CTRL> DEV float dppf(float v){ return BC(float,__builtin_amdgcn_update_dpp(0u,BC(u32,v),CTRL,0xf,0xf,false)); }
template<int CTRL> DEV u32 dppu(u32 v){ return (u32)__builtin_amdgcn_update_dpp(0u,v,CTRL,0xf,0xf,false); }
DEV float sum16(float v){ v+=dppf<0xB1>(v); v+=dppf<0x4E>(v); v+=dppf<0x141>(v); v+=dppf<0x140>(v); return v; }
DEV float add16x(float v){ auto r=__builtin_amdgcn_permlane16_swap(BC(u32,v),BC(u32,v),false,false); u32 a=r[0], b=r[1]; return BC(float,a)+BC(float,b); }
DEV u32 wmax(u32 v){
  v=umax_(v,(u32)__builtin_amdgcn_update_dpp(0u,v,0x111,0xf,0xf,false));
  v=umax_(v,(u32)__builtin_amdgcn_update_dpp(0u,v,0x112,0xf,0xf,false));
  v=umax_(v,(u32)__builtin_amdgcn_update_dpp(0u,v,0x114,0xf,0xf,false));
  v=umax_(v,(u32)__builtin_amdgcn_update_dpp(0u,v,0x118,0xf,0xf,false));
  v=umax_(v,(u32)__builtin_amdgcn_update_dpp(0u,v,0x142,0xa,0xf,false));
  v=umax_(v,(u32)__builtin_amdgcn_update_dpp(0u,v,0x143,0xc,0xf,false));
  return (u32)__builtin_amdgcn_readlane((int)v,63); }

DEV void garrive(lch* sm,int c){ u32 ad=(u32)(size_t)sm+c*4, one=1;
  asm volatile("s_mov_b64 exec, 1\n\tds_add_u32 %0, %1\n\ts_mov_b64 exec, -1" :: "v"(ad),"v"(one) : "memory"); }
template<int SLP> DEV void gwait(lch* sm,int c,u32 target){ u32 ad=(u32)(size_t)sm+c*4, tv, cnt, sv; target=RFL(target);
  asm volatile("s_mov_b32 %1, 0x20000\n"
    "1:\n\tds_read_b32 %0, %3\n\ts_waitcnt lgkmcnt(0)\n\tv_readfirstlane_b32 %2, %0\n\ts_cmp_ge_u32 %2, %4\n\ts_cbranch_scc1 2f\n\ts_sleep %5\n\ts_sub_u32 %1, %1, 1\n\ts_cbranch_scc0 1b\n"
    "2:" : "=&v"(tv),"=&s"(cnt),"=&s"(sv) : "v"(ad),"s"(target),"i"(SLP) : "memory","scc"); }
DEV u32 now_(){ u32 t; asm volatile("s_memrealtime s[98:99]\n\ts_waitcnt lgkmcnt(0)\n\ts_mov_b32 %0, s98" : "=s"(t) :: "memory"); return t; }
DEV void tgate2(u32 t0,u32 d){ u32 cnt; asm volatile("s_mov_b32 %0, 100\n1:\n\ts_memrealtime s[98:99]\n\ts_waitcnt lgkmcnt(0)\n\ts_sub_u32 s98, s98, %1\n\ts_cmp_ge_u32 s98, %2\n\ts_cbranch_scc1 2f\n\ts_sleep 1\n\ts_sub_u32 %0, %0, 1\n\ts_cbranch_scc0 1b\n2:" : "=&s"(cnt) : "s"(t0),"s"(d) : "memory","scc"); }
DEV void gsync(lch* sm,int c,u32 target){ garrive(sm,c); gwait<0>(sm,c,target); }
template<int SH> DEV u64 poll1(const char* p,u64 want){ u64 f=GLD(p);
  _Pragma("unroll 1") for(int sp=0;sp<SPINCAP;sp++){ bool bad=((f^want)>>SH)!=0ull; if(BALLOT(bad)==0ull) break; __builtin_amdgcn_s_sleep(1); if(bad) f=GLD(p); }
  __builtin_amdgcn_fence(__ATOMIC_ACQUIRE,"wavefront"); return f; }
#define STR_(x) #x
#define STR(x) STR_(x)
DEV u32x4 mkrs(const void* p,u32 n){ u64 a=(u64)p; return u32x4{(u32)RFL((int)(u32)a),(u32)RFL((int)((u32)(a>>32)&0xffffu)),n,0x00020000u}; }
#define VMW(n) asm volatile("s_waitcnt vmcnt(%0)" :: "i"(n) : "memory")
#define YSLP 1
#define GSLP 1
#define QSLP 1
#define PSLP 3
#define POLLASM(LOADS,CHECK,K) "s_mov_b32 %[cnt], 0x20000\n1:\n\ts_waitcnt vmcnt(0)\n\t" CHECK "s_cbranch_vccz 9f\n\ts_sleep " STR(K) "\n\ts_mov_b64 exec, vcc\n\t" LOADS \
  "s_mov_b64 exec, -1\n\ts_sub_u32 %[cnt], %[cnt], 1\n\ts_cbranch_scc0 1b\n9:\n\t"
#define LD_DUP2 "buffer_load_dwordx4 v[112:115], %[of], %[rs], 0 offen sc1\n\tbuffer_load_dwordx4 v[116:119], %[of], %[rs], %[so] offen sc1\n\t"
#define MX2(a,b) "v_max3_u32 %[t0], %[t0], v" #a ", v" #b "\n\t"
#define CHK_I0 "v_max_u32 %[t0], v112, v113\n\t" MX2(114,115)
#define CHK_IE "v_cmp_eq_u32 vcc, -1, %[t0]\n\t"
#define PCLOB "memory","vcc","scc"
#define UMASK 0x40074007u
DEV u32 upat(u32 tag,int k){ return ((((tag*0xea959c21u)>>(8*k))&255u)*0x04000801u)&UMASK; }
#define UD(t,a,c) "v_bitop3_b32 " t ", " t ", v" #a ", %[" #c "] bitop3:0xf6\n\t"
#define YMASK 0x60006000u
DEV u32 ypat(u32 tag,int odd,int k){ return (((tag*0xea959c21u)>>(8*odd))<<(13-2*k))&YMASK; }
#define Y96 1.262177448353619e-29f
DEV u32 pkz(float a,float b){ return BC(u32,__builtin_amdgcn_cvt_pkrtz(a,b)); }
#define INV 0xffffffffu
DEV u32 nv(u32 v){ return umin_(v,0xfffeffffu); }
DEV u32x4 nv4(u32x4 v){ return u32x4{nv(v[0]),nv(v[1]),nv(v[2]),nv(v[3])}; }
#define OOR 0x40000000

#define CHSZ 36864
#define O_CH 512
#define O_CND 256
#define SLSZ 38080
#define O_R2 (O_CH+2*SLSZ)
#define O_X2 (O_R2+CHSZ)
#define O_TILE (O_X2+CHSZ)
#define XSTR 1216
#define TSTR2 1216
#define O_CAND (O_TILE+9728)
#define LDS_TOTAL (O_CAND+128)
#define C_G 0
#define C_U 8
#define C_L 4
#define C_D 5
#define C_X 12
#define C_P 13
#define C_H 14
#define C_I 17
#define C_E 18
#define C_IX 19
#define C_RS 22
#define O_IDX 96
#define O_EXP 144
#define O_RMX 192
#define O_RV 128
#define O_MD 400
#define O_RC (O_CAND+64)

struct C2 { float r,i; };
DEV C2 cmul(C2 a,C2 b){ C2 c; c.r=__builtin_fmaf(a.r,b.r,-(a.i*b.i)); c.i=__builtin_fmaf(a.r,b.i,a.i*b.r); return c; }
DEV h8 ldA(const lch* p){ u32x2 a=*(const volatile LAS u32x2*)p, b=*(const volatile LAS u32x2*)(p+8); return BC(h8,(u32x4{a[0],a[1],b[0],b[1]})); }
DEV bf16x8 trfrag(const lch* p0,const lch* p1){
  u32x2 b0=BC(u32x2,__builtin_amdgcn_ds_read_tr16_b64_v4i16((LAS v4s*)p0)), b1=BC(u32x2,__builtin_amdgcn_ds_read_tr16_b64_v4i16((LAS v4s*)p1));
  return BC(bf16x8,(u32x4{b0[0],b0[1],b1[0],b1[1]})); }

struct FP { float gg[24], bs[24]; };
template<int Z0,int Z1> DEV void final_pre(FP& f,int w4,const u32x4* xq,const float* ds,const f32x2* rc){
  #pragma unroll
  for(int z=Z0;z<Z1;z++) if(z<2||w4==0){ float ea=rc[z][0]*-1.4426950408889634f, eb=__builtin_fmaf(rc[z][1],-1.4426950408889634f,-96.f);
    #pragma unroll
    for(int e=0;e<4;e++){ u32 xw=xq[z][e]; int i=8*z+2*e, j=8*(z>>1)+2*e; float a=blo(xw), b=bhi(xw);
      float pa=__builtin_fmaf(a,rc[z][0],rc[z][1]), pb=__builtin_fmaf(b,rc[z][0],rc[z][1]);
      float ga=pa*rcpf_(Y96+__builtin_amdgcn_exp2f(__builtin_fmaf(a,ea,eb))), gb=pb*rcpf_(Y96+__builtin_amdgcn_exp2f(__builtin_fmaf(b,ea,eb)));
      f.gg[i]=ga; f.gg[i+1]=gb; f.bs[i]=__builtin_fmaf(pa*ds[j],ga,a); f.bs[i+1]=__builtin_fmaf(pb*ds[j+1],gb,b);
      asm volatile("" : "+v"(f.gg[i]),"+v"(f.gg[i+1]),"+v"(f.bs[i]),"+v"(f.bs[i+1])); } }
}
#define FTS 544
DEV u32 pkmin(u32 a,u32 b){ u32 r; asm("v_pk_min_u16 %0, %1, %2" : "=v"(r) : "v"(a),"v"(b)); return r; }
struct FA { int yo,yo3; lch *wp,*wp3; const lch* tp; u32 tt,yp[4]; };
DEV void final_addr(FA& a,lch* sm,int w4,u32 tg){ LANE(ln); int bid=BID, xg=bid&7, lb=bid>>3, cg=lb&3, T=8*xg+(lb>>2);
  a.yo=(128*cg+32*w4+(ln>>2)*2)*YST+(32*T+8*(ln&3))*2; a.yo3= ln<32? (512+32*(cg&1)+(ln&30))*YST+(32*T+16*(cg>>1)+8*(ln&1))*2 : OOR;
  lch* fb=sm+O_X2+2048+w4*4608; a.wp=fb+(ln&3)*FTS+(ln&60); a.wp3=fb+(4+(ln&1)+(ln>>5)*2)*FTS+(ln&30)*2; a.tp=fb+(ln>>5)*FTS+(ln&31)*16;
  a.tt=(u32)(32*T+(ln>>2))*0x10001u;
  #pragma unroll
  for(int k=0;k<4;k++){ a.yp[k]=ypat(tg,ln&1,k); PIN(a.yp[k]); }
  asm volatile("" : "+v"(a.yo),"+v"(a.yo3),"+v"(a.wp),"+v"(a.wp3),"+v"(a.tp),"+v"(a.tt)); }
DEV void final_post(lch* sm,FP& f,const FA& fa,int w4,u16* __restrict__ out,char* ws,int xr,u32 tk,u32 td,const u32x4* xq,const float* dq,const f32x2* rc,int ye){
  int lb=BID>>3, cg=lb&3, ob[3], yo=fa.yo, yo3=fa.yo3; lch *wp=fa.wp, *wp3=fa.wp3; const lch* tp=fa.tp; u32 tt=fa.tt;
  u32x4 rs=mkrs(ws+W_Y,D_*YST); rsrc_t ro=RSRC(out,L_*D_*2); u32 ml=0x9fffu; PIN(ml); bool odd=BALLOT(ye!=0)!=0ull;
  #define RMASK { gwait<2>(sm,C_IX,1); u32x4 iq=LD(u32x4,sm+O_IDX); \
    _Pragma("unroll") for(int z=0;z<2;z++){ u32 t2=tt+z*0x100010u, m=pkmin(pkmin(iq[0]^t2,iq[1]^t2),pkmin(iq[2]^t2,iq[3]^t2)); ob[z]= umin_(m&0xffffu,m>>16)==0u? OOR : xr+z*(16*D_*2); } \
    ob[2]=((cg&2)? ob[1]:ob[0])+(512+32*(cg&1)-128*cg)*2; asm volatile("" : "+v"(ob[0]),"+v"(ob[1]),"+v"(ob[2])); }
  tgate2(tk,td);
  if(lb>=8) gwait<1>(sm,C_H,4);
  { u32 t0,t1,cnt;
    #define LD_Y "buffer_load_dwordx4 v[112:115], %[of], %[rs], 0 offen sc1\n\tbuffer_load_dwordx4 v[116:119], %[of], %[rs], %[s1] offen sc1\n\ts_cmp_lg_u32 %[w], 0\n\ts_cbranch_scc1 5f\n\t" \
      "buffer_load_dwordx4 v[120:123], %[o3], %[rs], 0 offen sc1\n\tbuffer_load_dwordx4 v[124:127], %[o3], %[rs], %[s1] offen sc1\n5:\n\t"
    #define YIN [of]"v"(yo),[rs]"s"(rs),[s1]"s"(YST),[o3]"v"(yo3),[w]"s"(w4)
    asm volatile(LD_Y :: YIN : "memory","scc");
    PRIO(3); final_pre<1,2>(f,w4,xq,dq,rc);
    RMASK
    #define YW(p,a,b,o) "v_perm_b32 %[t0], v" #b ", v" #a ", %[lo]\n\tv_perm_b32 %[t1], v" #b ", v" #a ", %[hi]\n\tds_write2_b32 %[" #p "], %[t0], %[t1] offset0:" #o " offset1:" #o "+16\n\t"
    #define UC(t,a,b,c) "v_bitop3_b32 " t ", v" #a ", v" #b ", %[" #c "] bitop3:0x7e\n\t"
    #define CHK_Y UC("%[t0]",112,116,c0) UC("%[t1]",113,117,c1) UC("%[t2]",114,118,c2) UC("%[t3]",115,119,c3) "s_cmp_lg_u32 %[w], 0\n\ts_cbranch_scc1 6f\n\ts_mov_b32 exec_hi, 0\n\t" \
      UD("%[t0]",120,c0) UD("%[t1]",121,c1) UD("%[t2]",122,c2) UD("%[t3]",123,c3) UD("%[t0]",124,c0) UD("%[t1]",125,c1) UD("%[t2]",126,c2) UD("%[t3]",127,c3) "s_mov_b32 exec_hi, -1\n6:\n\t" \
      "v_or3_b32 %[t0], %[t0], %[t1], %[t2]\n\tv_bitop3_b32 %[t0], %[t0], %[t3], %[mk] bitop3:0xa8\n\tv_cmp_ne_u32 vcc, 0, %[t0]\n\t"
    u32 t2,t3;
    asm volatile(POLLASM("s_wqm_b64 exec, exec\n\t" LD_Y,CHK_Y,YSLP)
      YW(wp,112,116,0) YW(wp,113,117,32) YW(wp,114,118,64) YW(wp,115,119,96) "s_cmp_lg_u32 %[w], 0\n\ts_cbranch_scc1 7f\n\t" YW(w3,120,124,0) YW(w3,121,125,32) YW(w3,122,126,64) YW(w3,123,127,96) "7:"
      : [t0]"=&v"(t0),[t1]"=&v"(t1),[t2]"=&v"(t2),[t3]"=&v"(t3),[cnt]"=&s"(cnt) : YIN,[wp]"v"(wp),[w3]"v"(wp3),[lo]"s"(0x05040100u),[hi]"s"(0x07060302u),[c0]"v"(fa.yp[0]),[c1]"v"(fa.yp[1]),[c2]"v"(fa.yp[2]),[c3]"v"(fa.yp[3]),[mk]"s"(YMASK),"v"(ml) : PCLOB); }
  u32x4 r[3];
  #pragma unroll
  for(int z=0;z<3;z++) r[z]=LD(u32x4,tp+z*(2*FTS));
  #define YLO(w) ({ float r_; asm("v_and_b32_sdwa %0, %1, %2 dst_sel:WORD_1 dst_unused:UNUSED_PAD src0_sel:WORD_0 src1_sel:WORD_0" : "=v"(r_) : "v"(w),"v"(ml)); r_; })
  if(__builtin_expect(odd,0)){
    LANE(ln); lch* eb=sm+O_X2+2048+w4*4608+8*FTS; ST(int,eb+ln*4)=ye;
    #pragma unroll
    for(int z=0;z<3;z++) if(z<2||w4==0){ u32x4 ov;
      #pragma unroll
      for(int e=0;e<4;e++){ u32 rw=r[z][e]&~YMASK; int i=8*z+2*e; u32x2 ex=LD(u32x2,eb+(z>>1)*128+(ln&3)*32+e*8); ov[e]=pk(__builtin_amdgcn_ldexpf(blo(rw)*f.gg[i],(int)ex[0])+f.bs[i],__builtin_amdgcn_ldexpf(bhi(rw)*f.gg[i+1],(int)ex[1])+f.bs[i+1]); }
      BST128(ov,ro,ob[z],0,SC1); }
  } else {
  #pragma unroll
  for(int z=0;z<3;z++) if(z<2||w4==0){ u32x4 ov; float v[8];
    #pragma unroll
    for(int e=0;e<4;e++){ u32 rw=r[z][e]; v[2*e]=YLO(rw); v[2*e+1]=BC(float,rw&0x9fff0000u); }
    SCHED();
    #pragma unroll
    for(int e=0;e<4;e++){ int i=8*z+2*e; ov[e]=pk(__builtin_fmaf(v[2*e],f.gg[i],f.bs[i]),__builtin_fmaf(v[2*e+1],f.gg[i+1],f.bs[i+1])); }
    BST128(ov,ro,ob[z],0,SC1); SCHED(); } }
}

#define TROW 528
#define O_TB 4640
#define O_DE 34752
#define O_DO 35968
#define O_R2K 37120
#define O_R3K 37632
#define O_DUM 37952
#define O_RED O_TB
#define MH(a,b,c) __builtin_amdgcn_mfma_f32_16x16x32_f16(a,b,c,0,0,0)
DEV h8 ld4w(const lch* p){ return BC(h8,(u32x4{LD(u32,p),LD(u32,p+4),LD(u32,p+8),LD(u32,p+12)})); }
DEV u32 __umul24_(u32 a,u32 b){ u32 r; asm("v_mul_u32_u24 %0, %1, %2" : "=v"(r) : "s"(b),"v"(a)); return r; }
DEV u32x4 band(u32 mask,int g,u32 val){ u32 by=__builtin_amdgcn_ubfe(mask,8*g,8), X=by|(by<<15), k=0x10001u; u32x4 r; asm volatile("" : "+s"(k),"+s"(val));
  #pragma unroll
  for(int d=0;d<4;d++) r[d]=__umul24_((X>>(2*d))&k,val);
  return r; }
DEV h8 zpack(f32x4 e,f32x4 o){ return BC(h8,(u32x4{pkh(e[0],o[0]),pkh(e[1],o[1]),pkh(e[2],o[2]),pkh(e[3],o[3])})); }
struct C3 { float r,i,n; };
DEV C3 c3(C2 a){ C3 c; c.r=a.r; c.i=a.i; PIN(c.i); c.n=-c.i; PIN(c.n); return c; }
DEV C2 cm(C2 a,C3 b){ C2 c; c.r=__builtin_fmaf(a.r,b.r,a.i*b.n); c.i=__builtin_fmaf(a.r,b.i,a.i*b.r); return c; }
DEV u32 ph(C2 a){ return pkh(a.r,a.i); }
#define W2(p,a,b) asm volatile("ds_write2_b32 %0, %1, %2 offset1:132" :: "v"(p),"v"(a),"v"(b) : "memory")
struct CP { const lch *x0,*x1,*bd; lch *p0,*p1,*p2,*wp; u32x4 Be,Bo,AE; int yo; };
template<int ROLE> DEV void lanes(CP& c,lch* cb,int ln){ int m=ln&15, g=ln>>4;
  const lch* ad=cb+((m&1)? O_DO+2*527 : O_DE+2*536)+2*(8*g-m);
  const lch* bd=cb+2*(256+16*m+8*g); lch* dum=cb+O_DUM;
  if(ROLE==0){ int l0=128*(g>>1)+8*m+4*(g&1); c.p0=cb+O_DE+(549-l0)*2; PIN(c.p0); c.p1=c.p0+6; PIN(c.p1); c.p2=c.p0+(O_DO-O_DE-18); c.x0=ad-640; c.bd=bd-64; c.x1=cb+O_R3K+2*(126-2*m+8*g)-192; PIN(c.x1);
    c.Be=band(0x1ffu<<(m+4),g,0x3c00u); PIN(c.Be); }
  if(ROLE==1){ lch* r=cb+O_R2K+(192-64*(g-2)-2*m)*2; c.p0= g<2? dum : r-66; c.p1= ln==0? cb+O_DE+(552-256)*2 : (ln<=32? dum : r); c.p2=cb+O_DE+(544-256-8*ln)*2; c.x0=ad-128-640; c.bd=bd-128-128; PIN(c.p0); PIN(c.p1); PIN(c.p2); }
  if(ROLE==2){ int i0=16*m+4*g; c.p0= m<8? cb+O_R3K+(125-i0)*2 : dum+2; c.p1= ln==0? cb+O_R2K+128 : (ln==8? cb+O_R3K : c.p0+6); c.x0=ad-320-640; c.bd=bd-320-128; PIN(c.p0); PIN(c.p1); }
  if(ROLE==3){ c.x0=cb+O_R2K+2*(188-4*m+8*g)-256; c.bd=cb+2*(248+8*m+8*g);
    c.Be=band(0x1fu<<(m+6),g,0x3c00u); c.Bo=band(0x7u<<(m+11),g,0x3c00u); PIN(c.Be); PIN(c.Bo); }
  PIN(c.x0); PIN(c.bd); }
template<int ROLE> DEV void lanes2(CP& c,lch* cb,int ln){ int m=ln&15, g=ln>>4;
  if(ROLE==0){ c.Bo=band(0x7fu<<(m+5),g,0x3c00u); c.AE=band(3u<<(m+12),g,0xffffu); PIN(c.Bo); PIN(c.AE); }
  c.wp=cb+O_RED+ln*16; c.yo=(g&1)*512+m*32+(g>>1)*16; PIN(c.wp); PIN(c.yo); }
template<int ROLE> DEV void chan2(CP& c,lch* sm,lch* cb,const lch*& fa,const lch*& fb,int grp,int d,int one_,int emax,u64 tag,char* ws){
  const f32x4 Z4={0,0,0,0}; h8 A[6]; f32x4 acc[8]; u32x4 rsY; float sc; u32 nvm, yp[4]; int ca, ye= emax<-24||emax>9? emax:0;
  #define YPRE rsY=mkrs(ws+W_Y+d*YST,4096); sc=__builtin_amdgcn_ldexpf(1.f,emax-ye-YSH-96); nvm=~YMASK; _Pragma("unroll") for(int k=0;k<4;k++){ yp[k]=ypat((u32)tag,(c.yo>>4)&1,k); PIN(yp[k]); } asm volatile("" : "+s"(rsY),"+s"(nvm)); PIN(sc); ca=(C_G+grp)*4; PIN(ca);
  #define LD_U "buffer_load_dwordx4 v[112:115], %[of], %[rs], 0 offen sc1\n\tbuffer_load_dwordx4 v[116:119], %[of], %[rs], %[s1] offen sc1\n\tbuffer_load_dwordx4 v[120:123], %[of], %[rs], %[s2] offen sc1\n\tbuffer_load_dwordx4 v[124:127], %[of], %[rs], %[s3] offen sc1\n\t"
  #define UX(a,b,c,d,o) "v_xor_b32 v" #a ", %[e0], v" #a "\n\tv_xor_b32 v" #b ", %[e1], v" #b "\n\tv_xor_b32 v" #c ", %[e2], v" #c "\n\tv_xor_b32 v" #d ", %[e3], v" #d "\n\tds_write_b128 %[up], v[" #a ":" #d "] offset:" #o "\n\t"
  #define UQ(n,a,b,c,d,o) "s_waitcnt vmcnt(" #n ")\n\t" UD("%[t0]",a,c0) UD("%[t1]",b,c1) UD("%[t0]",c,c2) UD("%[t1]",d,c3) UX(a,b,c,d,o)
  #define UPOLL { u32 t0,t1,cnt; asm volatile("s_mov_b32 %[cnt], 0x20000\n1:\n\tv_mov_b32 %[t0], 0\n\tv_mov_b32 %[t1], 0\n\t" UQ(3,112,113,114,115,544) UQ(2,116,117,118,119,1056) UQ(1,120,121,122,123,1568) UQ(0,124,125,126,127,2080) \
      "v_bitop3_b32 %[t0], %[t0], %[t1], %[mk] bitop3:0xa8\n\tv_cmp_ne_u32 vcc, 0, %[t0]\n\ts_cbranch_vccz 9f\n\ts_sleep " STR(PSLP) "\n\ts_mov_b64 exec, vcc\n\t" LD_U "s_sub_u32 %[cnt], %[cnt], 1\n\ts_cbranch_scc0 1b\n9:\n\t" \
      "s_mov_b64 exec, 1\n\tds_add_u32 %[cu], %[on]\n\ts_mov_b64 exec, -1" \
      : [t0]"=&v"(t0),[t1]"=&v"(t1),[cnt]"=&s"(cnt) : [of]"v"(c.p0),[rs]"s"(rsU),[s1]"s"((32>>TL)*GST_),[s2]"s"((64>>TL)*GST_),[s3]"s"((96>>TL)*GST_),[c0]"s"(c0),[c1]"s"(c1),[c2]"s"(c2),[c3]"s"(c3_), \
        [e0]"s"(c0^0x40004u),[e1]"s"(c1^0x40004u),[e2]"s"(c2^0x40004u),[e3]"s"(c3_^0x40004u),[mk]"s"(UMASK),[up]"v"(c.p1),[cu]"v"((C_U+one_)*4),[on]"v"(1) : PCLOB); }
  if(ROLE<3){
    h8 a[8], b[8];
    #pragma unroll
    for(int ks=0;ks<8;ks++){ a[ks]=LD(h8,fa+ks*64); b[ks]=LD(h8,fb+ks*64); }
    SCHED();
    LANE(ln); float fz= ln==32? 0.f : 4.76837158203125e-07f; if(ROLE==1) PIN(fz);
    SCHED();
    f32x4 k=Z4;
    #pragma unroll
    for(int ks=0;ks<8;ks++) k=MH(a[ks],b[ks],k);
    const float f=4.76837158203125e-07f;
    float k0=k[0]*f, k1=k[1]*f, k2=k[2]*f, k3=k[3]*f;
    u32 h32=pkh(k3,k2), h10=pkh(k1,k0);
    if(ROLE!=1){
      ST(u16,c.p0)=(u16)h32; ST(u32,c.p0+2)=pkh(k2,k1); ST(u16,c.p1)=(u16)(h10>>16);
      if(ROLE==0) ST(u32x2,c.p2)=u32x2{h32,h10};
    } else {
      float n0=dppf<0x130>(k[0])*fz, z1= fz==0.f? k0:k1, z2=k[2]*fz, z3=k[3]*fz; u32 h22=pkh(z2,z2);
      ST(u16,c.p0+64)=(u16)h10; ST(u16,c.p0+2)=(u16)(h32>>16); ST(u16,c.p0)=(u16)h32; ST(u16,c.p1)=(u16)(h10>>16);
      u32x4 vo={pkh(n0,z3),h22,pkh(z2,z1),pkh(k0,k0)}, ve={pkh(n0,n0),pkh(z3,z2),h22,pkh(z1,k0)};
      asm volatile("s_mov_b32 exec_hi, 1\n\tds_write_b128 %0, %1\n\tds_write_b128 %0, %2 offset:1200\n\ts_mov_b32 exec_hi, -1" :: "v"(c.p2),"v"(ve),"v"(vo) : "memory"); }
    gsync(sm,C_G+grp,7);
    u32 kv; A[0]=ld4w(c.x0+640); A[1]=ld4w(c.x0+128);
    if(ROLE==0){ A[2]=ld4w(c.x1+192); A[3]=ld4w(c.x1+128); A[4]=ld4w(c.x1+64); A[5]=ld4w(c.x1); kv=LD(u16,cb+O_R2K+128); }
    else { A[2]=ld4w(c.x0+576); A[3]=ld4w(c.x0+64); A[4]=ld4w(c.x0+512); A[5]=ld4w(c.x0); }
    SCHED();
    if(ROLE==0){ kv|=kv<<16; c.AE=u32x4{c.AE[0]&kv,c.AE[1]&kv,c.AE[2]&kv,c.AE[3]&kv}; PIN(c.AE); }
    int ha=W_HT+d*8; u32 hd= one_? HD3 : (grp==0? HD0:HD1); if(ROLE==2) asm volatile("v_mov_b32 v113, %2" : "+v"(ha),"+v"(hd) : "s"((u32)tag));
    YPRE
    PRIO(2);
    if(BID>=112) gwait<4>(sm,C_D,4);
    gwait<1>(sm,C_U+one_,2-one_);
    if(ROLE==2) asm volatile("s_memrealtime s[98:99]\n\ts_waitcnt lgkmcnt(0)\n\tv_add_u32 v112, s98, %1\n\ts_mov_b64 exec, 1\n\tglobal_store_dwordx2 %0, v[112:113], %2 sc1\n\ts_mov_b64 exec, -1" :: "v"(ha),"v"(hd),"s"(ws) : "memory");
  } else {
    u32 tg=(u32)tag, c0=upat(tg,0), c1=upat(tg,1), c2=upat(tg,2), c3_=upat(tg,3); u32x4 rsU=mkrs(ws+W_U,256*UST);
    if(!one_) PRIO(2);
    c.p0=(lch*)fa; c.p1=(lch*)fb;
    bool late= grp==0||BID>=112;
    asm volatile("s_mov_b64 exec, 1\n\tglobal_store_dwordx2 %0, %1, %2 sc1\n\ts_mov_b64 exec, -1" :: "v"(W_FL+d*8),"v"((tag<<16)|(u64)(ye&0xffff)),"s"(ws) : "memory");
    if(!late) asm volatile(LD_U :: [of]"v"(c.p0),[rs]"s"(rsU),[s1]"s"((32>>TL)*GST_),[s2]"s"((64>>TL)*GST_),[s3]"s"((96>>TL)*GST_) : "memory");
    #pragma unroll
    for(int j=2;j<8;j++){ acc[j]=Z4; PIN(acc[j]); }
    YPRE
    if(late){ if(BID<112) gwait<2>(sm,C_G,UGATE); else { gwait<1>(sm,C_RS,4); __builtin_amdgcn_s_sleep(UE); } asm volatile(LD_U :: [of]"v"(c.p0),[rs]"s"(rsU),[s1]"s"((32>>TL)*GST_),[s2]"s"((64>>TL)*GST_),[s3]"s"((96>>TL)*GST_) : "memory"); }
    if(!late) UPOLL
    gwait<1>(sm,C_G+grp,7);
    #pragma unroll
    for(int i=0;i<6;i++) A[i]=ldA(c.x0+320-64*i);
    if(late) UPOLL
    PRIO(2);
    gwait<1>(sm,C_U+one_,2-one_);
  }
  const lch* bd=c.bd; h8 Be=BC(h8,c.Be), Bo=BC(h8,c.Bo);
  #pragma unroll
  for(int j=0;j<(ROLE==3? 2:8);j++) acc[j]=Z4;
  #define RW(j) if((j)>=0&&((j)>>1)!=ROLE&&!(ROLE==3&&(j)<2)){ ST(f32x4,c.wp+((j)>>1)*6144+((ROLE-((j)>>1)-1)&3)*2048+((j)&1)*1024)=acc[(j)<0? 0:(j)]; SCHED(); }
  if(ROLE==1||ROLE==2){
    h8 B[8];
    #pragma unroll
    for(int q=0;q<8;q++) B[q]=LD(h8,bd+128+512*q);
    #pragma unroll
    for(int r=0;r<3;r++){
      #pragma unroll
      for(int dl=0;dl<8;dl++){ if(dl<7) acc[dl+1]=MH(A[2*r+1],B[dl],acc[dl+1]); SCHED(); acc[dl]=MH(A[2*r],B[dl],acc[dl]); SCHED();
        if(r<2){ B[dl]=LD(h8,bd+64-64*r+512*dl); SCHED(); } else RW(dl-2) }
      } }
  if(ROLE==0){
    h8 B[8], ue[4];
    #pragma unroll
    for(int kt=0;kt<4;kt++){ ue[kt]=LD(h8,bd+48+512*kt); B[kt]=LD(h8,bd+64+512*kt); }
    f32x4 ce[2],co[2]; h8 Z[2];
    ce[0]=MH(ue[0],Be,Z4); co[0]=MH(B[0],Bo,Z4); ce[1]=MH(ue[1],Be,Z4); co[1]=MH(B[1],Bo,Z4); Z[0]=zpack(ce[0],co[0]);
    #pragma unroll
    for(int kt=0;kt<4;kt++){
      SCHED();
      if(kt+2<4){ ce[kt&1]=MH(ue[kt+2],Be,Z4); co[kt&1]=MH(B[kt+2],Bo,Z4); }
      if(kt+1<4) Z[(kt+1)&1]=zpack(ce[(kt+1)&1],co[(kt+1)&1]);
      #pragma unroll
      for(int rho=0;rho+kt<4;rho++) acc[4+kt+rho]=MH(Z[kt&1],A[2+rho],acc[4+kt+rho]);
      SCHED(); B[4+kt]=LD(h8,bd+64+512*(4+kt)); if(kt==0) A[5]=BC(h8,c.AE); if(kt==1) A[4]=ld4w(c.x0-384); if(kt==2) A[3]=ld4w(c.x0+64); if(kt==3) A[2]=ld4w(c.x0+576); SCHED(); }
    #pragma unroll
    for(int dl=0;dl<8;dl++){ if(dl<4) acc[dl+4]=MH(A[5],B[dl],acc[dl+4]); SCHED(); if(dl<6) acc[dl+2]=MH(A[4],B[dl],acc[dl+2]); SCHED(); if(dl<7) acc[dl+1]=MH(A[1],B[dl],acc[dl+1]); SCHED(); acc[dl]=MH(A[0],B[dl],acc[dl]);
      SCHED(); B[dl]=LD(h8,bd+512*dl); SCHED(); }
    #pragma unroll
    for(int dl=0;dl<8;dl++){ if(dl<7) acc[dl+1]=MH(A[3],B[dl],acc[dl+1]); SCHED(); acc[dl]=MH(A[2],B[dl],acc[dl]); SCHED(); RW(dl-2) } }
  if(ROLE==3){
    h8 U[12];
    #pragma unroll
    for(int kt=0;kt<6;kt++) U[kt]=LD(h8,bd+256*kt);
    f32x4 ce[2],co[2]; h8 Z[2];
    ce[0]=MH(U[0],Be,Z4); co[0]=MH(U[0],Bo,Z4); ce[1]=MH(U[1],Be,Z4); co[1]=MH(U[1],Bo,Z4); Z[0]=zpack(ce[0],co[0]);
    #pragma unroll
    for(int kt=0;kt<12;kt++){
      SCHED();
      if(kt+2<12){ ce[kt&1]=MH(U[kt+2],Be,Z4); co[kt&1]=MH(U[kt+2],Bo,Z4); }
      if(kt+1<12) Z[(kt+1)&1]=zpack(ce[(kt+1)&1],co[(kt+1)&1]);
      #pragma unroll
      for(int rho=-1;rho<5;rho++){ int j=(kt+4+rho)>>1; if(((kt+rho)&1)==0&&j>=2&&j<8) acc[j]=MH(Z[kt&1],A[rho+1],acc[j]); }
      if(kt<6){ SCHED(); U[kt+6]=LD(h8,bd+256*(kt+6)); }
      if(kt>=3&&kt<=9&&(kt&1)){ SCHED(); RW((kt+1)/2) } }
  }
  PRIO(1);
  RW(6) RW(7)
  asm volatile("s_mov_b64 exec, 1\n\tds_add_u32 %0, %1\n\ts_mov_b64 exec, -1" :: "v"(ca),"v"(1) : "memory");
  { float s0[4], s1[4];
    f32x4 o0=acc[2*ROLE], o1=acc[2*ROLE+1];
    #define RD6 "ds_read_b128 v[112:115], %[rp] offset:%[o]\n\tds_read_b128 v[116:119], %[rp] offset:%[o]+1024\n\tds_read_b128 v[120:123], %[rp] offset:%[o]+2048\n\tds_read_b128 v[124:127], %[rp] offset:%[o]+3072\n\t.if %[o]\n\tds_read_b128 v[104:107], %[rp] offset:%[o]+4096\n\tds_read_b128 v[108:111], %[rp] offset:%[o]+5120\n\t.endif\n\t"
    #define MU4(p) "v_mul_f32 %[" #p "0], %[sc], %[i" #p "0]\n\tv_mul_f32 %[" #p "1], %[sc], %[i" #p "1]\n\tv_mul_f32 %[" #p "2], %[sc], %[i" #p "2]\n\tv_mul_f32 %[" #p "3], %[sc], %[i" #p "3]\n\t"
    #define FM4(p,r,k) "s_waitcnt lgkmcnt(%[n]-" #k ")\n\tv_fmac_f32 %[" #p "0], %[sc], v[" #r "]\n\tv_fmac_f32 %[" #p "1], %[sc], v[" #r "+1]\n\tv_fmac_f32 %[" #p "2], %[sc], v[" #r "+2]\n\tv_fmac_f32 %[" #p "3], %[sc], v[" #r "+3]\n\t"
    { u32 cnt,sv; asm volatile("ds_read_b32 v103, %[ca]\n\ts_nop 1\n\t" MU4(a) MU4(b) "s_mov_b32 %[cnt], 0x20000\n\ts_branch 4f\n"
        "1:\n\tds_read_b32 v103, %[ca]\n4:\n\ts_waitcnt lgkmcnt(0)\n\tv_readfirstlane_b32 %[sv], v103\n\ts_cmp_ge_u32 %[sv], 11\n\ts_cbranch_scc1 2f\n\ts_sleep 0\n\ts_sub_u32 %[cnt], %[cnt], 1\n\ts_cbranch_scc0 1b\n2:\n\t" RD6
        FM4(a,112,1) FM4(b,116,2) FM4(a,120,3) FM4(b,124,4) ".if %[o]\n\t" FM4(a,104,5) FM4(b,108,6) ".endif"
        : [cnt]"=&s"(cnt),[sv]"=&s"(sv),[a0]"=&v"(s0[0]),[a1]"=&v"(s0[1]),[a2]"=&v"(s0[2]),[a3]"=&v"(s0[3]),[b0]"=&v"(s1[0]),[b1]"=&v"(s1[1]),[b2]"=&v"(s1[2]),[b3]"=&v"(s1[3])
        : [ia0]"v"(o0[0]),[ia1]"v"(o0[1]),[ia2]"v"(o0[2]),[ia3]"v"(o0[3]),[ib0]"v"(o1[0]),[ib1]"v"(o1[1]),[ib2]"v"(o1[2]),[ib3]"v"(o1[3]),[ca]"v"(ca),[rp]"v"(c.wp),[sc]"v"(sc),[o]"i"(ROLE*6144),[n]"i"(ROLE==0? 4:6) : "memory","scc","v103","v104","v105","v106","v107","v108","v109","v110","v111"); }
    auto r0=__builtin_amdgcn_permlane16_swap(pk(s0[0],s0[1]),pk(s1[0],s1[1]),false,false); auto r1=__builtin_amdgcn_permlane16_swap(pk(s0[2],s0[3]),pk(s1[2],s1[3]),false,false);
    asm volatile("buffer_store_dwordx4 %0, %1, %2, 0 offen offset:%3 sc1\n\ts_nop 1" :: "v"(u32x4{(r0[0]&nvm)|yp[0],(r1[0]&nvm)|yp[1],(r0[1]&nvm)|yp[2],(r1[1]&nvm)|yp[3]}),"v"(c.yo),"s"(rsY),"i"(ROLE*1024) : "memory"); }
  garrive(sm,C_E);

  asm volatile("s_endpgm" : "=v"(fa),"=v"(fb),"=v"(c.x0),"=v"(c.x1),"=v"(c.bd),"=v"(c.p0),"=v"(c.p1),"=v"(c.p2),"=v"(c.wp),"=v"(c.Be),"=v"(c.Bo),"=v"(c.AE),"=v"(c.yo) :: "memory");
}
#define GETEP asm volatile("s_waitcnt lgkmcnt(0)\n\ts_mov_b64 %0, s[96:97]\n\ts_mov_b32 %1, s98" : "=s"(ep),"=s"(ldw));
DEV void chan(lch* sm,int grp,int w4,int d,int one_,char* ws,u32x4 q0,u32x4 q1,u32x4 q2,u32x4 q3,u32x4 q4){
  lch* cb=sm+O_CH+grp*SLSZ; int role=w4, emax;
  #define LP(v) ((lch*)(size_t)(u32)(v))
  const lch* fa=LP(q4[0]); const lch* fb=LP(q4[1]);
  u64 tag; CP c;
  { float a2,bp,br,bi,cr,ci;
    u32 fq=q3[2], fr=q3[3], one=q4[2]; float f64=BC(float,fq), f128=BC(float,fr);
    lch *t8[4]={LP(q0[0]),LP(q0[1]),LP(q0[2]),LP(q0[3])}, *tB[4]={LP(q1[0]),LP(q1[1]),LP(q1[2]),LP(q1[3])}, *t4[2]={LP(q2[0]),LP(q2[1])}, *t2[2]={LP(q2[2]),LP(q2[3])}, *t1=LP(q3[0]), *te=LP(q3[1]);
    { LANE(ln); asm volatile("" : "=v"(c.x1),"=v"(c.p0),"=v"(c.p1),"=v"(c.p2),"=v"(c.Be),"=v"(c.Bo),"=v"(c.AE));
      if(role==0){ lanes<0>(c,cb,ln); lanes2<0>(c,cb,ln); } else { if(role==1) lanes<1>(c,cb,ln); else if(role==2) lanes<2>(c,cb,ln); else lanes<3>(c,cb,ln); lanes2<1>(c,cb,ln); }
      asm volatile("" : "+v"(c.x0),"+v"(c.x1),"+v"(c.bd),"+v"(c.p0),"+v"(c.p1),"+v"(c.p2),"+v"(c.wp),"+v"(c.Be),"+v"(c.Bo),"+v"(c.AE),"+v"(c.yo)); }
    if(grp!=0) PRIO(1);
    u64 ep; u32 ldw; GETEP tag=ep+1ull;
    float dt=rb(expf_((d&1)? bhi(ldw):blo(ldw))), ca=dt*1.4426950408889634f, cb_=dt*0.15915494309189535f;
    asm volatile("s_waitcnt vmcnt(0)\n\tv_mul_f32 %2, %8, v114\n\tv_mul_f32 %3, %8, v115\n\tv_mul_f32 %0, %6, v112\n\tv_mul_f32 %1, %7, v113\n\tv_mov_b32 %4, v116\n\tv_mov_b32 %5, v117"
      : "=&v"(a2),"=&v"(bp),"=&v"(br),"=&v"(bi),"=&v"(cr),"=&v"(ci) : "v"(ca),"v"(cb_),"v"(dt) : "memory");
    C2 W; W.r=cr*br-ci*bi; W.i=cr*bi+ci*br;
    u32 mx=BC(u32,__builtin_fmaxf(__builtin_fabsf(W.r),__builtin_fabsf(W.i)));
    #define MXS(C) mx=umax_(mx,dppu<C>(mx));
    float e8=a2*8.f, eB=__builtin_fmaf(a2,f64,14.f), p8=bp*8.f, pB=bp*f64; SCHED();
    MXS(0xB1) p8=__builtin_amdgcn_fractf(p8); pB=__builtin_amdgcn_fractf(pB); e8=__builtin_amdgcn_exp2f(e8); SCHED();
    MXS(0x4E) eB=__builtin_amdgcn_exp2f(eB); float c8=__builtin_amdgcn_cosf(p8); SCHED();
    MXS(0x141) float s8=__builtin_amdgcn_sinf(p8), cB=__builtin_amdgcn_cosf(pB); SCHED();
    float sB=__builtin_amdgcn_sinf(pB); PIN(sB); MXS(0x140) float e1=__builtin_amdgcn_exp2f(a2); PIN(e1); SCHED();
    asm volatile("s_mov_b64 exec, 0x10001\n\tds_max_u32 %0, %1\n\tds_add_u32 %0, %2 offset:4\n\ts_mov_b64 exec, -1" :: "v"(te),"v"(mx),"v"(one) : "memory");
    C3 z8; z8.r=e8*c8; z8.n=e8*s8; z8.i=-z8.n; PIN(z8.i); C2 B; B.r=eB*cB; B.i=-(eB*sB);
    float p1=__builtin_amdgcn_fractf(bp), pE=__builtin_amdgcn_fractf(bp*256.f), pV=__builtin_amdgcn_fractf(bp*f128), eE=a2*256.f, eV=a2*f128, tr[8];
    #pragma unroll
    for(int k=0;k<4;k++){ SCHED(); u32 h0=ph(B); B=cm(B,z8);
      if(k==0){ tr[0]=__builtin_amdgcn_cosf(pV); tr[1]=__builtin_amdgcn_sinf(pV); } if(k==1){ tr[2]=__builtin_amdgcn_exp2f(eV); tr[3]=__builtin_amdgcn_cosf(p1); }
      if(k==2){ tr[4]=__builtin_amdgcn_sinf(p1); tr[5]=__builtin_amdgcn_exp2f(eE); } if(k==3){ tr[6]=__builtin_amdgcn_cosf(pE); tr[7]=__builtin_amdgcn_sinf(pE); }
      PIN(tr[2*k]); PIN(tr[2*k+1]);
      u32 h1=ph(B); if(k<3) B=cm(B,z8); W2(tB[k],h0,h1); }
    asm volatile("ds_read_b64 v[118:119], %0" :: "v"(te) : "memory");
    SCHED();
    C3 z1=c3(C2{e1*tr[3],e1*tr[4]}), E=c3(C2{tr[5]*tr[6],tr[5]*tr[7]}); C2 V[8]; V[0]=cmul(W,C2{tr[2]*tr[0],tr[2]*tr[1]});
    { u32 cnt,n; PIN(V[0].r); PIN(V[0].i); PIN(z1.r); PIN(E.r);
      asm volatile("s_mov_b32 %1, 0x20000\n1:\n\ts_waitcnt lgkmcnt(0)\n\tv_readfirstlane_b32 %2, v119\n\tv_readfirstlane_b32 %0, v118\n\ts_cmp_eq_u32 %2, 8\n\ts_cbranch_scc1 2f\n\tds_read_b64 v[118:119], %3\n\ts_sub_u32 %1, %1, 1\n\ts_cbranch_scc0 1b\n2:"
        : "=&s"(emax),"=&s"(cnt),"=&s"(n) : "v"(te) : "memory","scc");
      emax=RFL(emax); emax=(emax>>23)-126; }
    V[0].r=__builtin_amdgcn_ldexpf(V[0].r,14-emax); V[0].i=__builtin_amdgcn_ldexpf(V[0].i,14-emax);
    C2 Y=cm(V[0],E), Y4; u32 y0=ph(Y);
    V[1]=cm(V[0],z1); W2(t8[0],ph(V[0]),ph(V[1])); Y=cm(Y,E); u32 y1=ph(Y);
    V[2]=cm(V[1],z1); Y=cm(Y,E); u32 y2=ph(Y);
    V[3]=cm(V[2],z1); W2(t8[1],ph(V[2]),ph(V[3])); W2(t4[0],y0,ph(cm(V[2],E)));
    V[4]=cm(V[3],z1); Y=cm(Y,E); ST(u32,t1)=ph(Y);
    V[5]=cm(V[4],z1); W2(t8[2],ph(V[4]),ph(V[5])); Y4=cm(V[4],E); u32 y4=ph(Y4);
    V[6]=cm(V[5],z1); Y=cm(Y,E); ST(u32,t1+2*TROW)=ph(Y);
    V[7]=cm(V[6],z1); W2(t8[3],ph(V[6]),ph(V[7])); Y4=cm(Y4,E); W2(t2[0],y1,ph(Y4));
    W2(t4[1],y4,ph(cm(V[6],E))); Y=cm(Y,E); ST(u32,t1+4*TROW)=ph(Y);
    Y4=cm(Y4,E); W2(t2[1],y2,ph(Y4)); Y=cm(Y,E); ST(u32,t1+6*TROW)=ph(Y);
    Y=cm(Y,E); asm volatile("s_mov_b32 exec_hi, 0\n\tds_write_b32 %0, %1 offset:4224\n\ts_mov_b32 exec_hi, -1" :: "v"(t1),"v"(ph(Y)) : "memory");
  }
  if(one_) PRIO(3);
  gsync(sm,C_G+grp,4);
  if(role==0) chan2<0>(c,sm,cb,fa,fb,grp,d,one_,emax,tag,ws);
  if(role==1) chan2<1>(c,sm,cb,fa,fb,grp,d,one_,emax,tag,ws);
  if(role==2) chan2<2>(c,sm,cb,fa,fb,grp,d,one_,emax,tag,ws);
  chan2<3>(c,sm,cb,fa,fb,grp,d,one_,emax,tag,ws);
}

struct XR { int so,x0,x2; u32x4 rx,q[4]; };
DEV void xreq(XR& X,char* ws){ asm volatile("global_load_dwordx2 v[108:109], %[so], %[ws] sc1\n\tglobal_load_dwordx2 v[110:111], %[so], %[ws] offset:128 sc1\n\tbuffer_load_dwordx4 %[a], %[x0], %[rx], 0 offen\n\tbuffer_load_dwordx4 %[b], %[x0], %[rx], %[s1] offen\n\tbuffer_load_dwordx4 %[c], %[x2], %[rx], 0 offen"
  : [a]"=&{v[96:99]}"(X.q[0]),[b]"=&{v[100:103]}"(X.q[1]),[c]"=&{v[104:107]}"(X.q[2]),"=&{v[108:111]}"(X.q[3]) : [so]"v"(X.so),[ws]"s"(ws),[x0]"v"(X.x0),[x2]"v"(X.x2),[rx]"s"(X.rx),[s1]"s"(16*D_*2) : "memory"); }
DEV u32 med3u(u32 a,u32 b,u32 c){ u32 r; asm("v_med3_u32 %0, %1, %2, %3" : "=v"(r) : "v"(a),"v"(b),"v"(c)); return r; }
DEV u32 max3u(u32 a,u32 b,u32 c){ return umax_(umax_(a,b),c); }
#define GTC(r,s) "v_cmp_gt_u32 %[" #s "], v" #r ", %[c]\n\t"
#define GTA(s) "v_addc_co_u32 %[rk], vcc, 0, %[rk], %[" #s "]\n\t"
#define GT4(a,b,c,d) GTC(a,s0) GTC(b,s1) GTC(c,s2) GTC(d,s3) GTA(s0) GTA(s1) GTA(s2) GTA(s3)
#define GTS [s0]"=&s"(q0),[s1]"=&s"(q1),[s2]"=&s"(q2),[s3]"=&s"(q3)
template<class F> DEV void top8(lch* sm,int w4,int ln,u32 M,char* ws,u32& rank_,u32& key_,F& prep){
  u32 k[8], rc[8], hm=0xffff0000u; u64 em=0x0001000100010001ull,q0,q1,q2,q3;
  u32x4 rs=mkrs(ws+W_RG+(BID&(NRG-1))*8192,8192); int ro=w4*1024+ln*16;
  #pragma unroll
  for(int e=0;e<8;e++){ rc[e]=(u32)(0xfff-(w4*256+ln*4+(e&3)+(e>>2)*1024)); PIN(rc[e]); }
  lch *aW=sm+O_RMX+(w4*4+(ln>>4))*4, *aC=sm+O_RMX+(ln>>2)*4, *aO=sm+O_RMX+(ln&3)*16, *dC=sm+O_CND+(ln>>1)*4, *dO=sm+O_CND+(ln&1)*64; PIN(aW); PIN(aC); PIN(aO); PIN(dC); PIN(dO); PIN(ro); PIN(ln);
  asm volatile("" : "+s"(hm),"+s"(em),"+s"(rs));
  gwait<2>(sm,C_G,RGATE); __builtin_amdgcn_s_sleep(RDL);
  { u32 cnt;
    #define KL(e,r) "v_lshl_or_b32 %[k" #e "], v" #r ", 16, %[k" #e "]\n\t"
    #define KH(e,r) "v_and_or_b32 %[k" #e "], v" #r ", %[hm], %[k" #e "]\n\t"
    #define CHK_R "v_bitop3_b32 %[t0], v113, v115, %[M] bitop3:0x7e\n\tv_bitop3_b32 %[t1], v117, v119, %[M] bitop3:0x7e\n\tv_or_b32 %[t0], %[t0], %[t1]\n\tv_cmp_ne_u32 vcc, 0, %[t0]\n\t"
    u32 t0,t1;
    asm volatile(LD_DUP2 :: [of]"v"(ro),[rs]"s"(rs),[so]"s"(4096) : "memory");
    PRIO(PPR); prep(); PRIO(3);
    asm volatile(
      "s_sleep " STR(R2D) "\n\tbuffer_load_dwordx4 v[120:123], %[of], %[rs], 0 offen sc1\n\tbuffer_load_dwordx4 v[124:127], %[of], %[rs], %[so] offen sc1\n\ts_mov_b32 %[cnt], 0x20000\n\ts_waitcnt vmcnt(2)\n\t" CHK_R "s_cbranch_vccz 9f\n\t"
      "s_waitcnt vmcnt(0)\n\tv_mov_b64 v[112:113], v[120:121]\n\tv_mov_b64 v[114:115], v[122:123]\n\tv_mov_b64 v[116:117], v[124:125]\n\tv_mov_b64 v[118:119], v[126:127]\n\t"
      POLLASM(LD_DUP2,CHK_R,PSLP)
      KL(0,112) KH(1,112) KL(2,114) KH(3,114) KL(4,116) KH(5,116) KL(6,118) KH(7,118)
      : [k0]"+v"(rc[0]),[k1]"+v"(rc[1]),[k2]"+v"(rc[2]),[k3]"+v"(rc[3]),[k4]"+v"(rc[4]),[k5]"+v"(rc[5]),[k6]"+v"(rc[6]),[k7]"+v"(rc[7]),[cnt]"=&s"(cnt),[t0]"=&v"(t0),[t1]"=&v"(t1) : [of]"v"(ro),[rs]"s"(rs),[so]"s"(4096),[M]"s"(M),[hm]"s"(hm) : PCLOB); }
  garrive(sm,C_RS);
  #pragma unroll
  for(int e=0;e<8;e++) k[e]=rc[e];
  u32 h=max3u(k[0],k[1],k[2]), m1=max3u(max3u(k[3],k[4],k[5]),max3u(k[6],k[7],h),h), mx=m1, l=med3u(k[0],k[1],k[2]), t=umin_(umin_(k[0],k[1]),k[2]);
  SCHED();
  #define INS(e) t=med3u(t,l,k[e]); l=med3u(h,l,k[e]); if(e<7) h=umax_(h,k[e]); PIN(t); PIN(l); SCHED();
  mx=umax_(mx,dppu<0xB1>(mx)); INS(3) mx=umax_(mx,dppu<0x4E>(mx)); INS(4) mx=umax_(mx,dppu<0x141>(mx)); INS(5) mx=umax_(mx,dppu<0x140>(mx));
  asm volatile("s_mov_b64 exec, %2\n\tds_write_b32 %0, %1\n\ts_mov_b64 exec, -1\n\tds_read_b32 v116, %3\n\tds_read_b128 v[112:115], %4" :: "v"(aW),"v"(mx),"s"(em),"v"(aC),"v"(aO) : "memory");
  INS(6) INS(7)
  u32 m2=l, m3=t, T;
  { u32 c,rk,cnt;
    asm volatile("s_mov_b32 %[cnt], 0x20000\n1:\n\ts_waitcnt lgkmcnt(0)\n\tv_cmp_eq_u32 vcc, 0, v116\n\ts_cbranch_vccz 2f\n\tds_read_b32 v116, %[aC]\n\tds_read_b128 v[112:115], %[aO]\n\ts_sub_u32 %[cnt], %[cnt], 1\n\ts_cbranch_scc0 1b\n2:\n\t"
      "v_mov_b32 %[c], v116\n\tv_mov_b32 %[rk], 0\n\t" GT4(112,113,114,115) "s_nop 1\n\tv_add_u32_dpp %[rk], %[rk], %[rk] quad_perm:[1,0,3,2] row_mask:0xf bank_mask:0xf\n\ts_nop 1\n\tv_add_u32_dpp %[rk], %[rk], %[rk] quad_perm:[2,3,0,1] row_mask:0xf bank_mask:0xf\n\t"
      "v_cmp_eq_u32 vcc, 7, %[rk]\n\ts_ff1_i32_b64 %[cnt], vcc\n\ts_nop 0\n\tv_readlane_b32 %[T], %[c], %[cnt]\n\ts_nop 1"
      : [c]"=&v"(c),[rk]"=&v"(rk),[cnt]"=&s"(cnt),[T]"=&s"(T),GTS : [aC]"v"(aC),[aO]"v"(aO) : PCLOB); T=RFL(T); }
  { u64 b1=BALLOT(m1>=T), b2=BALLOT(m2>=T), b3=BALLOT(m3>=T); int n1=__builtin_popcountll(b1), n2=n1+__builtin_popcountll(b2);
    lch* cw=sm+O_CND+w4*32;
    if(__builtin_expect(b3==0ull&&n2<=8,1)){
      int p1=(int)__builtin_amdgcn_mbcnt_hi((u32)(b1>>32),__builtin_amdgcn_mbcnt_lo((u32)b1,0u)), pf=n2+ln-p1; bool c1=m1>=T;
      if(__builtin_expect(b2!=0ull,0)){ int p2=(int)__builtin_amdgcn_mbcnt_hi((u32)(b2>>32),__builtin_amdgcn_mbcnt_lo((u32)b2,(u32)n1)); if(m2>=T) ST(u32,cw+p2*4)=m2; }
      int ps= c1? p1:pf; ps= ps<8? ps:32-8*w4; ST(u32,cw+ps*4)= c1? m1:1u;
    } else {
      u32 mine=0;
      _Pragma("unroll 1") for(int c=0;c<8;c++){ u32 lm=umax_(umax_(umax_(k[0],k[1]),umax_(k[2],k[3])),umax_(umax_(k[4],k[5]),umax_(k[6],k[7]))); u32 wm=wmax(lm); if(ln==c) mine=wm;
        #pragma unroll
        for(int e=0;e<8;e++) k[e]=(k[e]==wm)?0u:k[e]; }
      if(ln<8) ST(u32,cw+ln*4)=mine; } }
  { u32 c,rk,cnt;
    asm volatile("s_mov_b32 %[cnt], 0x20000\n\tv_mov_b32 %[rk], 0\n\ts_waitcnt vmcnt(0)\n1:\n\tds_read_b32 %[c], %[dC]\n\tds_read_b128 v[112:115], %[dO]\n\tds_read_b128 v[116:119], %[dO] offset:16\n\tds_read_b128 v[120:123], %[dO] offset:32\n\tds_read_b128 v[124:127], %[dO] offset:48\n\t"
      "s_waitcnt lgkmcnt(4)\n\tv_cmp_eq_u32 vcc, 0, %[c]\n\ts_cbranch_vccz 2f\n\ts_sub_u32 %[cnt], %[cnt], 1\n\ts_cbranch_scc0 1b\n2:\n\t"
      "s_waitcnt lgkmcnt(3)\n\t" GT4(112,113,114,115) "s_waitcnt lgkmcnt(2)\n\t" GT4(116,117,118,119) "s_waitcnt lgkmcnt(1)\n\t" GT4(120,121,122,123) "s_waitcnt lgkmcnt(0)\n\t" GT4(124,125,126,127)
      "s_nop 1\n\tv_add_u32_dpp %[rk], %[rk], %[rk] quad_perm:[1,0,3,2] row_mask:0xf bank_mask:0xf"
      : [c]"=&v"(c),[rk]"=&v"(rk),[cnt]"=&s"(cnt),GTS : [dC]"v"(dC),[dO]"v"(dO) : PCLOB);
    rank_=rk; key_=c; }
}

#define FILL_(n,WAIT) "s_mov_b32 m0, %[ld]\n\ts_mov_b32 %[c], " STR(n) "-1\n3:\n\t" WAIT "buffer_load_dwordx4 %[vo], %[rs], %[so] offen nt lds\n\ts_add_u32 m0, m0, 0x1000\n\ts_add_u32 %[so], %[so], %[st]\n\t" \
  "s_sub_u32 %[c], %[c], 1\n\ts_cbranch_scc0 3b\n\ts_mov_b32 %[ld], m0\n\t"
#define FILL(n,WAIT) do{ u32 c_; asm volatile(FILL_(n,WAIT) : [c]"=&s"(c_),[so]"+s"(wso),[ld]"+s"(wld) : [vo]"v"(wvo),[rs]"s"(wrs),[st]"s"(wst) : "memory","scc","m0"); }while(0)
#define FILLD(n,WN,WW_) do{ u32 c_,t_,s_; asm volatile(FILL_(n,"ds_read_b32 %[t], %[ad]\n\ts_waitcnt lgkmcnt(0)\n\tv_readfirstlane_b32 %[s], %[t]\n\ts_cmp_ge_u32 %[s], 4\n\ts_cbranch_scc1 4f\n\t" VW(WN) "4:\n\t" VW(WW_)) \
  : [c]"=&s"(c_),[so]"+s"(wso),[ld]"+s"(wld),[t]"=&v"(t_),[s]"=&s"(s_) : [vo]"v"(wvo),[rs]"s"(wrs),[st]"s"(wst),[ad]"v"(C_D*4) : "memory","scc","m0"); wso=RFL(wso); wld=RFL(wld); }while(0)
#define VW(n) "s_waitcnt vmcnt(" STR(n) ")\n\t"

__global__ NOPK __attribute__((amdgpu_num_vgpr(56),amdgpu_num_sgpr(102))) __launch_bounds__(1024,4) void kA(const u16* x_,char* ws_,const u16* wr_,const u16* Dsk_,const u16* wg_,const u16* wu_,const u16* wd_,u16* out_,KP kp){
  const u16 *x,*Dsk,*wg,*wu,*wd; char* ws; u16* out;
  __shared__ __attribute__((aligned(16))) char smem[LDS_TOTAL];
  lch* sm=(lch*)smem;
  u32x4 xa,xb_,wa,wb,dq[3],la,lb_; u32x2 lc; u32 xc,wc;
  #define SARR "v_mov_b32 v121, 80\n\tv_mov_b32 v122, 1\n\ts_mov_b64 exec, 1\n\tds_add_u32 v121, v122\n\ts_mov_b64 exec, -1\n\t"
  asm volatile(
    "v_readfirstlane_b32 s60, %[t]\n\tv_lshlrev_b32 v119, 1, %[t]\n\tv_mov_b32 v118, 0\n\ts_cmpk_ge_u32 s60, 0x100\n\ts_cbranch_scc1 5f\n\tds_write_b16 v119, v118\n\ts_waitcnt lgkmcnt(0)\n5:\n\t"
    "s_load_dwordx16 s[64:79], %[ka], 0x0\n\ts_load_dwordx16 s[80:95], %[ka], 0x40\n\t"
    "s_and_b32 s61, %[wg], 7\n\ts_lshr_b32 s62, %[wg], 3\n\tv_and_b32 v118, 31, %[t]\n\ts_bfe_u32 s63, s60, 0x20006\n\ts_cmpk_ge_u32 s60, 0x100\n\ts_cbranch_scc1 1f\n\t"
    "s_barrier\n\ts_setprio 3\n\ts_lshl_b32 s56, s61, 5\n\ts_add_u32 s56, s56, s62\n\ts_mulk_i32 s56, 0x2400\n\tv_lshrrev_b32 v119, 5, %[t]\n\tv_mul_u32_u24 v119, 0x480, v119\n\tv_add_u32 v119, s56, v119\n\t"
    "v_lshl_add_u32 v116, v118, 4, v119\n\tv_lshl_add_u32 v117, v118, 2, v119\n\tv_lshlrev_b32 v114, 4, v118\n\tv_lshlrev_b32 v115, 2, v118\n\t"
    "s_and_b32 s57, s62, 3\n\ts_lshl2_add_u32 s57, s57, s63\n\ts_lshl_b32 s57, s57, 6\n\ts_and_b32 s58, s62, 1\n\ts_lshl_b32 s58, s58, 6\n\tv_and_b32 v112, 3, %[t]\n\tv_lshl_add_u32 v113, v112, 4, s57\n\tv_lshl_add_u32 v112, v112, 4, s58\n\t"
    "s_lshl_b32 s52, s61, 5\n\ts_add_u32 s52, s52, s62\n\ts_lshr_b32 s53, s52, 1\n\ts_mulk_i32 s53, %[ust]\n\ts_add_u32 s53, s53, s58\n\ts_add_u32 s53, s53, %[wu]\n\tv_and_b32 v41, 3, %[t]\n\tv_lshrrev_b32 v40, 2, %[t]\n\tv_lshl_add_u32 v32, v41, 4, s53\n\tv_lshl_add_u32 v32, v40, 7, v32\n\t"
    "v_add_u32 v33, 0x2000, v32\n\tv_add_u32 v34, 0x4000, v32\n\tv_lshrrev_b32 v35, 1, %[t]\n\tv_and_b32 v35, 16, v35\n\t"
    "v_lshrrev_b32 v39, 5, %[t]\n\ts_movk_i32 s54, 1216\n\tv_mov_b32 v38, %[ti]\n\tv_mad_u32_u24 v37, v39, s54, v38\n\tv_add_u32 v36, v37, v114\n\tv_add_u32 v37, v37, v115\n\tv_lshlrev_b32 v39, 1, v39\n\tv_add_u32 v39, 128, v39\n\t"
    "v_and_b32 v119, 3, v40\n\tv_mad_u32_u24 v38, v119, s54, v38\n\tv_and_b32 v119, 0xf0, %[t]\n\tv_lshl_add_u32 v38, v119, 1, v38\n\tv_lshl_add_u32 v38, v41, 3, v38\n\t"
    "s_lshl_b32 s53, s52, 5\n\ts_add_u32 s53, s53, %[rg]\n\tv_and_b32 v40, 7, v40\n\tv_lshl_add_u32 v40, v40, 13, s53\n\tv_lshl_add_u32 v40, v41, 3, v40\n\tv_lshlrev_b32 v41, 2, v41\n\tv_add_u32 v41, 128, v41\n\t"
    "s_lshl_b32 s53, s52, 6\n\ts_add_u32 s53, s53, %[st]\n\ts_mov_b32 exec_lo, 0\n\tv_and_b32 v41, 7, %[t]\n\tv_lshl_add_u32 v40, v41, 3, s53\n\tv_lshlrev_b32 v41, 2, v41\n\tv_add_u32 v41, 400, v41\n\ts_mov_b32 exec_lo, -1\n\t"
    "s_waitcnt lgkmcnt(0)\n\t"
    "s_memrealtime s[98:99]\n\ts_load_dwordx2 s[96:97], s[66:67], 0x0\n\t"
    "global_load_dwordx4 %[wa], v114, s[68:69]\n\tglobal_load_dwordx4 %[wb], v114, s[68:69] offset:512\n\tglobal_load_dword %[wc], v115, s[68:69] offset:1024\n\t"
    "global_load_dwordx4 %[xa], v116, s[64:65]\n\tglobal_load_dwordx4 %[xb], v116, s[64:65] offset:512\n\tglobal_load_dword %[xc], v117, s[64:65] offset:1024\n\t"
    "global_load_dwordx4 %[d0], v113, s[70:71]\n\tglobal_load_dwordx4 %[d1], v112, s[70:71] offset:1024\n\t" SARR
    "s_branch 8f\n"
    "1:\n\t" "s_cmp_ge_u32 s62, " STR(CPF) "\n\ts_cbranch_scc1 6f\n\ts_getpc_b64 s[56:57]\n.Lkpc:\n\ts_and_b32 s58, s62, 3\n\ts_lshl_b32 s58, s58, 13\n\ts_sub_u32 s58, s58, 0x1000\n\tv_lshl_add_u32 v119, %[t], 4, s58\n\tv_min_u32 v119, .Lkend-.Lkpc-16, v119\n\t"
    "global_load_dword v120, v119, s[56:57]\n\t.subsection 1\n.Lkend:\n\t.subsection 0\n6:\n\ts_barrier\n\t"
    "v_lshlrev_b32 v119, 4, %[t]\n\tv_and_b32 v119, 0xff0, v119\n\t"
    "s_lshr_b32 s59, s60, 8\n\ts_cmp_lt_u32 s62, 8\n\ts_cselect_b32 s58, 2, 1\n\ts_cbranch_scc1 2f\n\ts_cmp_eq_u32 s59, 3\n\ts_cbranch_scc1 4f\n"
    "2:\n\ts_sub_u32 s57, s59, 2\n\ts_cmp_eq_u32 s59, 1\n\ts_cselect_b32 s58, s58, s57\n\ts_cmp_lt_u32 s62, 8\n\ts_cselect_b32 s56, 0, 8\n\ts_lshl1_add_u32 s56, s62, s56\n\ts_mulk_i32 s61, 72\n\ts_add_u32 s51, s56, s61\n\ts_sub_u32 s56, 2, s59\n\ts_sub_u32 s57, 16, s62\n\ts_cmp_eq_u32 s59, 3\n\ts_cselect_b32 s50, s57, s56\n\ts_add_u32 s56, s50, s51\n\t"
    "v_mov_b32 v112, 0\n\tv_mov_b32 v113, 0\n\tv_mov_b32 v114, 0\n\tv_mov_b32 v115, 0\n\tv_mov_b32 v116, 0\n\tv_mov_b32 v117, 0\n\ts_setprio 2\n\t"
    "s_lshl_b32 s57, s56, 2\n\ts_add_u32 s57, s57, s63\n\ts_lshl_b32 s57, s57, 6\n\tv_lshl_add_u32 v22, v118, 1, s57\n\ts_lshl_b32 s56, s56, 1\n\ts_and_b32 s56, s56, -4\n\t"
    "s_waitcnt lgkmcnt(0)\n\t"
    "global_load_short_d16_hi v112, v22, s[80:81] nt\n\tglobal_load_short_d16_hi v113, v22, s[82:83] nt\n\tglobal_load_short_d16_hi v114, v22, s[84:85] nt\n\t"
    "global_load_short_d16_hi v115, v22, s[86:87] nt\n\tglobal_load_short_d16_hi v116, v22, s[88:89] nt\n\tglobal_load_short_d16_hi v117, v22, s[90:91] nt\n\t"
    "s_load_dwordx2 s[96:97], s[66:67], 0x0\n\ts_load_dword s98, s[92:93], s56\n\t"
    SARR
    "s_mul_i32 s52, s58, %[sl]\n\ts_add_u32 s52, s52, %[tb]\n\ts_lshl_b32 s53, s63, 7\n\ts_add_u32 s53, s53, s52\n\tv_lshl_add_u32 v30, v118, 2, s53\n\tv_bfe_u32 v31, %[t], 5, 1\n\ts_movk_i32 s54, 528\n\tv_mul_u32_u24 v29, s54, v31\n\t"
    "v_lshl_add_u32 v2, v29, 3, v30\n\ts_movk_i32 s53, 1056\n\tv_add_u32 v3, s53, v2\n\tv_add_u32 v4, s53, v3\n\tv_add_u32 v5, s53, v4\n\tv_add_u32 v6, 41*528, v2\n\tv_add_u32 v7, s53, v6\n\tv_add_u32 v8, s53, v7\n\tv_add_u32 v9, s53, v8\n\t"
    "v_lshl_add_u32 v10, v29, 2, v30\n\tv_add_u32 v10, 16*528, v10\n\tv_add_u32 v11, s53, v10\n\tv_lshl_add_u32 v12, v29, 1, v30\n\tv_add_u32 v12, 24*528, v12\n\tv_add_u32 v13, 2112, v12\n\tv_add_u32 v14, v29, v30\n\tv_add_u32 v14, 32*528, v14\n\t"
    "s_lshl_b32 s53, s58, 4\n\ts_add_u32 s53, s53, %[ex]\n\tv_mov_b32 v15, s53\n\tv_cvt_f32_ubyte0 v31, v31\n\tv_mul_f32 v16, 0x42800000, v31\n\tv_add_f32 v17, v16, v16\n\tv_mov_b32 v20, 1\n\t"
    "v_and_b32 v29, 15, %[t]\n\tv_and_b32 v30, 48, %[t]\n\tv_mad_u32_u24 v30, v29, s54, v30\n\ts_lshl_b32 s55, s59, 1\n\ts_lshr_b32 s55, 0x48, s55\n\ts_add_u32 s55, s55, s63\n\ts_and_b32 s55, s55, 3\n\ts_mul_i32 s53, s55, 16*528\n\ts_movk_i32 s57, 41*528\n\ts_cmp_eq_u32 s55, 2\n\ts_cselect_b32 s53, s57, s53\n\ts_cselect_b32 s57, 32*528, s57\n\t"
    "s_add_u32 s53, s53, s52\n\ts_add_u32 s57, s57, s52\n\tv_add_u32 v18, s53, v30\n\tv_add_u32 v19, s57, v30\n\t"
    "v_min_u32 v29, 237*16, v119\n\tv_subrev_u32 v30, 204*16, v29\n\tv_add_u32 v29, %[de], v29\n\tv_min_u32 v29, v29, v30\n\ts_sub_u32 s53, s52, 4640\n\tv_add_u32 v29, s53, v29\n\tv_mov_b32 v24, 0\n\tv_mov_b32 v25, 0\n\tv_mov_b32 v26, 0\n\tv_mov_b32 v27, 0\n\tds_write_b128 v29, v[24:27]\n\t"
    "s_cmp_lg_u32 s55, 3\n\ts_cbranch_scc1 10f\n\t"
    "v_bfe_u32 v18, %[t], 1, 5\n\tv_and_b32 v19, 1, %[t]\n\ts_cmp_lt_u32 s62, 8\n\ts_cselect_b32 s53, 2, 1\n\ts_mul_i32 s53, s53, %[sl]\n\tv_mul_u32_u24 v21, s53, v19\n\ts_sub_u32 s55, 2, s59\n\ts_lshl_b32 s55, s55, 7\n\tv_add_u32 v18, s55, v18\n\ts_cmp_lg_u32 s59, 3\n\ts_cbranch_scc1 7f\n\t"
    "v_bfe_u32 v18, %[t], 5, 1\n\tv_lshl_add_u32 v18, v18, 7, v118\n\tv_mov_b32 v19, s50\n\tv_mov_b32 v21, %[sl]\n"
    "7:\n\tv_add_u32 v19, s51, v19\n\tv_lshl_add_u32 v21, v18, 4, v21\n\tv_lshrrev_b32 v24, 3-" STR(TL) ", v19\n\tv_and_b32 v19, 7>>" STR(TL) ", v19\n\tv_lshlrev_b32 v19, 4, v19\n\tv_lshl_add_u32 v19, v24, 7, v19\n\t"
    "v_and_b32 v24, (1<<" STR(TL) ")-1, v18\n\tv_lshl_add_u32 v19, v24, 7-" STR(TL) ", v19\n\tv_lshrrev_b32 v18, " STR(TL) ", v18\n\tv_mul_u32_u24 v18, %[ust], v18\n\tv_add_u32 v18, v18, v19\n\tv_add_u32 v19, 512, v21\n10:\n\t"
    "s_branch 9f\n"
    "4:\n\t"
    "v_bfe_u32 v118, %[t], 3, 5\n\tv_lshrrev_b32 v117, 1, v118\n\tv_and_b32 v116, 6, v117\n\tv_and_b32 v115, 7, %[t]\n\tv_xor_b32 v115, v115, v116\n\tv_lshlrev_b32 v115, 4, v115\n\t"
    "s_mul_i32 s54, s61, 9\n\ts_sub_u32 s55, s62, 14\n\ts_lshr_b32 s55, s55, 1\n\ts_add_u32 s54, s54, s55\n\ts_lshl_b32 s54, s54, 7\n\t"
    "s_mul_i32 s52, s61, 0xa2000\n\ts_mul_i32 s53, s62, 0x180\n\ts_add_u32 s52, s52, s53\n\ts_cmp_ge_u32 s62, 11\n\ts_cselect_b32 s53, 0x51000-0x480, 0\n\ts_add_u32 s52, s52, s53\n\ts_sub_u32 s52, s52, 0xc00\n\t"
    "s_movk_i32 s55, 0x480\n\ts_cmp_ge_u32 s62, 14\n\ts_cselect_b32 s54, s54, s52\n\ts_cselect_b32 s55, 0x2400, s55\n\tv_mad_u32_u24 v118, v118, s55, v115\n\ts_lshl_b32 s55, s55, 5\n\t"
    "s_lshl_b32 s52, s63, 10\n\ts_add_u32 m0, s52, %[r2]\n\ts_mov_b32 s58, 0x510000\n\ts_mov_b32 s59, 0x20000\n\ts_cmp_ge_u32 s62, 14\n\ts_cselect_b32 s53, " STR(PRE1) "-1, " STR(PRE2) "-1\n\ts_setprio 3\n\t"
    "s_waitcnt lgkmcnt(0)\n\t" "s_memrealtime s[98:99]\n\t"
    "s_bitcmp1_b32 %[wg], 3\n\ts_cselect_b64 s[56:57], s[74:75], s[72:73]\n\ts_cmp_ge_u32 s62, 14\n\ts_cselect_b64 s[56:57], s[56:57], s[76:77]\n\ts_cselect_b32 s52, 0x180, -1\n\ts_and_b32 s57, s57, 0xffff\n\ts_add_u32 s60, s56, s54\n\ts_and_b32 s60, s60, 0x380\n\ts_cmp_eq_u32 s60, s52\n\ts_cselect_b32 s53, " STR(PRE3) "-1, s53\n\t"
    "v_mov_b32 v121, 80\n\ts_movk_i32 s52, 0x400\n12:\n\tds_read_b32 v122, v121\n\ts_waitcnt lgkmcnt(0)\n\tv_readfirstlane_b32 s60, v122\n\ts_cmp_ge_u32 s60, 12\n\ts_cbranch_scc1 13f\n\ts_sleep 1\n\ts_sub_u32 s52, s52, 1\n\ts_cbranch_scc0 12b\n13:\n\t"
    "s_load_dwordx2 s[96:97], s[66:67], 0x0\n\t"
    "\n3:\n\t" "buffer_load_dwordx4 v118, s[56:59], s54 offen nt lds\n\ts_add_u32 m0, m0, 0x1000\n\ts_add_u32 s54, s54, s55\n\ts_sub_u32 s53, s53, 1\n\ts_cbranch_scc0 3b\n"
    "9:\n8:"
    : [xa]"=&{v[2:5]}"(xa),[xb]"=&{v[6:9]}"(xb_),[xc]"=&{v30}"(xc),[wa]"=&{v[10:13]}"(wa),[wb]"=&{v[14:17]}"(wb),[wc]"=&{v31}"(wc),[d0]"=&{v[18:21]}"(dq[0]),[d1]"=&{v[22:25]}"(dq[1]),[d2]"=&{v[26:29]}"(dq[2]),"=&{v[32:35]}"(la),"=&{v[36:39]}"(lb_),"=&{v[40:41]}"(lc),
      "=&{s[64:65]}"(x),"=&{s[66:67]}"(ws),"=&{s[70:71]}"(Dsk),"=&{s[72:73]}"(wg),"=&{s[74:75]}"(wu),"=&{s[76:77]}"(wd),"=&{s[78:79]}"(out)
    : [ka]"s"(__builtin_amdgcn_kernarg_segment_ptr()),[wg]"s"(BID),[t]"v"(TID),[r2]"i"(O_R2),[sl]"i"(SLSZ),[tb]"i"(O_CH+O_TB),[ex]"i"(O_EXP),[de]"i"(O_DE),[ust]"i"(GST_),[wu]"i"(W_U),[ti]"i"(O_TILE),[rg]"i"(W_RG),[st]"i"(W_ST)
    : "memory","scc","m0","v127","s50","s51","s52","s53","s54","s55","s56","s57","s58","s59","s60","s61","s62","s63","s68","s69",
      "s80","s81","s82","s83","s84","s85","s86","s87","s88","s89","s90","s91","s92","s93","s94","s95","s99");
  int wv=RFL(TID>>6), bid=BID, q4=wv>>2, w4=wv&3;
  int xg=bid&7, lb=bid>>3, tb=32*xg+lb, nch=2+(lb<8);
  u64 ep; u32 ldw;
  {
  }
  #define KILLIN "=v"(xa),"=v"(xb_),"=v"(xc),"=v"(wa),"=v"(wb),"=v"(wc),"=v"(dq[0]),"=v"(dq[1]),"=v"(dq[2]),"=v"(la),"=v"(lb_),"=v"(lc)
  if(__builtin_expect(q4==3&&lb>=8,0)){
    int fu=18*xg+lb-14, kq=2*xg+(lb>=11), cgp=lb-8-3*(lb>=11);
  u32 wst= lb>=14? F_*64:D_*64;
  u32 wso=(lb>=14? (fu>>1)*128 : kq*(288*D_*2)+384*cgp)+(lb>=14? PRE1:PRE2)*wst, wld=(u32)(size_t)sm+O_R2+w4*1024+(lb>=14? PRE1:PRE2)*4096; u32x4 wrs=mkrs(lb>=14? ((fu&1)? wu:wg):wd,D_*F_*2);
  GETEP
  #define FLANE(RS) LANE(ln); int g=ln>>4, li=ln&15, qq=li>>2, pq=li&3; u32 wvo=(u32)(8*w4+(ln>>3))*(RS)+(u32)((ln&7)^(((ln>>4)&2)|((w4&1)<<2)))*16u; \
    const lch* tp=sm+O_R2+(16*(g>>1)+8*(qq>>1)+4*(g&1)+(qq&1))*128+((w4^((g&1)|(qq&2)))*32)+pq*8;
  #define WFR(img,ks) BC(u32x4,trfrag(tp+(img)*CHSZ+(ks)*4096,tp+(img)*CHSZ+(ks)*4096+256))
  #define WF2(img,s) __builtin_shufflevector(WFR(img,2*(s)),WFR(img,2*(s)+1),0,1,2,3,4,5,6,7)
  f32x4 ac[3]={{0,0,0,0},{0,0,0,0},{0,0,0,0}};
  #define AC0 "+{v[100:103]}"(ac[0])
  #define AC1 "+{v[104:107]}"(ac[1])
  #define AC2 "+{v[108:111]}"(ac[2])
  #define SMF(i,a,b) asm volatile("v_smfmac_f32_16x16x64_bf16 %0, %1, %2, %3" : AC##i : "v"(a),"v"(b),"v"(ix) : "memory"); SCHED();
  #define SMR(i,r,b) asm volatile("v_smfmac_f32_16x16x64_bf16 %0, v[" #r ":" #r "+3], %1, %2" : AC##i : "v"(b),"v"(ix) : "memory"); SCHED();
  #define GW4(c,p) { u32 tv,cnt,sv; asm volatile("s_mov_b32 %1, 0x20000\n1:\n\tds_read_b32 %0, %3\n\tds_read_b128 v[112:115], %4\n\tds_read_b128 v[116:119], %4 offset:128\n\tds_read_b128 v[120:123], %4 offset:256\n\tds_read_b128 v[124:127], %4 offset:384\n\ts_waitcnt lgkmcnt(4)\n\t" \
    "v_readfirstlane_b32 %2, %0\n\ts_cmp_ge_u32 %2, 4\n\ts_cbranch_scc1 2f\n\ts_sleep 1\n\ts_sub_u32 %1, %1, 1\n\ts_cbranch_scc0 1b\n2:" : "=&v"(tv),"=&s"(cnt),"=&s"(sv) : "v"((c)*4),"v"(p) : "memory","scc"); }
  #define LGW(n) asm volatile("s_waitcnt lgkmcnt(" #n ")" ::: "memory");
  u64 tag=ep+1ull; const u32x4 I4={INV,INV,INV,INV};
  if(lb>=14){
    FLANE(F_*2)
    int mt=fu&1;
    const lch* xp=sm+O_TILE+(li&7)*XSTR+g*32+(li>>3)*16; int of=(mt*288+(fu>>1)*4+w4)*256+ln*4; u32 ix= (li&8)? 0xeeeeu:0x4444u;
    const lch* mp=sm+O_RC+(g&1)*16+(g>>1)*8;
    asm volatile("" : "+v"(xp),"+v"(of),"+v"(mp),"+v"(ix));
    rsrc_t rsG=RSRC(ws+W_GU,2*8*F_*2);
    if(w4==0) BST128(I4,rsG,(mt*288+(fu>>1)*4)*256+ln*16,0,SC1);
    bool slow=(((u32)wrs[0]+wso)&0x380u)==0x180u;
    if(slow){ wso-=(PRE1-PRE3)*wst; wld-=(PRE1-PRE3)*4096; FILL(S3-PRE3,VW(W3)); } else FILLD(5,W1,W1B);
    if(w4==0){ if(slow) VMW(S3-PRE3<5? S3-PRE3:5); else VMW(5); if(ln==0) GST(ws+W_FL+(576+fu)*8,tag); }
    if(slow){ wso=RFL(wso); wld=RFL(wld); FILL(18-S3,VW(W3B)); } else FILLD(13-PRE1,W1,W1B);
    u32x8 fB[9]; u32x4 one4={0x3f803f80u,0x3f803f80u,0x3f803f80u,0x3f803f80u}; const lch* tq=tp+65536; asm volatile("" : AC0,AC1,"+v"(one4),"+v"(tq));
    VMW(4); gsync(sm,C_P,4);
    #pragma unroll
    for(int s=0;s<7;s++) fB[s]=WF2(0,s);
    #pragma unroll
    for(int s=0;s<7;s++){ SMF(1,one4,fB[s]) }
    DRAIN(); gsync(sm,C_P,8);
    fB[7]=WF2(0,7); { const lch* tp=tq; fB[8]=WF2(0,0); } SMF(1,one4,fB[7]) SMF(1,one4,fB[8])
    { u32x4 xb[5]; u32x2 md;
      GW4(C_X,xp)
      md=LD(u32x2,mp);
      #pragma unroll
      for(int s=0;s<5;s++) xb[s]=LD(u32x4,xp+512+s*128);
      SCHED(); LGW(6)
      SMR(0,112,fB[0])
      float S; asm volatile("v_permlane32_swap_b32 v104, v105\n\tv_add_f32 %0, v104, v105" : "=v"(S),AC1); SCHED();
      SMR(0,116,fB[1]) SMR(0,120,fB[2])
      float ra=rcpf_(bhi(md[0])), rb_=rcpf_(bhi(md[1])); PIN(ra); PIN(rb_); SCHED();
      SMR(0,124,fB[3])
      float na=-blo(md[0])*S, nb=-blo(md[1])*S; PIN(na); PIN(nb); SCHED();
      SMF(0,xb[0],fB[4])
      float ea=ra*-1.4426950408889634f, eb=rb_*-1.4426950408889634f; PIN(ea); PIN(eb); SCHED();
      SMF(0,xb[1],fB[5]) SMF(0,xb[2],fB[6]) SMF(0,xb[3],fB[7]) SMF(0,xb[4],fB[8])
      float v0,v1,s0,s1;
      asm volatile("s_nop 7\n\tv_permlane32_swap_b32 v100, v102\n\tv_permlane32_swap_b32 v101, v103\n\tv_add_f32 v100, %5, v100\n\tv_add_f32 v101, %6, v101\n\tv_add_f32 v100, v100, v102\n\tv_add_f32 v101, v101, v103\n\t"
        "v_mul_f32 %2, %9, v100\n\tv_mul_f32 %3, %10, v101\n\tv_mul_f32 %0, %7, v100\n\tv_mul_f32 %1, %8, v101" : "=&v"(v0),"=&v"(v1),"=&v"(s0),"=&v"(s1),AC0 : "v"(na),"v"(nb),"v"(ra),"v"(rb_),"v"(ea),"v"(eb));
      if(mt==0){ v0*=rcpf_(1.f+__builtin_amdgcn_exp2f(s0)); v1*=rcpf_(1.f+__builtin_amdgcn_exp2f(s1)); }
      __builtin_amdgcn_raw_buffer_store_b32(nv(pk(v0,v1)),rsG,of,0,SC1); garrive(sm,C_H); }
    if(bid==112&&w4==1){
      _Pragma("unroll 1") for(int sp=0;sp<SPINCAP;sp++){ u64 bad=0, f[12];
        #pragma unroll
        for(int i=0;i<12;i++) f[i]=GLD(ws+W_FL+(i*64+ln)*8);
        #pragma unroll
        for(int i=0;i<12;i++) bad|= i<9? (f[i]^(tag<<16))>>16 : f[i]^tag;
        if(BALLOT(bad!=0ull)==0ull) break; __builtin_amdgcn_s_sleep(8); }
      if(ln==0) GST(ws+W_EPOCH,tag); }
    if(mt==0&&w4==0){
    LANE(l2); ln=l2; li=ln&15; g=ln>>4; gwait<1>(sm,C_IX,1);
    int c9=(lb-14)>>1, t=RFL((int)LD(u16,sm+O_IDX+xg*2)), c=64*c9+4*li+g, ey; f32x4 pr;
    { u32 u=LD(u16,sm+O_IDX+16+xg*2)^0x8000u, xb=LD(u16,sm+O_TILE+xg*XSTR+c*2), sv=LD(u32,sm+O_RC+xg*4); float ds=b2f(((const GAS u16*)Dsk)[c]); u^=(u32)(((int)(u<<16))>>31)&0x7fffu;
      ey=96+(short)poll1<16>(ws+W_FL+c*8,tag<<16); poll1<0>(ws+W_FL+(720+(ln<48? ln:47))*8,tag);
      float xn=(b2f(xb)-blo(sv))*rcpf_(bhi(sv)); pr=f32x4{b2f(xb),xn,xn*ds,sigm_f(b2f(u))}; }
    { gwait<2>(sm,C_E,4); u32 ti=now_(), mx=0;
      #pragma unroll
      for(int k=0;k<3;k++){ int i=ln+64*k; i= i<144? i:143; int q=(i*3641)>>16; u64 h=GLD(ws+W_HT+(72*q+36+2*(i-18*q))*8); mx=umax_(mx,(u32)(h>>32)==(u32)tag? (u32)h-ldw : ti-ldw+HM); }
      u32 tr=umin_(wmax(mx),ti-ldw+160u); u32 hk=ti-ldw+HKR; if(tr>hk) tr-=(tr-hk)>>1; tgate2(ldw,tr+HR); }
    { u32x4 rs=mkrs(ws+W_PART+(xg*9+c9)*4096,4096), ry=mkrs(ws+W_Y,D_*YST);
      int yo=c*YST+(t&~15)*2, po=ln*16; u32 a[4], t0,cnt, tg=(u32)tag; u32x8 yq; float yb; u16* op=out+t*D_+c;
      #define LD_P "buffer_load_dwordx4 v[112:115], %[of], %[rs], 0 offen sc1\n\tbuffer_load_dwordx4 v[116:119], %[of], %[rs], 0 offen offset:1024 sc1\n\tbuffer_load_dwordx4 v[120:123], %[of], %[rs], 0 offen offset:2048 sc1\n\tbuffer_load_dwordx4 v[124:127], %[of], %[rs], 0 offen offset:3072 sc1\n\t"
      #define LD_YB "buffer_load_dwordx4 v[96:99], %[yo], %[ry], 0 offen sc1\n\tbuffer_load_dwordx4 v[100:103], %[yo], %[ry], 0 offen offset:16 sc1\n\t"
      #define AD4(o,a,b,c,d) "v_add_f32 %[t0], v" #a ", v" #b "\n\tv_add_f32 %[" #o "], v" #c ", v" #d "\n\tv_add_f32 %[" #o "], %[t0], %[" #o "]\n\t"
      u64 ym;
      asm volatile(LD_YB LD_P "s_mov_b32 %[cnt], 0x20000\n1:\n\ts_waitcnt vmcnt(4)\n\tv_xor_b32 %[t0], %[c0], v96\n\t" UD("%[t0]",97,c1) UD("%[t0]",98,c2) UD("%[t0]",99,c3) UD("%[t0]",100,c4) UD("%[t0]",101,c5) UD("%[t0]",102,c6) UD("%[t0]",103,c7) "v_and_b32 %[t0], %[mk], %[t0]\n\tv_cmp_ne_u32 %[ym], 0, %[t0]\n\t"
        "s_set_gpr_idx_on %[k], gpr_idx(SRC0)\n\tv_mov_b32 %[yb], v96\n\ts_set_gpr_idx_off\n\tv_lshlrev_b32 %[yb], %[sh], %[yb]\n\tv_and_b32 %[yb], 0x9fff0000, %[yb]\n\tv_ldexp_f32 %[yb], %[yb], %[ey]\n\ts_waitcnt vmcnt(0)\n\t" CHK_I0 MX2(116,117) MX2(118,119) MX2(120,121) MX2(122,123) MX2(124,125) MX2(126,127) CHK_IE
        "s_or_b64 exec, vcc, %[ym]\n\ts_cbranch_scc0 9f\n\ts_sleep " STR(QSLP) "\n\ts_mov_b64 exec, %[ym]\n\ts_cbranch_execz 2f\n\t" LD_YB "2:\n\ts_mov_b64 exec, vcc\n\ts_cbranch_execz 3f\n\t" LD_P
        "3:\n\ts_mov_b64 exec, -1\n\ts_waitcnt vmcnt(0)\n\ts_sub_u32 %[cnt], %[cnt], 1\n\ts_cbranch_scc0 1b\n9:\n\ts_mov_b64 exec, -1\n\t"
        AD4(a0,112,116,120,124) AD4(a1,113,117,121,125) AD4(a2,114,118,122,126) AD4(a3,115,119,123,127) "s_nop 1"
        : [t0]"=&v"(t0),[cnt]"=&s"(cnt),[a0]"=&v"(a[0]),[a1]"=&v"(a[1]),[a2]"=&v"(a[2]),[a3]"=&v"(a[3]),"=&{v[96:103]}"(yq),[yb]"=&v"(yb),[ym]"=&s"(ym) : [of]"v"(po),[rs]"s"(rs),[yo]"v"(yo),[ry]"s"(ry),[c0]"s"(ypat(tg,0,0)),[c1]"s"(ypat(tg,0,1)),[c2]"s"(ypat(tg,0,2)),[c3]"s"(ypat(tg,0,3)),
          [c4]"s"(ypat(tg,1,0)),[c5]"s"(ypat(tg,1,1)),[c6]"s"(ypat(tg,1,2)),[c7]"s"(ypat(tg,1,3)),[mk]"s"(YMASK),[ey]"v"(ey),[k]"s"((t>>1)&7),[sh]"s"((~t&1)*16),"v"(op),"v"(pr) : PCLOB,"m0");
      auto s0=__builtin_amdgcn_permlane32_swap(a[0],a[2],false,false); auto s1=__builtin_amdgcn_permlane32_swap(a[1],a[3],false,false);
      u32 x0=s0[0],x1=s0[1],y0=s1[0],y1=s1[1]; u32 p0=BC(u32,BC(float,x0)+BC(float,x1)), p1=BC(u32,BC(float,y0)+BC(float,y1));
      auto s2=__builtin_amdgcn_permlane16_swap(p0,p1,false,false); u32 z0=s2[0], z1=s2[1]; float s=BC(float,z0)+BC(float,z1);
      float gq=silu_f(__builtin_fmaf(s,pr[3],pr[1]));
      float sy=yb+pr[2];
      float o=__builtin_fmaf(sy,gq,pr[0]);
      asm volatile("global_store_short %0, %1, off sc1" :: "v"(op),"v"(pk(o,o)) : "memory");
      }
    }
  } else
  {
    FLANE(D_*2)
    u32x8 fr[12]; u32x4 fl[3];
    rsrc_t rsP=RSRC(ws+W_PART+cgp*12288+kq*256+w4*64,8*9*4096-2*12288);
    #pragma unroll
    for(int i=0;i<3;i++) BST128(I4,rsP,li<8? li*(9*4096)+g*16+i*4096 : OOR,0,SC1);
    if(PRE2<9) FILLD(9-PRE2,W2_,W2B);
    wso+=128u-9u*wst;
    FILLD(9,W2_,W2B);
    VMW(9); gsync(sm,C_P,4);
    #pragma unroll
    for(int s=0;s<4;s++) fr[s]=WF2(0,s);
    fl[0]=WFR(0,8);
    gsync(sm,C_P,8); if(w4==0&&ln==0) GST(ws+W_FL+(720+6*xg+lb-8)*8,tag);
    wso+=128u-9u*wst; wld-=2*CHSZ;
    int fo=W_FL+(576+((9*kq)&~1)+(ln<9? ln:9))*8;
    FILL(9,VW(W2C));
    if(w4==0) asm volatile("global_load_dwordx2 v[112:113], %0, %1 sc1\n\ts_waitcnt vmcnt(10)" :: "v"(fo),"s"(ws) : "memory"); else VMW(9);
    gsync(sm,C_P,12);
    #pragma unroll
    for(int s=0;s<4;s++) fr[4+s]=WF2(1,s);
    fl[1]=WFR(1,8);
    { int t2=TID; asm volatile("" : "+v"(t2)); ln=t2&63; li=ln&15; g=ln>>4; }
    const lch* hp=sm+O_TILE+(li&7)*576+g*32+(li>>3)*16; int po=(4*(g&1)+2*(g>>1))*(9*4096)+li*4; u32 ix= (li&8)? 0xeeeeu:0x4444u; PIN(hp); PIN(po); PIN(ix);
    const lch* hq=hp+512-(g>>1)*32-(g&1)*24; PIN(hq);
    u32x4 rs=mkrs(ws+W_GU,2*8*F_*2);
    int of=((ln>>5)*288+18*kq+2*w4)*256+(ln&31)*16; lch* hd=sm+O_TILE+((ln&4)+((ln>>2)&2))*576+w4*64+(ln&16)*2+(ln&3)*8+(ln>>5)*4; PIN(of); PIN(hd); asm volatile("" : "+s"(rs));
    u32x4 rp=mkrs(ws+W_PART+cgp*12288+kq*256+w4*64,8*9*4096-2*12288); int po1=po+9*4096; u32 nvm=0xfffeffffu; asm volatile("" : AC0,AC1,AC2,"+v"(po1),"+s"(rp),"+s"(nvm));
    DRAIN();
    if(w4==0){ u64 bad; asm volatile("v_cmp_ne_u64 %0, %1, v[112:113]" : "=s"(bad) : "s"(tag) : "memory");
      if(bad!=0ull) poll1<0>(ws+fo,tag); }
    gsync(sm,C_P,16);
    { u32 t0,cnt;
      #define LD_G "buffer_load_dwordx4 v[112:115], %[of], %[rs], 0 offen sc1\n\tbuffer_load_dwordx4 v[116:119], %[of], %[rs], 0 offen offset:2048 sc1\n\tbuffer_load_dwordx4 v[120:123], %[of], %[rs], %[so] offen sc1\n\t"
      #define HM1(t,a,b) "v_lshlrev_b32 " t ", 16, v" #a "\n\tv_lshlrev_b32 %[t1], 16, v" #b "\n\tv_and_b32 v" #a ", %[hm], v" #a "\n\tv_and_b32 v" #b ", %[hm], v" #b "\n\tv_mul_f32 " t ", " t ", %[t1]\n\tv_mul_f32 v" #a ", v" #a ", v" #b "\n\t"
      #define HMUL(a,b,c,d,o) "v_permlane32_swap_b32 v" #a ", v" #c "\n\tv_permlane32_swap_b32 v" #b ", v" #d "\n\t" HM1("%[t0]",a,c) HM1("v" #c,b,d) "v_cvt_pk_bf16_f32 %[t0], %[t0], v" #c "\n\tv_cvt_pk_bf16_f32 v" #a ", v" #a ", v" #b "\n\tds_write2_b32 %[hd], %[t0], v" #a " offset0:" #o " offset1:" #o "+144\n\t"
      u32 t1;
      asm volatile(LD_G :: [of]"v"(of),[rs]"s"(rs),[so]"s"(w4==0? 4096:0) : "memory");
      fr[8]=WF2(0,0); PIN(fr[8]); fl[2]=WFR(0,8); PIN(fl[2]);
      asm volatile(POLLASM(LD_G,CHK_I0 MX2(116,117) MX2(118,119) MX2(120,121) MX2(122,123) CHK_IE,GSLP)
        HMUL(112,113,114,115,0) HMUL(116,117,118,119,64) "s_cmp_eq_u32 %[so], 0\n\ts_cbranch_scc1 8f\n\tv_add_u32 %[hd], 512, %[hd]\n\t" HMUL(120,121,122,123,0) "8:\n\ts_mov_b64 exec, 1\n\tds_add_u32 %[ch], %[one]\n\ts_mov_b64 exec, -1"
        : [t0]"=&v"(t0),[t1]"=&v"(t1),[cnt]"=&s"(cnt),[hd]"+v"(hd) : [of]"v"(of),[rs]"s"(rs),[so]"s"(w4==0? 4096:0),[hm]"s"(0xffff0000u),[ch]"v"(C_H*4),[one]"v"(1),"v"(hp),"v"(po) : PCLOB);
      }
    {
      GW4(C_H,hp)
      u32x2 h8=LD(u32x2,hq); SCHED(); LGW(1)
      #define SM3(i,b) asm volatile("v_smfmac_f32_16x16x32_bf16 %0, %1, %2, %3" : AC##i : "v"(h8),"v"(b),"v"(ix) : "memory"); SCHED();
      #define PST(a,b,c,d,o) "v_permlane32_swap_b32 v" #a ", v" #c "\n\tv_permlane32_swap_b32 v" #b ", v" #d "\n\tv_add_f32 v" #a ", v" #a ", v" #c "\n\tv_add_f32 v" #b ", v" #b ", v" #d "\n\tv_min_u32 v" #a ", %[nv], v" #a "\n\tv_min_u32 v" #b ", %[nv], v" #b "\n\t" \
        "buffer_store_dword v" #a ", %[p0], %[rp], " o " offen sc1\n\tbuffer_store_dword v" #b ", %[p1], %[rp], " o " offen sc1\n\t"
      SMR(0,112,fr[0]) fr[9]=WF2(0,1); SCHED(); SMR(1,112,fr[4]) fr[10]=WF2(0,2); SCHED(); SMR(2,112,fr[8]) fr[11]=WF2(0,3); SCHED();
      SMR(0,116,fr[1]) SMR(1,116,fr[5]) SMR(0,120,fr[2]) SM3(2,fl[2]) SMR(0,124,fr[3]) SMR(1,120,fr[6]) SM3(0,fl[0]) SMR(1,124,fr[7]) SMR(2,116,fr[9]) SM3(1,fl[1]) SMR(2,120,fr[10]) SMR(2,124,fr[11])
      asm volatile("s_nop 2\n\t" PST(100,101,102,103,"0") PST(104,105,106,107,"%[o1]") PST(108,109,110,111,"%[o2]") : AC0,AC1,AC2 : [p0]"v"(po),[p1]"v"(po1),[rp]"s"(rp),[nv]"s"(nvm),[o1]"s"(4096),[o2]"s"(8192) : "memory"); }

  }
  asm volatile("s_endpgm" : KILLIN :: "memory"); }
  {
  if(__builtin_expect(q4!=0,0)){ int grp= q4==1? nch-1 : q4-2;
    chan(sm,grp,(w4+(0x48>>(2*q4)))&3,72*xg+(q4==3? 16+lb : 2*lb+(lb<8? 0:8)+2-q4),(q4+1)>>2,ws,xa,xb_,wa,wb,dq[0]);
    asm volatile("s_endpgm" : KILLIN :: "memory"); }
  u32x4 xq[3]; f32x2 rc[3]; int xr_,xr2_;
  {
    LANE(ln); int g=ln>>4, li=ln&15, qq=li>>2, pq=li&3, j=ln&31, rl=2*w4+(ln>>5);
    static_assert(TL==1&&O_RV==128&&O_MD==400&&TSTR2==1216,"prologue");
    u32 Tr=la[3], one=0x3f803f80u; int uo=la[0], uo1=la[1], uo2=la[2], go=lc[0], xr=0, xr2=0;
    lch *tw=LP(lb_[0]), *tw2=LP(lb_[1])+1024, *kw=LP(lb_[3]); const lch *tp=LP(lb_[2]), *kr=LP(lc[1]);
    rsrc_t rx=RSRC(x,L_*D_*2); u64 gm=((1ull<<(4*NRG))-1ull)|0xff00000000ull; u32 um=~UMASK, km=0x80008000u; asm volatile("" : "+s"(gm),"+s"(um),"+s"(km),"+s"(ws));
    #define D2(a,b,c) __builtin_amdgcn_fdot2_f32_bf16(BC(bf2,a),BC(bf2,b),c,false)
    asm volatile("s_waitcnt vmcnt(5)" : "+v"(wa),"+v"(wb),"+v"(wc),"+v"(one)
      :: "memory");
    u32 wq[9]={wa[0],wa[1],wa[2],wa[3],wb[0],wb[1],wb[2],wb[3],wc}; float sw;
    { float a[3];
      #pragma unroll
      for(int i=0;i<9;i++) a[i%3]=D2(wq[i],one,i<3? 0.f:a[i%3]);
      sw=add16x(sum16((a[0]+a[1])+a[2])); }
    GETEP u32 tg=(u32)ep+1u; Tr=((upat(tg,w4)>>Tr)&0xffffu)*0x10001u; u32 tgv=tg;
    float z_[9]={0,0,0,0,0,0,0,0,0};
    asm volatile("s_waitcnt vmcnt(2)" : "+v"(xa),"+v"(xb_),"+v"(xc),"+v"(Tr),"+v"(sw),"+v"(tgv),"+v"(z_[0]),"+v"(z_[1]),"+v"(z_[2]),"+v"(z_[3]),"+v"(z_[4]),"+v"(z_[5]) :: "memory");
    u32 v[9]={xa[0],xa[1],xa[2],xa[3],xb_[0],xb_[1],xb_[2],xb_[3],xc};
    { float xf[18], s1, s2, s3;
      #pragma unroll
      for(int i=0;i<9;i++){ z_[i&1]=D2(v[i],one,z_[i&1]); z_[2+(i&1)]=D2(v[i],v[i],z_[2+(i&1)]); z_[4+(i&1)]=D2(v[i],wq[i],z_[4+(i&1)]); }
      s1=z_[0]+z_[1]; s2=z_[2]+z_[3]; s3=z_[4]+z_[5];
      SCHED();
      #define RSTEP(C) s1+=dppf<C>(s1); s2+=dppf<C>(s2); s3+=dppf<C>(s3); SCHED();
      RSTEP(0xB1) RSTEP(0x4E) RSTEP(0x141) RSTEP(0x140)
      s1=add16x(s1)*(1.f/576.f); s2=add16x(s2)*(1.f/576.f); s3=add16x(s3);
      float mm=s1*s1, var=s2-mm;
      if(__builtin_expect(BALLOT(!(mm<=64.f*var))!=0ull,0)){
        float q=0.f;
        #pragma unroll
        for(int i=0;i<9;i++){ u32 t=v[i]; PIN(t); float d0=blo(t)-s1, d1=bhi(t)-s1; q=__builtin_fmaf(d0,d0,q); q=__builtin_fmaf(d1,d1,q); }
        var=add16x(sum16(q))*(1.f/576.f); }
      #define UNP(i) PIN(v[i]); xf[2*(i)]=blo(v[i]); xf[2*(i)+1]=bhi(v[i]); PIN(xf[2*(i)]); PIN(xf[2*(i)+1]); SCHED();
      u32 vb=pk(var,s1); UNP(0)
      float den=blo(vb)+1e-5f, m=bhi(vb); UNP(1)
      vb=pk(den,den); s3=__builtin_fmaf(-m,sw,s3); UNP(2)
      den=__builtin_amdgcn_sqrtf(bhi(vb)); UNP(3)
      u32 md=pk(m,den); UNP(4)
      float ri=rcpf_(bhi(md)); UNP(5)
      s3*=ri; float rs_=ri*0.0625f; UNP(6)
      u32 u=pk(s3,s3); float cs_=-m*rs_; UNP(7)
      { u32 sg=(u32)((int)u>>31); PIN(sg); u^=sg|km; } UNP(8)
      asm volatile("s_mov_b64 exec, 0x1\n\ts_mov_b32 exec_hi, 1\n\tds_write_b16 %0, %1\n\tv_add_u32 %0, %0, %0\n\tds_write_b32 %0, %2 offset:%3\n\ts_mov_b32 exec_hi, 0\n\tds_add_u32 %4, %5\n\ts_mov_b64 exec, -1" : "+v"(kw) : "v"(u),"v"(md),"i"(O_MD-2*O_RV),"v"(C_I*4),"v"(1) : "memory");
      if(w4==3){ gwait<0>(sm,C_I,4); asm volatile("ds_read_b32 v118, %4\n\tv_mov_b32 v119, %1\n\ts_mov_b64 exec, %3\n\ts_waitcnt lgkmcnt(0)\n\tglobal_store_dwordx2 %0, v[118:119], %2 sc1\n\ts_mov_b64 exec, -1" :: "v"(go),"v"(tgv),"s"(ws),"s"(gm),"v"(kr) : "memory"); }
      SCHED();
      u32 h[9];
      #define UH(i) h[i]=(pkz(__builtin_fmaf(xf[2*(i)],rs_,cs_),__builtin_fmaf(xf[2*(i)+1],rs_,cs_))&um)|Tr;
      UH(0) UH(1) UH(2) UH(3) ST(u32x4,tw)=u32x4{h[0],h[1],h[2],h[3]}; UH(4) UH(5) UH(6) UH(7) ST(u32x4,tw+512)=u32x4{h[4],h[5],h[6],h[7]};
      UH(8) ST(u32,tw2)=h[8]; }
    gsync(sm,C_L,4);
    { u32x4 uq[3];
      #pragma unroll
      for(int z=0;z<3;z++) uq[z]=BC(u32x4,trfrag(tp+z*512,tp+z*512+4*TSTR2));
      asm volatile("global_store_dwordx4 %0, %1, %2 sc1" :: "v"(uo),"v"(uq[0]),"s"(ws) : "memory");
      asm volatile("global_store_dwordx4 %0, %1, %2 sc1" :: "v"(uo1),"v"(uq[1]),"s"(ws) : "memory");
      asm volatile("s_cmp_lg_u32 %3, 0\n\ts_cbranch_scc1 1f\n\tglobal_store_dwordx4 %0, %1, %2 sc1\n1:\n\ts_nop 1" :: "v"(uo2),"v"(uq[2]),"s"(ws),"s"(w4),"v"(uq[0]),"v"(uq[1]) : "memory","scc");
      { LANE(l2); xr=(32*(tb>>2)+(l2>>2))*(D_*2)+256*(lb&3)+64*w4+(l2&3)*16; xr2= w4==0? xr+(lb&2)*(8*D_*2)+1024+64*(lb&1)-256*(lb&3):OOR; }
      asm volatile("s_waitcnt vmcnt(3)\n\ts_bitcmp1_b32 9, %2\n\ts_cbranch_scc1 1f\n\ts_waitcnt vmcnt(2)\n1:" : "+v"(dq[0]),"+v"(dq[1]) : "s"(w4) : "memory","scc");
      xr_=xr; xr2_=xr2;
    }
    if(lb<8){
      const u16* wm=(lb&1)? wu:wg; int co=(3-(int)((u64)wm>>7)-xg)&7, r_=576-16*PFN+((lb>>1)*4+w4)+16*j;
      asm volatile("buffer_load_dword v120, %0, %1, 0 offen" :: "v"(j<PFN&&(ln<32||co==0)? r_*(F_*2)+(9*xg+co)*128+(ln>>5)*1024 : OOR),"s"(mkrs(wm,D_*F_*2)) : "memory"); }
    if(lb>=8&&lb<14) garrive(sm,C_D);
  }
  FP fp; FA fa; u32 ti,td; float ds[16]; int ye=0;
  { LANE(ln); u64 tag=ep+1ull;
    int so_=W_ST+(32*(tb>>2)+(ln>>2))*8, fo_=W_FL+((ln>=32&&w4==0? 512+32*(lb&1) : 128*(lb&3)+32*w4)+(ln&31))*8; u32x4 rx=mkrs(x,L_*D_*2); PIN(so_); PIN(fo_);
    XR X; X.so=so_; X.x0=xr_; X.x2=xr2_; X.rx=rx;
    bool ix= w4==3&&lb<14; int ho_, pq_;
    auto prep=[&]() NOPK __attribute__((always_inline)) {
      final_addr(fa,sm,w4,(u32)tag);
      #pragma unroll
      for(int i=0;i<8;i++) if(i<4||w4==0){ ds[2*i]=blo(dq[i>>2][i&3])*Y96; ds[2*i+1]=bhi(dq[i>>2][i&3])*Y96; PIN(ds[2*i]); PIN(ds[2*i+1]); }
      ho_=fo_+(W_HT-W_FL); pq_= ix? W_IDX+(xg*8+(lb&3))*128+(ln&7)*8 : fo_; PIN(ho_); PIN(pq_); };
    if(lb>=14){
      u32 M=(u32)tag, rank,key; int j=ln&31, j16=j*16, j12=j*12, st0=W_ST; lch* xw=sm+O_TILE+(2*w4+(ln>>5))*XSTR+j*16; lch* rcw=sm+O_RC+(2*w4+(ln>>5))*4; PIN(rcw); PIN(j12); PIN(xw); PIN(j16); asm volatile("" : "+s"(rx),"+s"(st0));
      top8(sm,w4,ln,M,ws,rank,key,prep);
      {
        int t0=__builtin_amdgcn_readlane((int)key,__builtin_ctzll(BALLOT(rank==(u32)(2*w4)))), t1=__builtin_amdgcn_readlane((int)key,__builtin_ctzll(BALLOT(rank==(u32)(2*w4+1))));
        int tsel=~((ln>>5)? t1:t0)&2047, ro=tsel*1152+j16, sr=tsel*8+st0;
        u32x4 a,b; u32 c; u64 f1;
        asm volatile("buffer_load_dwordx4 %[a], %[ro], %[rx], 0 offen\n\tbuffer_load_dwordx4 %[b], %[ro], %[rx], 0 offen offset:512\n\tbuffer_load_dword %[c], %[r2], %[rx], 0 offen offset:1024\n\tglobal_load_dwordx2 %[f], %[sr], %[ws] sc1\n\t"
          "s_mov_b64 exec, 1\n\tds_add_u32 %[cd], %[one]\n\ts_mov_b64 exec, -1\n\ts_waitcnt vmcnt(0)"
          : [a]"=&v"(a),[b]"=&v"(b),[c]"=&v"(c),[f]"=&v"(f1) : [ro]"v"(ro),[r2]"v"(ro-j12),[sr]"v"(sr),[cd]"v"(C_D*4),[one]"v"(1),[rx]"s"(rx),[ws]"s"(ws) : "memory");
        if(__builtin_expect(BALLOT((u32)(f1>>32)!=(u32)tag)!=0ull,0)){ f1=poll1<32>(ws+sr,tag<<32); __builtin_amdgcn_s_waitcnt(0x0f70); }
        ST(u32x4,xw)=a; ST(u32x4,xw+512)=b; ST(u32,xw+1024-j12)=c; ST(u32,rcw)=(u32)f1;
        garrive(sm,C_X);
        if(w4==0){ if(rank<8) ST(u16,sm+O_IDX+(ln&1)*16+rank*2)=(u16)((ln&1)? key>>16 : ~key&2047u); garrive(sm,C_IX); }
        if(lb==14&&rank<8&&!(ln&1)) GST(ws+W_IDX+xg*1024+w4*128+rank*8,(tag<<16)|(~key&2047u));
      }
      PRIO(0); xreq(X,ws);
    } else { PRIO(0); prep(); gwait<8>(sm,C_U,2); xreq(X,ws); }
    { u32 e=now_()-ldw, nm= lb<8? ANA : (lb<14? ANC:ANB); if(e>nm) ldw+=e-nm; }
    { u64 fs,fs1;
      asm volatile("s_waitcnt vmcnt(0)" : "+{v[96:99]}"(X.q[0]),"+{v[100:103]}"(X.q[1]),"+{v[104:107]}"(X.q[2]),"+{v[108:111]}"(X.q[3]) :: "memory");
      fs=(u64)X.q[3][0]|((u64)X.q[3][1]<<32); fs1=(u64)X.q[3][2]|((u64)X.q[3][3]<<32);
      if(__builtin_expect(BALLOT((u32)(fs>>32)!=(u32)tag||(u32)(fs1>>32)!=(u32)tag)!=0ull,0)){ fs=poll1<32>(ws+so_,tag<<32); fs1=poll1<32>(ws+so_+128,tag<<32); __builtin_amdgcn_s_waitcnt(0x0f70); }
      float ri=rcpf_(bhi((u32)fs)), rj=rcpf_(bhi((u32)fs1)); rc[0]=f32x2{ri,-blo((u32)fs)*ri}; rc[1]=f32x2{rj,-blo((u32)fs1)*rj}; rc[2]= (lb&2)? rc[1]:rc[0];
      #pragma unroll
      for(int z=0;z<3;z++) xq[z]=X.q[z]; }
    final_pre<2,3>(fp,w4,xq,ds,rc);
    ti=now_()-ldw;
    asm volatile("global_load_dwordx2 v[112:113], %0, %2 sc1\n\tglobal_load_dwordx2 v[114:115], %1, %2 sc1\n\tv_mov_b32 v117, %5\n\ts_cmp_lt_u32 %4, " STR(HT) "\n\ts_cbranch_scc1 1f\n\tglobal_load_dwordx2 v[116:117], %3, %2 sc1\n1:"
      :: "v"(fo_),"v"(pq_),"s"(ws),"v"(ho_),"s"(ti),"s"(~(u32)tag) : "memory","scc");
    final_pre<0,1>(fp,w4,xq,ds,rc);
    { u64 ff,f; u32x2 h;
      asm volatile("s_waitcnt vmcnt(0)\n\tv_mov_b64 %0, v[112:113]\n\tv_mov_b64 %1, v[114:115]\n\tv_mov_b64 %2, v[116:117]" : "=v"(ff),"=v"(f),"=v"(h) :: "memory");
      _Pragma("unroll 1") for(int k=0;k<2;k++){ if(BALLOT(h[1]!=(u32)tag)==0ull||ti>=H2) break;
        tgate2(ldw,ti<HT? HT:H2); ti=now_()-ldw; if(ti>=HL){ h=u32x2{ldw,(u32)tag}; break; }
        asm volatile("global_load_dwordx2 %0, %1, %2 sc1\n\ts_waitcnt vmcnt(0)" : "=&v"(h) : "v"(ho_),"s"(ws) : "memory"); }
      u32 rel= h[1]==(u32)tag? h[0]-ldw : ti+HM; td=umin_(wmax(rel),ti+160u);
      if(td>HK) td-=(td-HK)>>1;
      if(__builtin_expect(BALLOT(ff!=(tag<<16))!=0ull,0)) ye=(short)poll1<16>(ws+fo_,tag<<16);
      if(ix){ if(BALLOT(((f^(tag<<16))>>16)!=0ull)!=0ull) f=poll1<16>(ws+pq_,tag<<16);
        if(ln<8) ST(u16,sm+O_IDX+ln*2)=(u16)f; garrive(sm,C_IX); }
      __builtin_amdgcn_s_waitcnt(0x0f70); }
  }
  final_post(sm,fp,fa,w4,out,ws,xr_,ldw,td,xq,ds,rc,ye);
  }
}

extern "C" void kernel_launch(void* const* d_in, const int* in_sizes, int n_in, void* d_out, int out_size, void* d_ws, size_t ws_size, hipStream_t stream) {
  const u16** I=(const u16**)d_in;
  KP kp; kp.Are=I[1]; kp.Aim=I[2]; kp.Bre=I[3]; kp.Bim=I[4]; kp.Cre=I[5]; kp.Cim=I[6]; kp.ldt=I[7]; kp.pad=I[7];
  kA<<<256,1024,0,stream>>>(I[0],(char*)d_ws,I[10],I[8],I[11],I[12],I[13],(u16*)d_out,kp);
}
```
